```python
import torch

def _softmax(x, axis=-1):
    return torch.softmax(x, dim=axis)

def _gelu(x):
    return torch.nn.functional.gelu(x, approximate='tanh')

def _silu(x):
    return torch.nn.functional.silu(x)

def _sigmoid(x):
    return torch.sigmoid(x)

def _layernorm(x, axis=-1, eps=1e-05):
    m = x.mean(dim=axis, keepdim=True)
    v = x.var(dim=axis, keepdim=True, unbiased=False)
    return (x - m) / torch.sqrt(v + eps)

def _scatter_add(tgt, idx, src, axis=0):
    return tgt.clone().index_add_(axis, idx, src)

def _segment_sum(src, seg, n):
    out = torch.zeros((n,) + src.shape[1:], dtype=src.dtype)
    return out.index_add_(0, seg, src)

def _pad(x, pw):
    flat = tuple((v for pair in reversed(pw) for v in pair))
    return torch.nn.functional.pad(x, flat)
kernel_name = 'adaLN_cond__sliding_block_cached__mixer_s_mlp__prompt'

def setup_inputs():

    _w = lambda a: (lambda t: t.to(torch.bfloat16) if t.is_floating_point() else t)(torch.as_tensor(a))

    def _A_setup():
        B, Sq, D = (1, 2048, 512)
        Hq, Hkv, Sk, Dh = (32, 8, 2048, 128)
        assert Hq % Hkv == 0, (Hq, Hkv)
        return {'x': _w((lambda *s: torch.randn(*s, dtype=torch.bfloat16) * 0.05)(B, Sq, D)), 'k_cache': _w((lambda *s: torch.randn(*s, dtype=torch.bfloat16) * 3.2)(B, Hkv, Sk, Dh)), 'v_cache': _w((lambda *s: torch.randn(*s, dtype=torch.bfloat16) * 0.4)(B, Hkv, Sk, Dh)), 'wq': _w((lambda *s: torch.randn(*s, dtype=torch.bfloat16) * 3.2)(D, Hq * Dh) * D ** (-0.5)), 'wo': _w((lambda *s: torch.randn(*s, dtype=torch.bfloat16) * 0.4)(Hq * Dh, D) * (Hq * Dh) ** (-0.5))}
    _A_in = _A_setup()

    def _B_setup():
        B, S, D, F = (1, 2048, 512, 1792)
        return {'x': _w((lambda *s: torch.randn(*s, dtype=torch.bfloat16) * 0.05)(B, S, D)), 'w1': _w((lambda *s: torch.randn(*s, dtype=torch.bfloat16) * 3.2)(D, D) * D ** (-0.5)), 'w2': _w((lambda *s: torch.randn(*s, dtype=torch.bfloat16) * 3.2)(D, D) * D ** (-0.5)), 'w3': _w((lambda *s: torch.randn(*s, dtype=torch.bfloat16) * 3.2)(D, F) * D ** (-0.5)), 'w4': _w((lambda *s: torch.randn(*s, dtype=torch.bfloat16) * 3.2)(F, D) * F ** (-0.5))}
    _B_in = _B_setup()
    return {'A_x': _A_in['x'], 'A_k_cache': _A_in['k_cache'], 'A_v_cache': _A_in['v_cache'], 'A_wq': _A_in['wq'], 'A_wo': _A_in['wo'], 'B_x': _B_in['x'], 'B_w1': _B_in['w1'], 'B_w2': _B_in['w2'], 'B_w3': _B_in['w3'], 'B_w4': _B_in['w4'], 'c': _w((lambda *s: torch.randn(*s, dtype=torch.bfloat16) * 0.1)(1, 512) * 8.0), 'w_mod': _w((lambda *s: torch.randn(*s, dtype=torch.bfloat16) * 0.1)(512, 6 * 512) * 512 ** (-0.5) * 8.0)}

def reference(A_x, A_k_cache, A_v_cache, A_wq, A_wo, B_x, B_w1, B_w2, B_w3, B_w4, c, w_mod):

    def _A_ref(x, k_cache, v_cache, wq, wo):
        B, Sq, D = x.shape
        Hq, Dh = (32, 128)
        Hkv, Sk = (k_cache.shape[1], k_cache.shape[2])
        W = min(4096, max(256, Sk // 4))
        rep = Hq // Hkv
        q = (lambda x, *d: x.permute(*d))((x @ wq).reshape(B, Sq, Hq, Dh), 0, 2, 1, 3)
        k = (lambda x, n, axis: torch.repeat_interleave(x, n, dim=axis))(k_cache[:, :, Sk - W:, :], rep, axis=1)
        v = (lambda x, n, axis: torch.repeat_interleave(x, n, dim=axis))(v_cache[:, :, Sk - W:, :], rep, axis=1)
        scores = torch.einsum('bhqd,bhkd->bhqk', q, k) * Dh ** (-0.5)
        p = _softmax(scores, axis=-1)
        o = torch.einsum('bhqk,bhkd->bhqd', p, v)
        return (lambda x, *d: x.permute(*d))(o, 0, 2, 1, 3).reshape(B, Sq, Hq * Dh) @ wo

    def _B_ref(x, w1, w2, w3, w4):
        B, S, D = x.shape
        h = _gelu(_layernorm(x, axis=-1) @ w1)
        c = D // 4
        g0 = _pad(h[:, :, 0 * c:1 * c], ((0, 0), (1, 0), (0, 0)))[:, :S, :]
        g1 = _pad(h[:, :, 1 * c:2 * c], ((0, 0), (0, 1), (0, 0)))[:, 1:, :]
        g2 = _pad(h[:, :, 2 * c:3 * c], ((0, 0), (2, 0), (0, 0)))[:, :S, :]
        g3 = _pad(h[:, :, 3 * c:], ((0, 0), (0, 2), (0, 0)))[:, 2:, :]
        hs = torch.concatenate([g0, g1, g2, g3], axis=-1) @ w2
        x1 = x + hs
        h2 = _gelu(_layernorm(x1, axis=-1) @ w3) @ w4
        return x1 + h2
    x = A_x
    mod = (_silu(c) @ w_mod).reshape(c.shape[0], 6, 1, -1)
    sa, ca, ga, sb, cb, gb = (mod[:, 0].clone(), mod[:, 1].clone(), mod[:, 2].clone(), mod[:, 3].clone(), mod[:, 4].clone(), mod[:, 5].clone())
    h = _layernorm(x, axis=-1) * (1 + ca) + sa
    x = x + ga * _A_ref(h, A_k_cache, A_v_cache, A_wq, A_wo)
    h = _layernorm(x, axis=-1) * (1 + cb) + sb
    x = x + gb * _B_ref(h, B_w1, B_w2, B_w3, B_w4)
    return x
```

```cpp
#include <hip/hip_runtime.h>
#include <hip/hip_bf16.h>
#include <stdint.h>

typedef short bf16x8 __attribute__((ext_vector_type(8)));
typedef short s16x4 __attribute__((ext_vector_type(4)));
typedef float f32x16 __attribute__((ext_vector_type(16)));
typedef float f32x4 __attribute__((ext_vector_type(4)));
typedef float f32x2 __attribute__((ext_vector_type(2)));
typedef unsigned u32x4 __attribute__((ext_vector_type(4)));
typedef unsigned u32x2 __attribute__((ext_vector_type(2)));
typedef __bf16 bf16x2_t __attribute__((ext_vector_type(2)));
typedef short v4i16_t __attribute__((ext_vector_type(4)));
#define LAS __attribute__((address_space(3)))
typedef LAS const char* lds_cptr;
typedef LAS char* lds_ptr;
#define DI __device__ __forceinline__
#define SBAR() __builtin_amdgcn_sched_barrier(0)
#define MFMA32(a, b, c) __builtin_amdgcn_mfma_f32_32x32x16_bf16((a), (b), (c), 0, 0, 0)
#define WAIT_V(n) asm volatile("s_waitcnt vmcnt(" #n ")" ::: "memory")
#define WAIT_L0() asm volatile("s_waitcnt lgkmcnt(0)" ::: "memory")
#define BAR_L() asm volatile("s_waitcnt lgkmcnt(0)\n\ts_barrier" ::: "memory")
#define WAIT_BAR(n) asm volatile("s_waitcnt vmcnt(" #n ") lgkmcnt(0)\n\ts_barrier" ::: "memory")

constexpr int T = 2048, D = 512, HKV = 8, SK = 2048, DH = 128, WIN = 512, F = 1792;
constexpr int H1P = T + 4;
constexpr float LN_EPS = 1e-5f;
constexpr float QSCALE = 0.088388347648318440f * 1.4426950408889634f;

constexpr size_t WS_MOD = 0;
constexpr size_t WS_P1 = 16384;
constexpr size_t WS_X2 = WS_P1 + (size_t)8 * 64 * T * 16;
constexpr size_t WS_HB = WS_X2 + (size_t)64 * T * 32;
constexpr size_t WS_U = WS_HB + (size_t)64 * T * 32;
constexpr size_t WS_H1 = WS_U + (size_t)64 * T * 16;
constexpr size_t WS_X1 = WS_H1 + (size_t)64 * H1P * 16;
constexpr size_t WS_P2 = WS_X1 + (size_t)64 * T * 32;
constexpr size_t WS_END = WS_P2 + (size_t)8 * 64 * T * 16;

constexpr int LDS_X = 0;
constexpr int LDS_R = 65536;
constexpr int RING = 5, RINGB = RING * 2048;
constexpr int LDS_D = LDS_R + 8 * RINGB;
constexpr int LDS_TOTAL = LDS_D + 16384;
static_assert(LDS_TOTAL == 163840, "lds");

DI unsigned cvtpk(float lo, float hi) {
  f32x2 v = {lo, hi};
  bf16x2_t b = __builtin_convertvector(v, bf16x2_t);
  return __builtin_bit_cast(unsigned, b);
}
DI float bflo(unsigned u) { return __uint_as_float(u << 16); }
DI float bfhi(unsigned u) { return __uint_as_float(u & 0xffff0000u); }
DI unsigned rfl(unsigned x) { return __builtin_amdgcn_readfirstlane(x); }
DI __amdgpu_buffer_rsrc_t mksrd(const void* p, unsigned bytes) {
  return __builtin_amdgcn_make_buffer_rsrc((void*)p, (short)0, (int)bytes, 0x00020000);
}
DI void dma16(unsigned lds, unsigned voff, __amdgpu_buffer_rsrc_t srd, unsigned soff) {
  asm volatile("s_mov_b32 m0, %0\n\ts_nop 0\n\tbuffer_load_dwordx4 %1, %2, %3 offen lds"
               :: "s"(lds), "v"(voff), "s"(srd), "s"(soff) : "memory");
}
DI bf16x8 lds_b128(lds_cptr p) { return *(const LAS bf16x8*)p; }
DI s16x4 lds_tr(lds_cptr p) {
  return __builtin_bit_cast(s16x4, __builtin_amdgcn_ds_read_tr16_b64_v4i16((LAS v4i16_t*)p));
}
DI bf16x8 cat8(s16x4 lo, s16x4 hi) { return __builtin_shufflevector(lo, hi, 0, 1, 2, 3, 4, 5, 6, 7); }
DI float swap32_add(float x) {
  auto r = __builtin_amdgcn_permlane32_swap(__float_as_uint(x), __float_as_uint(x), false, false);
  return __uint_as_float(r[0]) + __uint_as_float(r[1]);
}
DI float swap32_max(float x) {
  auto r = __builtin_amdgcn_permlane32_swap(__float_as_uint(x), __float_as_uint(x), false, false);
  return __builtin_fmaxf(__uint_as_float(r[0]), __uint_as_float(r[1]));
}
template <int CTRL> DI float dpp_add(float x) {
  int y = __builtin_amdgcn_update_dpp(0, __float_as_int(x), CTRL, 0xF, 0xF, true);
  return x + __int_as_float(y);
}
DI float gelu_tanh(float x) {
  const float k0 = -2.0f * 0.7978845608028654f * 1.4426950408889634f;
  const float t = x * x;
  const float z = x * __builtin_fmaf(t, 0.044715f * k0, k0);
  const float e = __builtin_amdgcn_exp2f(z);
  return x * __builtin_amdgcn_rcpf(1.0f + e);
}
DI void wave_sum8(const float (&a)[8], float (&out)[8]) {
  float b[4], c[2];
#pragma unroll
  for (int i = 0; i < 4; ++i) {
    auto r = __builtin_amdgcn_permlane32_swap(__float_as_uint(a[i]), __float_as_uint(a[i + 4]), false, false);
    b[i] = __uint_as_float(r[0]) + __uint_as_float(r[1]);
  }
#pragma unroll
  for (int i = 0; i < 2; ++i) {
    auto r = __builtin_amdgcn_permlane16_swap(__float_as_uint(b[i]), __float_as_uint(b[i + 2]), false, false);
    c[i] = __uint_as_float(r[0]) + __uint_as_float(r[1]);
  }
#pragma unroll
  for (int i = 0; i < 2; ++i) {
    c[i] = dpp_add<0xB1>(c[i]);
    c[i] = dpp_add<0x4E>(c[i]);
    c[i] = dpp_add<0x141>(c[i]);
    c[i] = dpp_add<0x140>(c[i]);
  }
#pragma unroll
  for (int q = 0; q < 4; ++q)
#pragma unroll
    for (int i = 0; i < 2; ++i)
      out[i + 2 * (q & 1) + 4 * (q >> 1)] = __int_as_float(__builtin_amdgcn_readlane(__float_as_int(c[i]), 16 * q));
}

__global__ __launch_bounds__(256) void k0_mod(const unsigned short* __restrict__ c, const unsigned short* __restrict__ wmod,
                                              float* __restrict__ mod, u32x4* __restrict__ h1) {
  const int tid = threadIdx.x;
  if (blockIdx.x == 96) {
    const int ch = tid >> 2, i = tid & 3;
    const int row = i < 2 ? i : T + i;
    h1[(size_t)ch * H1P + row] = (u32x4){0u, 0u, 0u, 0u};
    return;
  }
  __shared__ float red[4][32];
  const int rg = tid >> 2, cc = tid & 3, n0 = blockIdx.x * 32 + cc * 8;
  u32x4 w[8];
  float s[8];
#pragma unroll
  for (int i = 0; i < 8; ++i) w[i] = *(const u32x4*)(wmod + (size_t)(rg + 64 * i) * 3072 + n0);
#pragma unroll
  for (int i = 0; i < 8; ++i) {
    const float v = __uint_as_float((unsigned)c[rg + 64 * i] << 16);
    s[i] = v / (1.0f + __expf(-v));
  }
  float acc[8] = {0, 0, 0, 0, 0, 0, 0, 0};
#pragma unroll
  for (int i = 0; i < 8; ++i)
#pragma unroll
    for (int j = 0; j < 4; ++j) {
      acc[2 * j] = __builtin_fmaf(s[i], bflo(w[i][j]), acc[2 * j]);
      acc[2 * j + 1] = __builtin_fmaf(s[i], bfhi(w[i][j]), acc[2 * j + 1]);
    }
#pragma unroll
  for (int j = 0; j < 8; ++j) {
    float v = acc[j];
    v += __shfl_xor(v, 4); v += __shfl_xor(v, 8); v += __shfl_xor(v, 16); v += __shfl_xor(v, 32);
    acc[j] = v;
  }
  const int lane = tid & 63, wid = tid >> 6;
  if (lane < 4) {
#pragma unroll
    for (int j = 0; j < 8; ++j) red[wid][lane * 8 + j] = acc[j];
  }
  __syncthreads();
  if (tid < 32) mod[blockIdx.x * 32 + tid] = red[0][tid] + red[1][tid] + red[2][tid] + red[3][tid];
}

struct LaneC {
  unsigned lane, r, h, q, p, blk;
  unsigned wv;
  unsigned afr[2];
  unsigned xb[4];
};
DI void lane_consts(LaneC& c) {
  const unsigned l = threadIdx.x & 63;
  c.lane = l; c.r = l & 31; c.h = l >> 5; c.q = (l & 15) >> 2; c.p = l & 3; c.blk = (l >> 4) & 1;
  const unsigned sp = 2 * (c.p & 1) + (c.p >> 1);
#pragma unroll
  for (int nt = 0; nt < 2; ++nt) c.afr[nt] = (8 * c.h + c.q) * 128 + 64 * (nt ^ (c.q >> 1)) + 32 * c.blk + 8 * sp;
#pragma unroll
  for (int s = 0; s < 4; ++s) c.xb[s] = 1024 * c.h + ((c.r ^ (2 * s + c.h)) << 4);
}
DI unsigned wdma_voff(unsigned l, unsigned ld) { return (l >> 3) * ld + (((l & 7) ^ (((l >> 4) & 1) << 2)) << 4); }

DI void wring_issue(int ks, unsigned ring, unsigned voff, __amdgpu_buffer_rsrc_t srd, unsigned soff0, unsigned ld) {
  const unsigned slot = ring + (unsigned)(ks % RING) * 2048u;
  dma16(slot, voff, srd, soff0 + (unsigned)(16 * ks) * ld);
  dma16(slot + 1024u, voff, srd, soff0 + (unsigned)(16 * ks + 8) * ld);
}
template <int N> DI void wait_vm() {
  if constexpr (N == 0) WAIT_V(0); else if constexpr (N == 1) WAIT_V(1); else if constexpr (N == 2) WAIT_V(2);
  else if constexpr (N == 3) WAIT_V(3); else if constexpr (N == 4) WAIT_V(4); else if constexpr (N == 5) WAIT_V(5);
  else if constexpr (N == 6) WAIT_V(6); else if constexpr (N == 7) WAIT_V(7); else if constexpr (N == 8) WAIT_V(8);
  else if constexpr (N == 9) WAIT_V(9); else if constexpr (N == 10) WAIT_V(10); else if constexpr (N == 11) WAIT_V(11);
  else if constexpr (N == 12) WAIT_V(12); else if constexpr (N == 13) WAIT_V(13); else if constexpr (N == 14) WAIT_V(14);
  else if constexpr (N == 15) WAIT_V(15); else if constexpr (N == 16) WAIT_V(16);
  else static_assert(N < 0, "wait_vm");
}

struct Frag { bf16x8 a[2], b[2]; };
template <int KS> DI void load_frags(Frag& f, lds_cptr lds, unsigned ringoff, const LaneC& c) {
  const lds_cptr sl = lds + ringoff + (KS % RING) * 2048;
#pragma unroll
  for (int nt = 0; nt < 2; ++nt) f.a[nt] = cat8(lds_tr(sl + c.afr[nt]), lds_tr(sl + c.afr[nt] + 512));
#pragma unroll
  for (int rt = 0; rt < 2; ++rt) f.b[rt] = lds_b128(lds + LDS_X + c.xb[KS & 3] + 2048 * KS + 512 * rt);
}
template <int NKS, int KS, int EXTRA>
DI void gemm_step(f32x16 (&acc)[2][2], Frag& cur, Frag& nxt, lds_cptr lds, unsigned ringoff, unsigned ring, unsigned voff,
                  __amdgpu_buffer_rsrc_t srd, unsigned soff0, unsigned ld, const LaneC& c) {
  if constexpr (KS + 1 < NKS) {
    constexpr int last = (KS + RING < NKS ? KS + RING : NKS) - 1;
    constexpr int after = 2 * (last - (KS + 1)) + ((KS + 1 < RING) ? EXTRA : 0);
    wait_vm<after>();
    load_frags<KS + 1>(nxt, lds, ringoff, c);
  }
  SBAR();
#pragma unroll
  for (int nt = 0; nt < 2; ++nt)
#pragma unroll
    for (int rt = 0; rt < 2; ++rt) acc[nt][rt] = MFMA32(cur.a[nt], cur.b[rt], acc[nt][rt]);
  if constexpr (KS + RING < NKS) {
    asm volatile("" :: "v"(cur.a[0]), "v"(cur.a[1]));
    wring_issue(KS + RING, ring, voff, srd, soff0, ld);
  }
  SBAR();
  if constexpr (KS + 1 < NKS) gemm_step<NKS, KS + 1, EXTRA>(acc, nxt, cur, lds, ringoff, ring, voff, srd, soff0, ld, c);
}
template <int NKS, int EXTRA>
DI void gemm_phase(f32x16 (&acc)[2][2], lds_cptr lds, unsigned ringoff, unsigned ring, unsigned voff,
                   __amdgpu_buffer_rsrc_t srd, unsigned soff0, unsigned ld, const LaneC& c) {
  Frag f0, f1;
  wait_vm<2 * (RING - 1) + EXTRA>();
  load_frags<0>(f0, lds, ringoff, c);
  gemm_step<NKS, 0, EXTRA>(acc, f0, f1, lds, ringoff, ring, voff, srd, soff0, ld, c);
}
DI u32x4 pack8(const f32x16& a, int s, float sc) {
  return (u32x4){cvtpk(a[8 * s] * sc, a[8 * s + 1] * sc), cvtpk(a[8 * s + 2] * sc, a[8 * s + 3] * sc),
                 cvtpk(a[8 * s + 4] * sc, a[8 * s + 5] * sc), cvtpk(a[8 * s + 6] * sc, a[8 * s + 7] * sc)};
}
DI unsigned ximg(unsigned chunk, unsigned row) { return chunk * 1024u + ((row ^ (chunk & 7u)) << 4); }

__global__ __launch_bounds__(512, 2) void k1_attn(const unsigned short* __restrict__ x, const unsigned short* __restrict__ kc,
                                                  const unsigned short* __restrict__ vc, const unsigned short* __restrict__ wq,
                                                  const unsigned short* __restrict__ wo, const float* __restrict__ mod,
                                                  u32x4* __restrict__ p1) {
  extern __shared__ __attribute__((aligned(16))) char smem[];
  const lds_ptr lds = (lds_ptr)smem;
  const unsigned lds0 = (unsigned)(uintptr_t)smem;
  const unsigned g = blockIdx.x & 7, pn = blockIdx.x >> 3;
  const unsigned w = rfl(threadIdx.x >> 6);
  LaneC c; lane_consts(c);
  const unsigned l = c.lane;
  const unsigned ringoff = LDS_R + w * RINGB, ring = lds0 + ringoff;

  const __amdgpu_buffer_rsrc_t srdQ = mksrd(wq, 512u * 4096u * 2u);
  const unsigned vq = wdma_voff(l, 8192u), sq0 = (g * 512u + w * 64u) * 2u;
#pragma unroll
  for (int ks = 0; ks < RING; ++ks) wring_issue(ks, ring, vq, srdQ, sq0, 8192u);
  const __amdgpu_buffer_rsrc_t srdK = mksrd(kc + ((size_t)g * SK + (SK - WIN)) * DH, WIN * DH * 2u);
  const __amdgpu_buffer_rsrc_t srdV = mksrd(vc + ((size_t)g * SK + (SK - WIN)) * DH, WIN * DH * 2u);
  const unsigned vk = ((l >> 2) & 7) * 256u + (l >> 5) * 64u + (((l & 3) ^ (2 * (w & 1) + ((l >> 4) & 1))) << 4);
  const unsigned vv = wdma_voff(l, 256u);
  auto dma_k = [&](int t, unsigned slot) {
    dma16(lds0 + slot + 2048u * w, vk, srdK, (unsigned)t * 16384u + 2048u * w);
    dma16(lds0 + slot + 2048u * w + 1024u, vk, srdK, (unsigned)t * 16384u + 2048u * w + 128u);
  };
  auto dma_v = [&](int t, unsigned slot) {
    dma16(lds0 + slot + 1024u * w, vv, srdV, (unsigned)t * 16384u + 2048u * w);
    dma16(lds0 + slot + 8192u + 1024u * w, vv, srdV, (unsigned)t * 16384u + 2048u * w + 128u);
  };
  dma_k(0, LDS_D);

  {
    const unsigned row0 = pn * 64 + w * 8;
    u32x4 xr[8];
#pragma unroll
    for (int i = 0; i < 8; ++i) xr[i] = *(const u32x4*)(x + (size_t)(row0 + i) * D + l * 8);
    const f32x4 sa0 = *(const f32x4*)(mod + l * 8), sa1 = *(const f32x4*)(mod + l * 8 + 4);
    const f32x4 ca0 = *(const f32x4*)(mod + 512 + l * 8), ca1 = *(const f32x4*)(mod + 512 + l * 8 + 4);
    float v[8][8], s[8], m[8];
#pragma unroll
    for (int i = 0; i < 8; ++i) {
#pragma unroll
      for (int j = 0; j < 4; ++j) { v[i][2 * j] = bflo(xr[i][j]); v[i][2 * j + 1] = bfhi(xr[i][j]); }
      s[i] = ((v[i][0] + v[i][1]) + (v[i][2] + v[i][3])) + ((v[i][4] + v[i][5]) + (v[i][6] + v[i][7]));
    }
    wave_sum8(s, m);
#pragma unroll
    for (int i = 0; i < 8; ++i) {
      const float mu = m[i] * (1.0f / D);
      float q = 0.f;
#pragma unroll
      for (int j = 0; j < 8; ++j) { v[i][j] -= mu; q = __builtin_fmaf(v[i][j], v[i][j], q); }
      s[i] = q;
    }
    wave_sum8(s, m);
    const float c1[8] = {1.f + ca0[0], 1.f + ca0[1], 1.f + ca0[2], 1.f + ca0[3], 1.f + ca1[0], 1.f + ca1[1], 1.f + ca1[2], 1.f + ca1[3]};
    const float sa[8] = {sa0[0], sa0[1], sa0[2], sa0[3], sa1[0], sa1[1], sa1[2], sa1[3]};
#pragma unroll
    for (int i = 0; i < 8; ++i) {
      const float rs = __builtin_amdgcn_rsqf(m[i] * (1.0f / D) + LN_EPS);
      float hh[8];
#pragma unroll
      for (int j = 0; j < 8; ++j) hh[j] = __builtin_fmaf(v[i][j] * rs, c1[j], sa[j]);
      const u32x4 o = {cvtpk(hh[0], hh[1]), cvtpk(hh[2], hh[3]), cvtpk(hh[4], hh[5]), cvtpk(hh[6], hh[7])};
      *(LAS u32x4*)(lds + LDS_X + ximg(l, w * 8 + i)) = o;
    }
  }
  BAR_L();

  {
    f32x16 acc[2][2] = {};
    gemm_phase<32, 2>(acc, lds, ringoff, ring, vq, srdQ, sq0, 8192u, c);
    WAIT_BAR(0);
    dma_v(0, LDS_R); dma_k(1, LDS_R + 16384);
#pragma unroll
    for (int nt = 0; nt < 2; ++nt)
#pragma unroll
      for (int rt = 0; rt < 2; ++rt)
#pragma unroll
        for (int s = 0; s < 2; ++s)
          *(LAS u32x4*)(lds + LDS_X + ximg(8 * w + 4 * nt + 2 * s + c.h, 32 * rt + c.r)) = pack8(acc[nt][rt], s, QSCALE);
    BAR_L();
  }

  const unsigned hh = w >> 1, rt = w & 1;
  bf16x8 qf[8];
#pragma unroll
  for (int s = 0; s < 8; ++s) qf[s] = lds_b128(lds + LDS_X + ximg(16 * hh + 2 * s + c.h, 32 * rt + c.r));
  unsigned kb[2], vb[2];
#pragma unroll
  for (int i = 0; i < 2; ++i) {
    kb[i] = 2048 * (c.r >> 3) + 64 * (c.r & 7) + (((2 * i + c.h) ^ ((c.r >> 2) & 3)) << 4);
    vb[i] = (4 * c.h + c.q) * 128 + 64 * (i ^ (c.q >> 1)) + 32 * c.blk + 8 * (2 * (c.p & 1) + (c.p >> 1));
  }
  const __amdgpu_buffer_rsrc_t srdO = mksrd(wo, 4096u * 512u * 2u);
  const unsigned vo = wdma_voff(l, 1024u), so0 = g * 512u * 1024u + w * 128u;

  f32x16 o[4] = {};
  float mrun = -INFINITY, lsum = 0.f;
  constexpr int KSLOT[8] = {LDS_D, LDS_R + 16384, LDS_X, LDS_X + 32768, LDS_X, LDS_X + 32768, LDS_X, LDS_X + 32768};
  constexpr int VSLOT[8] = {LDS_R, LDS_X + 16384, LDS_X + 49152, LDS_X + 16384, LDS_X + 49152, LDS_X + 16384, LDS_X + 49152, LDS_D};
  u32x4 pk[4];
#pragma unroll
  for (int t = 0; t <= 8; ++t) {
    f32x16 s0 = {}, s1 = {};
    if (t < 8) {
      const lds_cptr kp = lds + KSLOT[t < 8 ? t : 0];
#pragma unroll
      for (int s = 0; s < 8; ++s) {
        const bf16x8 k0 = lds_b128(kp + kb[s & 1] + 512 * (s >> 1));
        const bf16x8 k1 = lds_b128(kp + kb[s & 1] + 512 * (s >> 1) + 8192);
        s0 = MFMA32(k0, qf[s], s0);
        s1 = MFMA32(k1, qf[s], s1);
      }
    }
    if (t > 0) {
      const lds_cptr vp = lds + VSLOT[t > 0 ? t - 1 : 0];
#pragma unroll
      for (int ks = 0; ks < 4; ++ks)
#pragma unroll
        for (int dt = 0; dt < 4; ++dt) {
          const lds_cptr a = vp + vb[dt & 1] + 8192 * (dt >> 1) + 2048 * ks;
          o[dt] = MFMA32(cat8(lds_tr(a), lds_tr(a + 1024)), __builtin_bit_cast(bf16x8, pk[ks]), o[dt]);
        }
    }
    if (t < 8) {
      float tm = __builtin_fmaxf(s0[0], s1[0]);
#pragma unroll
      for (int i = 1; i < 16; ++i) tm = __builtin_fmaxf(tm, __builtin_fmaxf(s0[i], s1[i]));
      tm = swap32_max(tm);
      if (t == 0) mrun = tm;
      else if (__any(tm > mrun)) {
        const float mn = __builtin_fmaxf(mrun, tm);
        const float al = __builtin_amdgcn_exp2f(mrun - mn);
        mrun = mn; lsum *= al;
#pragma unroll
        for (int dt = 0; dt < 4; ++dt)
#pragma unroll
          for (int i = 0; i < 16; ++i) o[dt][i] *= al;
      }
      float ps = 0.f;
#pragma unroll
      for (int i = 0; i < 16; ++i) {
        s0[i] = __builtin_amdgcn_exp2f(s0[i] - mrun); s1[i] = __builtin_amdgcn_exp2f(s1[i] - mrun);
        ps += s0[i] + s1[i];
      }
      lsum += ps;
      pk[0] = pack8(s0, 0, 1.f); pk[1] = pack8(s0, 1, 1.f); pk[2] = pack8(s1, 0, 1.f); pk[3] = pack8(s1, 1, 1.f);
      if (t < 7) { WAIT_BAR(0); } else { WAIT_BAR(10); }
      if (t + 2 < 8) dma_k(t + 2, KSLOT[t + 2 < 8 ? t + 2 : 0]);
      if (t + 1 < 8) dma_v(t + 1, VSLOT[t + 1 < 8 ? t + 1 : 0]);
      if (t == 6) {
#pragma unroll
        for (int ks = 0; ks < RING; ++ks) wring_issue(ks, ring, vo, srdO, so0, 1024u);
      }
    }
  }
  {
    const float il = __builtin_amdgcn_rcpf(swap32_add(lsum));
#pragma unroll
    for (int dt = 0; dt < 4; ++dt)
#pragma unroll
      for (int s = 0; s < 2; ++s)
        *(LAS u32x4*)(lds + LDS_X + ximg(16 * hh + 4 * dt + 2 * s + c.h, 32 * rt + c.r)) = pack8(o[dt], s, il);
  }
  BAR_L();
  {
    f32x16 acc[2][2] = {};
    gemm_phase<32, 0>(acc, lds, ringoff, ring, vo, srdO, so0, 1024u, c);
    u32x4* dst = p1 + ((size_t)g * 64 + 8 * w + c.h) * T + pn * 64 + c.r;
#pragma unroll
    for (int nt = 0; nt < 2; ++nt)
#pragma unroll
      for (int r2 = 0; r2 < 2; ++r2)
#pragma unroll
        for (int s = 0; s < 2; ++s) dst[(size_t)(4 * nt + 2 * s) * T + 32 * r2] = pack8(acc[nt][r2], s, 1.f);
  }
}

DI float row_sum512(float v, float* red  , unsigned tid) {
  v += __shfl_xor(v, 8); v += __shfl_xor(v, 16); v += __shfl_xor(v, 32);
  __syncthreads();
  if ((tid & 63) < 8) red[(tid >> 6) * 8 + (tid & 7)] = v;
  __syncthreads();
  float s = 0.f;
#pragma unroll
  for (int i = 0; i < 8; ++i) s += red[i * 8 + (tid & 7)];
  return s;
}
DI void ln8(float (&v)[8], float* red, unsigned tid) {
  float s = ((v[0] + v[1]) + (v[2] + v[3])) + ((v[4] + v[5]) + (v[6] + v[7]));
  const float mu = row_sum512(s, red, tid) * (1.0f / D);
  float q = 0.f;
#pragma unroll
  for (int j = 0; j < 8; ++j) { v[j] -= mu; q = __builtin_fmaf(v[j], v[j], q); }
  const float rs = __builtin_amdgcn_rsqf(row_sum512(q, red, tid) * (1.0f / D) + LN_EPS);
#pragma unroll
  for (int j = 0; j < 8; ++j) v[j] *= rs;
}

__global__ __launch_bounds__(512) void k2_red1(const unsigned short* __restrict__ x, const float* __restrict__ mod,
                                               const u32x4* __restrict__ p1, f32x4* __restrict__ x2o, f32x4* __restrict__ hbo,
                                               u32x4* __restrict__ uo) {
  __shared__ float red[64];
  const unsigned tid = threadIdx.x, row = blockIdx.x * 8 + (tid & 7), ch = tid >> 3;
  u32x4 p[8];
#pragma unroll
  for (int g = 0; g < 8; ++g) p[g] = p1[((size_t)g * 64 + ch) * T + row];
  const u32x4 xr = *(const u32x4*)(x + (size_t)row * D + ch * 8);
  float ga[8], sb[8], cb[8];
#pragma unroll
  for (int j = 0; j < 2; ++j) {
    const f32x4 a = *(const f32x4*)(mod + 2 * 512 + ch * 8 + 4 * j), b = *(const f32x4*)(mod + 3 * 512 + ch * 8 + 4 * j),
                cc = *(const f32x4*)(mod + 4 * 512 + ch * 8 + 4 * j);
#pragma unroll
    for (int e = 0; e < 4; ++e) { ga[4 * j + e] = a[e]; sb[4 * j + e] = b[e]; cb[4 * j + e] = 1.f + cc[e]; }
  }
  float v[8];
#pragma unroll
  for (int j = 0; j < 4; ++j) {
    float lo = 0.f, hi = 0.f;
#pragma unroll
    for (int g = 0; g < 8; ++g) { lo += bflo(p[g][j]); hi += bfhi(p[g][j]); }
    v[2 * j] = __builtin_fmaf(ga[2 * j], lo, bflo(xr[j]));
    v[2 * j + 1] = __builtin_fmaf(ga[2 * j + 1], hi, bfhi(xr[j]));
  }
  const size_t o = (size_t)ch * T + row;
  x2o[2 * o] = (f32x4){v[0], v[1], v[2], v[3]}; x2o[2 * o + 1] = (f32x4){v[4], v[5], v[6], v[7]};
  ln8(v, red, tid);
#pragma unroll
  for (int j = 0; j < 8; ++j) v[j] = __builtin_fmaf(v[j], cb[j], sb[j]);
  hbo[2 * o] = (f32x4){v[0], v[1], v[2], v[3]}; hbo[2 * o + 1] = (f32x4){v[4], v[5], v[6], v[7]};
  ln8(v, red, tid);
  uo[o] = (u32x4){cvtpk(v[0], v[1]), cvtpk(v[2], v[3]), cvtpk(v[4], v[5]), cvtpk(v[6], v[7])};
}

template <int MODE>
__global__ __launch_bounds__(512) void k3_gemm(const u32x4* __restrict__ xin, const unsigned short* __restrict__ wt,
                                               const f32x4* __restrict__ hb, u32x4* __restrict__ h1o, f32x4* __restrict__ x1o) {
  extern __shared__ __attribute__((aligned(16))) char smem[];
  const lds_ptr lds = (lds_ptr)smem;
  const unsigned lds0 = (unsigned)(uintptr_t)smem;
  const unsigned j = blockIdx.x & 7, pn = blockIdx.x >> 3;
  const unsigned w = rfl(threadIdx.x >> 6);
  LaneC c; lane_consts(c);
  const unsigned l = c.lane;
  constexpr int LW = 65536;
  constexpr unsigned pitch = MODE ? H1P : T;
  const __amdgpu_buffer_rsrc_t srdX = mksrd(xin, 64u * pitch * 16u);
  const __amdgpu_buffer_rsrc_t srdW = mksrd(wt, 512u * 512u * 2u);
  const unsigned vw = wdma_voff(l, 1024u);
#pragma unroll
  for (int st = 0; st < 2; ++st)
#pragma unroll
    for (int kh = 0; kh < 2; ++kh)
#pragma unroll
      for (int i = 0; i < 2; ++i) {
        const unsigned ch = 32 * kh + 16 * st + 2 * w + i;
        int shift = 0;
        if (MODE) { const int grp = 2 * kh + st; shift = grp == 0 ? -1 : grp == 1 ? 1 : grp == 2 ? -2 : 2; shift += 2; }
        dma16(lds0 + LDS_X + ch * 1024u, (l ^ (ch & 7u)) << 4, srdX, (ch * pitch + pn * 64u + (unsigned)shift) * 16u);
        dma16(lds0 + LW + ch * 1024u, vw, srdW, ch * 8192u + j * 128u);
      }
  const unsigned nt = w & 1, rt = (w >> 1) & 1, kh = w >> 2;
  f32x16 acc = {};
  const unsigned af = nt ? c.afr[1] : c.afr[0];
#pragma unroll
  for (int st = 0; st < 2; ++st) {
    if (st == 0) { WAIT_BAR(8); } else { WAIT_BAR(0); }
#pragma unroll
    for (int i = 0; i < 8; ++i) {
      const unsigned ks = 16 * kh + 8 * st + i;
      const lds_cptr a = lds + LW + ks * 2048 + af;
      const bf16x8 fa = cat8(lds_tr(a), lds_tr(a + 512));
      const bf16x8 fb = lds_b128(lds + LDS_X + c.xb[i & 3] + 2048 * ks + 512 * rt);
      acc = MFMA32(fa, fb, acc);
    }
  }
  BAR_L();
  {
    const lds_ptr e = lds + w * 2048 + l * 16;
#pragma unroll
    for (int i = 0; i < 2; ++i) {
      f32x4 t;
#pragma unroll
      for (int k = 0; k < 4; ++k) t[k] = kh ? acc[4 * i + k] : acc[8 + 4 * i + k];
      *(LAS f32x4*)(e + 1024 * i) = t;
    }
  }
  BAR_L();
  float v[8];
  {
    const lds_cptr e = lds + (w ^ 4) * 2048 + l * 16;
#pragma unroll
    for (int i = 0; i < 2; ++i) {
      const f32x4 t = *(const LAS f32x4*)(e + 1024 * i);
#pragma unroll
      for (int k = 0; k < 4; ++k) v[4 * i + k] = t[k] + (kh ? acc[8 + 4 * i + k] : acc[4 * i + k]);
    }
  }
  const unsigned ch = 8 * j + 4 * nt + 2 * kh + c.h, row = pn * 64 + 32 * rt + c.r;
  if (MODE == 0) {
#pragma unroll
    for (int k = 0; k < 8; ++k) v[k] = gelu_tanh(v[k]);
    h1o[(size_t)ch * H1P + 2 + row] = (u32x4){cvtpk(v[0], v[1]), cvtpk(v[2], v[3]), cvtpk(v[4], v[5]), cvtpk(v[6], v[7])};
  } else {
    const size_t o = (size_t)ch * T + row;
    const f32x4 a = hb[2 * o], b = hb[2 * o + 1];
    x1o[2 * o] = (f32x4){a[0] + v[0], a[1] + v[1], a[2] + v[2], a[3] + v[3]};
    x1o[2 * o + 1] = (f32x4){b[0] + v[4], b[1] + v[5], b[2] + v[6], b[3] + v[7]};
  }
}

struct Frag3 { bf16x8 a, b[2]; };
constexpr int RING3 = 10;
template <int KS> DI void load_frags3(Frag3& f, lds_cptr lds, unsigned ringoff, unsigned a3, const LaneC& c) {
  const lds_cptr sl = lds + ringoff + (KS % RING3) * 1024 + a3;
  f.a = cat8(lds_tr(sl), lds_tr(sl + 256));
#pragma unroll
  for (int rt = 0; rt < 2; ++rt) f.b[rt] = lds_b128(lds + LDS_X + c.xb[KS & 3] + 2048 * KS + 512 * rt);
}
template <int KS>
DI void g3_step(f32x16 (&acc)[2], Frag3& cur, Frag3& nxt, lds_cptr lds, unsigned ringoff, unsigned ring, unsigned voff,
                __amdgpu_buffer_rsrc_t srd, unsigned soff0, unsigned a3, const LaneC& c) {
  constexpr int NKS = 32;
  if constexpr (KS + 1 < NKS) {
    constexpr int last = (KS + RING3 < NKS ? KS + RING3 : NKS) - 1;
    wait_vm<last - (KS + 1)>();
    load_frags3<KS + 1>(nxt, lds, ringoff, a3, c);
  }
  SBAR();
  acc[0] = MFMA32(cur.a, cur.b[0], acc[0]);
  acc[1] = MFMA32(cur.a, cur.b[1], acc[1]);
  if constexpr (KS + RING3 < NKS) {
    asm volatile("" :: "v"(cur.a));
    dma16(ring + (KS % RING3) * 1024u, voff, srd, soff0 + (unsigned)(16 * (KS + RING3)) * 3584u);
  }
  SBAR();
  if constexpr (KS + 1 < NKS) g3_step<KS + 1>(acc, nxt, cur, lds, ringoff, ring, voff, srd, soff0, a3, c);
}

__global__ __launch_bounds__(512, 2) void k5_mlp(const f32x4* __restrict__ x1, const unsigned short* __restrict__ w3,
                                                 const unsigned short* __restrict__ w4, u32x4* __restrict__ p2) {
  extern __shared__ __attribute__((aligned(16))) char smem[];
  const lds_ptr lds = (lds_ptr)smem;
  const unsigned lds0 = (unsigned)(uintptr_t)smem;
  const unsigned j = blockIdx.x & 7, pn = blockIdx.x >> 3;
  const unsigned w = rfl(threadIdx.x >> 6);
  LaneC c; lane_consts(c);
  const unsigned l = c.lane;
  const unsigned ringoff = LDS_R + w * RINGB, ring = lds0 + ringoff;
  const __amdgpu_buffer_rsrc_t srd3 = mksrd(w3, 512u * F * 2u);
  const __amdgpu_buffer_rsrc_t srd4 = mksrd(w4, (unsigned)F * 512u * 2u);
  const unsigned v3 = (l >> 2) * 3584u + ((l & 3) << 4), s30 = (j * 224u + w * 32u) * 2u;
  if (w < 7) {
#pragma unroll
    for (int ks = 0; ks < RING3; ++ks) dma16(ring + ks * 1024u, v3, srd3, s30 + (unsigned)(16 * ks) * 3584u);
  }
  {
    const unsigned row = pn * 64 + w * 8 + (l & 7);
    float v[8][8];
#pragma unroll
    for (int i = 0; i < 8; ++i) {
      const size_t o = (size_t)((l >> 3) + 8 * i) * T + row;
      const f32x4 a = x1[2 * o], b = x1[2 * o + 1];
#pragma unroll
      for (int k = 0; k < 4; ++k) { v[i][k] = a[k]; v[i][4 + k] = b[k]; }
    }
    float s = 0.f;
#pragma unroll
    for (int i = 0; i < 8; ++i) s += ((v[i][0] + v[i][1]) + (v[i][2] + v[i][3])) + ((v[i][4] + v[i][5]) + (v[i][6] + v[i][7]));
    s += __shfl_xor(s, 8); s += __shfl_xor(s, 16); s += __shfl_xor(s, 32);
    const float mu = s * (1.0f / D);
    float q = 0.f;
#pragma unroll
    for (int i = 0; i < 8; ++i)
#pragma unroll
      for (int k = 0; k < 8; ++k) { v[i][k] -= mu; q = __builtin_fmaf(v[i][k], v[i][k], q); }
    q += __shfl_xor(q, 8); q += __shfl_xor(q, 16); q += __shfl_xor(q, 32);
    const float rs = __builtin_amdgcn_rsqf(q * (1.0f / D) + LN_EPS);
#pragma unroll
    for (int i = 0; i < 8; ++i) {
      const u32x4 o = {cvtpk(v[i][0] * rs, v[i][1] * rs), cvtpk(v[i][2] * rs, v[i][3] * rs),
                       cvtpk(v[i][4] * rs, v[i][5] * rs), cvtpk(v[i][6] * rs, v[i][7] * rs)};
      *(LAS u32x4*)(lds + LDS_X + ximg((l >> 3) + 8 * i, w * 8 + (l & 7))) = o;
    }
  }
  BAR_L();
  u32x4 hp[2][2];
  if (w < 7) {
    f32x16 acc[2] = {};
    const unsigned a3 = (8 * c.h + c.q) * 64 + 32 * c.blk + 8 * (2 * (c.p & 1) + (c.p >> 1));
    Frag3 f0, f1;
    wait_vm<RING3 - 1>();
    load_frags3<0>(f0, lds, ringoff, a3, c);
    g3_step<0>(acc, f0, f1, lds, ringoff, ring, v3, srd3, s30, a3, c);
#pragma unroll
    for (int rt = 0; rt < 2; ++rt) {
#pragma unroll
      for (int i = 0; i < 16; ++i) acc[rt][i] = gelu_tanh(acc[rt][i]);
      hp[rt][0] = pack8(acc[rt], 0, 1.f); hp[rt][1] = pack8(acc[rt], 1, 1.f);
    }
  }
  const unsigned v4 = wdma_voff(l, 1024u), s40 = j * 224u * 1024u + w * 128u;
  WAIT_L0();
#pragma unroll
  for (int ks = 0; ks < RING; ++ks) wring_issue(ks, ring, v4, srd4, s40, 1024u);
  BAR_L();
  if (w < 7) {
#pragma unroll
    for (int rt = 0; rt < 2; ++rt)
#pragma unroll
      for (int s = 0; s < 2; ++s) *(LAS u32x4*)(lds + LDS_X + ximg(4 * w + 2 * s + c.h, 32 * rt + c.r)) = hp[rt][s];
  }
  BAR_L();
  {
    f32x16 acc[2][2] = {};
    gemm_phase<14, 0>(acc, lds, ringoff, ring, v4, srd4, s40, 1024u, c);
    u32x4* dst = p2 + ((size_t)j * 64 + 8 * w + c.h) * T + pn * 64 + c.r;
#pragma unroll
    for (int nt = 0; nt < 2; ++nt)
#pragma unroll
      for (int r2 = 0; r2 < 2; ++r2)
#pragma unroll
        for (int s = 0; s < 2; ++s) dst[(size_t)(4 * nt + 2 * s) * T + 32 * r2] = pack8(acc[nt][r2], s, 1.f);
  }
}

__global__ __launch_bounds__(512) void k6_red2(const float* __restrict__ mod, const u32x4* __restrict__ p2,
                                               const f32x4* __restrict__ x2, const f32x4* __restrict__ x1,
                                               u32x4* __restrict__ out) {
  const unsigned tid = threadIdx.x, row = blockIdx.x * 8 + (tid & 7), ch = tid >> 3;
  u32x4 p[8];
#pragma unroll
  for (int g = 0; g < 8; ++g) p[g] = p2[((size_t)g * 64 + ch) * T + row];
  const size_t o = (size_t)ch * T + row;
  const f32x4 a0 = x2[2 * o], a1 = x2[2 * o + 1], b0 = x1[2 * o], b1 = x1[2 * o + 1];
  const f32x4 g0 = *(const f32x4*)(mod + 5 * 512 + ch * 8), g1 = *(const f32x4*)(mod + 5 * 512 + ch * 8 + 4);
  float v[8];
#pragma unroll
  for (int jj = 0; jj < 4; ++jj) {
    float lo = 0.f, hi = 0.f;
#pragma unroll
    for (int g = 0; g < 8; ++g) { lo += bflo(p[g][jj]); hi += bfhi(p[g][jj]); }
    v[2 * jj] = lo; v[2 * jj + 1] = hi;
  }
#pragma unroll
  for (int k = 0; k < 4; ++k) {
    v[k] = __builtin_fmaf(g0[k], b0[k] + v[k], a0[k]);
    v[4 + k] = __builtin_fmaf(g1[k], b1[k] + v[4 + k], a1[k]);
  }
  out[(size_t)row * 64 + ch] = (u32x4){cvtpk(v[0], v[1]), cvtpk(v[2], v[3]), cvtpk(v[4], v[5]), cvtpk(v[6], v[7])};
}

extern "C" void kernel_launch(void* const* d_in, const int* in_sizes, int n_in, void* d_out, int out_size, void* d_ws,
                              size_t ws_size, hipStream_t stream) {
  const unsigned short* x = (const unsigned short*)d_in[0];
  const unsigned short* kc = (const unsigned short*)d_in[1];
  const unsigned short* vc = (const unsigned short*)d_in[2];
  const unsigned short* wq = (const unsigned short*)d_in[3];
  const unsigned short* wo = (const unsigned short*)d_in[4];
  const unsigned short* w1 = (const unsigned short*)d_in[6];
  const unsigned short* w2 = (const unsigned short*)d_in[7];
  const unsigned short* w3 = (const unsigned short*)d_in[8];
  const unsigned short* w4 = (const unsigned short*)d_in[9];
  const unsigned short* cc = (const unsigned short*)d_in[10];
  const unsigned short* wm = (const unsigned short*)d_in[11];
  char* ws = (char*)d_ws;
  float* mod = (float*)(ws + WS_MOD);
  u32x4* p1 = (u32x4*)(ws + WS_P1);
  f32x4* x2 = (f32x4*)(ws + WS_X2);
  f32x4* hb = (f32x4*)(ws + WS_HB);
  u32x4* u = (u32x4*)(ws + WS_U);
  u32x4* h1 = (u32x4*)(ws + WS_H1);
  f32x4* x1 = (f32x4*)(ws + WS_X1);
  u32x4* p2 = (u32x4*)(ws + WS_P2);
  (void)hipFuncSetAttribute((const void*)k1_attn, hipFuncAttributeMaxDynamicSharedMemorySize, LDS_TOTAL);
  (void)hipFuncSetAttribute((const void*)k5_mlp, hipFuncAttributeMaxDynamicSharedMemorySize, LDS_TOTAL);
  (void)hipFuncSetAttribute((const void*)k3_gemm<0>, hipFuncAttributeMaxDynamicSharedMemorySize, 131072);
  (void)hipFuncSetAttribute((const void*)k3_gemm<1>, hipFuncAttributeMaxDynamicSharedMemorySize, 131072);
  k0_mod<<<97, 256, 0, stream>>>(cc, wm, mod, h1);
  k1_attn<<<256, 512, LDS_TOTAL, stream>>>(x, kc, vc, wq, wo, mod, p1);
  k2_red1<<<256, 512, 0, stream>>>(x, mod, p1, x2, hb, u);
  k3_gemm<0><<<256, 512, 131072, stream>>>(u, w1, hb, h1, x1);
  k3_gemm<1><<<256, 512, 131072, stream>>>(h1, w2, hb, h1, x1);
  k5_mlp<<<256, 512, LDS_TOTAL, stream>>>(x1, w3, w4, p2);
  k6_red2<<<256, 512, 0, stream>>>(mod, p2, x2, x1, (u32x4*)d_out);
}
```

```cpp
#include <hip/hip_runtime.h>
#include <hip/hip_bf16.h>
#include <stdint.h>

#if defined(__HIP_DEVICE_COMPILE__)
#pragma clang attribute push(__attribute__((target("no-packed-fp32-ops"))), apply_to = function)
#endif
typedef short bf16x8 __attribute__((ext_vector_type(8)));
typedef short s16x4 __attribute__((ext_vector_type(4)));
typedef float f32x16 __attribute__((ext_vector_type(16)));
typedef float f32x4 __attribute__((ext_vector_type(4)));
typedef float f32x2 __attribute__((ext_vector_type(2)));
typedef unsigned u32x4 __attribute__((ext_vector_type(4)));
typedef unsigned u32x2 __attribute__((ext_vector_type(2)));
typedef __bf16 bf16x2_t __attribute__((ext_vector_type(2)));
typedef short v4i16_t __attribute__((ext_vector_type(4)));
#define LAS __attribute__((address_space(3)))
typedef LAS const char* lds_cptr;
typedef LAS char* lds_ptr;
#define DI __device__ __forceinline__
#define F2U(x) __builtin_bit_cast(unsigned, (float)(x))
#define U2F(x) __builtin_bit_cast(float, (unsigned)(x))
#define F2I(x) __builtin_bit_cast(int, (float)(x))
#define I2F(x) __builtin_bit_cast(float, (int)(x))
#define TIDX __builtin_amdgcn_workitem_id_x()
#define BIDX __builtin_amdgcn_workgroup_id_x()
#define ANY(p) (__builtin_amdgcn_ballot_w64(p) != 0ull)
#define SBAR() __builtin_amdgcn_sched_barrier(0)
#define MFMA32(a, b, c) __builtin_amdgcn_mfma_f32_32x32x16_bf16((a), (b), (c), 0, 0, 0)
#define WAIT_V(n) asm volatile("s_waitcnt vmcnt(" #n ")" ::: "memory")
#define WAIT_L0() asm volatile("s_waitcnt lgkmcnt(0)" ::: "memory")
#define BAR_L() asm volatile("s_waitcnt lgkmcnt(0)\n\ts_barrier" ::: "memory")
#define WAIT_BAR(n) asm volatile("s_waitcnt vmcnt(" #n ") lgkmcnt(0)\n\ts_barrier" ::: "memory")

constexpr int T = 2048, D = 512, HKV = 8, SK = 2048, DH = 128, WIN = 512, F = 1792;
constexpr float LN_EPS = 1e-5f;
constexpr float QSCALE = 0.088388347648318440f * 1.4426950408889634f;

constexpr size_t WS_MOD = 0;
constexpr size_t WS_P1 = 32768;
constexpr size_t WS_S = WS_P1 + (size_t)8 * 64 * T * 16;
constexpr unsigned S_U = 0;
constexpr unsigned S_H1 = S_U + 64u * T * 16u;
constexpr unsigned S_X1 = S_H1 + 64u * T * 16u;
constexpr unsigned S_HZ = S_X1 + 64u * T * 16u;
constexpr unsigned S_P2 = S_HZ + 256u * T * 16u;
constexpr unsigned S_BYTES = S_P2 + 512u * T * 16u;
constexpr int FILL_PER_THREAD = 15;
static_assert(256u * FILL_PER_THREAD * 8192u >= S_BYTES, "fill");
#define K1MAP 0
#define P1_AUX 16
#define PRIO_S 1
#define ATHR 32.0f
constexpr float THR = ATHR;
constexpr unsigned SENT = 0xFFFFFFFFu;
constexpr unsigned SPIN_CAP = 1u << 16;

constexpr int LDS_X = 0;
constexpr int LDS_R = 65536;
constexpr int RING = 5, RINGB = RING * 2048;
constexpr int LDS_D = LDS_R + 8 * RINGB;
constexpr int LDS_TOTAL = LDS_D + 16384;
static_assert(LDS_TOTAL == 163840, "lds");

#define STAMP(kid, i) do {} while (0)
DI unsigned cvtpk(float lo, float hi) {
  f32x2 v = {lo, hi};
  bf16x2_t b = __builtin_convertvector(v, bf16x2_t);
  return __builtin_bit_cast(unsigned, b);
}
DI float bflo(unsigned u) { return U2F(u << 16); }
DI float bfhi(unsigned u) { return U2F(u & 0xffff0000u); }
DI unsigned rfl(unsigned x) { return __builtin_amdgcn_readfirstlane(x); }
DI __amdgpu_buffer_rsrc_t mksrd(const void* p, unsigned bytes) {
  return __builtin_amdgcn_make_buffer_rsrc((void*)p, (short)0, (int)bytes, 0x00020000);
}
DI void dma16(unsigned lds, unsigned voff, __amdgpu_buffer_rsrc_t srd, unsigned soff) {
  asm volatile("s_mov_b32 m0, %0\n\ts_nop 0\n\tbuffer_load_dwordx4 %1, %2, %3 offen lds"
               :: "s"(lds), "v"(voff), "s"(srd), "s"(soff) : "memory");
}
DI void gld16(u32x4& d, const void* p) { asm volatile("global_load_dwordx4 %0, %1, off" : "=v"(d) : "v"(p) : "memory"); }
DI bf16x8 lds_b128(lds_cptr p) { return *(const LAS bf16x8*)p; }
DI s16x4 lds_tr(lds_cptr p) {
  return __builtin_bit_cast(s16x4, __builtin_amdgcn_ds_read_tr16_b64_v4i16((LAS v4i16_t*)p));
}
DI bf16x8 cat8(s16x4 lo, s16x4 hi) { return __builtin_shufflevector(lo, hi, 0, 1, 2, 3, 4, 5, 6, 7); }
DI float swap32_add(float x) {
  auto r = __builtin_amdgcn_permlane32_swap(F2U(x), F2U(x), false, false);
  return U2F(r[0]) + U2F(r[1]);
}
DI float swap32_max(float x) {
  auto r = __builtin_amdgcn_permlane32_swap(F2U(x), F2U(x), false, false);
  return __builtin_fmaxf(U2F(r[0]), U2F(r[1]));
}
template <int CTRL> DI float dpp_add(float x) {
  int y = __builtin_amdgcn_update_dpp(0, F2I(x), CTRL, 0xF, 0xF, true);
  return x + I2F(y);
}
DI float max3f(float a, float b, float c) { float r; asm("v_max3_f32 %0, %1, %2, %3" : "=v"(r) : "v"(a), "v"(b), "v"(c)); return r; }
DI float max2f(float a, float b) { float r; asm("v_max_f32_e32 %0, %1, %2" : "=v"(r) : "v"(a), "v"(b)); return r; }
DI float dot2bf(unsigned a, unsigned b, float c) {
  return __builtin_amdgcn_fdot2_f32_bf16(__builtin_bit_cast(bf16x2_t, a), __builtin_bit_cast(bf16x2_t, b), c, false);
}
#define PIN(x) asm volatile("" : "+v"(x))
DI float swap16_add(float x) {
  auto r = __builtin_amdgcn_permlane16_swap(F2U(x), F2U(x), false, false);
  return U2F(r[0]) + U2F(r[1]);
}
DI float sum_hi3(float x) { return swap32_add(swap16_add(dpp_add<0x128>(x))); }
DI float gelu_tanh(float x) {
  const float k0 = -2.0f * 0.7978845608028654f * 1.4426950408889634f;
  const float t = x * x;
  const float z = x * __builtin_fmaf(t, 0.044715f * k0, k0);
  const float e = __builtin_amdgcn_exp2f(z);
  return x * __builtin_amdgcn_rcpf(1.0f + e);
}
DI void wave_sum8(const float (&a)[8], float (&out)[8]) {
  float b[4], c[2];
#pragma unroll
  for (int i = 0; i < 4; ++i) {
    auto r = __builtin_amdgcn_permlane32_swap(F2U(a[i]), F2U(a[i + 4]), false, false);
    b[i] = U2F(r[0]) + U2F(r[1]);
  }
#pragma unroll
  for (int i = 0; i < 2; ++i) {
    auto r = __builtin_amdgcn_permlane16_swap(F2U(b[i]), F2U(b[i + 2]), false, false);
    c[i] = U2F(r[0]) + U2F(r[1]);
  }
#pragma unroll
  for (int i = 0; i < 2; ++i) {
    c[i] = dpp_add<0xB1>(c[i]);
    c[i] = dpp_add<0x4E>(c[i]);
    c[i] = dpp_add<0x141>(c[i]);
    c[i] = dpp_add<0x140>(c[i]);
  }
#pragma unroll
  for (int q = 0; q < 4; ++q)
#pragma unroll
    for (int i = 0; i < 2; ++i)
      out[i + 2 * (q & 1) + 4 * (q >> 1)] = I2F(__builtin_amdgcn_readlane(F2I(c[i]), 16 * q));
}

#if defined(__HIP_DEVICE_COMPILE__)
#pragma clang attribute pop
#endif
__global__ __launch_bounds__(256) void k0_mod(const unsigned short* __restrict__ c, const unsigned short* __restrict__ wmod,
                                              const unsigned short* __restrict__ w3, float* __restrict__ mod,
                                              const unsigned short* __restrict__ wq, const unsigned short* __restrict__ kc,
                                              const unsigned short* __restrict__ vc) {
  const int tid = TIDX;
  __shared__ float red[4][32];
  if (BIDX >= 152) {
    const unsigned b = BIDX - 152, g = BIDX & 7, i = b >> 3;
    u32x4 a[12];
#pragma unroll
    for (int k = 0; k < 8; ++k) a[k] = *(const u32x4*)(wq + (size_t)(i * 32 + k * 4 + (tid >> 6)) * 4096 + g * 512 + (tid & 63) * 8);
#pragma unroll
    for (int k = 0; k < 2; ++k) {
      a[8 + k] = *(const u32x4*)(kc + ((size_t)g * SK + (SK - WIN)) * DH + (i * 2 + k) * 2048 + tid * 8);
      a[10 + k] = *(const u32x4*)(vc + ((size_t)g * SK + (SK - WIN)) * DH + (i * 2 + k) * 2048 + tid * 8);
    }
#pragma unroll
    for (int k = 0; k < 12; ++k) asm volatile("" :: "v"(a[k]));
    return;
  }
  const bool isw3 = BIDX >= 96;
  const int rg = tid >> 2, cc = tid & 3;
  const unsigned short* src = isw3 ? w3 + (BIDX - 96) * 32 + cc * 8 : wmod + BIDX * 32 + cc * 8;
  const int ld = isw3 ? F : 3072;
  u32x4 w[8];
  float s[8];
#pragma unroll
  for (int i = 0; i < 8; ++i) w[i] = *(const u32x4*)(src + (size_t)(rg + 64 * i) * ld);
#pragma unroll
  for (int i = 0; i < 8; ++i) {
    const float v = U2F((unsigned)c[rg + 64 * i] << 16);
    s[i] = isw3 ? 1.0f : v / (1.0f + __expf(-v));
  }
  float acc[8] = {0, 0, 0, 0, 0, 0, 0, 0};
#pragma unroll
  for (int i = 0; i < 8; ++i)
#pragma unroll
    for (int j = 0; j < 4; ++j) {
      acc[2 * j] = __builtin_fmaf(s[i], bflo(w[i][j]), acc[2 * j]);
      acc[2 * j + 1] = __builtin_fmaf(s[i], bfhi(w[i][j]), acc[2 * j + 1]);
    }
#pragma unroll
  for (int j = 0; j < 8; ++j) {
    float v = acc[j];
    v += __shfl_xor(v, 4); v += __shfl_xor(v, 8); v += __shfl_xor(v, 16); v += __shfl_xor(v, 32);
    acc[j] = v;
  }
  const int lane = tid & 63, wid = tid >> 6;
  if (lane < 4) {
#pragma unroll
    for (int j = 0; j < 8; ++j) red[wid][lane * 8 + j] = acc[j];
  }
  __syncthreads();
  if (tid < 32) mod[BIDX * 32 + tid] = red[0][tid] + red[1][tid] + red[2][tid] + red[3][tid];
}

#if defined(__HIP_DEVICE_COMPILE__)
#pragma clang attribute push(__attribute__((target("no-packed-fp32-ops"))), apply_to = function)
#endif
struct LaneC {
  unsigned lane, r, h, q, p, blk;
  unsigned wv;
  unsigned afr[2];
  unsigned xb[4];
};
DI void lane_consts(LaneC& c) {
  const unsigned l = TIDX & 63;
  c.lane = l; c.r = l & 31; c.h = l >> 5; c.q = (l & 15) >> 2; c.p = l & 3; c.blk = (l >> 4) & 1;
  const unsigned sp = 2 * (c.p & 1) + (c.p >> 1);
#pragma unroll
  for (int nt = 0; nt < 2; ++nt) c.afr[nt] = (8 * c.h + c.q) * 128 + 64 * (nt ^ (c.q >> 1)) + 32 * c.blk + 8 * sp;
#pragma unroll
  for (int s = 0; s < 4; ++s) c.xb[s] = 1024 * c.h + ((c.r ^ (2 * s + c.h)) << 4);
}
DI unsigned wdma_voff(unsigned l, unsigned ld) { return (l >> 3) * ld + (((l & 7) ^ (((l >> 4) & 1) << 2)) << 4); }

DI void wring_issue(int ks, unsigned ring, unsigned voff, __amdgpu_buffer_rsrc_t srd, unsigned soff0, unsigned ld) {
  const unsigned slot = ring + (unsigned)(ks % RING) * 2048u;
  dma16(slot, voff, srd, soff0 + (unsigned)(16 * ks) * ld);
  dma16(slot + 1024u, voff, srd, soff0 + (unsigned)(16 * ks + 8) * ld);
}
template <int N> DI void wait_vm() {
  if constexpr (N == 0) WAIT_V(0); else if constexpr (N == 1) WAIT_V(1); else if constexpr (N == 2) WAIT_V(2);
  else if constexpr (N == 3) WAIT_V(3); else if constexpr (N == 4) WAIT_V(4); else if constexpr (N == 5) WAIT_V(5);
  else if constexpr (N == 6) WAIT_V(6); else if constexpr (N == 7) WAIT_V(7); else if constexpr (N == 8) WAIT_V(8);
  else if constexpr (N == 9) WAIT_V(9); else if constexpr (N == 10) WAIT_V(10); else if constexpr (N == 11) WAIT_V(11);
  else if constexpr (N == 12) WAIT_V(12); else if constexpr (N == 13) WAIT_V(13); else if constexpr (N == 14) WAIT_V(14);
  else if constexpr (N == 15) WAIT_V(15); else if constexpr (N == 16) WAIT_V(16);
  else static_assert(N < 0, "wait_vm");
}

struct Frag { bf16x8 a[2], b[2]; };
template <int KS> DI void load_frags(Frag& f, lds_cptr lds, unsigned ringoff, const LaneC& c) {
  const lds_cptr sl = lds + ringoff + (KS % RING) * 2048;
#pragma unroll
  for (int nt = 0; nt < 2; ++nt) f.a[nt] = cat8(lds_tr(sl + c.afr[nt]), lds_tr(sl + c.afr[nt] + 512));
#pragma unroll
  for (int rt = 0; rt < 2; ++rt) f.b[rt] = lds_b128(lds + LDS_X + c.xb[KS & 3] + 2048 * KS + 512 * rt);
}
template <int NKS, int KS, int EXTRA>
DI void gemm_step(f32x16 (&acc)[2][2], Frag& cur, Frag& nxt, lds_cptr lds, unsigned ringoff, unsigned ring, unsigned voff,
                  __amdgpu_buffer_rsrc_t srd, unsigned soff0, unsigned ld, const LaneC& c) {
  if constexpr (KS + 1 < NKS) {
    constexpr int last = (KS + RING < NKS ? KS + RING : NKS) - 1;
    constexpr int after = 2 * (last - (KS + 1)) + ((KS + 1 < RING) ? EXTRA : 0);
    wait_vm<after>();
    load_frags<KS + 1>(nxt, lds, ringoff, c);
  }
  SBAR();
#pragma unroll
  for (int nt = 0; nt < 2; ++nt)
#pragma unroll
    for (int rt = 0; rt < 2; ++rt) acc[nt][rt] = MFMA32(cur.a[nt], cur.b[rt], acc[nt][rt]);
  if constexpr (KS + RING < NKS) {
    asm volatile("" :: "v"(cur.a[0]), "v"(cur.a[1]));
    wring_issue(KS + RING, ring, voff, srd, soff0, ld);
  }
  SBAR();
  if constexpr (KS + 1 < NKS) gemm_step<NKS, KS + 1, EXTRA>(acc, nxt, cur, lds, ringoff, ring, voff, srd, soff0, ld, c);
}
template <int NKS, int EXTRA>
DI void gemm_phase(f32x16 (&acc)[2][2], lds_cptr lds, unsigned ringoff, unsigned ring, unsigned voff,
                   __amdgpu_buffer_rsrc_t srd, unsigned soff0, unsigned ld, const LaneC& c) {
  Frag f0, f1;
  wait_vm<2 * (RING - 1) + EXTRA>();
  load_frags<0>(f0, lds, ringoff, c);
  gemm_step<NKS, 0, EXTRA>(acc, f0, f1, lds, ringoff, ring, voff, srd, soff0, ld, c);
}
DI u32x4 pack8(const f32x16& a, int s, float sc) {
  return (u32x4){cvtpk(a[8 * s] * sc, a[8 * s + 1] * sc), cvtpk(a[8 * s + 2] * sc, a[8 * s + 3] * sc),
                 cvtpk(a[8 * s + 4] * sc, a[8 * s + 5] * sc), cvtpk(a[8 * s + 6] * sc, a[8 * s + 7] * sc)};
}
DI unsigned ximg(unsigned chunk, unsigned row) { return chunk * 1024u + ((row ^ (chunk & 7u)) << 4); }

constexpr int KSLOT[8] = {LDS_D, LDS_R, LDS_R + 32768, LDS_X, LDS_X + 32768, LDS_D, LDS_X, LDS_X + 32768};
constexpr int VSLOT[8] = {LDS_R + 16384, LDS_R + 49152, LDS_X + 16384, LDS_X + 49152, LDS_R + 65536, LDS_X + 16384, LDS_X + 49152, LDS_D};
__global__ __launch_bounds__(512, 2) void k1_attn(const unsigned short* __restrict__ x, const unsigned short* __restrict__ kc,
                                                  const unsigned short* __restrict__ vc, const unsigned short* __restrict__ wq,
                                                  const unsigned short* __restrict__ wo, const float* __restrict__ mod,
                                                  void* __restrict__ p1, void* __restrict__ sreg,
                                                  const unsigned short* __restrict__ w1, const unsigned short* __restrict__ w2,
                                                  const unsigned short* __restrict__ w3, const unsigned short* __restrict__ w4) {
  extern __shared__ __attribute__((aligned(16))) char smem[];
  const lds_ptr lds = (lds_ptr)smem;
  const unsigned lds0 = (unsigned)(uintptr_t)smem;
  const unsigned g = BIDX & 7, pn = BIDX >> 3;
  const unsigned w = rfl(TIDX >> 6);
  LaneC c; lane_consts(c);
  const unsigned l = c.lane;
  const unsigned ringoff = LDS_R + w * RINGB, ring = lds0 + ringoff;
  STAMP(1, 0);

  u32x4 xr[8], mo[4];
#pragma unroll
  for (int i = 0; i < 8; ++i) gld16(xr[i], x + (size_t)(pn * 64 + w * 8 + i) * D + l * 8);
  gld16(mo[0], mod + l * 8); gld16(mo[1], mod + l * 8 + 4); gld16(mo[2], mod + 512 + l * 8); gld16(mo[3], mod + 512 + l * 8 + 4);
  const __amdgpu_buffer_rsrc_t srdQ = mksrd(wq, 512u * 4096u * 2u);
  const unsigned vq = wdma_voff(l, 8192u), sq0 = (g * 512u + w * 64u) * 2u;
  wring_issue(0, ring, vq, srdQ, sq0, 8192u);
  const __amdgpu_buffer_rsrc_t srdK = mksrd(kc + ((size_t)g * SK + (SK - WIN)) * DH, WIN * DH * 2u);
  const __amdgpu_buffer_rsrc_t srdV = mksrd(vc + ((size_t)g * SK + (SK - WIN)) * DH, WIN * DH * 2u);
  const unsigned vk = ((l >> 2) & 7) * 256u + (l >> 5) * 64u + (((l & 3) ^ (2 * (w & 1) + ((l >> 4) & 1))) << 4);
  const unsigned vv = wdma_voff(l, 256u);
  auto dma_k = [&](int t, unsigned slot) {
    dma16(lds0 + slot + 2048u * w, vk, srdK, (unsigned)t * 16384u + 2048u * w);
    dma16(lds0 + slot + 2048u * w + 1024u, vk, srdK, (unsigned)t * 16384u + 2048u * w + 128u);
  };
  auto dma_v = [&](int t, unsigned slot) {
    dma16(lds0 + slot + 1024u * w, vv, srdV, (unsigned)t * 16384u + 2048u * w);
    dma16(lds0 + slot + 8192u + 1024u * w, vv, srdV, (unsigned)t * 16384u + 2048u * w + 128u);
  };

  {
    asm volatile("s_waitcnt vmcnt(2)" : "+v"(xr[0]), "+v"(xr[1]), "+v"(xr[2]), "+v"(xr[3]), "+v"(xr[4]), "+v"(xr[5]), "+v"(xr[6]),
                 "+v"(xr[7]), "+v"(mo[0]), "+v"(mo[1]), "+v"(mo[2]), "+v"(mo[3]) :: "memory");
    const f32x4 sa0 = __builtin_bit_cast(f32x4, mo[0]), sa1 = __builtin_bit_cast(f32x4, mo[1]);
    const f32x4 ca0 = __builtin_bit_cast(f32x4, mo[2]), ca1 = __builtin_bit_cast(f32x4, mo[3]);
    float v[8][8], s[8], m[8];
#pragma unroll
    for (int i = 0; i < 8; ++i) {
#pragma unroll
      for (int j = 0; j < 4; ++j) { v[i][2 * j] = bflo(xr[i][j]); v[i][2 * j + 1] = bfhi(xr[i][j]); }
      s[i] = ((v[i][0] + v[i][1]) + (v[i][2] + v[i][3])) + ((v[i][4] + v[i][5]) + (v[i][6] + v[i][7]));
    }
    wring_issue(1, ring, vq, srdQ, sq0, 8192u);
    wave_sum8(s, m);
#pragma unroll
    for (int i = 0; i < 8; ++i) {
      const float mu = m[i] * (1.0f / D);
      float q = 0.f;
#pragma unroll
      for (int j = 0; j < 8; ++j) { v[i][j] -= mu; q = __builtin_fmaf(v[i][j], v[i][j], q); }
      s[i] = q;
    }
    wave_sum8(s, m);
    const float c1[8] = {1.f + ca0[0], 1.f + ca0[1], 1.f + ca0[2], 1.f + ca0[3], 1.f + ca1[0], 1.f + ca1[1], 1.f + ca1[2], 1.f + ca1[3]};
    const float sa[8] = {sa0[0], sa0[1], sa0[2], sa0[3], sa1[0], sa1[1], sa1[2], sa1[3]};
#pragma unroll
    for (int i = 0; i < 8; ++i) {
      const float rs = __builtin_amdgcn_rsqf(m[i] * (1.0f / D) + LN_EPS);
      float hh[8];
#pragma unroll
      for (int j = 0; j < 8; ++j) hh[j] = __builtin_fmaf(v[i][j] * rs, c1[j], sa[j]);
      const u32x4 o = {cvtpk(hh[0], hh[1]), cvtpk(hh[2], hh[3]), cvtpk(hh[4], hh[5]), cvtpk(hh[6], hh[7])};
      *(LAS u32x4*)(lds + LDS_X + ximg(l, w * 8 + i)) = o;
      if (i == 0 || i == 2 || i == 4) wring_issue(2 + i / 2, ring, vq, srdQ, sq0, 8192u);
      if (i == 6) dma_k(0, LDS_D);
    }
  }
  BAR_L();
  STAMP(1, 1);

  {
    f32x16 acc[2][2] = {};
    gemm_phase<32, 2>(acc, lds, ringoff, ring, vq, srdQ, sq0, 8192u, c);
    STAMP(1, 2);
    WAIT_BAR(0);
    dma_k(1, KSLOT[1]); dma_v(0, VSLOT[0]); dma_k(2, KSLOT[2]); dma_v(1, VSLOT[1]);
#pragma unroll
    for (int nt = 0; nt < 2; ++nt)
#pragma unroll
      for (int rt = 0; rt < 2; ++rt)
#pragma unroll
        for (int s = 0; s < 2; ++s)
          *(LAS u32x4*)(lds + LDS_X + ximg(8 * w + 4 * nt + 2 * s + c.h, 32 * rt + c.r)) = pack8(acc[nt][rt], s, QSCALE);
    BAR_L();
  }

  STAMP(1, 3);
  const unsigned hh = w >> 1, rt = w & 1;
  bf16x8 qf[8];
#pragma unroll
  for (int s = 0; s < 8; ++s) qf[s] = lds_b128(lds + LDS_X + ximg(16 * hh + 2 * s + c.h, 32 * rt + c.r));
  unsigned kb[2], vb[2];
#pragma unroll
  for (int i = 0; i < 2; ++i) {
    kb[i] = 2048 * (c.r >> 3) + 64 * (c.r & 7) + (((2 * i + c.h) ^ ((c.r >> 2) & 3)) << 4);
    vb[i] = (4 * c.h + c.q) * 128 + 64 * (i ^ (c.q >> 1)) + 32 * c.blk + 8 * (2 * (c.p & 1) + (c.p >> 1));
  }
  const __amdgpu_buffer_rsrc_t srdO = mksrd(wo, 4096u * 512u * 2u);
  const unsigned vo = wdma_voff(l, 1024u), so0 = g * 512u * 1024u + w * 128u;
  const __amdgpu_buffer_rsrc_t srdS = mksrd(sreg, S_BYTES);

  f32x16 o[4] = {};
  f32x16 s0, s1;
  f32x16 negm;
  float lsum = 0.f;
  u32x4 pk[4];
  constexpr int PD = 6;
  bf16x8 fr[PD];
  const bool lag = w >= 4;
  auto frag = [&](int t, int it) -> bf16x8 {
    if (it < 16) {
      const lds_cptr a = lds + VSLOT[t > 0 ? t - 1 : 0] + vb[it & 1] + 8192 * ((it >> 1) & 1) + 2048 * (it >> 2);
      return cat8(lds_tr(a), lds_tr(a + 1024));
    }
    const int s = (it - 16) >> 1;
    return lds_b128(lds + KSLOT[t < 8 ? t : 0] + kb[s & 1] + 512 * (s >> 1) + 8192 * (it & 1));
  };
  auto seam = [&](int t) {
    if (t == 0) { WAIT_BAR(0); } else if (t < 7) { WAIT_BAR(4); } else { BAR_L(); }
    if (t + 3 < 8) dma_k(t + 3, KSLOT[t + 3 < 8 ? t + 3 : 0]);
    if (t + 2 < 8) dma_v(t + 2, VSLOT[t + 2 < 8 ? t + 2 : 0]);
    if (t == 6) {
#pragma unroll
      for (int ks = 0; ks < RING; ++ks) wring_issue(ks, ring, vo, srdO, so0, 1024u);
    }
  };
  auto extra = [&](int e) {
    const unsigned dump = lds0 + LDS_R + w * 3072u + (unsigned)(e % 3) * 1024u;
    const unsigned id = (BIDX >> 3) * 8 + w, jh = BIDX & 1;
    const unsigned vrow = (l >> 5) * 1024u + (l & 31) * 16u + jh * 512u;
    auto w3v = [&](unsigned piece) { const unsigned byte = piece * 1024u + l * 16u, row = byte / 1792u;
                                     return row * 3584u + jh * 1792u + (byte - row * 1792u); };
    const unsigned r4 = jh * 896u * 1024u;
    if (e < 4 || e >= 15) {
      const int i = e < 4 ? e : e - 11;
      const u32x4 sv = {SENT, SENT, SENT, SENT};
      __builtin_amdgcn_raw_buffer_store_b128(sv, srdS, TIDX * 16u + (BIDX * FILL_PER_THREAD + i) * 8192u, 0, 16);
    } else if (e < 6) dma16(dump, l * 16u, srdO, (g * 512u + (pn * 8 + w) * 2 + (e - 4)) * 1024u);
    else if (e == 6) dma16(dump, vrow, mksrd(w1, 512u * 1024u), id * 2048u);
    else if (e == 7) dma16(dump, vrow, mksrd(w2, 512u * 1024u), id * 2048u);
    else if (e < 11) dma16(dump, w3v(id + 256u * (e - 8)), mksrd(w3, 512u * 3584u), 0u);
    else if (e == 11) {
      if (id < 128) dma16(dump, w3v(id + 768u), mksrd(w3, 512u * 3584u), 0u);
      else dma16(dump, l * 16u, mksrd(w4, 1792u * 1024u), r4 + (id - 128u + 768u) * 1024u);
    } else dma16(dump, l * 16u, mksrd(w4, 1792u * 1024u), r4 + (id + 256u * (e - 12)) * 1024u);
  };
#pragma unroll
  for (int t = 0; t <= 8; ++t) {
    const int i0 = t == 0 ? 16 : 0, i1 = t == 8 ? 16 : 32;
    if (t <= 1) {
#pragma unroll
      for (int it = i0; it < i0 + PD; ++it) fr[it % PD] = frag(t, it);
    }
    if (t == 0) { s0 = (f32x16){}; s1 = (f32x16){}; } else if (t < 8) { s0 = negm; s1 = negm; }
    __builtin_amdgcn_s_setprio(1);
    SBAR();
#pragma unroll
    for (int it = i0; it < i1; ++it) {
      if (it < 16) o[it & 3] = MFMA32(fr[it % PD], __builtin_bit_cast(bf16x8, pk[it >> 2]), o[it & 3]);
      else if (it & 1) s1 = MFMA32(fr[it % PD], qf[(it - 16) >> 1], s1);
      else s0 = MFMA32(fr[it % PD], qf[(it - 16) >> 1], s0);
      if (it + PD < i1) fr[it % PD] = frag(t, it + PD);
      if (t >= 1 && t <= 7 && (it & 7) == 3 && 4 * (t - 1) + (it >> 3) < 26) extra(4 * (t - 1) + (it >> 3));
      SBAR();
    }
    if (t == 8) break;
    __builtin_amdgcn_s_setprio(0);
    if (lag) seam(t);
    asm volatile("s_nop 15" : "+v"(s0), "+v"(s1));
    float ta = max3f(s0[0], s0[1], s1[0]), tb = max3f(s0[2], s0[3], s1[1]);
    ta = max3f(ta, s1[2], s1[3]);
#pragma unroll
    for (int i = 4; i < 16; i += 4) {
      ta = max3f(ta, s0[i], s0[i + 1]); tb = max3f(tb, s0[i + 2], s0[i + 3]);
      ta = max3f(ta, s1[i], s1[i + 1]); tb = max3f(tb, s1[i + 2], s1[i + 3]);
    }
    float tm = max2f(ta, tb);
    {
      auto r = __builtin_amdgcn_permlane32_swap(F2U(tm), F2U(tm), false, false);
      tm = max2f(U2F(r[0]), U2F(r[1]));
    }
    if (t == 0 || ANY(tm > THR)) {
      const float ndl = t == 0 ? -tm : -max2f(tm, 0.f);
      const float aone = c.h ? 0.f : 1.f;
      if (t == 0) negm = (f32x16){};
      __builtin_amdgcn_s_setprio(2);
      s0 = __builtin_amdgcn_mfma_f32_32x32x2f32(aone, ndl, s0, 0, 0, 0);
      s1 = __builtin_amdgcn_mfma_f32_32x32x2f32(aone, ndl, s1, 0, 0, 0);
      negm = __builtin_amdgcn_mfma_f32_32x32x2f32(aone, ndl, negm, 0, 0, 0);
      __builtin_amdgcn_s_setprio(0);
      if (t > 0) {
        const float al = __builtin_amdgcn_exp2f(ndl);
        lsum *= al;
#pragma unroll
        for (int dt = 0; dt < 4; ++dt)
#pragma unroll
          for (int i = 0; i < 16; ++i) o[dt][i] *= al;
      }
    }
    SBAR();
    if (t >= 1) {
#pragma unroll
      for (int it = 0; it < PD; ++it) fr[it] = frag(t + 1, it);
    }
#pragma unroll
    for (int i = 0; i < 16; ++i) { s0[i] = __builtin_amdgcn_exp2f(s0[i]); s1[i] = __builtin_amdgcn_exp2f(s1[i]); }
    pk[0] = pack8(s0, 0, 1.f); pk[1] = pack8(s0, 1, 1.f); pk[2] = pack8(s1, 0, 1.f); pk[3] = pack8(s1, 1, 1.f);
    {
      float l0 = lsum, l1 = 0.f, l2 = 0.f, l3 = 0.f;
#pragma unroll
      for (int i = 0; i < 16; i += 2) { l0 += s0[i]; l1 += s0[i + 1]; l2 += s1[i]; l3 += s1[i + 1]; }
      lsum = (l0 + l1) + (l2 + l3);
    }
    PIN(pk[0]); PIN(pk[1]); PIN(pk[2]); PIN(pk[3]); PIN(lsum);
    SBAR();
    if (!lag) seam(t);
  }
  __builtin_amdgcn_s_setprio(0);
  STAMP(1, 4);
  {
    const float il = __builtin_amdgcn_rcpf(swap32_add(lsum));
#pragma unroll
    for (int dt = 0; dt < 4; ++dt)
#pragma unroll
      for (int s = 0; s < 2; ++s)
        *(LAS u32x4*)(lds + LDS_X + ximg(16 * hh + 4 * dt + 2 * s + c.h, 32 * rt + c.r)) = pack8(o[dt], s, il);
  }
  BAR_L();
  STAMP(1, 5);
  {
    f32x16 acc[2][2] = {};
    gemm_phase<32, 2>(acc, lds, ringoff, ring, vo, srdO, so0, 1024u, c);
    STAMP(1, 6);
    const __amdgpu_buffer_rsrc_t srdP = mksrd(p1, 8u * 64u * T * 16u);
    const unsigned dst = ((((pn * 8 + (c.r >> 3)) * 8 + g) * 64 + 8 * w + c.h) * 8 + (c.r & 7)) * 16u;
#pragma unroll
    for (int nt = 0; nt < 2; ++nt)
#pragma unroll
      for (int r2 = 0; r2 < 2; ++r2)
#pragma unroll
        for (int s = 0; s < 2; ++s)
          __builtin_amdgcn_raw_buffer_store_b128(pack8(acc[nt][r2], s, 1.f), srdP, dst + ((4 * nt + 2 * s) * 8 + r2 * 4 * 8 * 64 * 8) * 16u, 0, P1_AUX);
  }
  STAMP(1, 7);
}

DI unsigned nosent(unsigned v) { return v < 0xFFFFFFFEu ? v : 0xFFFFFFFEu; }
DI void pub16(__amdgpu_buffer_rsrc_t srd, unsigned voff, unsigned soff, u32x4 v) {
  v = (u32x4){nosent(v[0]), nosent(v[1]), nosent(v[2]), nosent(v[3])};
  __builtin_amdgcn_raw_buffer_store_b128(v, srd, voff + soff, 0, 16);
}
DI unsigned umax3(unsigned a, unsigned b, unsigned c) { const unsigned m = a > b ? a : b; return m > c ? m : c; }
template <int N> DI void poll_load(u32x4 (&v)[N], __amdgpu_buffer_rsrc_t srd, unsigned voff, unsigned soff0, unsigned step) {
  for (unsigned spin = 0;; ++spin) {
    asm volatile("" ::: "memory");
#pragma unroll
    for (int i = 0; i < N; ++i) v[i] = __builtin_amdgcn_raw_buffer_load_b128(srd, voff, soff0 + (unsigned)i * step, 16);
    unsigned m = 0;
#pragma unroll
    for (int i = 0; i < N; ++i) { m = umax3(m, v[i][0], v[i][1]); m = umax3(m, v[i][2], v[i][3]); }
    if (!ANY(m == SENT) || spin > SPIN_CAP) break;
    __builtin_amdgcn_s_sleep(1);
  }
}
DI void ln8(float (&v)[8], LAS float* red  , unsigned tid) {
  float s = ((v[0] + v[1]) + (v[2] + v[3])) + ((v[4] + v[5]) + (v[6] + v[7]));
  float q = (__builtin_fmaf(v[0], v[0], v[1] * v[1]) + __builtin_fmaf(v[2], v[2], v[3] * v[3])) +
            (__builtin_fmaf(v[4], v[4], v[5] * v[5]) + __builtin_fmaf(v[6], v[6], v[7] * v[7]));
  s = sum_hi3(s); q = sum_hi3(q);
  if ((tid & 63) < 8) *(LAS f32x2*)(red + ((tid >> 6) * 8 + (tid & 7)) * 2) = (f32x2){s, q};
  BAR_L();
  f32x2 e[8];
#pragma unroll
  for (int i = 0; i < 8; ++i) e[i] = *(const LAS f32x2*)(red + (i * 8 + (tid & 7)) * 2);
  const float mu = (((e[0][0] + e[1][0]) + (e[2][0] + e[3][0])) + ((e[4][0] + e[5][0]) + (e[6][0] + e[7][0]))) * (1.0f / D);
  const float ms = (((e[0][1] + e[1][1]) + (e[2][1] + e[3][1])) + ((e[4][1] + e[5][1]) + (e[6][1] + e[7][1]))) * (1.0f / D);
  const float rs = __builtin_amdgcn_rsqf(__builtin_fmaxf(__builtin_fmaf(-mu, mu, ms), 0.f) + LN_EPS);
  const float nb = -mu * rs;
#pragma unroll
  for (int k = 0; k < 8; ++k) v[k] = __builtin_fmaf(v[k], rs, nb);
}
DI void red4_put(const f32x16& a, int nt, lds_ptr scr, unsigned rt, unsigned kq, unsigned l) {
#pragma unroll
  for (int g = 0; g < 2; ++g)
#pragma unroll
    for (int i = 0; i < 2; ++i) {
      const int o = 8 * g + 4 * i;
      *(LAS f32x4*)(scr + ((rt * 4 + 2 * nt + g) * 4 + kq) * 2048 + i * 1024 + l * 16) = (f32x4){a[o], a[o + 1], a[o + 2], a[o + 3]};
    }
}
DI void red4_get(float (&v)[8], lds_cptr scr, unsigned rt, unsigned kq, unsigned l) {
  f32x4 t[4][2];
#pragma unroll
  for (int s = 0; s < 4; ++s)
#pragma unroll
    for (int i = 0; i < 2; ++i) t[s][i] = *(const LAS f32x4*)(scr + ((rt * 4 + kq) * 4 + s) * 2048 + i * 1024 + l * 16);
#pragma unroll
  for (int i = 0; i < 2; ++i)
#pragma unroll
    for (int k = 0; k < 4; ++k) v[4 * i + k] = (t[0][i][k] + t[1][i][k]) + (t[2][i][k] + t[3][i][k]);
}
DI void tile_mm8(const bf16x8 (&af)[8][2], const u32x4 (&xb)[8], float (&v)[8], lds_ptr scr, unsigned rt, unsigned kq, unsigned l) {
  f32x16 a0 = {}, a1 = {};
#pragma unroll
  for (int i = 0; i < 8; ++i) a0 = MFMA32(af[i][0], __builtin_bit_cast(bf16x8, xb[i]), a0);
  SBAR();
#pragma unroll
  for (int i = 0; i < 4; ++i) a1 = MFMA32(af[i][1], __builtin_bit_cast(bf16x8, xb[i]), a1);
  SBAR();
  red4_put(a0, 0, scr, rt, kq, l);
#pragma unroll
  for (int i = 4; i < 8; ++i) a1 = MFMA32(af[i][1], __builtin_bit_cast(bf16x8, xb[i]), a1);
  SBAR();
  red4_put(a1, 1, scr, rt, kq, l);
  BAR_L();
  red4_get(v, scr, rt, kq, l);
}

struct Frag3 { bf16x8 a, b[2]; };
constexpr int RING3 = 13;
constexpr int LA = 0, LB = 65536, LC = 131072;
DI unsigned r3off(int s, unsigned rc, unsigned rb) { return s < 4 ? rc + s * 1024u : rb + (s - 4) * 1024u; }
template <int KS> DI void load_frags3(Frag3& f, lds_cptr lds, unsigned rc, unsigned rb, unsigned a3, const LaneC& c) {
  const lds_cptr sl = lds + r3off(KS % RING3, rc, rb) + a3;
  f.a = cat8(lds_tr(sl), lds_tr(sl + 256));
#pragma unroll
  for (int rt = 0; rt < 2; ++rt) f.b[rt] = lds_b128(lds + LA + c.xb[KS & 3] + 2048 * KS + 512 * rt);
}
template <int KS>
DI void g3_step(f32x16 (&acc)[2], Frag3& cur, Frag3& n1, Frag3& n2, lds_cptr lds, unsigned lds0, unsigned rc, unsigned rb,
                unsigned voff, __amdgpu_buffer_rsrc_t srd, unsigned soff0, unsigned a3, const LaneC& c) {
  constexpr int NKS = 32;
  if constexpr (KS + 2 < NKS) {
    constexpr int last = (KS + RING3 < NKS ? KS + RING3 : NKS) - 1;
    wait_vm<last - (KS + 2)>();
    load_frags3<KS + 2>(n2, lds, rc, rb, a3, c);
  }
  SBAR();
  acc[0] = MFMA32(cur.a, cur.b[0], acc[0]);
  acc[1] = MFMA32(cur.a, cur.b[1], acc[1]);
  if constexpr (KS + RING3 < NKS) {
    asm volatile("" :: "v"(cur.a));
    dma16(lds0 + r3off(KS % RING3, rc, rb), voff, srd, soff0 + (unsigned)(16 * (KS + RING3)) * 3584u);
  }
  SBAR();
  if constexpr (KS + 1 < NKS) g3_step<KS + 1>(acc, n1, n2, cur, lds, lds0, rc, rb, voff, srd, soff0, a3, c);
}
DI bool has_sent(const u32x4* v, int n) {
  unsigned m = 0;
#pragma unroll
  for (int i = 0; i < n; ++i) { m = umax3(m, v[i][0], v[i][1]); m = umax3(m, v[i][2], v[i][3]); }
  return ANY(m == SENT);
}

__global__ __launch_bounds__(512, 2) void kb_mlp(const unsigned short* __restrict__ x, const float* __restrict__ mod,
                                                 const u32x4* __restrict__ p1, const unsigned short* __restrict__ w1,
                                                 const unsigned short* __restrict__ w2, const unsigned short* __restrict__ w3,
                                                 const unsigned short* __restrict__ w4, void* sreg, u32x4* __restrict__ out) {
  extern __shared__ __attribute__((aligned(16))) char smem[];
  const lds_ptr lds = (lds_ptr)smem;
  const unsigned lds0 = (unsigned)(uintptr_t)smem;
  const unsigned idx = BIDX >> 3, pn = 8 * ((BIDX & 7) >> 1) + (idx >> 2), j = 4 * (BIDX & 1) + (idx & 3);
  const unsigned tid = TIDX, w = rfl(tid >> 6);
  LaneC c; lane_consts(c);
  const unsigned l = c.lane;
  const __amdgpu_buffer_rsrc_t srdS = mksrd(sreg, S_BYTES);
  const unsigned vw = wdma_voff(l, 1024u);
  const unsigned rt = w & 1, kq = w >> 1;
  constexpr int LMOD = LC;
  constexpr int LRED = LC + 28672;
  const unsigned prow = 32 * rt + c.r;
  const unsigned trow = pn * 64 + prow;
  const unsigned och = 8 * j + 2 * kq + c.h;
  const unsigned ovo = ((pn * 64 + och) * 64 + prow) * 16u;
  float gb[8];
  STAMP(2, 0);

  {
    const unsigned row = pn * 64 + j * 8 + (tid & 7), ch = tid >> 3;
    dma16(lds0 + LMOD + w * 1024u, l * 16u, mksrd(mod + 1024, 8192u), w * 1024u);
    u32x4 p[8], xr;
#pragma unroll
    for (int g = 0; g < 8; ++g) gld16(p[g], p1 + ((size_t)(pn * 8 + j) * 8 + g) * 512 + tid);
    gld16(xr, x + (size_t)row * D + ch * 8);
    asm volatile("s_waitcnt vmcnt(9)\n\ts_barrier" ::: "memory");
    float ga[8], sb[8], cb[8], g2[8];
#pragma unroll
    for (int i = 0; i < 2; ++i) {
      const f32x4 a = *(const LAS f32x4*)(lds + LMOD + ch * 32 + 16 * i), b = *(const LAS f32x4*)(lds + LMOD + 2048 + ch * 32 + 16 * i),
                  cc = *(const LAS f32x4*)(lds + LMOD + 4096 + ch * 32 + 16 * i), d = *(const LAS f32x4*)(lds + LMOD + 6144 + ch * 32 + 16 * i),
                  e = *(const LAS f32x4*)(lds + LMOD + 6144 + och * 32 + 16 * i);
#pragma unroll
      for (int k = 0; k < 4; ++k) { ga[4 * i + k] = a[k]; sb[4 * i + k] = b[k]; cb[4 * i + k] = 1.f + cc[k]; g2[4 * i + k] = d[k]; gb[4 * i + k] = e[k]; }
    }
    asm volatile("s_waitcnt vmcnt(0)" : "+v"(p[0]), "+v"(p[1]), "+v"(p[2]), "+v"(p[3]), "+v"(p[4]), "+v"(p[5]), "+v"(p[6]), "+v"(p[7]),
                 "+v"(xr) :: "memory");
    STAMP(2, 1);
    const __amdgpu_buffer_rsrc_t srd1 = mksrd(w1, 512u * 512u * 2u), srd2 = mksrd(w2, 512u * 512u * 2u);
#pragma unroll
    for (int i = 0; i < 4; ++i) dma16(lds0 + LA + (8 * w + i) * 1024u, vw, srd1, (8 * w + i) * 8192u + j * 128u);
    float v[8], x2[8];
#pragma unroll
    for (int i = 0; i < 4; ++i) {
      const float lo = ((bflo(p[0][i]) + bflo(p[1][i])) + (bflo(p[2][i]) + bflo(p[3][i]))) + ((bflo(p[4][i]) + bflo(p[5][i])) + (bflo(p[6][i]) + bflo(p[7][i])));
      const float hi = ((bfhi(p[0][i]) + bfhi(p[1][i])) + (bfhi(p[2][i]) + bfhi(p[3][i]))) + ((bfhi(p[4][i]) + bfhi(p[5][i])) + (bfhi(p[6][i]) + bfhi(p[7][i])));
      v[2 * i] = x2[2 * i] = __builtin_fmaf(ga[2 * i], lo, bflo(xr[i]));
      v[2 * i + 1] = x2[2 * i + 1] = __builtin_fmaf(ga[2 * i + 1], hi, bfhi(xr[i]));
    }
    ln8(v, (LAS float*)(lds + LRED), tid);
#pragma unroll
    for (int i = 4; i < 8; ++i) dma16(lds0 + LA + (8 * w + i) * 1024u, vw, srd1, (8 * w + i) * 8192u + j * 128u);
    float hb[8];
#pragma unroll
    for (int k = 0; k < 8; ++k) v[k] = hb[k] = __builtin_fmaf(v[k], cb[k], sb[k]);
    ln8(v, (LAS float*)(lds + LRED + 512), tid);
    const unsigned vo = ((pn * 64 + ch) * 64 + j * 8 + (tid & 7)) * 16u, vz = ((pn * 64 + ch) * 256 + j * 8 + (tid & 7)) * 16u;
    pub16(srdS, vo, S_U, (u32x4){cvtpk(v[0], v[1]), cvtpk(v[2], v[3]), cvtpk(v[4], v[5]), cvtpk(v[6], v[7])});
#pragma unroll
    for (int i = 0; i < 2; ++i) {
      pub16(srdS, vz, S_HZ + i * 1024u, (u32x4){F2U(hb[4 * i]), F2U(hb[4 * i + 1]),
                                                F2U(hb[4 * i + 2]), F2U(hb[4 * i + 3])});
      f32x4 z;
#pragma unroll
      for (int e = 0; e < 4; ++e) z[e] = __builtin_fmaf(g2[4 * i + e], hb[4 * i + e], x2[4 * i + e]);
      pub16(srdS, vz, S_HZ + (2 + i) * 1024u, __builtin_bit_cast(u32x4, z));
    }
    WAIT_BAR(5);
#pragma unroll
    for (int i = 0; i < 8; ++i) dma16(lds0 + LB + (8 * w + i) * 1024u, vw, srd2, (8 * w + i) * 8192u + j * 128u);
  }
  STAMP(2, 2);
  const __amdgpu_buffer_rsrc_t srd3 = mksrd(w3, 512u * F * 2u);
  const unsigned rc = LC + w * 4096u, rb = LB + w * 9216u;
  const unsigned v3 = (l >> 2) * 3584u + ((l & 3) << 4), s30 = (j * 224u + w * 32u) * 2u;
  float v[8];
  {
    bf16x8 af[8][2];
#pragma unroll
    for (int i = 0; i < 8; ++i)
#pragma unroll
      for (int nt = 0; nt < 2; ++nt) {
        const lds_cptr a = lds + LA + (8 * kq + i) * 2048 + c.afr[nt];
        af[i][nt] = cat8(lds_tr(a), lds_tr(a + 512));
      }
    BAR_L();
    u32x4 xb[8];
    poll_load<8>(xb, srdS, ((pn * 64 + 16 * kq + c.h) * 64 + prow) * 16u, S_U, 2048u);
    STAMP(2, 3);
    tile_mm8(af, xb, v, lds + LA, rt, kq, l);
#pragma unroll
    for (int k = 0; k < 8; ++k) v[k] = gelu_tanh(v[k]);
    pub16(srdS, ovo, S_H1, (u32x4){cvtpk(v[0], v[1]), cvtpk(v[2], v[3]), cvtpk(v[4], v[5]), cvtpk(v[6], v[7])});
    if (w < 7) {
#pragma unroll
      for (int ks = 0; ks < 4; ++ks) dma16(lds0 + r3off(ks, rc, rb), v3, srd3, s30 + (unsigned)(16 * ks) * 3584u);
    }
  }
  STAMP(2, 4);
  float y[8];
  {
    bf16x8 af[8][2];
#pragma unroll
    for (int i = 0; i < 8; ++i)
#pragma unroll
      for (int nt = 0; nt < 2; ++nt) {
        const lds_cptr a = lds + LB + (8 * kq + i) * 2048 + c.afr[nt];
        af[i][nt] = cat8(lds_tr(a), lds_tr(a + 512));
      }
    BAR_L();
    u32x4 hz[4];
    poll_load<4>(hz, srdS, ((pn * 64 + och) * 256 + prow) * 16u, S_HZ, 1024u);
    const int sh = kq == 0 ? -1 : kq == 1 ? 1 : kq == 2 ? -2 : 2;
    const int srow = (int)trow + sh;
    const unsigned xvo = (srow >= 0 && srow < T) ? ((((unsigned)srow >> 6) * 64 + 16 * kq + c.h) * 64 + ((unsigned)srow & 63)) * 16u : 0xF0000000u;
    u32x4 xb[8];
    poll_load<8>(xb, srdS, xvo, S_H1, 2048u);
    STAMP(2, 5);
    tile_mm8(af, xb, v, lds + LA, rt, kq, l);
    float x1[8];
#pragma unroll
    for (int k = 0; k < 8; ++k) {
      x1[k] = U2F(hz[k >> 2][k & 3]) + v[k];
      y[k] = __builtin_fmaf(gb[k], v[k], U2F(hz[2 + (k >> 2)][k & 3]));
    }
    pub16(srdS, ovo, S_X1, (u32x4){cvtpk(x1[0], x1[1]), cvtpk(x1[2], x1[3]), cvtpk(x1[4], x1[5]), cvtpk(x1[6], x1[7])});
    if (w < 7) {
#pragma unroll
      for (int ks = 4; ks < RING3; ++ks) dma16(lds0 + r3off(ks, rc, rb), v3, srd3, s30 + (unsigned)(16 * ks) * 3584u);
    }
  }
  STAMP(2, 6);
  const __amdgpu_buffer_rsrc_t srd4 = mksrd(w4 + (size_t)j * 224 * 512, 224u * 1024u);
  const unsigned ringoff = LB + w * (RING * 2048), ring = lds0 + ringoff;
  {
    f32x4 cs[4];
    if (w < 7) {
#pragma unroll
      for (int i = 0; i < 4; ++i) cs[i] = *(const f32x4*)(mod + 3072 + 224 * j + 32 * w + 16 * (i >> 1) + 8 * c.h + 4 * (i & 1));
    }
    u32x4 xr[8];
    BAR_L();
    poll_load<8>(xr, srdS, ((pn * 64 + 8 * w) * 64 + l) * 16u, S_X1, 1024u);
    STAMP(2, 7);
#pragma unroll
    for (int i = 0; i < 8; ++i) *(LAS u32x4*)(lds + LA + ximg(8 * w + i, l)) = xr[i];
    {
      float s0 = 0.f, s1 = 0.f, q0 = 0.f, q1 = 0.f;
#pragma unroll
      for (int i = 0; i < 8; ++i)
#pragma unroll
        for (int k = 0; k < 4; k += 2) {
          s0 = dot2bf(xr[i][k], 0x3F803F80u, s0); s1 = dot2bf(xr[i][k + 1], 0x3F803F80u, s1);
          q0 = dot2bf(xr[i][k], xr[i][k], q0); q1 = dot2bf(xr[i][k + 1], xr[i][k + 1], q1);
        }
      *(LAS f32x2*)(lds + LRED + (w * 64 + l) * 8) = (f32x2){s0 + s1, q0 + q1};
    }
    BAR_L();
    STAMP(2, 8);
    u32x4 hp[2][2];
    if (w < 7) {
      f32x16 acc[2] = {};
      const unsigned a3 = (8 * c.h + c.q) * 64 + 32 * c.blk + 8 * (2 * (c.p & 1) + (c.p >> 1));
      Frag3 f0, f1, f2;
      wait_vm<RING3 - 2>();
      load_frags3<0>(f0, lds, rc, rb, a3, c);
      load_frags3<1>(f1, lds, rc, rb, a3, c);
      float mu[2], rs[2];
#pragma unroll
      for (int r2 = 0; r2 < 2; ++r2) {
        float s = 0.f, q = 0.f;
#pragma unroll
        for (int i = 0; i < 8; ++i) {
          const f32x2 t = *(const LAS f32x2*)(lds + LRED + (i * 64 + 32 * r2 + c.r) * 8);
          s += t[0]; q += t[1];
        }
        mu[r2] = s * (1.0f / D);
        rs[r2] = __builtin_amdgcn_rsqf(__builtin_fmaxf(__builtin_fmaf(-mu[r2], mu[r2], q * (1.0f / D)), 0.f) + LN_EPS);
      }
      if (w < 4) __builtin_amdgcn_s_setprio(2); else __builtin_amdgcn_s_setprio(1);
      g3_step<0>(acc, f0, f1, f2, lds, lds0, rc, rb, v3, srd3, s30, a3, c);
      __builtin_amdgcn_s_setprio(0);
      STAMP(2, 9);
#pragma unroll
      for (int r2 = 0; r2 < 2; ++r2) {
#pragma unroll
        for (int i = 0; i < 16; ++i) acc[r2][i] = gelu_tanh(rs[r2] * __builtin_fmaf(-mu[r2], cs[i >> 2][i & 3], acc[r2][i]));
        hp[r2][0] = pack8(acc[r2], 0, 1.f); hp[r2][1] = pack8(acc[r2], 1, 1.f);
      }
    }
    BAR_L();
#pragma unroll
    for (int ks = 0; ks < RING; ++ks) wring_issue(ks, ring, vw, srd4, w * 128u, 1024u);
    if (w < 7) {
#pragma unroll
      for (int r2 = 0; r2 < 2; ++r2)
#pragma unroll
        for (int s2 = 0; s2 < 2; ++s2) *(LAS u32x4*)(lds + LA + ximg(4 * w + 2 * s2 + c.h, 32 * r2 + c.r)) = hp[r2][s2];
    }
    BAR_L();
  }
  STAMP(2, 10);
  {
    f32x16 acc[2][2] = {};
    gemm_phase<14, 0>(acc, lds, ringoff, ring, vw, srd4, w * 128u, 1024u, c);
    STAMP(2, 11);
    const unsigned dst = ((((pn * 8 + w) * 8 + j) * 8 + c.h) * 64 + c.r) * 16u;
#pragma unroll
    for (int nt = 0; nt < 2; ++nt)
#pragma unroll
      for (int r2 = 0; r2 < 2; ++r2)
#pragma unroll
        for (int s2 = 0; s2 < 2; ++s2) pub16(srdS, dst + ((4 * nt + 2 * s2) * 64 + 32 * r2) * 16u, S_P2, pack8(acc[nt][r2], s2, 1.f));
  }
  STAMP(2, 12);
  {
    u32x4 p[8];
    poll_load<8>(p, srdS, (((pn * 8 + j) * 64 + 2 * kq + c.h) * 64 + prow) * 16u, S_P2, 8192u);
    STAMP(2, 13);
#pragma unroll
    for (int i = 0; i < 4; ++i) {
      const float lo = ((bflo(p[0][i]) + bflo(p[1][i])) + (bflo(p[2][i]) + bflo(p[3][i]))) + ((bflo(p[4][i]) + bflo(p[5][i])) + (bflo(p[6][i]) + bflo(p[7][i])));
      const float hi = ((bfhi(p[0][i]) + bfhi(p[1][i])) + (bfhi(p[2][i]) + bfhi(p[3][i]))) + ((bfhi(p[4][i]) + bfhi(p[5][i])) + (bfhi(p[6][i]) + bfhi(p[7][i])));
      v[2 * i] = __builtin_fmaf(gb[2 * i], lo, y[2 * i]);
      v[2 * i + 1] = __builtin_fmaf(gb[2 * i + 1], hi, y[2 * i + 1]);
    }
    out[(size_t)trow * 64 + och] = (u32x4){cvtpk(v[0], v[1]), cvtpk(v[2], v[3]), cvtpk(v[4], v[5]), cvtpk(v[6], v[7])};
  }
  STAMP(2, 14);
}

#if defined(__HIP_DEVICE_COMPILE__)
#pragma clang attribute pop
#endif
extern "C" void kernel_launch(void* const* d_in, const int* in_sizes, int n_in, void* d_out, int out_size, void* d_ws,
                              size_t ws_size, hipStream_t stream) {
  const unsigned short* x = (const unsigned short*)d_in[0];
  const unsigned short* kc = (const unsigned short*)d_in[1];
  const unsigned short* vc = (const unsigned short*)d_in[2];
  const unsigned short* wq = (const unsigned short*)d_in[3];
  const unsigned short* wo = (const unsigned short*)d_in[4];
  const unsigned short* w1 = (const unsigned short*)d_in[6];
  const unsigned short* w2 = (const unsigned short*)d_in[7];
  const unsigned short* w3 = (const unsigned short*)d_in[8];
  const unsigned short* w4 = (const unsigned short*)d_in[9];
  const unsigned short* cc = (const unsigned short*)d_in[10];
  const unsigned short* wm = (const unsigned short*)d_in[11];
  char* ws = (char*)d_ws;
  float* mod = (float*)(ws + WS_MOD);
  u32x4* p1 = (u32x4*)(ws + WS_P1);
  void* sreg = (void*)(ws + WS_S);
  (void)hipFuncSetAttribute((const void*)k1_attn, hipFuncAttributeMaxDynamicSharedMemorySize, LDS_TOTAL);
  (void)hipFuncSetAttribute((const void*)kb_mlp, hipFuncAttributeMaxDynamicSharedMemorySize, LDS_TOTAL);
  k0_mod<<<280, 256, 0, stream>>>(cc, wm, w3, mod, wq, kc, vc);
  k1_attn<<<256, 512, LDS_TOTAL, stream>>>(x, kc, vc, wq, wo, mod, p1, sreg, w1, w2, w3, w4);
  kb_mlp<<<256, 512, LDS_TOTAL, stream>>>(x, mod, p1, w1, w2, w3, w4, sreg, (u32x4*)d_out);
}
```

```cpp
#include <hip/hip_runtime.h>
#include <hip/hip_bf16.h>
#include <stdint.h>

#if defined(__HIP_DEVICE_COMPILE__)
#pragma clang attribute push(__attribute__((target("no-packed-fp32-ops"))), apply_to = function)
#endif
typedef short bf16x8 __attribute__((ext_vector_type(8)));
typedef short s16x4 __attribute__((ext_vector_type(4)));
typedef float f32x16 __attribute__((ext_vector_type(16)));
typedef float f32x4 __attribute__((ext_vector_type(4)));
typedef float f32x2 __attribute__((ext_vector_type(2)));
typedef unsigned u32x4 __attribute__((ext_vector_type(4)));
typedef unsigned u32x2 __attribute__((ext_vector_type(2)));
typedef __bf16 bf16x2_t __attribute__((ext_vector_type(2)));
typedef short v4i16_t __attribute__((ext_vector_type(4)));
#define LAS __attribute__((address_space(3)))
typedef LAS const char* lds_cptr;
typedef LAS char* lds_ptr;
#define DI __device__ __forceinline__
#define F2U(x) __builtin_bit_cast(unsigned, (float)(x))
#define U2F(x) __builtin_bit_cast(float, (unsigned)(x))
#define F2I(x) __builtin_bit_cast(int, (float)(x))
#define I2F(x) __builtin_bit_cast(float, (int)(x))
#define TIDX __builtin_amdgcn_workitem_id_x()
#define BIDX __builtin_amdgcn_workgroup_id_x()
#define ANY(p) (__builtin_amdgcn_ballot_w64(p) != 0ull)
#define SBAR() __builtin_amdgcn_sched_barrier(0)
#define MFMA32(a, b, c) __builtin_amdgcn_mfma_f32_32x32x16_bf16((a), (b), (c), 0, 0, 0)
#define WAIT_V(n) asm volatile("s_waitcnt vmcnt(" #n ")" ::: "memory")
#define WAIT_L0() asm volatile("s_waitcnt lgkmcnt(0)" ::: "memory")
#define BAR_L() asm volatile("s_waitcnt lgkmcnt(0)\n\ts_barrier" ::: "memory")
#define WAIT_BAR(n) asm volatile("s_waitcnt vmcnt(" #n ") lgkmcnt(0)\n\ts_barrier" ::: "memory")

constexpr int T = 2048, D = 512, HKV = 8, SK = 2048, DH = 128, WIN = 512, F = 1792;
constexpr float LN_EPS = 1e-5f;
constexpr float QSCALE = 0.088388347648318440f * 1.4426950408889634f;

constexpr unsigned W_MOD = 0;
constexpr unsigned W_FLAG = 32768;
constexpr unsigned W_P1 = 65536;
constexpr unsigned S_U = W_P1 + 8u * 64u * T * 16u;
constexpr unsigned S_H1 = S_U + 64u * T * 16u;
constexpr unsigned S_X1 = S_H1 + 64u * T * 16u;
constexpr unsigned S_HZ = S_X1 + 64u * T * 16u;
constexpr unsigned S_H3 = S_HZ + 256u * T * 16u;
constexpr unsigned W_END = S_H3 + 224u * T * 16u;
constexpr unsigned OOB = 0xF0000000u;
#define ATHR 32.0f
constexpr float THR = ATHR;
constexpr unsigned SENT = 0xFFFFFFFFu;
constexpr unsigned SPIN_CAP = 1u << 16;

constexpr int LDS_X = 0;
constexpr int LDS_R = 65536;
constexpr int RING = 5, RINGB = RING * 2048;
constexpr int LDS_D = LDS_R + 8 * RINGB;
constexpr int LDS_TOTAL = LDS_D + 16384;
static_assert(LDS_TOTAL == 163840, "lds");

#define STAMP(kid, i) do {} while (0)
DI unsigned cvtpk(float lo, float hi) {
  f32x2 v = {lo, hi};
  bf16x2_t b = __builtin_convertvector(v, bf16x2_t);
  return __builtin_bit_cast(unsigned, b);
}
DI float bflo(unsigned u) { return U2F(u << 16); }
DI float bfhi(unsigned u) { return U2F(u & 0xffff0000u); }
DI unsigned rfl(unsigned x) { return __builtin_amdgcn_readfirstlane(x); }
DI __amdgpu_buffer_rsrc_t mksrd(const void* p, unsigned bytes) {
  return __builtin_amdgcn_make_buffer_rsrc((void*)p, (short)0, (int)bytes, 0x00020000);
}
DI void dma16(unsigned lds, unsigned voff, __amdgpu_buffer_rsrc_t srd, unsigned soff) {
  asm volatile("s_mov_b32 m0, %0\n\ts_nop 0\n\tbuffer_load_dwordx4 %1, %2, %3 offen lds"
               :: "s"(lds), "v"(voff), "s"(srd), "s"(soff) : "memory");
}
DI void gld16(u32x4& d, const void* p) { asm volatile("global_load_dwordx4 %0, %1, off" : "=v"(d) : "v"(p) : "memory"); }
DI void bld16_sc1(u32x4& d, unsigned voff, __amdgpu_buffer_rsrc_t srd, unsigned soff) {
  asm volatile("buffer_load_dwordx4 %0, %1, %2, %3 offen sc1" : "=v"(d) : "v"(voff), "s"(srd), "s"(soff) : "memory");
}
DI void dma16_sc1(unsigned lds, unsigned voff, __amdgpu_buffer_rsrc_t srd, unsigned soff) {
  asm volatile("s_mov_b32 m0, %0\n\ts_nop 0\n\tbuffer_load_dwordx4 %1, %2, %3 offen sc1 lds"
               :: "s"(lds), "v"(voff), "s"(srd), "s"(soff) : "memory");
}
DI bf16x8 lds_b128(lds_cptr p) { return *(const LAS bf16x8*)p; }
DI s16x4 lds_tr(lds_cptr p) {
  return __builtin_bit_cast(s16x4, __builtin_amdgcn_ds_read_tr16_b64_v4i16((LAS v4i16_t*)p));
}
DI bf16x8 cat8(s16x4 lo, s16x4 hi) { return __builtin_shufflevector(lo, hi, 0, 1, 2, 3, 4, 5, 6, 7); }
DI float swap32_add(float x) {
  auto r = __builtin_amdgcn_permlane32_swap(F2U(x), F2U(x), false, false);
  return U2F(r[0]) + U2F(r[1]);
}
DI float swap32_max(float x) {
  auto r = __builtin_amdgcn_permlane32_swap(F2U(x), F2U(x), false, false);
  return __builtin_fmaxf(U2F(r[0]), U2F(r[1]));
}
template <int CTRL> DI float dpp_add(float x) {
  int y = __builtin_amdgcn_update_dpp(0, F2I(x), CTRL, 0xF, 0xF, true);
  return x + I2F(y);
}
DI float max3f(float a, float b, float c) { float r; asm("v_max3_f32 %0, %1, %2, %3" : "=v"(r) : "v"(a), "v"(b), "v"(c)); return r; }
DI float max2f(float a, float b) { float r; asm("v_max_f32_e32 %0, %1, %2" : "=v"(r) : "v"(a), "v"(b)); return r; }
DI float dot2bf(unsigned a, unsigned b, float c) {
  return __builtin_amdgcn_fdot2_f32_bf16(__builtin_bit_cast(bf16x2_t, a), __builtin_bit_cast(bf16x2_t, b), c, false);
}
#define PIN(x) asm volatile("" : "+v"(x))
DI float swap16_add(float x) {
  auto r = __builtin_amdgcn_permlane16_swap(F2U(x), F2U(x), false, false);
  return U2F(r[0]) + U2F(r[1]);
}
DI float sum_hi3(float x) { return swap32_add(swap16_add(dpp_add<0x128>(x))); }
DI float gelu_tanh(float x) {
  const float k0 = -2.0f * 0.7978845608028654f * 1.4426950408889634f;
  const float t = x * x;
  const float z = x * __builtin_fmaf(t, 0.044715f * k0, k0);
  const float e = __builtin_amdgcn_exp2f(z);
  return x * __builtin_amdgcn_rcpf(1.0f + e);
}
DI void wave_sum8(const float (&a)[8], float (&out)[8]) {
  float b[4], c[2];
#pragma unroll
  for (int i = 0; i < 4; ++i) {
    auto r = __builtin_amdgcn_permlane32_swap(F2U(a[i]), F2U(a[i + 4]), false, false);
    b[i] = U2F(r[0]) + U2F(r[1]);
  }
#pragma unroll
  for (int i = 0; i < 2; ++i) {
    auto r = __builtin_amdgcn_permlane16_swap(F2U(b[i]), F2U(b[i + 2]), false, false);
    c[i] = U2F(r[0]) + U2F(r[1]);
  }
#pragma unroll
  for (int i = 0; i < 2; ++i) {
    c[i] = dpp_add<0xB1>(c[i]);
    c[i] = dpp_add<0x4E>(c[i]);
    c[i] = dpp_add<0x141>(c[i]);
    c[i] = dpp_add<0x140>(c[i]);
  }
#pragma unroll
  for (int q = 0; q < 4; ++q)
#pragma unroll
    for (int i = 0; i < 2; ++i)
      out[i + 2 * (q & 1) + 4 * (q >> 1)] = I2F(__builtin_amdgcn_readlane(F2I(c[i]), 16 * q));
}

DI unsigned nosent(unsigned v) { return v < 0xFFFFFFFEu ? v : 0xFFFFFFFEu; }
DI void pub16(__amdgpu_buffer_rsrc_t srd, unsigned voff, unsigned soff, u32x4 v) {
  v = (u32x4){nosent(v[0]), nosent(v[1]), nosent(v[2]), nosent(v[3])};
  __builtin_amdgcn_raw_buffer_store_b128(v, srd, voff + soff, 0, 16);
}
DI unsigned umax3(unsigned a, unsigned b, unsigned c) { const unsigned m = a > b ? a : b; return m > c ? m : c; }
DI bool has_sent(const u32x4* v, int n) {
  unsigned m = 0;
#pragma unroll
  for (int i = 0; i < n; ++i) { m = umax3(m, v[i][0], v[i][1]); m = umax3(m, v[i][2], v[i][3]); }
  return ANY(m == SENT);
}
template <int N> DI void poll_load(u32x4* v, __amdgpu_buffer_rsrc_t srd, unsigned voff, unsigned soff0, unsigned step) {
  for (unsigned spin = 0;; ++spin) {
    asm volatile("" ::: "memory");
#pragma unroll
    for (int i = 0; i < N; ++i) v[i] = __builtin_amdgcn_raw_buffer_load_b128(srd, voff, soff0 + (unsigned)i * step, 16);
    if (!has_sent(v, N) || spin > SPIN_CAP) break;
    __builtin_amdgcn_s_sleep(1);
  }
}
template <int N, int I0, int I1> DI void poll_ind(u32x4* v, __amdgpu_buffer_rsrc_t srd, unsigned voff, unsigned soff0, unsigned step) {
  u32x4 ind[2];
  poll_load<2>(ind, srd, voff, soff0 + (unsigned)I0 * step, (unsigned)(I1 - I0) * step);
  poll_load<N>(v, srd, voff, soff0, step);
}

__global__ __launch_bounds__(256) void k_init(void* ws) {
  const __amdgpu_buffer_rsrc_t srd = mksrd(ws, W_P1);
  const unsigned u = BIDX * 256 + TIDX;
  const unsigned v = u < 2048 ? SENT : 0u;
  __builtin_amdgcn_raw_buffer_store_b128((u32x4){v, v, v, v}, srd, u * 16u, 0, 16);
}

struct LaneC {
  unsigned lane, r, h, q, p, blk;
  unsigned wv;
  unsigned afr[2];
  unsigned xb[4];
};
DI void lane_consts(LaneC& c) {
  const unsigned l = TIDX & 63;
  c.lane = l; c.r = l & 31; c.h = l >> 5; c.q = (l & 15) >> 2; c.p = l & 3; c.blk = (l >> 4) & 1;
  const unsigned sp = 2 * (c.p & 1) + (c.p >> 1);
#pragma unroll
  for (int nt = 0; nt < 2; ++nt) c.afr[nt] = (8 * c.h + c.q) * 128 + 64 * (nt ^ (c.q >> 1)) + 32 * c.blk + 8 * sp;
#pragma unroll
  for (int s = 0; s < 4; ++s) c.xb[s] = 1024 * c.h + ((c.r ^ (2 * s + c.h)) << 4);
}
DI unsigned wdma_voff(unsigned l, unsigned ld) { return (l >> 3) * ld + (((l & 7) ^ (((l >> 4) & 1) << 2)) << 4); }

DI void wring_issue(int ks, unsigned ring, unsigned voff, __amdgpu_buffer_rsrc_t srd, unsigned soff0, unsigned ld) {
  const unsigned slot = ring + (unsigned)(ks % RING) * 2048u;
  dma16(slot, voff, srd, soff0 + (unsigned)(16 * ks) * ld);
  dma16(slot + 1024u, voff, srd, soff0 + (unsigned)(16 * ks + 8) * ld);
}
template <int N> DI void wait_vm() {
  if constexpr (N == 0) WAIT_V(0); else if constexpr (N == 1) WAIT_V(1); else if constexpr (N == 2) WAIT_V(2);
  else if constexpr (N == 3) WAIT_V(3); else if constexpr (N == 4) WAIT_V(4); else if constexpr (N == 5) WAIT_V(5);
  else if constexpr (N == 6) WAIT_V(6); else if constexpr (N == 7) WAIT_V(7); else if constexpr (N == 8) WAIT_V(8);
  else if constexpr (N == 9) WAIT_V(9); else if constexpr (N == 10) WAIT_V(10); else if constexpr (N == 11) WAIT_V(11);
  else if constexpr (N == 12) WAIT_V(12); else if constexpr (N == 13) WAIT_V(13); else if constexpr (N == 14) WAIT_V(14);
  else if constexpr (N == 15) WAIT_V(15); else if constexpr (N == 16) WAIT_V(16);
  else static_assert(N < 0, "wait_vm");
}

struct Frag { bf16x8 a[2], b[2]; };
template <int KS> DI void load_frags(Frag& f, lds_cptr lds, unsigned ringoff, const LaneC& c) {
  const lds_cptr sl = lds + ringoff + (KS % RING) * 2048;
#pragma unroll
  for (int nt = 0; nt < 2; ++nt) f.a[nt] = cat8(lds_tr(sl + c.afr[nt]), lds_tr(sl + c.afr[nt] + 512));
#pragma unroll
  for (int rt = 0; rt < 2; ++rt) f.b[rt] = lds_b128(lds + LDS_X + c.xb[KS & 3] + 2048 * KS + 512 * rt);
}
template <int NKS, int KS, int EXTRA>
DI void gemm_step(f32x16 (&acc)[2][2], Frag& cur, Frag& nxt, lds_cptr lds, unsigned ringoff, unsigned ring, unsigned voff,
                  __amdgpu_buffer_rsrc_t srd, unsigned soff0, unsigned ld, const LaneC& c) {
  if constexpr (KS + 1 < NKS) {
    constexpr int last = (KS + RING < NKS ? KS + RING : NKS) - 1;
    constexpr int after = 2 * (last - (KS + 1)) + ((KS + 1 < RING) ? EXTRA : 0);
    wait_vm<after>();
    load_frags<KS + 1>(nxt, lds, ringoff, c);
  }
  SBAR();
#pragma unroll
  for (int nt = 0; nt < 2; ++nt)
#pragma unroll
    for (int rt = 0; rt < 2; ++rt) acc[nt][rt] = MFMA32(cur.a[nt], cur.b[rt], acc[nt][rt]);
  if constexpr (KS + RING < NKS) {
    asm volatile("" :: "v"(cur.a[0]), "v"(cur.a[1]));
    wring_issue(KS + RING, ring, voff, srd, soff0, ld);
  }
  SBAR();
  if constexpr (KS + 1 < NKS) gemm_step<NKS, KS + 1, EXTRA>(acc, nxt, cur, lds, ringoff, ring, voff, srd, soff0, ld, c);
}
template <int NKS, int EXTRA>
DI void gemm_phase(f32x16 (&acc)[2][2], lds_cptr lds, unsigned ringoff, unsigned ring, unsigned voff,
                   __amdgpu_buffer_rsrc_t srd, unsigned soff0, unsigned ld, const LaneC& c) {
  Frag f0, f1;
  wait_vm<2 * (RING - 1) + EXTRA>();
  load_frags<0>(f0, lds, ringoff, c);
  gemm_step<NKS, 0, EXTRA>(acc, f0, f1, lds, ringoff, ring, voff, srd, soff0, ld, c);
}
DI u32x4 pack8(const f32x16& a, int s, float sc) {
  return (u32x4){cvtpk(a[8 * s] * sc, a[8 * s + 1] * sc), cvtpk(a[8 * s + 2] * sc, a[8 * s + 3] * sc),
                 cvtpk(a[8 * s + 4] * sc, a[8 * s + 5] * sc), cvtpk(a[8 * s + 6] * sc, a[8 * s + 7] * sc)};
}
DI unsigned ximg(unsigned chunk, unsigned row) { return chunk * 1024u + ((row ^ (chunk & 7u)) << 4); }

constexpr int KSLOT[8] = {LDS_D, LDS_R, LDS_R + 32768, LDS_X, LDS_X + 32768, LDS_D, LDS_X, LDS_X + 32768};
constexpr int VSLOT[8] = {LDS_R + 16384, LDS_R + 49152, LDS_X + 16384, LDS_X + 49152, LDS_R + 65536, LDS_X + 16384, LDS_X + 49152, LDS_D};
struct Params {
  const unsigned short *x, *kc, *vc, *wq, *wo, *w1, *w2, *w3, *w4, *c, *wmod;
  void* ws;
  u32x4* out;
};
DI void attn_part(const Params& P, lds_ptr lds, unsigned lds0, const LaneC& c, unsigned w, __amdgpu_buffer_rsrc_t srdW) {
  const unsigned g = BIDX & 7, pn = BIDX >> 3;
  const unsigned l = c.lane, tid = TIDX;
  const unsigned ringoff = LDS_R + w * RINGB, ring = lds0 + ringoff;
  STAMP(1, 0);

  const bool job = BIDX < 152, isw3 = BIDX >= 96;
  u32x4 jw[4]; unsigned jc[4];
  if (job) {
    const unsigned short* src = isw3 ? P.w3 + (BIDX - 96) * 32 + (tid & 3) * 8 : P.wmod + BIDX * 32 + (tid & 3) * 8;
    const unsigned ld = isw3 ? F : 3072;
#pragma unroll
    for (int i = 0; i < 4; ++i) gld16(jw[i], src + (size_t)((tid >> 2) + 128 * i) * ld);
#pragma unroll
    for (int i = 0; i < 4; ++i) asm volatile("global_load_ushort %0, %1, off" : "=v"(jc[i]) : "v"(P.c + (tid >> 2) + 128 * i) : "memory");
  }
  u32x4 xr[8];
#pragma unroll
  for (int i = 0; i < 8; ++i) gld16(xr[i], P.x + (size_t)(pn * 64 + w * 8 + i) * D + l * 8);
  const __amdgpu_buffer_rsrc_t srdQ = mksrd(P.wq, 512u * 4096u * 2u);
  const unsigned vq = wdma_voff(l, 8192u), sq0 = (g * 512u + w * 64u) * 2u;
  const __amdgpu_buffer_rsrc_t srdK = mksrd(P.kc + ((size_t)g * SK + (SK - WIN)) * DH, WIN * DH * 2u);
  const __amdgpu_buffer_rsrc_t srdV = mksrd(P.vc + ((size_t)g * SK + (SK - WIN)) * DH, WIN * DH * 2u);
  const unsigned vk = ((l >> 2) & 7) * 256u + (l >> 5) * 64u + (((l & 3) ^ (2 * (w & 1) + ((l >> 4) & 1))) << 4);
  const unsigned vv = wdma_voff(l, 256u);
  auto dma_k = [&](int t, unsigned slot) {
    dma16(lds0 + slot + 2048u * w, vk, srdK, (unsigned)t * 16384u + 2048u * w);
    dma16(lds0 + slot + 2048u * w + 1024u, vk, srdK, (unsigned)t * 16384u + 2048u * w + 128u);
  };
  auto dma_v = [&](int t, unsigned slot) {
    dma16(lds0 + slot + 1024u * w, vv, srdV, (unsigned)t * 16384u + 2048u * w);
    dma16(lds0 + slot + 8192u + 1024u * w, vv, srdV, (unsigned)t * 16384u + 2048u * w + 128u);
  };
  {
    const unsigned id = pn * 8 + w;
    dma16(ring + 6144u, l * 16u, srdQ, (2 * id) * 8192u + g * 1024u);
    dma16(ring + 7168u, l * 16u, srdQ, (2 * id + 1) * 8192u + g * 1024u);
    dma16(ring + 8192u, l * 16u, id < 128 ? srdK : srdV, (id & 127u) * 1024u);
  }
  if (job) {
    asm volatile("s_waitcnt vmcnt(11)" : "+v"(jw[0]), "+v"(jw[1]), "+v"(jw[2]), "+v"(jw[3]), "+v"(jc[0]), "+v"(jc[1]), "+v"(jc[2]), "+v"(jc[3]) :: "memory");
    float acc[8] = {0, 0, 0, 0, 0, 0, 0, 0};
#pragma unroll
    for (int i = 0; i < 4; ++i) {
      const float v = U2F(jc[i] << 16);
      const float sv = v * __builtin_amdgcn_rcpf(1.0f + __builtin_amdgcn_exp2f(-1.4426950408889634f * v));
      const float sc = isw3 ? 1.0f : sv;
#pragma unroll
      for (int k = 0; k < 4; ++k) {
        acc[2 * k] = __builtin_fmaf(sc, bflo(jw[i][k]), acc[2 * k]);
        acc[2 * k + 1] = __builtin_fmaf(sc, bfhi(jw[i][k]), acc[2 * k + 1]);
      }
    }
#pragma unroll
    for (int k = 0; k < 8; ++k) acc[k] = sum_hi3(dpp_add<0x124>(acc[k]));
    if (l < 4) {
      *(LAS f32x4*)(lds + LDS_X + w * 128 + l * 32) = (f32x4){acc[0], acc[1], acc[2], acc[3]};
      *(LAS f32x4*)(lds + LDS_X + w * 128 + l * 32 + 16) = (f32x4){acc[4], acc[5], acc[6], acc[7]};
    }
    BAR_L();
    if (tid < 8) {
      f32x4 t = *(const LAS f32x4*)(lds + LDS_X + tid * 16);
#pragma unroll
      for (int i = 1; i < 8; ++i) t += *(const LAS f32x4*)(lds + LDS_X + i * 128 + tid * 16);
      pub16(srdW, tid * 16u, W_MOD + BIDX * 128u, __builtin_bit_cast(u32x4, t));
    }
    BAR_L();
  }
  wring_issue(0, ring, vq, srdQ, sq0, 8192u);

  {
    asm volatile("s_waitcnt vmcnt(5)" : "+v"(xr[0]), "+v"(xr[1]), "+v"(xr[2]), "+v"(xr[3]), "+v"(xr[4]), "+v"(xr[5]), "+v"(xr[6]),
                 "+v"(xr[7]) :: "memory");
    float v[8][8], s[8], m[8];
#pragma unroll
    for (int i = 0; i < 8; ++i) {
#pragma unroll
      for (int j = 0; j < 4; ++j) { v[i][2 * j] = bflo(xr[i][j]); v[i][2 * j + 1] = bfhi(xr[i][j]); }
      s[i] = ((v[i][0] + v[i][1]) + (v[i][2] + v[i][3])) + ((v[i][4] + v[i][5]) + (v[i][6] + v[i][7]));
    }
    wring_issue(1, ring, vq, srdQ, sq0, 8192u);
    wave_sum8(s, m);
#pragma unroll
    for (int i = 0; i < 8; ++i) {
      const float mu = m[i] * (1.0f / D);
      float q = 0.f;
#pragma unroll
      for (int j = 0; j < 8; ++j) { v[i][j] -= mu; q = __builtin_fmaf(v[i][j], v[i][j], q); }
      s[i] = q;
      if (i == 1 || i == 3 || i == 5) wring_issue(2 + i / 2, ring, vq, srdQ, sq0, 8192u);
      if (i == 7) dma_k(0, LDS_D);
    }
    wave_sum8(s, m);
    float rs[8];
#pragma unroll
    for (int i = 0; i < 8; ++i) rs[i] = __builtin_amdgcn_rsqf(m[i] * (1.0f / D) + LN_EPS);
    STAMP(1, 8);
    u32x4 mo[4];
    poll_load<2>(mo, srdW, l * 32u, W_MOD, 16u);
    poll_load<2>(mo + 2, srdW, l * 32u, W_MOD + 2048u, 16u);
    STAMP(1, 9);
#pragma unroll
    for (int i = 0; i < 8; ++i) {
      float hh[8];
#pragma unroll
      for (int j = 0; j < 8; ++j) hh[j] = __builtin_fmaf(v[i][j] * rs[i], 1.f + U2F(mo[2 + (j >> 2)][j & 3]), U2F(mo[j >> 2][j & 3]));
      const u32x4 o = {cvtpk(hh[0], hh[1]), cvtpk(hh[2], hh[3]), cvtpk(hh[4], hh[5]), cvtpk(hh[6], hh[7])};
      *(LAS u32x4*)(lds + LDS_X + ximg(l, w * 8 + i)) = o;
    }
  }
  BAR_L();
  STAMP(1, 1);

  {
    f32x16 acc[2][2] = {};
    gemm_phase<32, 2>(acc, lds, ringoff, ring, vq, srdQ, sq0, 8192u, c);
    STAMP(1, 2);
    WAIT_BAR(0);
    dma_k(1, KSLOT[1]); dma_v(0, VSLOT[0]); dma_k(2, KSLOT[2]); dma_v(1, VSLOT[1]);
#pragma unroll
    for (int nt = 0; nt < 2; ++nt)
#pragma unroll
      for (int rt = 0; rt < 2; ++rt)
#pragma unroll
        for (int s = 0; s < 2; ++s)
          *(LAS u32x4*)(lds + LDS_X + ximg(8 * w + 4 * nt + 2 * s + c.h, 32 * rt + c.r)) = pack8(acc[nt][rt], s, QSCALE);
    BAR_L();
  }

  STAMP(1, 3);
  const unsigned hh = w >> 1, rt = w & 1;
  bf16x8 qf[8];
#pragma unroll
  for (int s = 0; s < 8; ++s) qf[s] = lds_b128(lds + LDS_X + ximg(16 * hh + 2 * s + c.h, 32 * rt + c.r));
  unsigned kb[2], vb[2];
#pragma unroll
  for (int i = 0; i < 2; ++i) {
    kb[i] = 2048 * (c.r >> 3) + 64 * (c.r & 7) + (((2 * i + c.h) ^ ((c.r >> 2) & 3)) << 4);
    vb[i] = (4 * c.h + c.q) * 128 + 64 * (i ^ (c.q >> 1)) + 32 * c.blk + 8 * (2 * (c.p & 1) + (c.p >> 1));
  }
  const __amdgpu_buffer_rsrc_t srdO = mksrd(P.wo, 4096u * 512u * 2u);
  const unsigned vo = wdma_voff(l, 1024u), so0 = g * 512u * 1024u + w * 128u;

  f32x16 o[4] = {};
  f32x16 s0, s1;
  f32x16 negm;
  float lsum = 0.f;
  u32x4 pk[4];
  constexpr int PD = 6;
  bf16x8 fr[PD];
  const bool lag = w >= 4;
  auto frag = [&](int t, int it) -> bf16x8 {
    if (it < 16) {
      const lds_cptr a = lds + VSLOT[t > 0 ? t - 1 : 0] + vb[it & 1] + 8192 * ((it >> 1) & 1) + 2048 * (it >> 2);
      return cat8(lds_tr(a), lds_tr(a + 1024));
    }
    const int s = (it - 16) >> 1;
    return lds_b128(lds + KSLOT[t < 8 ? t : 0] + kb[s & 1] + 512 * (s >> 1) + 8192 * (it & 1));
  };
  auto seam = [&](int t) {
    if (t == 0) { WAIT_BAR(0); } else if (t < 6) { WAIT_BAR(4); } else if (t == 6) { WAIT_BAR(2); } else { BAR_L(); }
    if (t + 3 < 8) dma_k(t + 3, KSLOT[t + 3 < 8 ? t + 3 : 0]);
    if (t + 2 < 8) dma_v(t + 2, VSLOT[t + 2 < 8 ? t + 2 : 0]);
    if (t == 6) {
#pragma unroll
      for (int ks = 0; ks < RING; ++ks) wring_issue(ks, ring, vo, srdO, so0, 1024u);
    }
  };
  auto extra = [&](int e) {
    const unsigned dump = lds0 + LDS_R + w * 3072u + (unsigned)(e % 3) * 1024u;
    const unsigned id = (BIDX >> 3) * 8 + w, jh = BIDX & 1;
    const unsigned vrow = (l >> 5) * 1024u + (l & 31) * 16u + jh * 512u;
    auto w3v = [&](unsigned piece) { const unsigned byte = piece * 1024u + l * 16u, row = byte / 1792u;
                                     return row * 3584u + jh * 1792u + (byte - row * 1792u); };
    if (e < 4 || e >= 15) {
      const int i = e < 4 ? e : e - 11;
      const unsigned off = i < 3 ? S_U + (unsigned)i * (64u * T * 16u) + pn * 65536u + g * 8192u
                         : i < 7 ? S_HZ + pn * 262144u + g * 32768u + (unsigned)(i - 3) * 8192u
                                 : S_H3 + pn * 229376u + g * 28672u + (unsigned)(i - 7) * 8192u;
      const u32x4 sv = {SENT, SENT, SENT, SENT};
      __builtin_amdgcn_raw_buffer_store_b128(sv, srdW, (i == 10 && tid >= 256) ? OOB : tid * 16u + off, 0, 16);
    } else if (e < 6) dma16(dump, l * 16u, srdO, (g * 512u + (pn * 8 + w) * 2 + (e - 4)) * 1024u);
    else if (e == 6) dma16(dump, vrow, mksrd(P.w1, 512u * 1024u), id * 2048u);
    else if (e == 7) dma16(dump, vrow, mksrd(P.w2, 512u * 1024u), id * 2048u);
    else if (e < 11) dma16(dump, w3v(id + 256u * (e - 8)), mksrd(P.w3, 512u * 3584u), 0u);
    else if (e == 11) {
      if (id < 128) dma16(dump, w3v(id + 768u), mksrd(P.w3, 512u * 3584u), 0u);
      else dma16(dump, vrow, mksrd(P.w4, 1792u * 1024u), (id - 128u + 768u) * 2048u);
    } else dma16(dump, vrow, mksrd(P.w4, 1792u * 1024u), (id + 256u * (e - 12)) * 2048u);
  };
#pragma unroll
  for (int t = 0; t <= 8; ++t) {
    const int i0 = t == 0 ? 16 : 0, i1 = t == 8 ? 16 : 32;
    if (t <= 1) {
#pragma unroll
      for (int it = i0; it < i0 + PD; ++it) fr[it % PD] = frag(t, it);
    }
    if (t == 0) { s0 = (f32x16){}; s1 = (f32x16){}; } else if (t < 8) { s0 = negm; s1 = negm; }
    __builtin_amdgcn_s_setprio(1);
    SBAR();
#pragma unroll
    for (int it = i0; it < i1; ++it) {
      if (it < 16) o[it & 3] = MFMA32(fr[it % PD], __builtin_bit_cast(bf16x8, pk[it >> 2]), o[it & 3]);
      else if (it & 1) s1 = MFMA32(fr[it % PD], qf[(it - 16) >> 1], s1);
      else s0 = MFMA32(fr[it % PD], qf[(it - 16) >> 1], s0);
      if (it + PD < i1) fr[it % PD] = frag(t, it + PD);
      if (t >= 1 && t <= 7 && (it & 7) == 3 && 4 * (t - 1) + (it >> 3) < 22) extra(4 * (t - 1) + (it >> 3));
      SBAR();
    }
    if (t == 8) break;
    __builtin_amdgcn_s_setprio(0);
    if (lag) seam(t);
    asm volatile("s_nop 15" : "+v"(s0), "+v"(s1));
    float ta = max3f(s0[0], s0[1], s1[0]), tb = max3f(s0[2], s0[3], s1[1]);
    ta = max3f(ta, s1[2], s1[3]);
#pragma unroll
    for (int i = 4; i < 16; i += 4) {
      ta = max3f(ta, s0[i], s0[i + 1]); tb = max3f(tb, s0[i + 2], s0[i + 3]);
      ta = max3f(ta, s1[i], s1[i + 1]); tb = max3f(tb, s1[i + 2], s1[i + 3]);
    }
    float tm = max2f(ta, tb);
    {
      auto r = __builtin_amdgcn_permlane32_swap(F2U(tm), F2U(tm), false, false);
      tm = max2f(U2F(r[0]), U2F(r[1]));
    }
    if (t == 0 || ANY(tm > THR)) {
      const float ndl = t == 0 ? -tm : -max2f(tm, 0.f);
      const float aone = c.h ? 0.f : 1.f;
      if (t == 0) negm = (f32x16){};
      __builtin_amdgcn_s_setprio(2);
      s0 = __builtin_amdgcn_mfma_f32_32x32x2f32(aone, ndl, s0, 0, 0, 0);
      s1 = __builtin_amdgcn_mfma_f32_32x32x2f32(aone, ndl, s1, 0, 0, 0);
      negm = __builtin_amdgcn_mfma_f32_32x32x2f32(aone, ndl, negm, 0, 0, 0);
      __builtin_amdgcn_s_setprio(0);
      if (t > 0) {
        const float al = __builtin_amdgcn_exp2f(ndl);
        lsum *= al;
#pragma unroll
        for (int dt = 0; dt < 4; ++dt)
#pragma unroll
          for (int i = 0; i < 16; ++i) o[dt][i] *= al;
      }
    }
    SBAR();
    if (t >= 1) {
#pragma unroll
      for (int it = 0; it < PD; ++it) fr[it] = frag(t + 1, it);
    }
#pragma unroll
    for (int i = 0; i < 16; ++i) { s0[i] = __builtin_amdgcn_exp2f(s0[i]); s1[i] = __builtin_amdgcn_exp2f(s1[i]); }
    pk[0] = pack8(s0, 0, 1.f); pk[1] = pack8(s0, 1, 1.f); pk[2] = pack8(s1, 0, 1.f); pk[3] = pack8(s1, 1, 1.f);
    {
      float l0 = lsum, l1 = 0.f, l2 = 0.f, l3 = 0.f;
#pragma unroll
      for (int i = 0; i < 16; i += 2) { l0 += s0[i]; l1 += s0[i + 1]; l2 += s1[i]; l3 += s1[i + 1]; }
      lsum = (l0 + l1) + (l2 + l3);
    }
    PIN(pk[0]); PIN(pk[1]); PIN(pk[2]); PIN(pk[3]); PIN(lsum);
    SBAR();
    if (!lag) seam(t);
  }
  __builtin_amdgcn_s_setprio(0);
  STAMP(1, 4);
  {
    const float il = __builtin_amdgcn_rcpf(swap32_add(lsum));
#pragma unroll
    for (int dt = 0; dt < 4; ++dt)
#pragma unroll
      for (int s = 0; s < 2; ++s)
        *(LAS u32x4*)(lds + LDS_X + ximg(16 * hh + 4 * dt + 2 * s + c.h, 32 * rt + c.r)) = pack8(o[dt], s, il);
  }
  BAR_L();
  STAMP(1, 5);
  {
    f32x16 acc[2][2] = {};
    gemm_phase<32, 0>(acc, lds, ringoff, ring, vo, srdO, so0, 1024u, c);
    STAMP(1, 6);
    const unsigned dst = W_P1 + ((((pn * 8 + (c.r >> 3)) * 8 + g) * 64 + 8 * w + c.h) * 8 + (c.r & 7)) * 16u;
#pragma unroll
    for (int nt = 0; nt < 2; ++nt)
#pragma unroll
      for (int r2 = 0; r2 < 2; ++r2)
#pragma unroll
        for (int s = 0; s < 2; ++s)
          __builtin_amdgcn_raw_buffer_store_b128(pack8(acc[nt][r2], s, 1.f), srdW, dst + ((4 * nt + 2 * s) * 8 + r2 * 4 * 8 * 64 * 8) * 16u, 0, 16);
  }
  WAIT_BAR(0);
  if (tid == 0) __builtin_amdgcn_raw_buffer_store_b32(1u, srdW, W_FLAG + BIDX * 64u, 0, 16);
  STAMP(1, 7);
}

DI void ln8(float (&v)[8], LAS float* red  , unsigned tid) {
  float s = ((v[0] + v[1]) + (v[2] + v[3])) + ((v[4] + v[5]) + (v[6] + v[7]));
  float q = (__builtin_fmaf(v[0], v[0], v[1] * v[1]) + __builtin_fmaf(v[2], v[2], v[3] * v[3])) +
            (__builtin_fmaf(v[4], v[4], v[5] * v[5]) + __builtin_fmaf(v[6], v[6], v[7] * v[7]));
  s = sum_hi3(s); q = sum_hi3(q);
  if ((tid & 63) < 8) *(LAS f32x2*)(red + ((tid >> 6) * 8 + (tid & 7)) * 2) = (f32x2){s, q};
  BAR_L();
  f32x2 e[8];
#pragma unroll
  for (int i = 0; i < 8; ++i) e[i] = *(const LAS f32x2*)(red + (i * 8 + (tid & 7)) * 2);
  const float mu = (((e[0][0] + e[1][0]) + (e[2][0] + e[3][0])) + ((e[4][0] + e[5][0]) + (e[6][0] + e[7][0]))) * (1.0f / D);
  const float ms = (((e[0][1] + e[1][1]) + (e[2][1] + e[3][1])) + ((e[4][1] + e[5][1]) + (e[6][1] + e[7][1]))) * (1.0f / D);
  const float rs = __builtin_amdgcn_rsqf(__builtin_fmaxf(__builtin_fmaf(-mu, mu, ms), 0.f) + LN_EPS);
  const float nb = -mu * rs;
#pragma unroll
  for (int k = 0; k < 8; ++k) v[k] = __builtin_fmaf(v[k], rs, nb);
}
DI void red4_put(const f32x16& a, int nt, lds_ptr scr, unsigned rt, unsigned kq, unsigned l) {
#pragma unroll
  for (int g = 0; g < 2; ++g)
#pragma unroll
    for (int i = 0; i < 2; ++i) {
      const int o = 8 * g + 4 * i;
      *(LAS f32x4*)(scr + ((rt * 4 + 2 * nt + g) * 4 + kq) * 2048 + i * 1024 + l * 16) = (f32x4){a[o], a[o + 1], a[o + 2], a[o + 3]};
    }
}
DI void red4_get(float (&v)[8], lds_cptr scr, unsigned rt, unsigned kq, unsigned l) {
  f32x4 t[4][2];
#pragma unroll
  for (int s = 0; s < 4; ++s)
#pragma unroll
    for (int i = 0; i < 2; ++i) t[s][i] = *(const LAS f32x4*)(scr + ((rt * 4 + kq) * 4 + s) * 2048 + i * 1024 + l * 16);
#pragma unroll
  for (int i = 0; i < 2; ++i)
#pragma unroll
    for (int k = 0; k < 4; ++k) v[4 * i + k] = (t[0][i][k] + t[1][i][k]) + (t[2][i][k] + t[3][i][k]);
}
DI void tile_mm8(const bf16x8 (&af)[8][2], const u32x4 (&xb)[8], float (&v)[8], lds_ptr scr, unsigned rt, unsigned kq, unsigned l) {
  f32x16 a0 = {}, a1 = {};
#pragma unroll
  for (int i = 0; i < 8; ++i) a0 = MFMA32(af[i][0], __builtin_bit_cast(bf16x8, xb[i]), a0);
  SBAR();
#pragma unroll
  for (int i = 0; i < 4; ++i) a1 = MFMA32(af[i][1], __builtin_bit_cast(bf16x8, xb[i]), a1);
  SBAR();
  red4_put(a0, 0, scr, rt, kq, l);
#pragma unroll
  for (int i = 4; i < 8; ++i) a1 = MFMA32(af[i][1], __builtin_bit_cast(bf16x8, xb[i]), a1);
  SBAR();
  red4_put(a1, 1, scr, rt, kq, l);
  BAR_L();
  red4_get(v, scr, rt, kq, l);
}

struct Frag3 { bf16x8 a, b[2]; };
constexpr int RING3 = 13;
constexpr int LA = 0, LB = 65536, LC = 131072;
DI unsigned r3off(int s, unsigned rc, unsigned rb) { return s < 4 ? rc + s * 1024u : rb + (s - 4) * 1024u; }
template <int KS> DI void load_frags3(Frag3& f, lds_cptr lds, unsigned rc, unsigned rb, unsigned a3, const LaneC& c) {
  const lds_cptr sl = lds + r3off(KS % RING3, rc, rb) + a3;
  f.a = cat8(lds_tr(sl), lds_tr(sl + 256));
#pragma unroll
  for (int rt = 0; rt < 2; ++rt) f.b[rt] = lds_b128(lds + LA + c.xb[KS & 3] + 2048 * KS + 512 * rt);
}
template <int KS>
DI void g3_step(f32x16 (&acc)[2], Frag3& cur, Frag3& n1, Frag3& n2, lds_cptr lds, unsigned lds0, unsigned rc, unsigned rb,
                unsigned voff, __amdgpu_buffer_rsrc_t srd, unsigned soff0, unsigned a3, const LaneC& c) {
  constexpr int NKS = 32;
  if constexpr (KS + 2 < NKS) {
    constexpr int last = (KS + RING3 < NKS ? KS + RING3 : NKS) - 1;
    wait_vm<last - (KS + 2)>();
    load_frags3<KS + 2>(n2, lds, rc, rb, a3, c);
  }
  SBAR();
  acc[0] = MFMA32(cur.a, cur.b[0], acc[0]);
  acc[1] = MFMA32(cur.a, cur.b[1], acc[1]);
  if constexpr (KS + RING3 < NKS) {
    asm volatile("" :: "v"(cur.a));
    dma16(lds0 + r3off(KS % RING3, rc, rb), voff, srd, soff0 + (unsigned)(16 * (KS + RING3)) * 3584u);
  }
  SBAR();
  if constexpr (KS + 1 < NKS) g3_step<KS + 1>(acc, n1, n2, cur, lds, lds0, rc, rb, voff, srd, soff0, a3, c);
}
DI void mlp_part(const Params& P, lds_ptr lds, unsigned lds0, const LaneC& c, unsigned w, __amdgpu_buffer_rsrc_t srdS) {
  const unsigned idx = BIDX >> 3, pn = 8 * ((BIDX & 7) >> 1) + (idx >> 2), j = 4 * (BIDX & 1) + (idx & 3);
  const unsigned tid = TIDX, l = c.lane;
  const unsigned vw = wdma_voff(l, 1024u);
  const unsigned rt = w & 1, kq = w >> 1;
  constexpr int LMOD = LC;
  constexpr int LRED = LC + 28672;
  const unsigned prow = 32 * rt + c.r;
  const unsigned trow = pn * 64 + prow;
  const unsigned och = 8 * j + 2 * kq + c.h;
  const unsigned ovo = ((pn * 64 + och) * 64 + prow) * 16u;
  float gb[8];
  STAMP(2, 0);

  {
    const unsigned row = pn * 64 + j * 8 + (tid & 7), ch = tid >> 3;
    dma16_sc1(lds0 + LMOD + w * 1024u, l * 16u, srdS, W_MOD + 4096u + w * 1024u);
    u32x4 xr;
    gld16(xr, P.x + (size_t)row * D + ch * 8);
    const __amdgpu_buffer_rsrc_t srd1 = mksrd(P.w1, 512u * 512u * 2u), srd2 = mksrd(P.w2, 512u * 512u * 2u);
#pragma unroll
    for (int i = 0; i < 8; ++i) dma16(lds0 + LA + (8 * w + i) * 1024u, vw, srd1, (8 * w + i) * 8192u + j * 128u);
    {
      const int fp = (int)pn - 1 + (int)(l >> 3);
      const unsigned fo = (l < 24 && fp >= 0 && fp < 32) ? W_FLAG + ((unsigned)fp * 8 + (l & 7)) * 64u : OOB;
      for (unsigned spin = 0;; ++spin) {
        asm volatile("" ::: "memory");
        const unsigned f = __builtin_amdgcn_raw_buffer_load_b32(srdS, fo, 0, 16);
        if (!ANY(fo != OOB && f != 1u) || spin > SPIN_CAP) break;
        __builtin_amdgcn_s_sleep(2);
      }
    }
    STAMP(2, 15);
    u32x4 p[8];
#pragma unroll
    for (int g = 0; g < 8; ++g) bld16_sc1(p[g], tid * 16u, srdS, W_P1 + ((pn * 8 + j) * 8 + g) * 8192u);
    asm volatile("s_waitcnt vmcnt(8)\n\ts_barrier" : "+v"(xr) :: "memory");
    float ga[8], sb[8], cb[8], g2[8];
    {
      u32x4 mv[10];
#pragma unroll
      for (int i = 0; i < 8; ++i) mv[i] = *(const LAS u32x4*)(lds + LMOD + (i >> 1) * 2048 + ch * 32 + 16 * (i & 1));
#pragma unroll
      for (int i = 0; i < 2; ++i) mv[8 + i] = *(const LAS u32x4*)(lds + LMOD + 6144 + och * 32 + 16 * i);
      if (has_sent(mv, 10)) {
#pragma unroll
        for (int i = 0; i < 4; ++i) poll_load<2>(mv + 2 * i, srdS, ch * 32u, W_MOD + 4096u + i * 2048u, 16u);
        poll_load<2>(mv + 8, srdS, och * 32u, W_MOD + 10240u, 16u);
      }
#pragma unroll
      for (int k = 0; k < 8; ++k) {
        ga[k] = U2F(mv[k >> 2][k & 3]); sb[k] = U2F(mv[2 + (k >> 2)][k & 3]); cb[k] = 1.f + U2F(mv[4 + (k >> 2)][k & 3]);
        g2[k] = U2F(mv[6 + (k >> 2)][k & 3]); gb[k] = U2F(mv[8 + (k >> 2)][k & 3]);
      }
    }
    asm volatile("s_waitcnt vmcnt(0)" : "+v"(p[0]), "+v"(p[1]), "+v"(p[2]), "+v"(p[3]), "+v"(p[4]), "+v"(p[5]), "+v"(p[6]), "+v"(p[7]) :: "memory");
    STAMP(2, 1);
    float v[8], x2[8];
#pragma unroll
    for (int i = 0; i < 4; ++i) {
      const float lo = ((bflo(p[0][i]) + bflo(p[1][i])) + (bflo(p[2][i]) + bflo(p[3][i]))) + ((bflo(p[4][i]) + bflo(p[5][i])) + (bflo(p[6][i]) + bflo(p[7][i])));
      const float hi = ((bfhi(p[0][i]) + bfhi(p[1][i])) + (bfhi(p[2][i]) + bfhi(p[3][i]))) + ((bfhi(p[4][i]) + bfhi(p[5][i])) + (bfhi(p[6][i]) + bfhi(p[7][i])));
      v[2 * i] = x2[2 * i] = __builtin_fmaf(ga[2 * i], lo, bflo(xr[i]));
      v[2 * i + 1] = x2[2 * i + 1] = __builtin_fmaf(ga[2 * i + 1], hi, bfhi(xr[i]));
    }
    ln8(v, (LAS float*)(lds + LRED), tid);
    float hb[8];
#pragma unroll
    for (int k = 0; k < 8; ++k) v[k] = hb[k] = __builtin_fmaf(v[k], cb[k], sb[k]);
    ln8(v, (LAS float*)(lds + LRED + 512), tid);
    const unsigned vo = ((pn * 64 + ch) * 64 + j * 8 + (tid & 7)) * 16u, vz = ((pn * 64 + ch) * 256 + j * 8 + (tid & 7)) * 16u;
    pub16(srdS, vo, S_U, (u32x4){cvtpk(v[0], v[1]), cvtpk(v[2], v[3]), cvtpk(v[4], v[5]), cvtpk(v[6], v[7])});
#pragma unroll
    for (int i = 0; i < 2; ++i) {
      pub16(srdS, vz, S_HZ + i * 1024u, (u32x4){F2U(hb[4 * i]), F2U(hb[4 * i + 1]),
                                                F2U(hb[4 * i + 2]), F2U(hb[4 * i + 3])});
      f32x4 z;
#pragma unroll
      for (int e = 0; e < 4; ++e) z[e] = __builtin_fmaf(g2[4 * i + e], hb[4 * i + e], x2[4 * i + e]);
      pub16(srdS, vz, S_HZ + (2 + i) * 1024u, __builtin_bit_cast(u32x4, z));
    }
#pragma unroll
    for (int i = 0; i < 8; ++i) dma16(lds0 + LB + (8 * w + i) * 1024u, vw, srd2, (8 * w + i) * 8192u + j * 128u);
  }
  STAMP(2, 2);
  const __amdgpu_buffer_rsrc_t srd3 = mksrd(P.w3, 512u * F * 2u);
  const unsigned rc = LC + w * 4096u, rb = LB + w * 9216u;
  const unsigned v3 = (l >> 2) * 3584u + ((l & 3) << 4), s30 = (j * 224u + w * 32u) * 2u;
  float v[8];
  {
    bf16x8 af[8][2];
#pragma unroll
    for (int i = 0; i < 8; ++i)
#pragma unroll
      for (int nt = 0; nt < 2; ++nt) {
        const lds_cptr a = lds + LA + (8 * kq + i) * 2048 + c.afr[nt];
        af[i][nt] = cat8(lds_tr(a), lds_tr(a + 512));
      }
    BAR_L();
    u32x4 xb[8];
    poll_ind<8, 3, 7>(xb, srdS, ((pn * 64 + 16 * kq + c.h) * 64 + prow) * 16u, S_U, 2048u);
    STAMP(2, 3);
    tile_mm8(af, xb, v, lds + LA, rt, kq, l);
#pragma unroll
    for (int k = 0; k < 8; ++k) v[k] = gelu_tanh(v[k]);
    pub16(srdS, ovo, S_H1, (u32x4){cvtpk(v[0], v[1]), cvtpk(v[2], v[3]), cvtpk(v[4], v[5]), cvtpk(v[6], v[7])});
    if (w < 7) {
#pragma unroll
      for (int ks = 0; ks < 4; ++ks) dma16(lds0 + r3off(ks, rc, rb), v3, srd3, s30 + (unsigned)(16 * ks) * 3584u);
    }
  }
  STAMP(2, 4);
  float y[8];
  u32x4 cs[4];
  const unsigned cso = w < 7 ? (224 * j + 32 * w + 8 * c.h) * 4u : OOB;
  {
    bf16x8 af[8][2];
#pragma unroll
    for (int i = 0; i < 8; ++i)
#pragma unroll
      for (int nt = 0; nt < 2; ++nt) {
        const lds_cptr a = lds + LB + (8 * kq + i) * 2048 + c.afr[nt];
        af[i][nt] = cat8(lds_tr(a), lds_tr(a + 512));
      }
    BAR_L();
    u32x4 hz[4];
    const unsigned hzo = ((pn * 64 + och) * 256 + prow) * 16u;
#pragma unroll
    for (int i = 0; i < 4; ++i) hz[i] = __builtin_amdgcn_raw_buffer_load_b128(srdS, hzo, S_HZ + i * 1024u, 16);
#pragma unroll
    for (int i = 0; i < 4; ++i) cs[i] = __builtin_amdgcn_raw_buffer_load_b128(srdS, cso, W_MOD + 12288u + 64u * (i >> 1) + 16u * (i & 1), 16);
    const int sh = kq == 0 ? -1 : kq == 1 ? 1 : kq == 2 ? -2 : 2;
    const int srow = (int)trow + sh;
    const unsigned xvo = (srow >= 0 && srow < T) ? ((((unsigned)srow >> 6) * 64 + 16 * kq + c.h) * 64 + ((unsigned)srow & 63)) * 16u : OOB;
    u32x4 xb[8];
    poll_ind<8, 3, 7>(xb, srdS, xvo, S_H1, 2048u);
    STAMP(2, 5);
    tile_mm8(af, xb, v, lds + LA, rt, kq, l);
    if (has_sent(hz, 4)) poll_load<4>(hz, srdS, hzo, S_HZ, 1024u);
    if (has_sent(cs, 4)) { poll_load<2>(cs, srdS, cso, W_MOD + 12288u, 16u); poll_load<2>(cs + 2, srdS, cso, W_MOD + 12352u, 16u); }
    float x1[8];
#pragma unroll
    for (int k = 0; k < 8; ++k) {
      x1[k] = U2F(hz[k >> 2][k & 3]) + v[k];
      y[k] = __builtin_fmaf(gb[k], v[k], U2F(hz[2 + (k >> 2)][k & 3]));
    }
    pub16(srdS, ovo, S_X1, (u32x4){cvtpk(x1[0], x1[1]), cvtpk(x1[2], x1[3]), cvtpk(x1[4], x1[5]), cvtpk(x1[6], x1[7])});
    if (w < 7) {
#pragma unroll
      for (int ks = 4; ks < RING3; ++ks) dma16(lds0 + r3off(ks, rc, rb), v3, srd3, s30 + (unsigned)(16 * ks) * 3584u);
    }
  }
  STAMP(2, 6);
  {
    u32x4 xr[8];
    BAR_L();
    poll_ind<8, 3, 7>(xr, srdS, ((pn * 64 + 8 * w) * 64 + l) * 16u, S_X1, 1024u);
    STAMP(2, 7);
#pragma unroll
    for (int i = 0; i < 8; ++i) *(LAS u32x4*)(lds + LA + ximg(8 * w + i, l)) = xr[i];
    {
      float s0 = 0.f, s1 = 0.f, q0 = 0.f, q1 = 0.f;
#pragma unroll
      for (int i = 0; i < 8; ++i)
#pragma unroll
        for (int k = 0; k < 4; k += 2) {
          s0 = dot2bf(xr[i][k], 0x3F803F80u, s0); s1 = dot2bf(xr[i][k + 1], 0x3F803F80u, s1);
          q0 = dot2bf(xr[i][k], xr[i][k], q0); q1 = dot2bf(xr[i][k + 1], xr[i][k + 1], q1);
        }
      *(LAS f32x2*)(lds + LRED + (w * 64 + l) * 8) = (f32x2){s0 + s1, q0 + q1};
    }
    BAR_L();
    STAMP(2, 8);
    if (w < 7) {
      f32x16 acc[2] = {};
      const unsigned a3 = (8 * c.h + c.q) * 64 + 32 * c.blk + 8 * (2 * (c.p & 1) + (c.p >> 1));
      Frag3 f0, f1, f2;
      wait_vm<RING3 - 2>();
      load_frags3<0>(f0, lds, rc, rb, a3, c);
      load_frags3<1>(f1, lds, rc, rb, a3, c);
      float mu[2], rs[2];
#pragma unroll
      for (int r2 = 0; r2 < 2; ++r2) {
        float s = 0.f, q = 0.f;
#pragma unroll
        for (int i = 0; i < 8; ++i) {
          const f32x2 t = *(const LAS f32x2*)(lds + LRED + (i * 64 + 32 * r2 + c.r) * 8);
          s += t[0]; q += t[1];
        }
        mu[r2] = s * (1.0f / D);
        rs[r2] = __builtin_amdgcn_rsqf(__builtin_fmaxf(__builtin_fmaf(-mu[r2], mu[r2], q * (1.0f / D)), 0.f) + LN_EPS);
      }
      if (w < 4) __builtin_amdgcn_s_setprio(2); else __builtin_amdgcn_s_setprio(1);
      g3_step<0>(acc, f0, f1, f2, lds, lds0, rc, rb, v3, srd3, s30, a3, c);
      __builtin_amdgcn_s_setprio(0);
      STAMP(2, 9);
#pragma unroll
      for (int r2 = 0; r2 < 2; ++r2) {
#pragma unroll
        for (int i = 0; i < 16; ++i) acc[r2][i] = gelu_tanh(rs[r2] * __builtin_fmaf(-mu[r2], U2F(cs[i >> 2][i & 3]), acc[r2][i]));
#pragma unroll
        for (int s2 = 0; s2 < 2; ++s2)
          pub16(srdS, ((pn * 224 + 28 * j + 4 * w + 2 * s2 + c.h) * 64 + 32 * r2 + c.r) * 16u, S_H3, pack8(acc[r2], s2, 1.f));
      }
    }
    BAR_L();
  }
  STAMP(2, 10);
  {
    const __amdgpu_buffer_rsrc_t srd4 = mksrd(P.w4, (unsigned)F * 512u * 2u);
    const unsigned qb = kq * 40960u;
    const unsigned s40 = (448u * kq + 8u * rt) * 1024u + j * 128u;
    auto w4dma = [&](int ks) { dma16(lds0 + qb + (ks % 20) * 2048u + rt * 1024u, vw, srd4, s40 + (unsigned)ks * 16384u); };
#pragma unroll
    for (int ks = 0; ks < 8; ++ks) w4dma(ks);
    const unsigned hvo = ((pn * 224 + 56 * kq + c.h) * 64 + prow) * 16u;
    u32x4 ha[14], hb[14];
    poll_ind<14, 9, 13>(ha, srdS, hvo, S_H3, 2048u);
    BAR_L();
    STAMP(2, 11);
    f32x16 acc[2] = {};
    bf16x8 fa[2][2];
    auto ld = [&](int ks) {
#pragma unroll
      for (int nt = 0; nt < 2; ++nt) {
        const lds_cptr a = lds + qb + (ks % 20) * 2048 + c.afr[nt];
        fa[ks & 1][nt] = cat8(lds_tr(a), lds_tr(a + 512));
      }
    };
    auto mm = [&](int ks, const u32x4& b) {
#pragma unroll
      for (int nt = 0; nt < 2; ++nt) acc[nt] = MFMA32(fa[ks & 1][nt], __builtin_bit_cast(bf16x8, b), acc[nt]);
    };
    auto hbl = [&](int i) { bld16_sc1(hb[i], hvo, srdS, S_H3 + (14u + i) * 2048u); };
    ld(0);
#pragma unroll
    for (int ks = 0; ks < 8; ++ks) {
      if (ks < 7) ld(ks + 1);
      SBAR(); mm(ks, ha[ks]);
      if (ks < 6) { w4dma(8 + ks); hbl(2 * ks); hbl(2 * ks + 1); }
      else if (ks == 6) { hbl(12); hbl(13); w4dma(14); w4dma(15); w4dma(16); }
      else { w4dma(17); w4dma(18); w4dma(19); }
      SBAR();
    }
    WAIT_BAR(10);
    ld(8);
#pragma unroll
    for (int ks = 8; ks < 14; ++ks) {
      if (ks < 13) ld(ks + 1);
      SBAR(); mm(ks, ha[ks]);
      if (ks < 12) { w4dma(20 + 2 * (ks - 8)); w4dma(21 + 2 * (ks - 8)); }
      SBAR();
    }
    asm volatile("s_waitcnt vmcnt(8) lgkmcnt(0)\n\ts_barrier" : "+v"(hb[0]), "+v"(hb[1]), "+v"(hb[2]), "+v"(hb[3]), "+v"(hb[4]), "+v"(hb[5]),
                 "+v"(hb[6]), "+v"(hb[7]), "+v"(hb[8]), "+v"(hb[9]), "+v"(hb[10]), "+v"(hb[11]), "+v"(hb[12]), "+v"(hb[13]) :: "memory");
    if (has_sent(hb, 14)) poll_load<14>(hb, srdS, hvo, S_H3 + 14u * 2048u, 2048u);
    STAMP(2, 12);
    ld(14);
#pragma unroll
    for (int ks = 14; ks < 19; ++ks) { ld(ks + 1); SBAR(); mm(ks, hb[ks - 14]); SBAR(); }
    mm(19, hb[5]);
    WAIT_BAR(0);
    ld(20);
#pragma unroll
    for (int ks = 20; ks < 27; ++ks) { ld(ks + 1); SBAR(); mm(ks, hb[ks - 14]); SBAR(); }
    mm(27, hb[13]);
    BAR_L();
    STAMP(2, 13);
    red4_put(acc[0], 0, lds + LA, rt, kq, l);
    red4_put(acc[1], 1, lds + LA, rt, kq, l);
    BAR_L();
    red4_get(v, lds + LA, rt, kq, l);
#pragma unroll
    for (int k = 0; k < 8; ++k) v[k] = __builtin_fmaf(gb[k], v[k], y[k]);
    P.out[(size_t)trow * 64 + och] = (u32x4){cvtpk(v[0], v[1]), cvtpk(v[2], v[3]), cvtpk(v[4], v[5]), cvtpk(v[6], v[7])};
  }
  STAMP(2, 14);
}

__global__ __launch_bounds__(512, 2) void k_fused(Params P) {
  extern __shared__ __attribute__((aligned(16))) char smem[];
  const lds_ptr lds = (lds_ptr)smem;
  const unsigned lds0 = (unsigned)(uintptr_t)smem;
  const unsigned w = rfl(TIDX >> 6);
  LaneC c; lane_consts(c);
  const __amdgpu_buffer_rsrc_t srdW = mksrd(P.ws, W_END);
  attn_part(P, lds, lds0, c, w, srdW);
  mlp_part(P, lds, lds0, c, w, srdW);
}

#if defined(__HIP_DEVICE_COMPILE__)
#pragma clang attribute pop
#endif
extern "C" void kernel_launch(void* const* d_in, const int* in_sizes, int n_in, void* d_out, int out_size, void* d_ws,
                              size_t ws_size, hipStream_t stream) {
  Params P;
  P.x = (const unsigned short*)d_in[0];
  P.kc = (const unsigned short*)d_in[1];
  P.vc = (const unsigned short*)d_in[2];
  P.wq = (const unsigned short*)d_in[3];
  P.wo = (const unsigned short*)d_in[4];
  P.w1 = (const unsigned short*)d_in[6];
  P.w2 = (const unsigned short*)d_in[7];
  P.w3 = (const unsigned short*)d_in[8];
  P.w4 = (const unsigned short*)d_in[9];
  P.c = (const unsigned short*)d_in[10];
  P.wmod = (const unsigned short*)d_in[11];
  P.ws = d_ws;
  P.out = (u32x4*)d_out;
  (void)hipFuncSetAttribute((const void*)k_fused, hipFuncAttributeMaxDynamicSharedMemorySize, LDS_TOTAL);
  k_init<<<12, 256, 0, stream>>>(d_ws);
  k_fused<<<256, 512, LDS_TOTAL, stream>>>(P);
}
```

```cpp
#include <hip/hip_runtime.h>
#include <hip/hip_bf16.h>
#include <stdint.h>

#if defined(__HIP_DEVICE_COMPILE__)
#pragma clang attribute push(__attribute__((target("no-packed-fp32-ops"))), apply_to = function)
#endif
typedef short bf16x8 __attribute__((ext_vector_type(8)));
typedef short s16x4 __attribute__((ext_vector_type(4)));
typedef float f32x16 __attribute__((ext_vector_type(16)));
typedef float f32x4 __attribute__((ext_vector_type(4)));
typedef float f32x2 __attribute__((ext_vector_type(2)));
typedef unsigned u32x4 __attribute__((ext_vector_type(4)));
typedef unsigned u32x2 __attribute__((ext_vector_type(2)));
typedef __bf16 bf16x2_t __attribute__((ext_vector_type(2)));
typedef short v4i16_t __attribute__((ext_vector_type(4)));
#define LAS __attribute__((address_space(3)))
typedef LAS const char* lds_cptr;
typedef LAS char* lds_ptr;
#define DI __device__ __forceinline__
#define F2U(x) __builtin_bit_cast(unsigned, (float)(x))
#define U2F(x) __builtin_bit_cast(float, (unsigned)(x))
#define F2I(x) __builtin_bit_cast(int, (float)(x))
#define I2F(x) __builtin_bit_cast(float, (int)(x))
#define TIDX __builtin_amdgcn_workitem_id_x()
#define BIDX __builtin_amdgcn_workgroup_id_x()
#define ANY(p) (__builtin_amdgcn_ballot_w64(p) != 0ull)
#define SBAR() __builtin_amdgcn_sched_barrier(0)
#define MFMA32(a, b, c) __builtin_amdgcn_mfma_f32_32x32x16_bf16((a), (b), (c), 0, 0, 0)
#define WAIT_V(n) asm volatile("s_waitcnt vmcnt(" #n ")" ::: "memory")
#define WAIT_L0() asm volatile("s_waitcnt lgkmcnt(0)" ::: "memory")
#define BAR_L() asm volatile("s_waitcnt lgkmcnt(0)\n\ts_barrier" ::: "memory")
#define WAIT_BAR(n) asm volatile("s_waitcnt vmcnt(" #n ") lgkmcnt(0)\n\ts_barrier" ::: "memory")

constexpr int T = 2048, D = 512, HKV = 8, SK = 2048, DH = 128, WIN = 512, F = 1792;
constexpr float LN_EPS = 1e-5f;
constexpr float QSCALE = 0.088388347648318440f * 1.4426950408889634f;

constexpr unsigned W_MOD = 0;
constexpr unsigned W_FLAG = 32768;
constexpr unsigned W_P1 = 65536;
constexpr unsigned S_U = W_P1 + 8u * 64u * T * 16u;
constexpr unsigned S_H1 = S_U + 64u * T * 16u;
constexpr unsigned S_X1 = S_H1 + 64u * T * 16u;
constexpr unsigned S_HZ = S_X1 + 64u * T * 16u;
constexpr unsigned S_H3 = S_HZ + 256u * T * 16u;
constexpr unsigned W_END = S_H3 + 224u * T * 16u;
static_assert(W_FLAG % 4096 == 0 && W_FLAG + 16384 <= W_P1, "ws");
constexpr unsigned OOB = 0xF0000000u;
#define K1MAP 0
#define ATHR 32.0f
constexpr float THR = ATHR;
constexpr unsigned SENT = 0xFFFFFFFFu;
constexpr unsigned SPIN_CAP = 1u << 16;

constexpr int LDS_X = 0;
constexpr int LDS_R = 65536;
constexpr int RING = 5, RINGB = RING * 2048;
constexpr int LDS_D = LDS_R + 8 * RINGB;
constexpr int LDS_TOTAL = LDS_D + 16384;
static_assert(LDS_TOTAL == 163840, "lds");

#define STAMP(kid, i) do {} while (0)
DI unsigned cvtpk(float lo, float hi) {
  f32x2 v = {lo, hi};
  bf16x2_t b = __builtin_convertvector(v, bf16x2_t);
  return __builtin_bit_cast(unsigned, b);
}
DI float bflo(unsigned u) { return U2F(u << 16); }
DI float bfhi(unsigned u) { return U2F(u & 0xffff0000u); }
DI unsigned rfl(unsigned x) { return __builtin_amdgcn_readfirstlane(x); }
DI __amdgpu_buffer_rsrc_t mksrd(const void* p, unsigned bytes) {
  return __builtin_amdgcn_make_buffer_rsrc((void*)p, (short)0, (int)bytes, 0x00020000);
}
DI void dma16(unsigned lds, unsigned voff, __amdgpu_buffer_rsrc_t srd, unsigned soff) {
  asm volatile("s_mov_b32 m0, %0\n\ts_nop 0\n\tbuffer_load_dwordx4 %1, %2, %3 offen lds"
               :: "s"(lds), "v"(voff), "s"(srd), "s"(soff) : "memory");
}
DI void gld16(u32x4& d, const void* p) { asm volatile("global_load_dwordx4 %0, %1, off" : "=v"(d) : "v"(p) : "memory"); }
DI void bld16_sc1(u32x4& d, unsigned voff, __amdgpu_buffer_rsrc_t srd, unsigned soff) {
  asm volatile("buffer_load_dwordx4 %0, %1, %2, %3 offen sc1" : "=v"(d) : "v"(voff), "s"(srd), "s"(soff) : "memory");
}
DI void dma16_sc1(unsigned lds, unsigned voff, __amdgpu_buffer_rsrc_t srd, unsigned soff) {
  asm volatile("s_mov_b32 m0, %0\n\ts_nop 0\n\tbuffer_load_dwordx4 %1, %2, %3 offen sc1 lds"
               :: "s"(lds), "v"(voff), "s"(srd), "s"(soff) : "memory");
}
DI bf16x8 lds_b128(lds_cptr p) { return *(const LAS bf16x8*)p; }
DI s16x4 lds_tr(lds_cptr p) {
  return __builtin_bit_cast(s16x4, __builtin_amdgcn_ds_read_tr16_b64_v4i16((LAS v4i16_t*)p));
}
DI bf16x8 cat8(s16x4 lo, s16x4 hi) { return __builtin_shufflevector(lo, hi, 0, 1, 2, 3, 4, 5, 6, 7); }
DI float swap32_add(float x) {
  auto r = __builtin_amdgcn_permlane32_swap(F2U(x), F2U(x), false, false);
  return U2F(r[0]) + U2F(r[1]);
}
DI float swap32_max(float x) {
  auto r = __builtin_amdgcn_permlane32_swap(F2U(x), F2U(x), false, false);
  return __builtin_fmaxf(U2F(r[0]), U2F(r[1]));
}
template <int CTRL> DI float dpp_add(float x) {
  int y = __builtin_amdgcn_update_dpp(0, F2I(x), CTRL, 0xF, 0xF, true);
  return x + I2F(y);
}
DI float max3f(float a, float b, float c) { float r; asm("v_max3_f32 %0, %1, %2, %3" : "=v"(r) : "v"(a), "v"(b), "v"(c)); return r; }
DI float max2f(float a, float b) { float r; asm("v_max_f32_e32 %0, %1, %2" : "=v"(r) : "v"(a), "v"(b)); return r; }
DI float dot2bf(unsigned a, unsigned b, float c) {
  return __builtin_amdgcn_fdot2_f32_bf16(__builtin_bit_cast(bf16x2_t, a), __builtin_bit_cast(bf16x2_t, b), c, false);
}
#define PIN(x) asm volatile("" : "+v"(x))
DI float swap16_add(float x) {
  auto r = __builtin_amdgcn_permlane16_swap(F2U(x), F2U(x), false, false);
  return U2F(r[0]) + U2F(r[1]);
}
DI float sum_hi3(float x) { return swap32_add(swap16_add(dpp_add<0x128>(x))); }
DI float gelu_tanh(float x) {
  const float k0 = -2.0f * 0.7978845608028654f * 1.4426950408889634f;
  const float t = x * x;
  const float z = x * __builtin_fmaf(t, 0.044715f * k0, k0);
  const float e = __builtin_amdgcn_exp2f(z);
  return x * __builtin_amdgcn_rcpf(1.0f + e);
}
DI void wave_sum8(const float (&a)[8], float (&out)[8]) {
  float b[4], c[2];
#pragma unroll
  for (int i = 0; i < 4; ++i) {
    auto r = __builtin_amdgcn_permlane32_swap(F2U(a[i]), F2U(a[i + 4]), false, false);
    b[i] = U2F(r[0]) + U2F(r[1]);
  }
#pragma unroll
  for (int i = 0; i < 2; ++i) {
    auto r = __builtin_amdgcn_permlane16_swap(F2U(b[i]), F2U(b[i + 2]), false, false);
    c[i] = U2F(r[0]) + U2F(r[1]);
  }
#pragma unroll
  for (int i = 0; i < 2; ++i) {
    c[i] = dpp_add<0xB1>(c[i]);
    c[i] = dpp_add<0x4E>(c[i]);
    c[i] = dpp_add<0x141>(c[i]);
    c[i] = dpp_add<0x140>(c[i]);
  }
#pragma unroll
  for (int q = 0; q < 4; ++q)
#pragma unroll
    for (int i = 0; i < 2; ++i)
      out[i + 2 * (q & 1) + 4 * (q >> 1)] = I2F(__builtin_amdgcn_readlane(F2I(c[i]), 16 * q));
}

DI unsigned nosent(unsigned v) { return v < 0xFFFFFFFEu ? v : 0xFFFFFFFEu; }
DI void pub16(__amdgpu_buffer_rsrc_t srd, unsigned voff, unsigned soff, u32x4 v) {
  v = (u32x4){nosent(v[0]), nosent(v[1]), nosent(v[2]), nosent(v[3])};
  __builtin_amdgcn_raw_buffer_store_b128(v, srd, voff + soff, 0, 16);
}
DI unsigned umax3(unsigned a, unsigned b, unsigned c) { const unsigned m = a > b ? a : b; return m > c ? m : c; }
DI bool has_sent(const u32x4* v, int n) {
  unsigned m = 0;
#pragma unroll
  for (int i = 0; i < n; ++i) { m = umax3(m, v[i][0], v[i][1]); m = umax3(m, v[i][2], v[i][3]); }
  return ANY(m == SENT);
}
template <int N> DI void poll_load(u32x4* v, __amdgpu_buffer_rsrc_t srd, unsigned voff, unsigned soff0, unsigned step) {
  for (unsigned spin = 0;; ++spin) {
    asm volatile("" ::: "memory");
#pragma unroll
    for (int i = 0; i < N; ++i) v[i] = __builtin_amdgcn_raw_buffer_load_b128(srd, voff, soff0 + (unsigned)i * step, 16);
    if (!has_sent(v, N) || spin > SPIN_CAP) break;
    __builtin_amdgcn_s_sleep(1);
  }
}
template <int N, int I0, int I1> DI void poll_ind(u32x4* v, __amdgpu_buffer_rsrc_t srd, unsigned voff, unsigned soff0, unsigned step) {
  u32x4 ind[2];
  poll_load<2>(ind, srd, voff, soff0 + (unsigned)I0 * step, (unsigned)(I1 - I0) * step);
  poll_load<N>(v, srd, voff, soff0, step);
}

__global__ __launch_bounds__(512) void k_init(const unsigned short* __restrict__ c, const unsigned short* __restrict__ wmod,
                                              const unsigned short* __restrict__ w3, void* ws) {
  __shared__ __attribute__((aligned(16))) float red[8][32];
  const unsigned tid = TIDX, l = tid & 63, w = tid >> 6;
  if (BIDX >= 152) {
    *(u32x4*)((char*)ws + W_FLAG + (BIDX - 152) * 8192 + tid * 16) = (u32x4){0u, 0u, 0u, 0u};
    return;
  }
  const bool isw3 = BIDX >= 96;
  const unsigned short* src = isw3 ? w3 + (BIDX - 96) * 32 + (tid & 3) * 8 : wmod + BIDX * 32 + (tid & 3) * 8;
  const unsigned ld = isw3 ? F : 3072;
  u32x4 jw[4]; unsigned jc[4];
#pragma unroll
  for (int i = 0; i < 4; ++i) jw[i] = *(const u32x4*)(src + (size_t)((tid >> 2) + 128 * i) * ld);
#pragma unroll
  for (int i = 0; i < 4; ++i) jc[i] = c[(tid >> 2) + 128 * i];
  float acc[8] = {0, 0, 0, 0, 0, 0, 0, 0};
#pragma unroll
  for (int i = 0; i < 4; ++i) {
    const float v = U2F(jc[i] << 16);
    const float sv = v * __builtin_amdgcn_rcpf(1.0f + __builtin_amdgcn_exp2f(-1.4426950408889634f * v));
    const float sc = isw3 ? 1.0f : sv;
#pragma unroll
    for (int k = 0; k < 4; ++k) {
      acc[2 * k] = __builtin_fmaf(sc, bflo(jw[i][k]), acc[2 * k]);
      acc[2 * k + 1] = __builtin_fmaf(sc, bfhi(jw[i][k]), acc[2 * k + 1]);
    }
  }
#pragma unroll
  for (int k = 0; k < 8; ++k) acc[k] = sum_hi3(dpp_add<0x124>(acc[k]));
  if (l < 4) {
    *(f32x4*)&red[w][l * 8] = (f32x4){acc[0], acc[1], acc[2], acc[3]};
    *(f32x4*)&red[w][l * 8 + 4] = (f32x4){acc[4], acc[5], acc[6], acc[7]};
  }
  BAR_L();
  if (tid < 8) {
    f32x4 t = *(const f32x4*)&red[0][tid * 4];
#pragma unroll
    for (int i = 1; i < 8; ++i) t += *(const f32x4*)&red[i][tid * 4];
    *(f32x4*)((char*)ws + W_MOD + BIDX * 128 + tid * 16) = t;
  }
}

struct LaneC {
  unsigned lane, r, h, q, p, blk;
  unsigned wv;
  unsigned afr[2];
  unsigned xb[4];
};
DI void lane_consts(LaneC& c) {
  const unsigned l = TIDX & 63;
  c.lane = l; c.r = l & 31; c.h = l >> 5; c.q = (l & 15) >> 2; c.p = l & 3; c.blk = (l >> 4) & 1;
  const unsigned sp = 2 * (c.p & 1) + (c.p >> 1);
#pragma unroll
  for (int nt = 0; nt < 2; ++nt) c.afr[nt] = (8 * c.h + c.q) * 128 + 64 * (nt ^ (c.q >> 1)) + 32 * c.blk + 8 * sp;
#pragma unroll
  for (int s = 0; s < 4; ++s) c.xb[s] = 1024 * c.h + ((c.r ^ (2 * s + c.h)) << 4);
}
DI unsigned wdma_voff(unsigned l, unsigned ld) { return (l >> 3) * ld + (((l & 7) ^ (((l >> 4) & 1) << 2)) << 4); }

DI void wring_issue(int ks, unsigned ring, unsigned voff, __amdgpu_buffer_rsrc_t srd, unsigned soff0, unsigned ld) {
  const unsigned slot = ring + (unsigned)(ks % RING) * 2048u;
  dma16(slot, voff, srd, soff0 + (unsigned)(16 * ks) * ld);
  dma16(slot + 1024u, voff, srd, soff0 + (unsigned)(16 * ks + 8) * ld);
}
template <int N> DI void wait_vm() {
  if constexpr (N == 0) WAIT_V(0); else if constexpr (N == 1) WAIT_V(1); else if constexpr (N == 2) WAIT_V(2);
  else if constexpr (N == 3) WAIT_V(3); else if constexpr (N == 4) WAIT_V(4); else if constexpr (N == 5) WAIT_V(5);
  else if constexpr (N == 6) WAIT_V(6); else if constexpr (N == 7) WAIT_V(7); else if constexpr (N == 8) WAIT_V(8);
  else if constexpr (N == 9) WAIT_V(9); else if constexpr (N == 10) WAIT_V(10); else if constexpr (N == 11) WAIT_V(11);
  else if constexpr (N == 12) WAIT_V(12); else if constexpr (N == 13) WAIT_V(13); else if constexpr (N == 14) WAIT_V(14);
  else if constexpr (N == 15) WAIT_V(15); else if constexpr (N == 16) WAIT_V(16);
  else if constexpr (N == 17) WAIT_V(17); else if constexpr (N == 18) WAIT_V(18);
  else static_assert(N < 0, "wait_vm");
}

struct Frag { bf16x8 a[2], b[2]; };
template <int KS> DI void load_frags(Frag& f, lds_cptr lds, unsigned ringoff, const LaneC& c) {
  const lds_cptr sl = lds + ringoff + (KS % RING) * 2048;
#pragma unroll
  for (int nt = 0; nt < 2; ++nt) f.a[nt] = cat8(lds_tr(sl + c.afr[nt]), lds_tr(sl + c.afr[nt] + 512));
#pragma unroll
  for (int rt = 0; rt < 2; ++rt) f.b[rt] = lds_b128(lds + LDS_X + c.xb[KS & 3] + 2048 * KS + 512 * rt);
}
template <int NKS, int KS, int EXTRA>
DI void gemm_step(f32x16 (&acc)[2][2], Frag& cur, Frag& nxt, lds_cptr lds, unsigned ringoff, unsigned ring, unsigned voff,
                  __amdgpu_buffer_rsrc_t srd, unsigned soff0, unsigned ld, const LaneC& c) {
  if constexpr (KS + 1 < NKS) {
    constexpr int last = (KS + RING < NKS ? KS + RING : NKS) - 1;
    constexpr int after = 2 * (last - (KS + 1)) + ((KS + 1 < RING) ? EXTRA : 0);
    wait_vm<after>();
    load_frags<KS + 1>(nxt, lds, ringoff, c);
  }
  SBAR();
#pragma unroll
  for (int nt = 0; nt < 2; ++nt)
#pragma unroll
    for (int rt = 0; rt < 2; ++rt) acc[nt][rt] = MFMA32(cur.a[nt], cur.b[rt], acc[nt][rt]);
  if constexpr (KS + RING < NKS) {
    asm volatile("" :: "v"(cur.a[0]), "v"(cur.a[1]));
    wring_issue(KS + RING, ring, voff, srd, soff0, ld);
  }
  SBAR();
  if constexpr (KS + 1 < NKS) gemm_step<NKS, KS + 1, EXTRA>(acc, nxt, cur, lds, ringoff, ring, voff, srd, soff0, ld, c);
}
template <int NKS, int EXTRA>
DI void gemm_phase(f32x16 (&acc)[2][2], lds_cptr lds, unsigned ringoff, unsigned ring, unsigned voff,
                   __amdgpu_buffer_rsrc_t srd, unsigned soff0, unsigned ld, const LaneC& c) {
  Frag f0, f1;
  wait_vm<2 * (RING - 1) + EXTRA>();
  load_frags<0>(f0, lds, ringoff, c);
  gemm_step<NKS, 0, EXTRA>(acc, f0, f1, lds, ringoff, ring, voff, srd, soff0, ld, c);
}
DI u32x4 pack8(const f32x16& a, int s, float sc) {
  return (u32x4){cvtpk(a[8 * s] * sc, a[8 * s + 1] * sc), cvtpk(a[8 * s + 2] * sc, a[8 * s + 3] * sc),
                 cvtpk(a[8 * s + 4] * sc, a[8 * s + 5] * sc), cvtpk(a[8 * s + 6] * sc, a[8 * s + 7] * sc)};
}
DI unsigned ximg(unsigned chunk, unsigned row) { return chunk * 1024u + ((row ^ (chunk & 7u)) << 4); }

constexpr int KSLOT[8] = {LDS_D, LDS_R, LDS_R + 32768, LDS_X, LDS_X + 32768, LDS_D, LDS_X, LDS_X + 32768};
constexpr int VSLOT[8] = {LDS_R + 16384, LDS_R + 49152, LDS_X + 16384, LDS_X + 49152, LDS_R + 65536, LDS_X + 16384, LDS_X + 49152, LDS_D};
struct Params {
  const unsigned short *x, *kc, *vc, *wq, *wo, *w1, *w2, *w3, *w4, *c, *wmod;
  void* ws;
  u32x4* out;
};
DI void attn_part(const Params& P, lds_ptr lds, unsigned lds0, const LaneC& c, unsigned w, __amdgpu_buffer_rsrc_t srdW) {
  const unsigned g = BIDX & 7, pn = BIDX >> 3;
  const unsigned l = c.lane, tid = TIDX;
  const unsigned ringoff = LDS_R + w * RINGB, ring = lds0 + ringoff;
  STAMP(1, 0);

  u32x4 xr[8];
#pragma unroll
  for (int i = 0; i < 8; ++i) gld16(xr[i], P.x + (size_t)(pn * 64 + w * 8 + i) * D + l * 8);
  if (w < 4) dma16(lds0 + LDS_D + w * 1024u, l * 16u, srdW, W_MOD + w * 1024u);
  const __amdgpu_buffer_rsrc_t srdQ = mksrd(P.wq, 512u * 4096u * 2u);
  const unsigned vq = wdma_voff(l, 8192u), sq0 = (g * 512u + w * 64u) * 2u;
  const __amdgpu_buffer_rsrc_t srdK = mksrd(P.kc + ((size_t)g * SK + (SK - WIN)) * DH, WIN * DH * 2u);
  const __amdgpu_buffer_rsrc_t srdV = mksrd(P.vc + ((size_t)g * SK + (SK - WIN)) * DH, WIN * DH * 2u);
  const unsigned vk = ((l >> 2) & 7) * 256u + (l >> 5) * 64u + (((l & 3) ^ (2 * (w & 1) + ((l >> 4) & 1))) << 4);
  const unsigned vv = wdma_voff(l, 256u);
  auto dma_k = [&](int t, unsigned slot) {
    dma16(lds0 + slot + 2048u * w, vk, srdK, (unsigned)t * 16384u + 2048u * w);
    dma16(lds0 + slot + 2048u * w + 1024u, vk, srdK, (unsigned)t * 16384u + 2048u * w + 128u);
  };
  auto dma_v = [&](int t, unsigned slot) {
    dma16(lds0 + slot + 1024u * w, vv, srdV, (unsigned)t * 16384u + 2048u * w);
    dma16(lds0 + slot + 8192u + 1024u * w, vv, srdV, (unsigned)t * 16384u + 2048u * w + 128u);
  };

  {
    asm volatile("s_waitcnt vmcnt(0)" : "+v"(xr[0]), "+v"(xr[1]), "+v"(xr[2]), "+v"(xr[3]), "+v"(xr[4]), "+v"(xr[5]), "+v"(xr[6]),
                 "+v"(xr[7]) :: "memory");
    dma16(ring + 8192u, l * 16u, srdQ, (2 * (pn * 8 + w)) * 8192u + g * 1024u);
    dma16(ring + 9216u, l * 16u, srdQ, (2 * (pn * 8 + w) + 1) * 8192u + g * 1024u);
    wring_issue(0, ring, vq, srdQ, sq0, 8192u);
    wring_issue(1, ring, vq, srdQ, sq0, 8192u);
    BAR_L();
    u32x4 mo[4];
#pragma unroll
    for (int i = 0; i < 4; ++i) mo[i] = *(const LAS u32x4*)(lds + LDS_D + (i >> 1) * 2048 + l * 32 + (i & 1) * 16);
    float v[8][8], s[8], m[8];
#pragma unroll
    for (int i = 0; i < 8; ++i) {
#pragma unroll
      for (int j = 0; j < 4; ++j) { v[i][2 * j] = bflo(xr[i][j]); v[i][2 * j + 1] = bfhi(xr[i][j]); }
      s[i] = ((v[i][0] + v[i][1]) + (v[i][2] + v[i][3])) + ((v[i][4] + v[i][5]) + (v[i][6] + v[i][7]));
    }
    wave_sum8(s, m);
#pragma unroll
    for (int i = 0; i < 8; ++i) {
      const float mu = m[i] * (1.0f / D);
      float q = 0.f;
#pragma unroll
      for (int j = 0; j < 8; ++j) { v[i][j] -= mu; q = __builtin_fmaf(v[i][j], v[i][j], q); }
      s[i] = q;
    }
    wave_sum8(s, m);
    float rs[8];
#pragma unroll
    for (int i = 0; i < 8; ++i) rs[i] = __builtin_amdgcn_rsqf(m[i] * (1.0f / D) + LN_EPS);
    STAMP(1, 8);
    STAMP(1, 9);
#pragma unroll
    for (int i = 0; i < 8; ++i) {
      float hh[8];
#pragma unroll
      for (int j = 0; j < 8; ++j) hh[j] = __builtin_fmaf(v[i][j] * rs[i], 1.f + U2F(mo[2 + (j >> 2)][j & 3]), U2F(mo[j >> 2][j & 3]));
      const u32x4 o = {cvtpk(hh[0], hh[1]), cvtpk(hh[2], hh[3]), cvtpk(hh[4], hh[5]), cvtpk(hh[6], hh[7])};
      *(LAS u32x4*)(lds + LDS_X + ximg(l, w * 8 + i)) = o;
      if (i == 1 || i == 3 || i == 5) wring_issue(2 + i / 2, ring, vq, srdQ, sq0, 8192u);
    }
  }
  BAR_L();
  dma_k(0, LDS_D);
  STAMP(1, 1);

  {
    f32x16 acc[2][2] = {};
    gemm_phase<32, 2>(acc, lds, ringoff, ring, vq, srdQ, sq0, 8192u, c);
    STAMP(1, 2);
    WAIT_BAR(0);
    dma_k(1, KSLOT[1]); dma_v(0, VSLOT[0]); dma_k(2, KSLOT[2]); dma_v(1, VSLOT[1]);
#pragma unroll
    for (int nt = 0; nt < 2; ++nt)
#pragma unroll
      for (int rt = 0; rt < 2; ++rt)
#pragma unroll
        for (int s = 0; s < 2; ++s)
          *(LAS u32x4*)(lds + LDS_X + ximg(8 * w + 4 * nt + 2 * s + c.h, 32 * rt + c.r)) = pack8(acc[nt][rt], s, QSCALE);
    BAR_L();
  }

  STAMP(1, 3);
  const unsigned hh = w >> 1, rt = w & 1;
  bf16x8 qf[8];
#pragma unroll
  for (int s = 0; s < 8; ++s) qf[s] = lds_b128(lds + LDS_X + ximg(16 * hh + 2 * s + c.h, 32 * rt + c.r));
  unsigned kb[2], vb[2];
#pragma unroll
  for (int i = 0; i < 2; ++i) {
    kb[i] = 2048 * (c.r >> 3) + 64 * (c.r & 7) + (((2 * i + c.h) ^ ((c.r >> 2) & 3)) << 4);
    vb[i] = (4 * c.h + c.q) * 128 + 64 * (i ^ (c.q >> 1)) + 32 * c.blk + 8 * (2 * (c.p & 1) + (c.p >> 1));
  }
  const __amdgpu_buffer_rsrc_t srdO = mksrd(P.wo, 4096u * 512u * 2u);
  const unsigned vo = wdma_voff(l, 1024u), so0 = g * 512u * 1024u + w * 128u;

  f32x16 o[4] = {};
  f32x16 s0, s1;
  f32x16 negm;
  float lsum = 0.f;
  u32x4 pk[4];
  constexpr int PD = 6;
  bf16x8 fr[PD];
  const bool lag = w >= 4;
  auto frag = [&](int t, int it) -> bf16x8 {
    if (it < 16) {
      const lds_cptr a = lds + VSLOT[t > 0 ? t - 1 : 0] + vb[it & 1] + 8192 * ((it >> 1) & 1) + 2048 * (it >> 2);
      return cat8(lds_tr(a), lds_tr(a + 1024));
    }
    const int s = (it - 16) >> 1;
    return lds_b128(lds + KSLOT[t < 8 ? t : 0] + kb[s & 1] + 512 * (s >> 1) + 8192 * (it & 1));
  };
  auto seam = [&](int t) {
    if (t == 0) { WAIT_BAR(0); } else if (t < 6) { WAIT_BAR(4); } else if (t == 6) { WAIT_BAR(2); } else { BAR_L(); }
    if (t + 3 < 8) dma_k(t + 3, KSLOT[t + 3 < 8 ? t + 3 : 0]);
    if (t + 2 < 8) dma_v(t + 2, VSLOT[t + 2 < 8 ? t + 2 : 0]);
    if (t == 6) {
#pragma unroll
      for (int ks = 0; ks < RING; ++ks) wring_issue(ks, ring, vo, srdO, so0, 1024u);
    }
  };
  auto extra = [&](int e) {
    const unsigned dump = lds0 + LDS_R + w * 3072u + (unsigned)(e % 3) * 1024u;
    const unsigned id = (BIDX >> 3) * 8 + w, jh = BIDX & 1;
    const unsigned vrow = (l >> 5) * 1024u + (l & 31) * 16u + jh * 512u;
    auto w3v = [&](unsigned piece) { const unsigned byte = piece * 1024u + l * 16u, row = byte / 1792u;
                                     return row * 3584u + jh * 1792u + (byte - row * 1792u); };
    if (e < 4 || e >= 15) {
      const int i = e < 4 ? e : e - 11;
      const unsigned off = i < 3 ? S_U + (unsigned)i * (64u * T * 16u) + pn * 65536u + g * 8192u
                         : i < 7 ? S_HZ + pn * 262144u + g * 32768u + (unsigned)(i - 3) * 8192u
                                 : S_H3 + pn * 229376u + g * 28672u + (unsigned)(i - 7) * 8192u;
      const u32x4 sv = {SENT, SENT, SENT, SENT};
      __builtin_amdgcn_raw_buffer_store_b128(sv, srdW, (i == 10 && tid >= 256) ? OOB : tid * 16u + off, 0, 16);
    } else if (e < 6) dma16(dump, l * 16u, srdO, (g * 512u + (pn * 8 + w) * 2 + (e - 4)) * 1024u);
    else if (e == 6) dma16(dump, vrow, mksrd(P.w1, 512u * 1024u), id * 2048u);
    else if (e == 7) dma16(dump, vrow, mksrd(P.w2, 512u * 1024u), id * 2048u);
    else if (e < 11) dma16(dump, w3v(id + 256u * (e - 8)), mksrd(P.w3, 512u * 3584u), 0u);
    else if (e == 11) {
      if (id < 128) dma16(dump, w3v(id + 768u), mksrd(P.w3, 512u * 3584u), 0u);
      else dma16(dump, vrow, mksrd(P.w4, 1792u * 1024u), (id - 128u + 768u) * 2048u);
    } else dma16(dump, vrow, mksrd(P.w4, 1792u * 1024u), (id + 256u * (e - 12)) * 2048u);
  };
#pragma unroll
  for (int t = 0; t <= 8; ++t) {
    const int i0 = t == 0 ? 16 : 0, i1 = t == 8 ? 16 : 32;
    if (t <= 1) {
#pragma unroll
      for (int it = i0; it < i0 + PD; ++it) fr[it % PD] = frag(t, it);
    }
    if (t == 0) { s0 = (f32x16){}; s1 = (f32x16){}; } else if (t < 8) { s0 = negm; s1 = negm; }
    __builtin_amdgcn_s_setprio(1);
    SBAR();
#pragma unroll
    for (int it = i0; it < i1; ++it) {
      if (it < 16) o[it & 3] = MFMA32(fr[it % PD], __builtin_bit_cast(bf16x8, pk[it >> 2]), o[it & 3]);
      else if (it & 1) s1 = MFMA32(fr[it % PD], qf[(it - 16) >> 1], s1);
      else s0 = MFMA32(fr[it % PD], qf[(it - 16) >> 1], s0);
      if (it + PD < i1) fr[it % PD] = frag(t, it + PD);
      if (t >= 1 && t <= 7 && (it & 7) == 3 && 4 * (t - 1) + (it >> 3) < 22) extra(4 * (t - 1) + (it >> 3));
      SBAR();
    }
    if (t == 8) break;
    __builtin_amdgcn_s_setprio(0);
    if (lag) seam(t);
    asm volatile("s_nop 15" : "+v"(s0), "+v"(s1));
    float ta = max3f(s0[0], s0[1], s1[0]), tb = max3f(s0[2], s0[3], s1[1]);
    ta = max3f(ta, s1[2], s1[3]);
#pragma unroll
    for (int i = 4; i < 16; i += 4) {
      ta = max3f(ta, s0[i], s0[i + 1]); tb = max3f(tb, s0[i + 2], s0[i + 3]);
      ta = max3f(ta, s1[i], s1[i + 1]); tb = max3f(tb, s1[i + 2], s1[i + 3]);
    }
    float tm = max2f(ta, tb);
    {
      auto r = __builtin_amdgcn_permlane32_swap(F2U(tm), F2U(tm), false, false);
      tm = max2f(U2F(r[0]), U2F(r[1]));
    }
    if (t == 0 || ANY(tm > THR)) {
      const float ndl = t == 0 ? -tm : -max2f(tm, 0.f);
      const float aone = c.h ? 0.f : 1.f;
      if (t == 0) negm = (f32x16){};
      __builtin_amdgcn_s_setprio(2);
      s0 = __builtin_amdgcn_mfma_f32_32x32x2f32(aone, ndl, s0, 0, 0, 0);
      s1 = __builtin_amdgcn_mfma_f32_32x32x2f32(aone, ndl, s1, 0, 0, 0);
      negm = __builtin_amdgcn_mfma_f32_32x32x2f32(aone, ndl, negm, 0, 0, 0);
      __builtin_amdgcn_s_setprio(0);
      if (t > 0) {
        const float al = __builtin_amdgcn_exp2f(ndl);
        lsum *= al;
#pragma unroll
        for (int dt = 0; dt < 4; ++dt)
#pragma unroll
          for (int i = 0; i < 16; ++i) o[dt][i] *= al;
      }
    }
    SBAR();
    if (t >= 1) {
#pragma unroll
      for (int it = 0; it < PD; ++it) fr[it] = frag(t + 1, it);
    }
#pragma unroll
    for (int i = 0; i < 16; ++i) { s0[i] = __builtin_amdgcn_exp2f(s0[i]); s1[i] = __builtin_amdgcn_exp2f(s1[i]); }
    pk[0] = pack8(s0, 0, 1.f); pk[1] = pack8(s0, 1, 1.f); pk[2] = pack8(s1, 0, 1.f); pk[3] = pack8(s1, 1, 1.f);
    {
      float l0 = lsum, l1 = 0.f, l2 = 0.f, l3 = 0.f;
#pragma unroll
      for (int i = 0; i < 16; i += 2) { l0 += s0[i]; l1 += s0[i + 1]; l2 += s1[i]; l3 += s1[i + 1]; }
      lsum = (l0 + l1) + (l2 + l3);
    }
    PIN(pk[0]); PIN(pk[1]); PIN(pk[2]); PIN(pk[3]); PIN(lsum);
    SBAR();
    if (!lag) seam(t);
  }
  __builtin_amdgcn_s_setprio(0);
  STAMP(1, 4);
  {
    const float il = __builtin_amdgcn_rcpf(swap32_add(lsum));
#pragma unroll
    for (int dt = 0; dt < 4; ++dt)
#pragma unroll
      for (int s = 0; s < 2; ++s)
        *(LAS u32x4*)(lds + LDS_X + ximg(16 * hh + 4 * dt + 2 * s + c.h, 32 * rt + c.r)) = pack8(o[dt], s, il);
  }
  BAR_L();
  STAMP(1, 5);
  {
    f32x16 acc[2][2] = {};
    gemm_phase<32, 0>(acc, lds, ringoff, ring, vo, srdO, so0, 1024u, c);
    STAMP(1, 6);
    const unsigned dst = W_P1 + ((((pn * 8 + (c.r >> 3)) * 8 + g) * 64 + 8 * w + c.h) * 8 + (c.r & 7)) * 16u;
#pragma unroll
    for (int nt = 0; nt < 2; ++nt)
#pragma unroll
      for (int r2 = 0; r2 < 2; ++r2)
#pragma unroll
        for (int s = 0; s < 2; ++s)
          __builtin_amdgcn_raw_buffer_store_b128(pack8(acc[nt][r2], s, 1.f), srdW, dst + ((4 * nt + 2 * s) * 8 + r2 * 4 * 8 * 64 * 8) * 16u, 0, 16);
  }
  WAIT_BAR(0);
  if (tid == 0) __builtin_amdgcn_raw_buffer_store_b32(1u, srdW, W_FLAG + (pn * 8 + g) * 64u, 0, 16);
  STAMP(1, 7);
}

DI void ln8(float (&v)[8], LAS float* red  , unsigned tid) {
  float s = ((v[0] + v[1]) + (v[2] + v[3])) + ((v[4] + v[5]) + (v[6] + v[7]));
  float q = (__builtin_fmaf(v[0], v[0], v[1] * v[1]) + __builtin_fmaf(v[2], v[2], v[3] * v[3])) +
            (__builtin_fmaf(v[4], v[4], v[5] * v[5]) + __builtin_fmaf(v[6], v[6], v[7] * v[7]));
  s = sum_hi3(s); q = sum_hi3(q);
  if ((tid & 63) < 8) *(LAS f32x2*)(red + ((tid >> 6) * 8 + (tid & 7)) * 2) = (f32x2){s, q};
  BAR_L();
  f32x2 e[8];
#pragma unroll
  for (int i = 0; i < 8; ++i) e[i] = *(const LAS f32x2*)(red + (i * 8 + (tid & 7)) * 2);
  const float mu = (((e[0][0] + e[1][0]) + (e[2][0] + e[3][0])) + ((e[4][0] + e[5][0]) + (e[6][0] + e[7][0]))) * (1.0f / D);
  const float ms = (((e[0][1] + e[1][1]) + (e[2][1] + e[3][1])) + ((e[4][1] + e[5][1]) + (e[6][1] + e[7][1]))) * (1.0f / D);
  const float rs = __builtin_amdgcn_rsqf(__builtin_fmaxf(__builtin_fmaf(-mu, mu, ms), 0.f) + LN_EPS);
  const float nb = -mu * rs;
#pragma unroll
  for (int k = 0; k < 8; ++k) v[k] = __builtin_fmaf(v[k], rs, nb);
}
DI void red4_put(const f32x16& a, int nt, lds_ptr scr, unsigned rt, unsigned kq, unsigned l) {
#pragma unroll
  for (int g = 0; g < 2; ++g)
#pragma unroll
    for (int i = 0; i < 2; ++i) {
      const int o = 8 * g + 4 * i;
      *(LAS f32x4*)(scr + ((rt * 4 + 2 * nt + g) * 4 + kq) * 2048 + i * 1024 + l * 16) = (f32x4){a[o], a[o + 1], a[o + 2], a[o + 3]};
    }
}
DI void red4_get(float (&v)[8], lds_cptr scr, unsigned rt, unsigned kq, unsigned l) {
  f32x4 t[4][2];
#pragma unroll
  for (int s = 0; s < 4; ++s)
#pragma unroll
    for (int i = 0; i < 2; ++i) t[s][i] = *(const LAS f32x4*)(scr + ((rt * 4 + kq) * 4 + s) * 2048 + i * 1024 + l * 16);
#pragma unroll
  for (int i = 0; i < 2; ++i)
#pragma unroll
    for (int k = 0; k < 4; ++k) v[4 * i + k] = (t[0][i][k] + t[1][i][k]) + (t[2][i][k] + t[3][i][k]);
}
DI void tile_mm8(const bf16x8 (&af)[8][2], const u32x4 (&xb)[8], float (&v)[8], lds_ptr scr, unsigned rt, unsigned kq, unsigned l) {
  f32x16 a0 = {}, a1 = {};
#pragma unroll
  for (int i = 0; i < 8; ++i) a0 = MFMA32(af[i][0], __builtin_bit_cast(bf16x8, xb[i]), a0);
  SBAR();
#pragma unroll
  for (int i = 0; i < 4; ++i) a1 = MFMA32(af[i][1], __builtin_bit_cast(bf16x8, xb[i]), a1);
  SBAR();
  red4_put(a0, 0, scr, rt, kq, l);
#pragma unroll
  for (int i = 4; i < 8; ++i) a1 = MFMA32(af[i][1], __builtin_bit_cast(bf16x8, xb[i]), a1);
  SBAR();
  red4_put(a1, 1, scr, rt, kq, l);
  BAR_L();
  red4_get(v, scr, rt, kq, l);
}

struct Frag3 { bf16x8 a, b[2]; };
constexpr int RING3 = 13;
constexpr int LA = 0, LB = 65536, LC = 131072;
DI unsigned r3off(int s, unsigned rc, unsigned rb) { return s < 4 ? rc + s * 1024u : rb + (s - 4) * 1024u; }
template <int KS> DI void load_frags3(Frag3& f, lds_cptr lds, unsigned rc, unsigned rb, unsigned a3, const LaneC& c) {
  const lds_cptr sl = lds + r3off(KS % RING3, rc, rb) + a3;
  f.a = cat8(lds_tr(sl), lds_tr(sl + 256));
#pragma unroll
  for (int rt = 0; rt < 2; ++rt) f.b[rt] = lds_b128(lds + LA + c.xb[KS & 3] + 2048 * KS + 512 * rt);
}
template <int KS>
DI void g3_step(f32x16 (&acc)[2], Frag3& cur, Frag3& n1, Frag3& n2, lds_cptr lds, unsigned lds0, unsigned rc, unsigned rb,
                unsigned voff, __amdgpu_buffer_rsrc_t srd, unsigned soff0, unsigned a3, const LaneC& c) {
  constexpr int NKS = 32;
  if constexpr (KS + 2 < NKS) {
    constexpr int last = (KS + RING3 < NKS ? KS + RING3 : NKS) - 1;
    wait_vm<last - (KS + 2)>();
    load_frags3<KS + 2>(n2, lds, rc, rb, a3, c);
  }
  SBAR();
  acc[0] = MFMA32(cur.a, cur.b[0], acc[0]);
  acc[1] = MFMA32(cur.a, cur.b[1], acc[1]);
  if constexpr (KS + RING3 < NKS) {
    asm volatile("" :: "v"(cur.a));
    dma16(lds0 + r3off(KS % RING3, rc, rb), voff, srd, soff0 + (unsigned)(16 * (KS + RING3)) * 3584u);
  }
  SBAR();
  if constexpr (KS + 1 < NKS) g3_step<KS + 1>(acc, n1, n2, cur, lds, lds0, rc, rb, voff, srd, soff0, a3, c);
}
DI void mlp_part(const Params& P, lds_ptr lds, unsigned lds0, const LaneC& c, unsigned w, __amdgpu_buffer_rsrc_t srdS) {
  const unsigned idx = BIDX >> 3, pn = 8 * ((BIDX & 7) >> 1) + (idx >> 2), j = 4 * (BIDX & 1) + (idx & 3);
  const unsigned tid = TIDX, l = c.lane;
  const unsigned vw = wdma_voff(l, 1024u);
  const unsigned rt = w & 1, kq = w >> 1;
  constexpr int LMOD = LC;
  constexpr int LRED = LC + 28672;
  const unsigned prow = 32 * rt + c.r;
  const unsigned trow = pn * 64 + prow;
  const unsigned och = 8 * j + 2 * kq + c.h;
  const unsigned ovo = ((pn * 64 + och) * 64 + prow) * 16u;
  float gb[8];
  STAMP(2, 0);

  {
    const unsigned row = pn * 64 + j * 8 + (tid & 7), ch = tid >> 3;
    dma16(lds0 + LMOD + w * 1024u, l * 16u, srdS, W_MOD + 4096u + w * 1024u);
    u32x4 xr;
    gld16(xr, P.x + (size_t)row * D + ch * 8);
    const __amdgpu_buffer_rsrc_t srd1 = mksrd(P.w1, 512u * 512u * 2u), srd2 = mksrd(P.w2, 512u * 512u * 2u);
#pragma unroll
    for (int i = 0; i < 8; ++i) dma16(lds0 + LA + (8 * w + i) * 1024u, vw, srd1, (8 * w + i) * 8192u + j * 128u);
    {
      const int fp = (int)pn - 1 + (int)(l >> 3);
      const unsigned fo = (l < 24 && fp >= 0 && fp < 32) ? W_FLAG + ((unsigned)fp * 8 + (l & 7)) * 64u : OOB;
      for (unsigned spin = 0;; ++spin) {
        asm volatile("" ::: "memory");
        const unsigned f = __builtin_amdgcn_raw_buffer_load_b32(srdS, fo, 0, 16);
        if (!ANY(fo != OOB && f != 1u) || spin > SPIN_CAP) break;
        __builtin_amdgcn_s_sleep(2);
      }
    }
    STAMP(2, 15);
    u32x4 p[8];
#pragma unroll
    for (int g = 0; g < 8; ++g) bld16_sc1(p[g], tid * 16u, srdS, W_P1 + ((pn * 8 + j) * 8 + g) * 8192u);
    asm volatile("s_waitcnt vmcnt(8)\n\ts_barrier" : "+v"(xr) :: "memory");
    float ga[8], sb[8], cb[8], g2[8];
    {
      u32x4 mv[10];
#pragma unroll
      for (int i = 0; i < 8; ++i) mv[i] = *(const LAS u32x4*)(lds + LMOD + (i >> 1) * 2048 + ch * 32 + 16 * (i & 1));
#pragma unroll
      for (int i = 0; i < 2; ++i) mv[8 + i] = *(const LAS u32x4*)(lds + LMOD + 6144 + och * 32 + 16 * i);
#pragma unroll
      for (int k = 0; k < 8; ++k) {
        ga[k] = U2F(mv[k >> 2][k & 3]); sb[k] = U2F(mv[2 + (k >> 2)][k & 3]); cb[k] = 1.f + U2F(mv[4 + (k >> 2)][k & 3]);
        g2[k] = U2F(mv[6 + (k >> 2)][k & 3]); gb[k] = U2F(mv[8 + (k >> 2)][k & 3]);
      }
    }
    asm volatile("s_waitcnt vmcnt(0)" : "+v"(p[0]), "+v"(p[1]), "+v"(p[2]), "+v"(p[3]), "+v"(p[4]), "+v"(p[5]), "+v"(p[6]), "+v"(p[7]) :: "memory");
    STAMP(2, 1);
    float v[8], x2[8];
#pragma unroll
    for (int i = 0; i < 4; ++i) {
      const float lo = ((bflo(p[0][i]) + bflo(p[1][i])) + (bflo(p[2][i]) + bflo(p[3][i]))) + ((bflo(p[4][i]) + bflo(p[5][i])) + (bflo(p[6][i]) + bflo(p[7][i])));
      const float hi = ((bfhi(p[0][i]) + bfhi(p[1][i])) + (bfhi(p[2][i]) + bfhi(p[3][i]))) + ((bfhi(p[4][i]) + bfhi(p[5][i])) + (bfhi(p[6][i]) + bfhi(p[7][i])));
      v[2 * i] = x2[2 * i] = __builtin_fmaf(ga[2 * i], lo, bflo(xr[i]));
      v[2 * i + 1] = x2[2 * i + 1] = __builtin_fmaf(ga[2 * i + 1], hi, bfhi(xr[i]));
    }
    ln8(v, (LAS float*)(lds + LRED), tid);
    float hb[8];
#pragma unroll
    for (int k = 0; k < 8; ++k) v[k] = hb[k] = __builtin_fmaf(v[k], cb[k], sb[k]);
    ln8(v, (LAS float*)(lds + LRED + 512), tid);
    const unsigned vo = ((pn * 64 + ch) * 64 + j * 8 + (tid & 7)) * 16u, vz = ((pn * 64 + ch) * 256 + j * 8 + (tid & 7)) * 16u;
    pub16(srdS, vo, S_U, (u32x4){cvtpk(v[0], v[1]), cvtpk(v[2], v[3]), cvtpk(v[4], v[5]), cvtpk(v[6], v[7])});
#pragma unroll
    for (int i = 0; i < 2; ++i) {
      pub16(srdS, vz, S_HZ + i * 1024u, (u32x4){F2U(hb[4 * i]), F2U(hb[4 * i + 1]),
                                                F2U(hb[4 * i + 2]), F2U(hb[4 * i + 3])});
      f32x4 z;
#pragma unroll
      for (int e = 0; e < 4; ++e) z[e] = __builtin_fmaf(g2[4 * i + e], hb[4 * i + e], x2[4 * i + e]);
      pub16(srdS, vz, S_HZ + (2 + i) * 1024u, __builtin_bit_cast(u32x4, z));
    }
#pragma unroll
    for (int i = 0; i < 8; ++i) dma16(lds0 + LB + (8 * w + i) * 1024u, vw, srd2, (8 * w + i) * 8192u + j * 128u);
  }
  STAMP(2, 2);
  const __amdgpu_buffer_rsrc_t srd3 = mksrd(P.w3, 512u * F * 2u);
  const unsigned rc = LC + w * 4096u, rb = LB + w * 9216u;
  const unsigned v3 = (l >> 2) * 3584u + ((l & 3) << 4), s30 = (j * 224u + w * 32u) * 2u;
  float v[8];
  {
    bf16x8 af[8][2];
#pragma unroll
    for (int i = 0; i < 8; ++i)
#pragma unroll
      for (int nt = 0; nt < 2; ++nt) {
        const lds_cptr a = lds + LA + (8 * kq + i) * 2048 + c.afr[nt];
        af[i][nt] = cat8(lds_tr(a), lds_tr(a + 512));
      }
    BAR_L();
    u32x4 xb[8];
    poll_ind<8, 3, 7>(xb, srdS, ((pn * 64 + 16 * kq + c.h) * 64 + prow) * 16u, S_U, 2048u);
    STAMP(2, 3);
    tile_mm8(af, xb, v, lds + LA, rt, kq, l);
#pragma unroll
    for (int k = 0; k < 8; ++k) v[k] = gelu_tanh(v[k]);
    pub16(srdS, ovo, S_H1, (u32x4){cvtpk(v[0], v[1]), cvtpk(v[2], v[3]), cvtpk(v[4], v[5]), cvtpk(v[6], v[7])});
    if (w < 7) {
#pragma unroll
      for (int ks = 0; ks < 4; ++ks) dma16(lds0 + r3off(ks, rc, rb), v3, srd3, s30 + (unsigned)(16 * ks) * 3584u);
    }
  }
  STAMP(2, 4);
  float y[8];
  u32x4 cs[4];
  const unsigned cso = w < 7 ? (224 * j + 32 * w + 8 * c.h) * 4u : OOB;
  {
    bf16x8 af[8][2];
#pragma unroll
    for (int i = 0; i < 8; ++i)
#pragma unroll
      for (int nt = 0; nt < 2; ++nt) {
        const lds_cptr a = lds + LB + (8 * kq + i) * 2048 + c.afr[nt];
        af[i][nt] = cat8(lds_tr(a), lds_tr(a + 512));
      }
    BAR_L();
    u32x4 hz[4];
    const unsigned hzo = ((pn * 64 + och) * 256 + prow) * 16u;
#pragma unroll
    for (int i = 0; i < 4; ++i) hz[i] = __builtin_amdgcn_raw_buffer_load_b128(srdS, hzo, S_HZ + i * 1024u, 16);
#pragma unroll
    for (int i = 0; i < 4; ++i) cs[i] = __builtin_amdgcn_raw_buffer_load_b128(srdS, cso, W_MOD + 12288u + 64u * (i >> 1) + 16u * (i & 1), 0);
    const int sh = kq == 0 ? -1 : kq == 1 ? 1 : kq == 2 ? -2 : 2;
    const int srow = (int)trow + sh;
    const unsigned xvo = (srow >= 0 && srow < T) ? ((((unsigned)srow >> 6) * 64 + 16 * kq + c.h) * 64 + ((unsigned)srow & 63)) * 16u : OOB;
    u32x4 xb[8];
    poll_ind<8, 3, 7>(xb, srdS, xvo, S_H1, 2048u);
    STAMP(2, 5);
    tile_mm8(af, xb, v, lds + LA, rt, kq, l);
    if (has_sent(hz, 4)) poll_load<4>(hz, srdS, hzo, S_HZ, 1024u);
    float x1[8];
#pragma unroll
    for (int k = 0; k < 8; ++k) {
      x1[k] = U2F(hz[k >> 2][k & 3]) + v[k];
      y[k] = __builtin_fmaf(gb[k], v[k], U2F(hz[2 + (k >> 2)][k & 3]));
    }
    pub16(srdS, ovo, S_X1, (u32x4){cvtpk(x1[0], x1[1]), cvtpk(x1[2], x1[3]), cvtpk(x1[4], x1[5]), cvtpk(x1[6], x1[7])});
    if (w < 7) {
#pragma unroll
      for (int ks = 4; ks < RING3; ++ks) dma16(lds0 + r3off(ks, rc, rb), v3, srd3, s30 + (unsigned)(16 * ks) * 3584u);
    }
  }
  STAMP(2, 6);
  {
    u32x4 xr[8];
    BAR_L();
    poll_ind<8, 3, 7>(xr, srdS, ((pn * 64 + 8 * w) * 64 + l) * 16u, S_X1, 1024u);
    STAMP(2, 7);
#pragma unroll
    for (int i = 0; i < 8; ++i) *(LAS u32x4*)(lds + LA + ximg(8 * w + i, l)) = xr[i];
    {
      float s0 = 0.f, s1 = 0.f, q0 = 0.f, q1 = 0.f;
#pragma unroll
      for (int i = 0; i < 8; ++i)
#pragma unroll
        for (int k = 0; k < 4; k += 2) {
          s0 = dot2bf(xr[i][k], 0x3F803F80u, s0); s1 = dot2bf(xr[i][k + 1], 0x3F803F80u, s1);
          q0 = dot2bf(xr[i][k], xr[i][k], q0); q1 = dot2bf(xr[i][k + 1], xr[i][k + 1], q1);
        }
      *(LAS f32x2*)(lds + LRED + (w * 64 + l) * 8) = (f32x2){s0 + s1, q0 + q1};
    }
    BAR_L();
    STAMP(2, 8);
    if (w < 7) {
      f32x16 acc[2] = {};
      const unsigned a3 = (8 * c.h + c.q) * 64 + 32 * c.blk + 8 * (2 * (c.p & 1) + (c.p >> 1));
      Frag3 f0, f1, f2;
      wait_vm<RING3 - 2>();
      load_frags3<0>(f0, lds, rc, rb, a3, c);
      load_frags3<1>(f1, lds, rc, rb, a3, c);
      float mu[2], rs[2];
#pragma unroll
      for (int r2 = 0; r2 < 2; ++r2) {
        float s = 0.f, q = 0.f;
#pragma unroll
        for (int i = 0; i < 8; ++i) {
          const f32x2 t = *(const LAS f32x2*)(lds + LRED + (i * 64 + 32 * r2 + c.r) * 8);
          s += t[0]; q += t[1];
        }
        mu[r2] = s * (1.0f / D);
        rs[r2] = __builtin_amdgcn_rsqf(__builtin_fmaxf(__builtin_fmaf(-mu[r2], mu[r2], q * (1.0f / D)), 0.f) + LN_EPS);
      }
      if (w < 4) __builtin_amdgcn_s_setprio(2); else __builtin_amdgcn_s_setprio(1);
      g3_step<0>(acc, f0, f1, f2, lds, lds0, rc, rb, v3, srd3, s30, a3, c);
      __builtin_amdgcn_s_setprio(0);
      STAMP(2, 9);
#pragma unroll
      for (int r2 = 0; r2 < 2; ++r2) {
#pragma unroll
        for (int i = 0; i < 16; ++i) acc[r2][i] = gelu_tanh(rs[r2] * __builtin_fmaf(-mu[r2], U2F(cs[i >> 2][i & 3]), acc[r2][i]));
#pragma unroll
        for (int s2 = 0; s2 < 2; ++s2)
          pub16(srdS, ((pn * 224 + 28 * j + 4 * w + 2 * s2 + c.h) * 64 + 32 * r2 + c.r) * 16u, S_H3, pack8(acc[r2], s2, 1.f));
      }
    }
    BAR_L();
  }
  STAMP(2, 10);
  {
    const __amdgpu_buffer_rsrc_t srd4 = mksrd(P.w4, (unsigned)F * 512u * 2u);
    const unsigned qb = kq * 40960u;
    const unsigned s40 = (448u * kq + 8u * rt) * 1024u + j * 128u;
    auto w4dma = [&](int ks) { dma16(lds0 + qb + (ks % 20) * 2048u + rt * 1024u, vw, srd4, s40 + (unsigned)ks * 16384u); };
#pragma unroll
    for (int ks = 0; ks < 8; ++ks) w4dma(ks);
    const unsigned hvo = ((pn * 224 + 56 * kq + c.h) * 64 + prow) * 16u;
    u32x4 ha[14], hb[14];
    poll_ind<14, 9, 13>(ha, srdS, hvo, S_H3, 2048u);
    BAR_L();
    STAMP(2, 11);
    f32x16 acc[2] = {};
    bf16x8 fa[2][2];
    auto ld = [&](int ks) {
#pragma unroll
      for (int nt = 0; nt < 2; ++nt) {
        const lds_cptr a = lds + qb + (ks % 20) * 2048 + c.afr[nt];
        fa[ks & 1][nt] = cat8(lds_tr(a), lds_tr(a + 512));
      }
    };
    auto mm = [&](int ks, const u32x4& b) {
#pragma unroll
      for (int nt = 0; nt < 2; ++nt) acc[nt] = MFMA32(fa[ks & 1][nt], __builtin_bit_cast(bf16x8, b), acc[nt]);
    };
    auto hbl = [&](int i) { bld16_sc1(hb[i], hvo, srdS, S_H3 + (14u + i) * 2048u); };
    ld(0);
#pragma unroll
    for (int ks = 0; ks < 8; ++ks) {
      if (ks < 7) ld(ks + 1);
      SBAR(); mm(ks, ha[ks]);
      if (ks < 6) { w4dma(8 + ks); hbl(2 * ks); hbl(2 * ks + 1); }
      else if (ks == 6) { hbl(12); hbl(13); w4dma(14); w4dma(15); w4dma(16); }
      else { w4dma(17); w4dma(18); w4dma(19); }
      SBAR();
    }
    WAIT_BAR(10);
    ld(8);
#pragma unroll
    for (int ks = 8; ks < 14; ++ks) {
      if (ks < 13) ld(ks + 1);
      SBAR(); mm(ks, ha[ks]);
      if (ks < 12) { w4dma(20 + 2 * (ks - 8)); w4dma(21 + 2 * (ks - 8)); }
      SBAR();
    }
    asm volatile("s_waitcnt vmcnt(8) lgkmcnt(0)\n\ts_barrier" : "+v"(hb[0]), "+v"(hb[1]), "+v"(hb[2]), "+v"(hb[3]), "+v"(hb[4]), "+v"(hb[5]),
                 "+v"(hb[6]), "+v"(hb[7]), "+v"(hb[8]), "+v"(hb[9]), "+v"(hb[10]), "+v"(hb[11]), "+v"(hb[12]), "+v"(hb[13]) :: "memory");
    if (has_sent(hb, 14)) poll_load<14>(hb, srdS, hvo, S_H3 + 14u * 2048u, 2048u);
    STAMP(2, 12);
    ld(14);
#pragma unroll
    for (int ks = 14; ks < 19; ++ks) { ld(ks + 1); SBAR(); mm(ks, hb[ks - 14]); SBAR(); }
    mm(19, hb[5]);
    WAIT_BAR(0);
    ld(20);
#pragma unroll
    for (int ks = 20; ks < 27; ++ks) { ld(ks + 1); SBAR(); mm(ks, hb[ks - 14]); SBAR(); }
    mm(27, hb[13]);
    BAR_L();
    STAMP(2, 13);
    red4_put(acc[0], 0, lds + LA, rt, kq, l);
    red4_put(acc[1], 1, lds + LA, rt, kq, l);
    BAR_L();
    red4_get(v, lds + LA, rt, kq, l);
#pragma unroll
    for (int k = 0; k < 8; ++k) v[k] = __builtin_fmaf(gb[k], v[k], y[k]);
    P.out[(size_t)trow * 64 + och] = (u32x4){cvtpk(v[0], v[1]), cvtpk(v[2], v[3]), cvtpk(v[4], v[5]), cvtpk(v[6], v[7])};
  }
  STAMP(2, 14);
}

__global__ __launch_bounds__(512, 2) void k_fused(Params P) {
  extern __shared__ __attribute__((aligned(16))) char smem[];
  const lds_ptr lds = (lds_ptr)smem;
  const unsigned lds0 = (unsigned)(uintptr_t)smem;
  const unsigned w = rfl(TIDX >> 6);
  LaneC c; lane_consts(c);
  const __amdgpu_buffer_rsrc_t srdW = mksrd(P.ws, W_END);
  attn_part(P, lds, lds0, c, w, srdW);
  mlp_part(P, lds, lds0, c, w, srdW);
}

#if defined(__HIP_DEVICE_COMPILE__)
#pragma clang attribute pop
#endif
extern "C" void kernel_launch(void* const* d_in, const int* in_sizes, int n_in, void* d_out, int out_size, void* d_ws,
                              size_t ws_size, hipStream_t stream) {
  Params P;
  P.x = (const unsigned short*)d_in[0];
  P.kc = (const unsigned short*)d_in[1];
  P.vc = (const unsigned short*)d_in[2];
  P.wq = (const unsigned short*)d_in[3];
  P.wo = (const unsigned short*)d_in[4];
  P.w1 = (const unsigned short*)d_in[6];
  P.w2 = (const unsigned short*)d_in[7];
  P.w3 = (const unsigned short*)d_in[8];
  P.w4 = (const unsigned short*)d_in[9];
  P.c = (const unsigned short*)d_in[10];
  P.wmod = (const unsigned short*)d_in[11];
  P.ws = d_ws;
  P.out = (u32x4*)d_out;
  (void)hipFuncSetAttribute((const void*)k_fused, hipFuncAttributeMaxDynamicSharedMemorySize, LDS_TOTAL);
  k_init<<<154, 512, 0, stream>>>(P.c, P.wmod, P.w3, d_ws);
  k_fused<<<256, 512, LDS_TOTAL, stream>>>(P);
}
```

```cpp
#include <hip/hip_runtime.h>
#include <hip/hip_bf16.h>
#include <stdint.h>

#if defined(__HIP_DEVICE_COMPILE__)
#pragma clang attribute push(__attribute__((target("no-packed-fp32-ops"))), apply_to = function)
#endif
typedef short bf16x8 __attribute__((ext_vector_type(8)));
typedef short s16x4 __attribute__((ext_vector_type(4)));
typedef float f32x16 __attribute__((ext_vector_type(16)));
typedef float f32x4 __attribute__((ext_vector_type(4)));
typedef float f32x2 __attribute__((ext_vector_type(2)));
typedef unsigned u32x4 __attribute__((ext_vector_type(4)));
typedef unsigned u32x2 __attribute__((ext_vector_type(2)));
typedef __bf16 bf16x2_t __attribute__((ext_vector_type(2)));
typedef short v4i16_t __attribute__((ext_vector_type(4)));
#define LAS __attribute__((address_space(3)))
typedef LAS const char* lds_cptr;
typedef LAS char* lds_ptr;
#define DI __device__ __forceinline__
#define F2U(x) __builtin_bit_cast(unsigned, (float)(x))
#define U2F(x) __builtin_bit_cast(float, (unsigned)(x))
#define F2I(x) __builtin_bit_cast(int, (float)(x))
#define I2F(x) __builtin_bit_cast(float, (int)(x))
#define TIDX __builtin_amdgcn_workitem_id_x()
#define BIDX __builtin_amdgcn_workgroup_id_x()
#define ANY(p) (__builtin_amdgcn_ballot_w64(p) != 0ull)
#define SBAR() __builtin_amdgcn_sched_barrier(0)
#define MFMA32(a, b, c) __builtin_amdgcn_mfma_f32_32x32x16_bf16((a), (b), (c), 0, 0, 0)
#define WAIT_V(n) asm volatile("s_waitcnt vmcnt(" #n ")" ::: "memory")
#define WAIT_L0() asm volatile("s_waitcnt lgkmcnt(0)" ::: "memory")
#define BAR_L() asm volatile("s_waitcnt lgkmcnt(0)\n\ts_barrier" ::: "memory")
#define WAIT_BAR(n) asm volatile("s_waitcnt vmcnt(" #n ") lgkmcnt(0)\n\ts_barrier" ::: "memory")

constexpr int T = 2048, D = 512, HKV = 8, SK = 2048, DH = 128, WIN = 512, F = 1792;
constexpr float LN_EPS = 1e-5f;
constexpr float QSCALE = 0.088388347648318440f * 1.4426950408889634f;

constexpr unsigned W_MOD = 0;
constexpr unsigned W_MP = 32768;
constexpr unsigned W_FLAG = W_MP + 8u * 4864u * 4u + 8192u;
constexpr unsigned W_P1 = 262144;
constexpr unsigned S_U = W_P1 + 8u * 64u * T * 16u;
constexpr unsigned S_H1 = S_U + 64u * T * 16u;
constexpr unsigned S_X1 = S_H1 + 64u * T * 16u;
constexpr unsigned S_HZ = S_X1 + 64u * T * 16u;
constexpr unsigned S_H3 = S_HZ + 256u * T * 16u;
constexpr unsigned W_END = S_H3 + 224u * T * 16u;
static_assert(W_FLAG % 4096 == 0 && W_FLAG + 16384 <= W_P1, "ws");
constexpr unsigned OOB = 0xF0000000u;
#define K1MAP 0
#define ATHR 32.0f
constexpr float THR = ATHR;
constexpr unsigned SENT = 0xFFFFFFFFu;
constexpr unsigned SPIN_CAP = 1u << 16;

constexpr int LDS_X = 0;
constexpr int LDS_R = 65536;
constexpr int RING = 5, RINGB = RING * 2048;
constexpr int LDS_D = LDS_R + 8 * RINGB;
constexpr int LDS_TOTAL = LDS_D + 16384;
static_assert(LDS_TOTAL == 163840, "lds");

#define STAMP(kid, i) do {} while (0)
DI unsigned cvtpk(float lo, float hi) {
  f32x2 v = {lo, hi};
  bf16x2_t b = __builtin_convertvector(v, bf16x2_t);
  return __builtin_bit_cast(unsigned, b);
}
DI float bflo(unsigned u) { return U2F(u << 16); }
DI float bfhi(unsigned u) { return U2F(u & 0xffff0000u); }
DI unsigned rfl(unsigned x) { return __builtin_amdgcn_readfirstlane(x); }
DI __amdgpu_buffer_rsrc_t mksrd(const void* p, unsigned bytes) {
  return __builtin_amdgcn_make_buffer_rsrc((void*)p, (short)0, (int)bytes, 0x00020000);
}
DI void dma16(unsigned lds, unsigned voff, __amdgpu_buffer_rsrc_t srd, unsigned soff) {
  asm volatile("s_mov_b32 m0, %0\n\ts_nop 0\n\tbuffer_load_dwordx4 %1, %2, %3 offen lds"
               :: "s"(lds), "v"(voff), "s"(srd), "s"(soff) : "memory");
}
DI void gld16(u32x4& d, const void* p) { asm volatile("global_load_dwordx4 %0, %1, off" : "=v"(d) : "v"(p) : "memory"); }
DI void bld16_sc1(u32x4& d, unsigned voff, __amdgpu_buffer_rsrc_t srd, unsigned soff) {
  asm volatile("buffer_load_dwordx4 %0, %1, %2, %3 offen sc1" : "=v"(d) : "v"(voff), "s"(srd), "s"(soff) : "memory");
}
DI void dma16_sc1(unsigned lds, unsigned voff, __amdgpu_buffer_rsrc_t srd, unsigned soff) {
  asm volatile("s_mov_b32 m0, %0\n\ts_nop 0\n\tbuffer_load_dwordx4 %1, %2, %3 offen sc1 lds"
               :: "s"(lds), "v"(voff), "s"(srd), "s"(soff) : "memory");
}
DI bf16x8 lds_b128(lds_cptr p) { return *(const LAS bf16x8*)p; }
DI s16x4 lds_tr(lds_cptr p) {
  return __builtin_bit_cast(s16x4, __builtin_amdgcn_ds_read_tr16_b64_v4i16((LAS v4i16_t*)p));
}
DI bf16x8 cat8(s16x4 lo, s16x4 hi) { return __builtin_shufflevector(lo, hi, 0, 1, 2, 3, 4, 5, 6, 7); }
DI float swap32_add(float x) {
  auto r = __builtin_amdgcn_permlane32_swap(F2U(x), F2U(x), false, false);
  return U2F(r[0]) + U2F(r[1]);
}
DI float swap32_max(float x) {
  auto r = __builtin_amdgcn_permlane32_swap(F2U(x), F2U(x), false, false);
  return __builtin_fmaxf(U2F(r[0]), U2F(r[1]));
}
template <int CTRL> DI float dpp_add(float x) {
  int y = __builtin_amdgcn_update_dpp(0, F2I(x), CTRL, 0xF, 0xF, true);
  return x + I2F(y);
}
DI float max3f(float a, float b, float c) { float r; asm("v_max3_f32 %0, %1, %2, %3" : "=v"(r) : "v"(a), "v"(b), "v"(c)); return r; }
DI float max2f(float a, float b) { float r; asm("v_max_f32_e32 %0, %1, %2" : "=v"(r) : "v"(a), "v"(b)); return r; }
DI float dot2bf(unsigned a, unsigned b, float c) {
  return __builtin_amdgcn_fdot2_f32_bf16(__builtin_bit_cast(bf16x2_t, a), __builtin_bit_cast(bf16x2_t, b), c, false);
}
#define PIN(x) asm volatile("" : "+v"(x))
DI float swap16_add(float x) {
  auto r = __builtin_amdgcn_permlane16_swap(F2U(x), F2U(x), false, false);
  return U2F(r[0]) + U2F(r[1]);
}
DI float sum_hi3(float x) { return swap32_add(swap16_add(dpp_add<0x128>(x))); }
DI float gelu_tanh(float x) {
  const float k0 = -2.0f * 0.7978845608028654f * 1.4426950408889634f;
  const float t = x * x;
  const float z = x * __builtin_fmaf(t, 0.044715f * k0, k0);
  const float e = __builtin_amdgcn_exp2f(z);
  return x * __builtin_amdgcn_rcpf(1.0f + e);
}
DI void wave_sum8(const float (&a)[8], float (&out)[8]) {
  float b[4], c[2];
#pragma unroll
  for (int i = 0; i < 4; ++i) {
    auto r = __builtin_amdgcn_permlane32_swap(F2U(a[i]), F2U(a[i + 4]), false, false);
    b[i] = U2F(r[0]) + U2F(r[1]);
  }
#pragma unroll
  for (int i = 0; i < 2; ++i) {
    auto r = __builtin_amdgcn_permlane16_swap(F2U(b[i]), F2U(b[i + 2]), false, false);
    c[i] = U2F(r[0]) + U2F(r[1]);
  }
#pragma unroll
  for (int i = 0; i < 2; ++i) {
    c[i] = dpp_add<0xB1>(c[i]);
    c[i] = dpp_add<0x4E>(c[i]);
    c[i] = dpp_add<0x141>(c[i]);
    c[i] = dpp_add<0x140>(c[i]);
  }
#pragma unroll
  for (int q = 0; q < 4; ++q)
#pragma unroll
    for (int i = 0; i < 2; ++i)
      out[i + 2 * (q & 1) + 4 * (q >> 1)] = I2F(__builtin_amdgcn_readlane(F2I(c[i]), 16 * q));
}

DI unsigned nosent(unsigned v) { return v < 0xFFFFFFFEu ? v : 0xFFFFFFFEu; }
DI void pub16(__amdgpu_buffer_rsrc_t srd, unsigned voff, unsigned soff, u32x4 v) {
  v = (u32x4){nosent(v[0]), nosent(v[1]), nosent(v[2]), nosent(v[3])};
  __builtin_amdgcn_raw_buffer_store_b128(v, srd, voff + soff, 0, 16);
}
DI unsigned umax3(unsigned a, unsigned b, unsigned c) { const unsigned m = a > b ? a : b; return m > c ? m : c; }
DI bool has_sent(const u32x4* v, int n) {
  unsigned m = 0;
#pragma unroll
  for (int i = 0; i < n; ++i) { m = umax3(m, v[i][0], v[i][1]); m = umax3(m, v[i][2], v[i][3]); }
  return ANY(m == SENT);
}
template <int N> DI void poll_load(u32x4* v, __amdgpu_buffer_rsrc_t srd, unsigned voff, unsigned soff0, unsigned step) {
  for (unsigned spin = 0;; ++spin) {
    asm volatile("" ::: "memory");
#pragma unroll
    for (int i = 0; i < N; ++i) v[i] = __builtin_amdgcn_raw_buffer_load_b128(srd, voff, soff0 + (unsigned)i * step, 16);
    if (!has_sent(v, N) || spin > SPIN_CAP) break;
    __builtin_amdgcn_s_sleep(1);
  }
}
template <int N, int I0, int I1> DI void poll_ind(u32x4* v, __amdgpu_buffer_rsrc_t srd, unsigned voff, unsigned soff0, unsigned step) {
  u32x4 ind[2];
  poll_load<2>(ind, srd, voff, soff0 + (unsigned)I0 * step, (unsigned)(I1 - I0) * step);
  poll_load<N>(v, srd, voff, soff0, step);
}

__global__ __launch_bounds__(256) void k_init(void* ws) {
  const __amdgpu_buffer_rsrc_t srd = mksrd(ws, W_P1);
  const unsigned u = BIDX * 256 + TIDX;
  const unsigned v = u < W_FLAG / 16u ? SENT : 0u;
  __builtin_amdgcn_raw_buffer_store_b128((u32x4){v, v, v, v}, srd, u * 16u, 0, 16);
}

struct LaneC {
  unsigned lane, r, h, q, p, blk;
  unsigned wv;
  unsigned afr[2];
  unsigned xb[4];
};
DI void lane_consts(LaneC& c) {
  const unsigned l = TIDX & 63;
  c.lane = l; c.r = l & 31; c.h = l >> 5; c.q = (l & 15) >> 2; c.p = l & 3; c.blk = (l >> 4) & 1;
  const unsigned sp = 2 * (c.p & 1) + (c.p >> 1);
#pragma unroll
  for (int nt = 0; nt < 2; ++nt) c.afr[nt] = (8 * c.h + c.q) * 128 + 64 * (nt ^ (c.q >> 1)) + 32 * c.blk + 8 * sp;
#pragma unroll
  for (int s = 0; s < 4; ++s) c.xb[s] = 1024 * c.h + ((c.r ^ (2 * s + c.h)) << 4);
}
DI unsigned wdma_voff(unsigned l, unsigned ld) { return (l >> 3) * ld + (((l & 7) ^ (((l >> 4) & 1) << 2)) << 4); }

DI void wring_issue(int ks, unsigned ring, unsigned voff, __amdgpu_buffer_rsrc_t srd, unsigned soff0, unsigned ld) {
  const unsigned slot = ring + (unsigned)(ks % RING) * 2048u;
  dma16(slot, voff, srd, soff0 + (unsigned)(16 * ks) * ld);
  dma16(slot + 1024u, voff, srd, soff0 + (unsigned)(16 * ks + 8) * ld);
}
template <int N> DI void wait_vm() {
  if constexpr (N == 0) WAIT_V(0); else if constexpr (N == 1) WAIT_V(1); else if constexpr (N == 2) WAIT_V(2);
  else if constexpr (N == 3) WAIT_V(3); else if constexpr (N == 4) WAIT_V(4); else if constexpr (N == 5) WAIT_V(5);
  else if constexpr (N == 6) WAIT_V(6); else if constexpr (N == 7) WAIT_V(7); else if constexpr (N == 8) WAIT_V(8);
  else if constexpr (N == 9) WAIT_V(9); else if constexpr (N == 10) WAIT_V(10); else if constexpr (N == 11) WAIT_V(11);
  else if constexpr (N == 12) WAIT_V(12); else if constexpr (N == 13) WAIT_V(13); else if constexpr (N == 14) WAIT_V(14);
  else if constexpr (N == 15) WAIT_V(15); else if constexpr (N == 16) WAIT_V(16);
  else if constexpr (N == 17) WAIT_V(17); else if constexpr (N == 18) WAIT_V(18);
  else static_assert(N < 0, "wait_vm");
}

struct Frag { bf16x8 a[2], b[2]; };
template <int KS> DI void load_frags(Frag& f, lds_cptr lds, unsigned ringoff, const LaneC& c) {
  const lds_cptr sl = lds + ringoff + (KS % RING) * 2048;
#pragma unroll
  for (int nt = 0; nt < 2; ++nt) f.a[nt] = cat8(lds_tr(sl + c.afr[nt]), lds_tr(sl + c.afr[nt] + 512));
#pragma unroll
  for (int rt = 0; rt < 2; ++rt) f.b[rt] = lds_b128(lds + LDS_X + c.xb[KS & 3] + 2048 * KS + 512 * rt);
}
template <int NKS, int KS, int EXTRA>
DI void gemm_step(f32x16 (&acc)[2][2], Frag& cur, Frag& nxt, lds_cptr lds, unsigned ringoff, unsigned ring, unsigned voff,
                  __amdgpu_buffer_rsrc_t srd, unsigned soff0, unsigned ld, const LaneC& c) {
  if constexpr (KS + 1 < NKS) {
    constexpr int last = (KS + RING < NKS ? KS + RING : NKS) - 1;
    constexpr int after = 2 * (last - (KS + 1)) + ((KS + 1 < RING) ? EXTRA : 0);
    wait_vm<after>();
    load_frags<KS + 1>(nxt, lds, ringoff, c);
  }
  SBAR();
#pragma unroll
  for (int nt = 0; nt < 2; ++nt)
#pragma unroll
    for (int rt = 0; rt < 2; ++rt) acc[nt][rt] = MFMA32(cur.a[nt], cur.b[rt], acc[nt][rt]);
  if constexpr (KS + RING < NKS) {
    asm volatile("" :: "v"(cur.a[0]), "v"(cur.a[1]));
    wring_issue(KS + RING, ring, voff, srd, soff0, ld);
  }
  SBAR();
  if constexpr (KS + 1 < NKS) gemm_step<NKS, KS + 1, EXTRA>(acc, nxt, cur, lds, ringoff, ring, voff, srd, soff0, ld, c);
}
template <int NKS, int EXTRA>
DI void gemm_phase(f32x16 (&acc)[2][2], lds_cptr lds, unsigned ringoff, unsigned ring, unsigned voff,
                   __amdgpu_buffer_rsrc_t srd, unsigned soff0, unsigned ld, const LaneC& c) {
  Frag f0, f1;
  wait_vm<2 * (RING - 1) + EXTRA>();
  load_frags<0>(f0, lds, ringoff, c);
  gemm_step<NKS, 0, EXTRA>(acc, f0, f1, lds, ringoff, ring, voff, srd, soff0, ld, c);
}
DI u32x4 pack8(const f32x16& a, int s, float sc) {
  return (u32x4){cvtpk(a[8 * s] * sc, a[8 * s + 1] * sc), cvtpk(a[8 * s + 2] * sc, a[8 * s + 3] * sc),
                 cvtpk(a[8 * s + 4] * sc, a[8 * s + 5] * sc), cvtpk(a[8 * s + 6] * sc, a[8 * s + 7] * sc)};
}
DI unsigned ximg(unsigned chunk, unsigned row) { return chunk * 1024u + ((row ^ (chunk & 7u)) << 4); }

constexpr int KSLOT[8] = {LDS_D, LDS_R, LDS_R + 32768, LDS_X, LDS_X + 32768, LDS_D, LDS_X, LDS_X + 32768};
constexpr int VSLOT[8] = {LDS_R + 16384, LDS_R + 49152, LDS_X + 16384, LDS_X + 49152, LDS_R + 65536, LDS_X + 16384, LDS_X + 49152, LDS_D};
struct Params {
  const unsigned short *x, *kc, *vc, *wq, *wo, *w1, *w2, *w3, *w4, *c, *wmod;
  void* ws;
  u32x4* out;
};
DI void attn_part(const Params& P, lds_ptr lds, unsigned lds0, const LaneC& c, unsigned w, __amdgpu_buffer_rsrc_t srdW) {
  const unsigned g = BIDX & 7, pn = BIDX >> 3;
  const unsigned l = c.lane, tid = TIDX;
  const unsigned ringoff = LDS_R + w * RINGB, ring = lds0 + ringoff;
  STAMP(1, 0);

  const unsigned jid = w == 0 ? BIDX : 256u + (w - 4u) * 256u + BIDX;
  const bool job = (w == 0 || w >= 4) && jid < 1216u;
  const unsigned jcol = jid < 256u ? (jid & 31u) : 32u + (jid - 256u) % 120u, jks = jid < 256u ? jid >> 5 : (jid - 256u) / 120u;
  const bool isw3 = jcol >= 96u;
  u32x4 jw[4]; unsigned jc[4];
  auto job_load = [&]() {
    const unsigned short* src = isw3 ? P.w3 + (jcol - 96u) * 32 + (l & 3) * 8 : P.wmod + jcol * 32 + (l & 3) * 8;
    const unsigned ld = isw3 ? F : 3072, r0 = jks * 64 + (l >> 2);
#pragma unroll
    for (int i = 0; i < 4; ++i) gld16(jw[i], src + (size_t)(r0 + 16 * i) * ld);
#pragma unroll
    for (int i = 0; i < 4; ++i) asm volatile("global_load_ushort %0, %1, off" : "=v"(jc[i]) : "v"(P.c + r0 + 16 * i) : "memory");
  };
  auto job_run = [&]() {
    asm volatile("s_waitcnt vmcnt(0)" : "+v"(jw[0]), "+v"(jw[1]), "+v"(jw[2]), "+v"(jw[3]), "+v"(jc[0]), "+v"(jc[1]), "+v"(jc[2]), "+v"(jc[3]) :: "memory");
    float acc[8] = {0, 0, 0, 0, 0, 0, 0, 0};
#pragma unroll
    for (int i = 0; i < 4; ++i) {
      const float v = U2F(jc[i] << 16);
      const float sv = v * __builtin_amdgcn_rcpf(1.0f + __builtin_amdgcn_exp2f(-1.4426950408889634f * v));
      const float sc = isw3 ? 1.0f : sv;
#pragma unroll
      for (int k = 0; k < 4; ++k) {
        acc[2 * k] = __builtin_fmaf(sc, bflo(jw[i][k]), acc[2 * k]);
        acc[2 * k + 1] = __builtin_fmaf(sc, bfhi(jw[i][k]), acc[2 * k + 1]);
      }
    }
#pragma unroll
    for (int k = 0; k < 8; ++k) acc[k] = sum_hi3(dpp_add<0x124>(acc[k]));
    const bool hi = (l >> 2) & 1;
    const f32x4 t = {hi ? acc[4] : acc[0], hi ? acc[5] : acc[1], hi ? acc[6] : acc[2], hi ? acc[7] : acc[3]};
    pub16(srdW, l < 8 ? (l & 3) * 32u + (l >> 2) * 16u : OOB, W_MP + (jks * 4864u + jcol * 32u) * 4u, __builtin_bit_cast(u32x4, t));
  };
  if (w == 0) { job_load(); job_run(); }
  u32x4 xr[8];
#pragma unroll
  for (int i = 0; i < 8; ++i) gld16(xr[i], P.x + (size_t)(pn * 64 + w * 8 + i) * D + l * 8);
  const __amdgpu_buffer_rsrc_t srdQ = mksrd(P.wq, 512u * 4096u * 2u);
  const unsigned vq = wdma_voff(l, 8192u), sq0 = (g * 512u + w * 64u) * 2u;
  const __amdgpu_buffer_rsrc_t srdK = mksrd(P.kc + ((size_t)g * SK + (SK - WIN)) * DH, WIN * DH * 2u);
  const __amdgpu_buffer_rsrc_t srdV = mksrd(P.vc + ((size_t)g * SK + (SK - WIN)) * DH, WIN * DH * 2u);
  const unsigned vk = ((l >> 2) & 7) * 256u + (l >> 5) * 64u + (((l & 3) ^ (2 * (w & 1) + ((l >> 4) & 1))) << 4);
  const unsigned vv = wdma_voff(l, 256u);
  auto dma_k = [&](int t, unsigned slot) {
    dma16(lds0 + slot + 2048u * w, vk, srdK, (unsigned)t * 16384u + 2048u * w);
    dma16(lds0 + slot + 2048u * w + 1024u, vk, srdK, (unsigned)t * 16384u + 2048u * w + 128u);
  };
  auto dma_v = [&](int t, unsigned slot) {
    dma16(lds0 + slot + 1024u * w, vv, srdV, (unsigned)t * 16384u + 2048u * w);
    dma16(lds0 + slot + 8192u + 1024u * w, vv, srdV, (unsigned)t * 16384u + 2048u * w + 128u);
  };

  {
    asm volatile("s_waitcnt vmcnt(0)" : "+v"(xr[0]), "+v"(xr[1]), "+v"(xr[2]), "+v"(xr[3]), "+v"(xr[4]), "+v"(xr[5]), "+v"(xr[6]),
                 "+v"(xr[7]) :: "memory");
    if (job && w != 0) job_load();
    float v[8][8], s[8], m[8];
#pragma unroll
    for (int i = 0; i < 8; ++i) {
#pragma unroll
      for (int j = 0; j < 4; ++j) { v[i][2 * j] = bflo(xr[i][j]); v[i][2 * j + 1] = bfhi(xr[i][j]); }
      s[i] = ((v[i][0] + v[i][1]) + (v[i][2] + v[i][3])) + ((v[i][4] + v[i][5]) + (v[i][6] + v[i][7]));
    }
    wave_sum8(s, m);
#pragma unroll
    for (int i = 0; i < 8; ++i) {
      const float mu = m[i] * (1.0f / D);
      float q = 0.f;
#pragma unroll
      for (int j = 0; j < 8; ++j) { v[i][j] -= mu; q = __builtin_fmaf(v[i][j], v[i][j], q); }
      s[i] = q;
    }
    wave_sum8(s, m);
    float rs[8];
#pragma unroll
    for (int i = 0; i < 8; ++i) rs[i] = __builtin_amdgcn_rsqf(m[i] * (1.0f / D) + LN_EPS);
    STAMP(1, 8);
    if (w < 4) {
      u32x4 mp[8];
      poll_load<8>(mp, srdW, tid * 16u, W_MP, 4864u * 4u);
      f32x4 t;
#pragma unroll
      for (int k = 0; k < 4; ++k)
        t[k] = ((U2F(mp[0][k]) + U2F(mp[1][k])) + (U2F(mp[2][k]) + U2F(mp[3][k]))) + ((U2F(mp[4][k]) + U2F(mp[5][k])) + (U2F(mp[6][k]) + U2F(mp[7][k])));
      *(LAS f32x4*)(lds + LDS_D + tid * 16) = t;
    } else if (job) job_run();
    dma16(ring + 8192u, l * 16u, srdQ, (2 * (pn * 8 + w)) * 8192u + g * 1024u);
    dma16(ring + 9216u, l * 16u, srdQ, (2 * (pn * 8 + w) + 1) * 8192u + g * 1024u);
    wring_issue(0, ring, vq, srdQ, sq0, 8192u);
    wring_issue(1, ring, vq, srdQ, sq0, 8192u);
    BAR_L();
    u32x4 mo[4];
#pragma unroll
    for (int i = 0; i < 4; ++i) mo[i] = *(const LAS u32x4*)(lds + LDS_D + (i >> 1) * 2048 + l * 32 + (i & 1) * 16);
    STAMP(1, 9);
#pragma unroll
    for (int i = 0; i < 8; ++i) {
      float hh[8];
#pragma unroll
      for (int j = 0; j < 8; ++j) hh[j] = __builtin_fmaf(v[i][j] * rs[i], 1.f + U2F(mo[2 + (j >> 2)][j & 3]), U2F(mo[j >> 2][j & 3]));
      const u32x4 o = {cvtpk(hh[0], hh[1]), cvtpk(hh[2], hh[3]), cvtpk(hh[4], hh[5]), cvtpk(hh[6], hh[7])};
      *(LAS u32x4*)(lds + LDS_X + ximg(l, w * 8 + i)) = o;
    }
  }
  BAR_L();
  wring_issue(2, ring, vq, srdQ, sq0, 8192u);
  wring_issue(3, ring, vq, srdQ, sq0, 8192u);
  wring_issue(4, ring, vq, srdQ, sq0, 8192u);
  dma_k(0, LDS_D);
  STAMP(1, 1);

  {
    f32x16 acc[2][2] = {};
    gemm_phase<32, 2>(acc, lds, ringoff, ring, vq, srdQ, sq0, 8192u, c);
    STAMP(1, 2);
    WAIT_BAR(0);
    dma_k(1, KSLOT[1]); dma_v(0, VSLOT[0]); dma_k(2, KSLOT[2]); dma_v(1, VSLOT[1]);
#pragma unroll
    for (int nt = 0; nt < 2; ++nt)
#pragma unroll
      for (int rt = 0; rt < 2; ++rt)
#pragma unroll
        for (int s = 0; s < 2; ++s)
          *(LAS u32x4*)(lds + LDS_X + ximg(8 * w + 4 * nt + 2 * s + c.h, 32 * rt + c.r)) = pack8(acc[nt][rt], s, QSCALE);
    BAR_L();
  }

  STAMP(1, 3);
  const unsigned hh = w >> 1, rt = w & 1;
  bf16x8 qf[8];
#pragma unroll
  for (int s = 0; s < 8; ++s) qf[s] = lds_b128(lds + LDS_X + ximg(16 * hh + 2 * s + c.h, 32 * rt + c.r));
  unsigned kb[2], vb[2];
#pragma unroll
  for (int i = 0; i < 2; ++i) {
    kb[i] = 2048 * (c.r >> 3) + 64 * (c.r & 7) + (((2 * i + c.h) ^ ((c.r >> 2) & 3)) << 4);
    vb[i] = (4 * c.h + c.q) * 128 + 64 * (i ^ (c.q >> 1)) + 32 * c.blk + 8 * (2 * (c.p & 1) + (c.p >> 1));
  }
  const __amdgpu_buffer_rsrc_t srdO = mksrd(P.wo, 4096u * 512u * 2u);
  const unsigned vo = wdma_voff(l, 1024u), so0 = g * 512u * 1024u + w * 128u;

  f32x16 o[4] = {};
  f32x16 s0, s1;
  f32x16 negm;
  float lsum = 0.f;
  u32x4 pk[4];
  constexpr int PD = 6;
  bf16x8 fr[PD];
  const bool lag = w >= 4;
  auto frag = [&](int t, int it) -> bf16x8 {
    if (it < 16) {
      const lds_cptr a = lds + VSLOT[t > 0 ? t - 1 : 0] + vb[it & 1] + 8192 * ((it >> 1) & 1) + 2048 * (it >> 2);
      return cat8(lds_tr(a), lds_tr(a + 1024));
    }
    const int s = (it - 16) >> 1;
    return lds_b128(lds + KSLOT[t < 8 ? t : 0] + kb[s & 1] + 512 * (s >> 1) + 8192 * (it & 1));
  };
  auto seam = [&](int t) {
    if (t == 0) { WAIT_BAR(0); } else if (t < 6) { WAIT_BAR(4); } else if (t == 6) { WAIT_BAR(2); } else { BAR_L(); }
    if (t + 3 < 8) dma_k(t + 3, KSLOT[t + 3 < 8 ? t + 3 : 0]);
    if (t + 2 < 8) dma_v(t + 2, VSLOT[t + 2 < 8 ? t + 2 : 0]);
    if (t == 6) {
#pragma unroll
      for (int ks = 0; ks < RING; ++ks) wring_issue(ks, ring, vo, srdO, so0, 1024u);
    }
  };
  auto extra = [&](int e) {
    const unsigned dump = lds0 + LDS_R + w * 3072u + (unsigned)(e % 3) * 1024u;
    const unsigned id = (BIDX >> 3) * 8 + w, jh = BIDX & 1;
    const unsigned vrow = (l >> 5) * 1024u + (l & 31) * 16u + jh * 512u;
    auto w3v = [&](unsigned piece) { const unsigned byte = piece * 1024u + l * 16u, row = byte / 1792u;
                                     return row * 3584u + jh * 1792u + (byte - row * 1792u); };
    if (e < 4 || e >= 15) {
      const int i = e < 4 ? e : e - 11;
      const unsigned off = i < 3 ? S_U + (unsigned)i * (64u * T * 16u) + pn * 65536u + g * 8192u
                         : i < 7 ? S_HZ + pn * 262144u + g * 32768u + (unsigned)(i - 3) * 8192u
                                 : S_H3 + pn * 229376u + g * 28672u + (unsigned)(i - 7) * 8192u;
      const u32x4 sv = {SENT, SENT, SENT, SENT};
      __builtin_amdgcn_raw_buffer_store_b128(sv, srdW, (i == 10 && tid >= 256) ? OOB : tid * 16u + off, 0, 16);
    } else if (e < 6) dma16(dump, l * 16u, srdO, (g * 512u + (pn * 8 + w) * 2 + (e - 4)) * 1024u);
    else if (e == 6) dma16(dump, vrow, mksrd(P.w1, 512u * 1024u), id * 2048u);
    else if (e == 7) dma16(dump, vrow, mksrd(P.w2, 512u * 1024u), id * 2048u);
    else if (e < 11) dma16(dump, w3v(id + 256u * (e - 8)), mksrd(P.w3, 512u * 3584u), 0u);
    else if (e == 11) {
      if (id < 128) dma16(dump, w3v(id + 768u), mksrd(P.w3, 512u * 3584u), 0u);
      else dma16(dump, vrow, mksrd(P.w4, 1792u * 1024u), (id - 128u + 768u) * 2048u);
    } else dma16(dump, vrow, mksrd(P.w4, 1792u * 1024u), (id + 256u * (e - 12)) * 2048u);
  };
#pragma unroll
  for (int t = 0; t <= 8; ++t) {
    const int i0 = t == 0 ? 16 : 0, i1 = t == 8 ? 16 : 32;
    if (t <= 1) {
#pragma unroll
      for (int it = i0; it < i0 + PD; ++it) fr[it % PD] = frag(t, it);
    }
    if (t == 0) { s0 = (f32x16){}; s1 = (f32x16){}; } else if (t < 8) { s0 = negm; s1 = negm; }
    __builtin_amdgcn_s_setprio(1);
    SBAR();
#pragma unroll
    for (int it = i0; it < i1; ++it) {
      if (it < 16) o[it & 3] = MFMA32(fr[it % PD], __builtin_bit_cast(bf16x8, pk[it >> 2]), o[it & 3]);
      else if (it & 1) s1 = MFMA32(fr[it % PD], qf[(it - 16) >> 1], s1);
      else s0 = MFMA32(fr[it % PD], qf[(it - 16) >> 1], s0);
      if (it + PD < i1) fr[it % PD] = frag(t, it + PD);
      if (t >= 1 && t <= 7 && (it & 7) == 3 && 4 * (t - 1) + (it >> 3) < 22) extra(4 * (t - 1) + (it >> 3));
      SBAR();
    }
    if (t == 8) break;
    __builtin_amdgcn_s_setprio(0);
    if (lag) seam(t);
    asm volatile("s_nop 15" : "+v"(s0), "+v"(s1));
    float ta = max3f(s0[0], s0[1], s1[0]), tb = max3f(s0[2], s0[3], s1[1]);
    ta = max3f(ta, s1[2], s1[3]);
#pragma unroll
    for (int i = 4; i < 16; i += 4) {
      ta = max3f(ta, s0[i], s0[i + 1]); tb = max3f(tb, s0[i + 2], s0[i + 3]);
      ta = max3f(ta, s1[i], s1[i + 1]); tb = max3f(tb, s1[i + 2], s1[i + 3]);
    }
    float tm = max2f(ta, tb);
    {
      auto r = __builtin_amdgcn_permlane32_swap(F2U(tm), F2U(tm), false, false);
      tm = max2f(U2F(r[0]), U2F(r[1]));
    }
    if (t == 0 || ANY(tm > THR)) {
      const float ndl = t == 0 ? -tm : -max2f(tm, 0.f);
      const float aone = c.h ? 0.f : 1.f;
      if (t == 0) negm = (f32x16){};
      __builtin_amdgcn_s_setprio(2);
      s0 = __builtin_amdgcn_mfma_f32_32x32x2f32(aone, ndl, s0, 0, 0, 0);
      s1 = __builtin_amdgcn_mfma_f32_32x32x2f32(aone, ndl, s1, 0, 0, 0);
      negm = __builtin_amdgcn_mfma_f32_32x32x2f32(aone, ndl, negm, 0, 0, 0);
      __builtin_amdgcn_s_setprio(0);
      if (t > 0) {
        const float al = __builtin_amdgcn_exp2f(ndl);
        lsum *= al;
#pragma unroll
        for (int dt = 0; dt < 4; ++dt)
#pragma unroll
          for (int i = 0; i < 16; ++i) o[dt][i] *= al;
      }
    }
    SBAR();
    if (t >= 1) {
#pragma unroll
      for (int it = 0; it < PD; ++it) fr[it] = frag(t + 1, it);
    }
#pragma unroll
    for (int i = 0; i < 16; ++i) { s0[i] = __builtin_amdgcn_exp2f(s0[i]); s1[i] = __builtin_amdgcn_exp2f(s1[i]); }
    pk[0] = pack8(s0, 0, 1.f); pk[1] = pack8(s0, 1, 1.f); pk[2] = pack8(s1, 0, 1.f); pk[3] = pack8(s1, 1, 1.f);
    {
      float l0 = lsum, l1 = 0.f, l2 = 0.f, l3 = 0.f;
#pragma unroll
      for (int i = 0; i < 16; i += 2) { l0 += s0[i]; l1 += s0[i + 1]; l2 += s1[i]; l3 += s1[i + 1]; }
      lsum = (l0 + l1) + (l2 + l3);
    }
    PIN(pk[0]); PIN(pk[1]); PIN(pk[2]); PIN(pk[3]); PIN(lsum);
    SBAR();
    if (!lag) seam(t);
  }
  __builtin_amdgcn_s_setprio(0);
  STAMP(1, 4);
  {
    const float il = __builtin_amdgcn_rcpf(swap32_add(lsum));
#pragma unroll
    for (int dt = 0; dt < 4; ++dt)
#pragma unroll
      for (int s = 0; s < 2; ++s)
        *(LAS u32x4*)(lds + LDS_X + ximg(16 * hh + 4 * dt + 2 * s + c.h, 32 * rt + c.r)) = pack8(o[dt], s, il);
  }
  BAR_L();
  STAMP(1, 5);
  if (BIDX < 120 && w == 0) {
    const unsigned fvo = l < 8 ? ((32u + BIDX) * 32u + l * 4u) * 4u : OOB;
    u32x4 mp[8];
    poll_load<8>(mp, srdW, fvo, W_MP, 4864u * 4u);
    f32x4 t;
#pragma unroll
    for (int k = 0; k < 4; ++k)
      t[k] = ((U2F(mp[0][k]) + U2F(mp[1][k])) + (U2F(mp[2][k]) + U2F(mp[3][k]))) + ((U2F(mp[4][k]) + U2F(mp[5][k])) + (U2F(mp[6][k]) + U2F(mp[7][k])));
    pub16(srdW, fvo, W_MOD, __builtin_bit_cast(u32x4, t));
  }
  {
    f32x16 acc[2][2] = {};
    gemm_phase<32, 0>(acc, lds, ringoff, ring, vo, srdO, so0, 1024u, c);
    STAMP(1, 6);
    const unsigned dst = W_P1 + ((((pn * 8 + (c.r >> 3)) * 8 + g) * 64 + 8 * w + c.h) * 8 + (c.r & 7)) * 16u;
#pragma unroll
    for (int nt = 0; nt < 2; ++nt)
#pragma unroll
      for (int r2 = 0; r2 < 2; ++r2)
#pragma unroll
        for (int s = 0; s < 2; ++s)
          __builtin_amdgcn_raw_buffer_store_b128(pack8(acc[nt][r2], s, 1.f), srdW, dst + ((4 * nt + 2 * s) * 8 + r2 * 4 * 8 * 64 * 8) * 16u, 0, 16);
  }
  WAIT_BAR(0);
  if (tid == 0) __builtin_amdgcn_raw_buffer_store_b32(1u, srdW, W_FLAG + (pn * 8 + g) * 64u, 0, 16);
  STAMP(1, 7);
}

DI void ln8(float (&v)[8], LAS float* red  , unsigned tid) {
  float s = ((v[0] + v[1]) + (v[2] + v[3])) + ((v[4] + v[5]) + (v[6] + v[7]));
  float q = (__builtin_fmaf(v[0], v[0], v[1] * v[1]) + __builtin_fmaf(v[2], v[2], v[3] * v[3])) +
            (__builtin_fmaf(v[4], v[4], v[5] * v[5]) + __builtin_fmaf(v[6], v[6], v[7] * v[7]));
  s = sum_hi3(s); q = sum_hi3(q);
  if ((tid & 63) < 8) *(LAS f32x2*)(red + ((tid >> 6) * 8 + (tid & 7)) * 2) = (f32x2){s, q};
  BAR_L();
  f32x2 e[8];
#pragma unroll
  for (int i = 0; i < 8; ++i) e[i] = *(const LAS f32x2*)(red + (i * 8 + (tid & 7)) * 2);
  const float mu = (((e[0][0] + e[1][0]) + (e[2][0] + e[3][0])) + ((e[4][0] + e[5][0]) + (e[6][0] + e[7][0]))) * (1.0f / D);
  const float ms = (((e[0][1] + e[1][1]) + (e[2][1] + e[3][1])) + ((e[4][1] + e[5][1]) + (e[6][1] + e[7][1]))) * (1.0f / D);
  const float rs = __builtin_amdgcn_rsqf(__builtin_fmaxf(__builtin_fmaf(-mu, mu, ms), 0.f) + LN_EPS);
  const float nb = -mu * rs;
#pragma unroll
  for (int k = 0; k < 8; ++k) v[k] = __builtin_fmaf(v[k], rs, nb);
}
DI void red4_put(const f32x16& a, int nt, lds_ptr scr, unsigned rt, unsigned kq, unsigned l) {
#pragma unroll
  for (int g = 0; g < 2; ++g)
#pragma unroll
    for (int i = 0; i < 2; ++i) {
      const int o = 8 * g + 4 * i;
      *(LAS f32x4*)(scr + ((rt * 4 + 2 * nt + g) * 4 + kq) * 2048 + i * 1024 + l * 16) = (f32x4){a[o], a[o + 1], a[o + 2], a[o + 3]};
    }
}
DI void red4_get(float (&v)[8], lds_cptr scr, unsigned rt, unsigned kq, unsigned l) {
  f32x4 t[4][2];
#pragma unroll
  for (int s = 0; s < 4; ++s)
#pragma unroll
    for (int i = 0; i < 2; ++i) t[s][i] = *(const LAS f32x4*)(scr + ((rt * 4 + kq) * 4 + s) * 2048 + i * 1024 + l * 16);
#pragma unroll
  for (int i = 0; i < 2; ++i)
#pragma unroll
    for (int k = 0; k < 4; ++k) v[4 * i + k] = (t[0][i][k] + t[1][i][k]) + (t[2][i][k] + t[3][i][k]);
}
DI void tile_mm8(const bf16x8 (&af)[8][2], const u32x4 (&xb)[8], float (&v)[8], lds_ptr scr, unsigned rt, unsigned kq, unsigned l) {
  f32x16 a0 = {}, a1 = {};
#pragma unroll
  for (int i = 0; i < 8; ++i) a0 = MFMA32(af[i][0], __builtin_bit_cast(bf16x8, xb[i]), a0);
  SBAR();
#pragma unroll
  for (int i = 0; i < 4; ++i) a1 = MFMA32(af[i][1], __builtin_bit_cast(bf16x8, xb[i]), a1);
  SBAR();
  red4_put(a0, 0, scr, rt, kq, l);
#pragma unroll
  for (int i = 4; i < 8; ++i) a1 = MFMA32(af[i][1], __builtin_bit_cast(bf16x8, xb[i]), a1);
  SBAR();
  red4_put(a1, 1, scr, rt, kq, l);
  BAR_L();
  red4_get(v, scr, rt, kq, l);
}

struct Frag3 { bf16x8 a, b[2]; };
constexpr int RING3 = 13;
constexpr int LA = 0, LB = 65536, LC = 131072;
DI unsigned r3off(int s, unsigned rc, unsigned rb) { return s < 4 ? rc + s * 1024u : rb + (s - 4) * 1024u; }
template <int KS> DI void load_frags3(Frag3& f, lds_cptr lds, unsigned rc, unsigned rb, unsigned a3, const LaneC& c) {
  const lds_cptr sl = lds + r3off(KS % RING3, rc, rb) + a3;
  f.a = cat8(lds_tr(sl), lds_tr(sl + 256));
#pragma unroll
  for (int rt = 0; rt < 2; ++rt) f.b[rt] = lds_b128(lds + LA + c.xb[KS & 3] + 2048 * KS + 512 * rt);
}
template <int KS>
DI void g3_step(f32x16 (&acc)[2], Frag3& cur, Frag3& n1, Frag3& n2, lds_cptr lds, unsigned lds0, unsigned rc, unsigned rb,
                unsigned voff, __amdgpu_buffer_rsrc_t srd, unsigned soff0, unsigned a3, const LaneC& c) {
  constexpr int NKS = 32;
  if constexpr (KS + 2 < NKS) {
    constexpr int last = (KS + RING3 < NKS ? KS + RING3 : NKS) - 1;
    wait_vm<last - (KS + 2)>();
    load_frags3<KS + 2>(n2, lds, rc, rb, a3, c);
  }
  SBAR();
  acc[0] = MFMA32(cur.a, cur.b[0], acc[0]);
  acc[1] = MFMA32(cur.a, cur.b[1], acc[1]);
  if constexpr (KS + RING3 < NKS) {
    asm volatile("" :: "v"(cur.a));
    dma16(lds0 + r3off(KS % RING3, rc, rb), voff, srd, soff0 + (unsigned)(16 * (KS + RING3)) * 3584u);
  }
  SBAR();
  if constexpr (KS + 1 < NKS) g3_step<KS + 1>(acc, n1, n2, cur, lds, lds0, rc, rb, voff, srd, soff0, a3, c);
}
DI void mlp_part(const Params& P, lds_ptr lds, unsigned lds0, const LaneC& c, unsigned w, __amdgpu_buffer_rsrc_t srdS) {
  const unsigned idx = BIDX >> 3, pn = 8 * ((BIDX & 7) >> 1) + (idx >> 2), j = 4 * (BIDX & 1) + (idx & 3);
  const unsigned tid = TIDX, l = c.lane;
  const unsigned vw = wdma_voff(l, 1024u);
  const unsigned rt = w & 1, kq = w >> 1;
  constexpr int LMOD = LC;
  constexpr int LRED = LC + 28672;
  const unsigned prow = 32 * rt + c.r;
  const unsigned trow = pn * 64 + prow;
  const unsigned och = 8 * j + 2 * kq + c.h;
  const unsigned ovo = ((pn * 64 + och) * 64 + prow) * 16u;
  float gb[8];
  STAMP(2, 0);

  {
    const unsigned row = pn * 64 + j * 8 + (tid & 7), ch = tid >> 3;
    dma16_sc1(lds0 + LMOD + w * 1024u, l * 16u, srdS, W_MOD + 4096u + w * 1024u);
    u32x4 xr;
    gld16(xr, P.x + (size_t)row * D + ch * 8);
    const __amdgpu_buffer_rsrc_t srd1 = mksrd(P.w1, 512u * 512u * 2u), srd2 = mksrd(P.w2, 512u * 512u * 2u);
#pragma unroll
    for (int i = 0; i < 8; ++i) dma16(lds0 + LA + (8 * w + i) * 1024u, vw, srd1, (8 * w + i) * 8192u + j * 128u);
    {
      const int fp = (int)pn - 1 + (int)(l >> 3);
      const unsigned fo = (l < 24 && fp >= 0 && fp < 32) ? W_FLAG + ((unsigned)fp * 8 + (l & 7)) * 64u : OOB;
      for (unsigned spin = 0;; ++spin) {
        asm volatile("" ::: "memory");
        const unsigned f = __builtin_amdgcn_raw_buffer_load_b32(srdS, fo, 0, 16);
        if (!ANY(fo != OOB && f != 1u) || spin > SPIN_CAP) break;
        __builtin_amdgcn_s_sleep(2);
      }
    }
    STAMP(2, 15);
    u32x4 p[8];
#pragma unroll
    for (int g = 0; g < 8; ++g) bld16_sc1(p[g], tid * 16u, srdS, W_P1 + ((pn * 8 + j) * 8 + g) * 8192u);
    asm volatile("s_waitcnt vmcnt(8)\n\ts_barrier" : "+v"(xr) :: "memory");
    float ga[8], sb[8], cb[8], g2[8];
    {
      u32x4 mv[10];
#pragma unroll
      for (int i = 0; i < 8; ++i) mv[i] = *(const LAS u32x4*)(lds + LMOD + (i >> 1) * 2048 + ch * 32 + 16 * (i & 1));
#pragma unroll
      for (int i = 0; i < 2; ++i) mv[8 + i] = *(const LAS u32x4*)(lds + LMOD + 6144 + och * 32 + 16 * i);
      if (has_sent(mv, 10)) {
#pragma unroll
        for (int i = 0; i < 4; ++i) poll_load<2>(mv + 2 * i, srdS, ch * 32u, W_MOD + 4096u + i * 2048u, 16u);
        poll_load<2>(mv + 8, srdS, och * 32u, W_MOD + 10240u, 16u);
      }
#pragma unroll
      for (int k = 0; k < 8; ++k) {
        ga[k] = U2F(mv[k >> 2][k & 3]); sb[k] = U2F(mv[2 + (k >> 2)][k & 3]); cb[k] = 1.f + U2F(mv[4 + (k >> 2)][k & 3]);
        g2[k] = U2F(mv[6 + (k >> 2)][k & 3]); gb[k] = U2F(mv[8 + (k >> 2)][k & 3]);
      }
    }
    asm volatile("s_waitcnt vmcnt(0)" : "+v"(p[0]), "+v"(p[1]), "+v"(p[2]), "+v"(p[3]), "+v"(p[4]), "+v"(p[5]), "+v"(p[6]), "+v"(p[7]) :: "memory");
    STAMP(2, 1);
    float v[8], x2[8];
#pragma unroll
    for (int i = 0; i < 4; ++i) {
      const float lo = ((bflo(p[0][i]) + bflo(p[1][i])) + (bflo(p[2][i]) + bflo(p[3][i]))) + ((bflo(p[4][i]) + bflo(p[5][i])) + (bflo(p[6][i]) + bflo(p[7][i])));
      const float hi = ((bfhi(p[0][i]) + bfhi(p[1][i])) + (bfhi(p[2][i]) + bfhi(p[3][i]))) + ((bfhi(p[4][i]) + bfhi(p[5][i])) + (bfhi(p[6][i]) + bfhi(p[7][i])));
      v[2 * i] = x2[2 * i] = __builtin_fmaf(ga[2 * i], lo, bflo(xr[i]));
      v[2 * i + 1] = x2[2 * i + 1] = __builtin_fmaf(ga[2 * i + 1], hi, bfhi(xr[i]));
    }
    ln8(v, (LAS float*)(lds + LRED), tid);
    float hb[8];
#pragma unroll
    for (int k = 0; k < 8; ++k) v[k] = hb[k] = __builtin_fmaf(v[k], cb[k], sb[k]);
    ln8(v, (LAS float*)(lds + LRED + 512), tid);
    const unsigned vo = ((pn * 64 + ch) * 64 + j * 8 + (tid & 7)) * 16u, vz = ((pn * 64 + ch) * 256 + j * 8 + (tid & 7)) * 16u;
    pub16(srdS, vo, S_U, (u32x4){cvtpk(v[0], v[1]), cvtpk(v[2], v[3]), cvtpk(v[4], v[5]), cvtpk(v[6], v[7])});
#pragma unroll
    for (int i = 0; i < 2; ++i) {
      pub16(srdS, vz, S_HZ + i * 1024u, (u32x4){F2U(hb[4 * i]), F2U(hb[4 * i + 1]),
                                                F2U(hb[4 * i + 2]), F2U(hb[4 * i + 3])});
      f32x4 z;
#pragma unroll
      for (int e = 0; e < 4; ++e) z[e] = __builtin_fmaf(g2[4 * i + e], hb[4 * i + e], x2[4 * i + e]);
      pub16(srdS, vz, S_HZ + (2 + i) * 1024u, __builtin_bit_cast(u32x4, z));
    }
#pragma unroll
    for (int i = 0; i < 8; ++i) dma16(lds0 + LB + (8 * w + i) * 1024u, vw, srd2, (8 * w + i) * 8192u + j * 128u);
  }
  STAMP(2, 2);
  const __amdgpu_buffer_rsrc_t srd3 = mksrd(P.w3, 512u * F * 2u);
  const unsigned rc = LC + w * 4096u, rb = LB + w * 9216u;
  const unsigned v3 = (l >> 2) * 3584u + ((l & 3) << 4), s30 = (j * 224u + w * 32u) * 2u;
  float v[8];
  {
    bf16x8 af[8][2];
#pragma unroll
    for (int i = 0; i < 8; ++i)
#pragma unroll
      for (int nt = 0; nt < 2; ++nt) {
        const lds_cptr a = lds + LA + (8 * kq + i) * 2048 + c.afr[nt];
        af[i][nt] = cat8(lds_tr(a), lds_tr(a + 512));
      }
    BAR_L();
    u32x4 xb[8];
    poll_ind<8, 3, 7>(xb, srdS, ((pn * 64 + 16 * kq + c.h) * 64 + prow) * 16u, S_U, 2048u);
    STAMP(2, 3);
    tile_mm8(af, xb, v, lds + LA, rt, kq, l);
#pragma unroll
    for (int k = 0; k < 8; ++k) v[k] = gelu_tanh(v[k]);
    pub16(srdS, ovo, S_H1, (u32x4){cvtpk(v[0], v[1]), cvtpk(v[2], v[3]), cvtpk(v[4], v[5]), cvtpk(v[6], v[7])});
    if (w < 7) {
#pragma unroll
      for (int ks = 0; ks < 4; ++ks) dma16(lds0 + r3off(ks, rc, rb), v3, srd3, s30 + (unsigned)(16 * ks) * 3584u);
    }
  }
  STAMP(2, 4);
  float y[8];
  u32x4 cs[4];
  const unsigned cso = w < 7 ? (224 * j + 32 * w + 8 * c.h) * 4u : OOB;
  {
    bf16x8 af[8][2];
#pragma unroll
    for (int i = 0; i < 8; ++i)
#pragma unroll
      for (int nt = 0; nt < 2; ++nt) {
        const lds_cptr a = lds + LB + (8 * kq + i) * 2048 + c.afr[nt];
        af[i][nt] = cat8(lds_tr(a), lds_tr(a + 512));
      }
    BAR_L();
    u32x4 hz[4];
    const unsigned hzo = ((pn * 64 + och) * 256 + prow) * 16u;
#pragma unroll
    for (int i = 0; i < 4; ++i) hz[i] = __builtin_amdgcn_raw_buffer_load_b128(srdS, hzo, S_HZ + i * 1024u, 16);
#pragma unroll
    for (int i = 0; i < 4; ++i) cs[i] = __builtin_amdgcn_raw_buffer_load_b128(srdS, cso, W_MOD + 12288u + 64u * (i >> 1) + 16u * (i & 1), 16);
    const int sh = kq == 0 ? -1 : kq == 1 ? 1 : kq == 2 ? -2 : 2;
    const int srow = (int)trow + sh;
    const unsigned xvo = (srow >= 0 && srow < T) ? ((((unsigned)srow >> 6) * 64 + 16 * kq + c.h) * 64 + ((unsigned)srow & 63)) * 16u : OOB;
    u32x4 xb[8];
    poll_ind<8, 3, 7>(xb, srdS, xvo, S_H1, 2048u);
    STAMP(2, 5);
    tile_mm8(af, xb, v, lds + LA, rt, kq, l);
    if (has_sent(hz, 4)) poll_load<4>(hz, srdS, hzo, S_HZ, 1024u);
    if (has_sent(cs, 4)) { poll_load<2>(cs, srdS, cso, W_MOD + 12288u, 16u); poll_load<2>(cs + 2, srdS, cso, W_MOD + 12352u, 16u); }
    float x1[8];
#pragma unroll
    for (int k = 0; k < 8; ++k) {
      x1[k] = U2F(hz[k >> 2][k & 3]) + v[k];
      y[k] = __builtin_fmaf(gb[k], v[k], U2F(hz[2 + (k >> 2)][k & 3]));
    }
    pub16(srdS, ovo, S_X1, (u32x4){cvtpk(x1[0], x1[1]), cvtpk(x1[2], x1[3]), cvtpk(x1[4], x1[5]), cvtpk(x1[6], x1[7])});
    if (w < 7) {
#pragma unroll
      for (int ks = 4; ks < RING3; ++ks) dma16(lds0 + r3off(ks, rc, rb), v3, srd3, s30 + (unsigned)(16 * ks) * 3584u);
    }
  }
  STAMP(2, 6);
  {
    u32x4 xr[8];
    BAR_L();
    poll_ind<8, 3, 7>(xr, srdS, ((pn * 64 + 8 * w) * 64 + l) * 16u, S_X1, 1024u);
    STAMP(2, 7);
#pragma unroll
    for (int i = 0; i < 8; ++i) *(LAS u32x4*)(lds + LA + ximg(8 * w + i, l)) = xr[i];
    {
      float s0 = 0.f, s1 = 0.f, q0 = 0.f, q1 = 0.f;
#pragma unroll
      for (int i = 0; i < 8; ++i)
#pragma unroll
        for (int k = 0; k < 4; k += 2) {
          s0 = dot2bf(xr[i][k], 0x3F803F80u, s0); s1 = dot2bf(xr[i][k + 1], 0x3F803F80u, s1);
          q0 = dot2bf(xr[i][k], xr[i][k], q0); q1 = dot2bf(xr[i][k + 1], xr[i][k + 1], q1);
        }
      *(LAS f32x2*)(lds + LRED + (w * 64 + l) * 8) = (f32x2){s0 + s1, q0 + q1};
    }
    BAR_L();
    STAMP(2, 8);
    if (w < 7) {
      f32x16 acc[2] = {};
      const unsigned a3 = (8 * c.h + c.q) * 64 + 32 * c.blk + 8 * (2 * (c.p & 1) + (c.p >> 1));
      Frag3 f0, f1, f2;
      wait_vm<RING3 - 2>();
      load_frags3<0>(f0, lds, rc, rb, a3, c);
      load_frags3<1>(f1, lds, rc, rb, a3, c);
      float mu[2], rs[2];
#pragma unroll
      for (int r2 = 0; r2 < 2; ++r2) {
        float s = 0.f, q = 0.f;
#pragma unroll
        for (int i = 0; i < 8; ++i) {
          const f32x2 t = *(const LAS f32x2*)(lds + LRED + (i * 64 + 32 * r2 + c.r) * 8);
          s += t[0]; q += t[1];
        }
        mu[r2] = s * (1.0f / D);
        rs[r2] = __builtin_amdgcn_rsqf(__builtin_fmaxf(__builtin_fmaf(-mu[r2], mu[r2], q * (1.0f / D)), 0.f) + LN_EPS);
      }
      if (w < 4) __builtin_amdgcn_s_setprio(2); else __builtin_amdgcn_s_setprio(1);
      g3_step<0>(acc, f0, f1, f2, lds, lds0, rc, rb, v3, srd3, s30, a3, c);
      __builtin_amdgcn_s_setprio(0);
      STAMP(2, 9);
#pragma unroll
      for (int r2 = 0; r2 < 2; ++r2) {
#pragma unroll
        for (int i = 0; i < 16; ++i) acc[r2][i] = gelu_tanh(rs[r2] * __builtin_fmaf(-mu[r2], U2F(cs[i >> 2][i & 3]), acc[r2][i]));
#pragma unroll
        for (int s2 = 0; s2 < 2; ++s2)
          pub16(srdS, ((pn * 224 + 28 * j + 4 * w + 2 * s2 + c.h) * 64 + 32 * r2 + c.r) * 16u, S_H3, pack8(acc[r2], s2, 1.f));
      }
    }
    BAR_L();
  }
  STAMP(2, 10);
  {
    const __amdgpu_buffer_rsrc_t srd4 = mksrd(P.w4, (unsigned)F * 512u * 2u);
    const unsigned qb = kq * 40960u;
    const unsigned s40 = (448u * kq + 8u * rt) * 1024u + j * 128u;
    auto w4dma = [&](int ks) { dma16(lds0 + qb + (ks % 20) * 2048u + rt * 1024u, vw, srd4, s40 + (unsigned)ks * 16384u); };
#pragma unroll
    for (int ks = 0; ks < 8; ++ks) w4dma(ks);
    const unsigned hvo = ((pn * 224 + 56 * kq + c.h) * 64 + prow) * 16u;
    u32x4 ha[14], hb[14];
    poll_ind<14, 9, 13>(ha, srdS, hvo, S_H3, 2048u);
    BAR_L();
    STAMP(2, 11);
    f32x16 acc[2] = {};
    bf16x8 fa[2][2];
    auto ld = [&](int ks) {
#pragma unroll
      for (int nt = 0; nt < 2; ++nt) {
        const lds_cptr a = lds + qb + (ks % 20) * 2048 + c.afr[nt];
        fa[ks & 1][nt] = cat8(lds_tr(a), lds_tr(a + 512));
      }
    };
    auto mm = [&](int ks, const u32x4& b) {
#pragma unroll
      for (int nt = 0; nt < 2; ++nt) acc[nt] = MFMA32(fa[ks & 1][nt], __builtin_bit_cast(bf16x8, b), acc[nt]);
    };
    auto hbl = [&](int i) { bld16_sc1(hb[i], hvo, srdS, S_H3 + (14u + i) * 2048u); };
    ld(0);
#pragma unroll
    for (int ks = 0; ks < 8; ++ks) {
      if (ks < 7) ld(ks + 1);
      SBAR(); mm(ks, ha[ks]);
      if (ks < 6) { w4dma(8 + ks); hbl(2 * ks); hbl(2 * ks + 1); }
      else if (ks == 6) { hbl(12); hbl(13); w4dma(14); w4dma(15); w4dma(16); }
      else { w4dma(17); w4dma(18); w4dma(19); }
      SBAR();
    }
    WAIT_BAR(10);
    ld(8);
#pragma unroll
    for (int ks = 8; ks < 14; ++ks) {
      if (ks < 13) ld(ks + 1);
      SBAR(); mm(ks, ha[ks]);
      if (ks < 12) { w4dma(20 + 2 * (ks - 8)); w4dma(21 + 2 * (ks - 8)); }
      SBAR();
    }
    asm volatile("s_waitcnt vmcnt(8) lgkmcnt(0)\n\ts_barrier" : "+v"(hb[0]), "+v"(hb[1]), "+v"(hb[2]), "+v"(hb[3]), "+v"(hb[4]), "+v"(hb[5]),
                 "+v"(hb[6]), "+v"(hb[7]), "+v"(hb[8]), "+v"(hb[9]), "+v"(hb[10]), "+v"(hb[11]), "+v"(hb[12]), "+v"(hb[13]) :: "memory");
    if (has_sent(hb, 14)) poll_load<14>(hb, srdS, hvo, S_H3 + 14u * 2048u, 2048u);
    STAMP(2, 12);
    ld(14);
#pragma unroll
    for (int ks = 14; ks < 19; ++ks) { ld(ks + 1); SBAR(); mm(ks, hb[ks - 14]); SBAR(); }
    mm(19, hb[5]);
    WAIT_BAR(0);
    ld(20);
#pragma unroll
    for (int ks = 20; ks < 27; ++ks) { ld(ks + 1); SBAR(); mm(ks, hb[ks - 14]); SBAR(); }
    mm(27, hb[13]);
    BAR_L();
    STAMP(2, 13);
    red4_put(acc[0], 0, lds + LA, rt, kq, l);
    red4_put(acc[1], 1, lds + LA, rt, kq, l);
    BAR_L();
    red4_get(v, lds + LA, rt, kq, l);
#pragma unroll
    for (int k = 0; k < 8; ++k) v[k] = __builtin_fmaf(gb[k], v[k], y[k]);
    P.out[(size_t)trow * 64 + och] = (u32x4){cvtpk(v[0], v[1]), cvtpk(v[2], v[3]), cvtpk(v[4], v[5]), cvtpk(v[6], v[7])};
  }
  STAMP(2, 14);
}

__global__ __launch_bounds__(512, 2) void k_fused(Params P) {
  extern __shared__ __attribute__((aligned(16))) char smem[];
  const lds_ptr lds = (lds_ptr)smem;
  const unsigned lds0 = (unsigned)(uintptr_t)smem;
  const unsigned w = rfl(TIDX >> 6);
  LaneC c; lane_consts(c);
  const __amdgpu_buffer_rsrc_t srdW = mksrd(P.ws, W_END);
  attn_part(P, lds, lds0, c, w, srdW);
  mlp_part(P, lds, lds0, c, w, srdW);
}

#if defined(__HIP_DEVICE_COMPILE__)
#pragma clang attribute pop
#endif
extern "C" void kernel_launch(void* const* d_in, const int* in_sizes, int n_in, void* d_out, int out_size, void* d_ws,
                              size_t ws_size, hipStream_t stream) {
  Params P;
  P.x = (const unsigned short*)d_in[0];
  P.kc = (const unsigned short*)d_in[1];
  P.vc = (const unsigned short*)d_in[2];
  P.wq = (const unsigned short*)d_in[3];
  P.wo = (const unsigned short*)d_in[4];
  P.w1 = (const unsigned short*)d_in[6];
  P.w2 = (const unsigned short*)d_in[7];
  P.w3 = (const unsigned short*)d_in[8];
  P.w4 = (const unsigned short*)d_in[9];
  P.c = (const unsigned short*)d_in[10];
  P.wmod = (const unsigned short*)d_in[11];
  P.ws = d_ws;
  P.out = (u32x4*)d_out;
  (void)hipFuncSetAttribute((const void*)k_fused, hipFuncAttributeMaxDynamicSharedMemorySize, LDS_TOTAL);
  k_init<<<(W_FLAG + 16384) / 4096, 256, 0, stream>>>(d_ws);
  k_fused<<<256, 512, LDS_TOTAL, stream>>>(P);
}
```

```cpp
#include <hip/hip_runtime.h>
#include <hip/hip_bf16.h>
#include <stdint.h>

#if defined(__HIP_DEVICE_COMPILE__)
#pragma clang attribute push(__attribute__((target("no-packed-fp32-ops"))), apply_to = function)
#endif
typedef short bf16x8 __attribute__((ext_vector_type(8)));
typedef short s16x4 __attribute__((ext_vector_type(4)));
typedef float f32x16 __attribute__((ext_vector_type(16)));
typedef float f32x4 __attribute__((ext_vector_type(4)));
typedef float f32x2 __attribute__((ext_vector_type(2)));
typedef unsigned u32x4 __attribute__((ext_vector_type(4)));
typedef unsigned u32x2 __attribute__((ext_vector_type(2)));
typedef __bf16 bf16x2_t __attribute__((ext_vector_type(2)));
typedef short v4i16_t __attribute__((ext_vector_type(4)));
#define LAS __attribute__((address_space(3)))
typedef LAS const char* lds_cptr;
typedef LAS char* lds_ptr;
#define DI __device__ __forceinline__
#define F2U(x) __builtin_bit_cast(unsigned, (float)(x))
#define U2F(x) __builtin_bit_cast(float, (unsigned)(x))
#define F2I(x) __builtin_bit_cast(int, (float)(x))
#define I2F(x) __builtin_bit_cast(float, (int)(x))
#define TIDX __builtin_amdgcn_workitem_id_x()
#define BIDX __builtin_amdgcn_workgroup_id_x()
#define ANY(p) (__builtin_amdgcn_ballot_w64(p) != 0ull)
#define SBAR() __builtin_amdgcn_sched_barrier(0)
#define MFMA32(a, b, c) __builtin_amdgcn_mfma_f32_32x32x16_bf16((a), (b), (c), 0, 0, 0)
#define WAIT_V(n) asm volatile("s_waitcnt vmcnt(" #n ")" ::: "memory")
#define WAIT_L0() asm volatile("s_waitcnt lgkmcnt(0)" ::: "memory")
#define BAR_L() asm volatile("s_waitcnt lgkmcnt(0)\n\ts_barrier" ::: "memory")
#define WAIT_BAR(n) asm volatile("s_waitcnt vmcnt(" #n ") lgkmcnt(0)\n\ts_barrier" ::: "memory")

constexpr int T = 2048, D = 512, HKV = 8, SK = 2048, DH = 128, WIN = 512, F = 1792;
constexpr float LN_EPS = 1e-5f;
constexpr float QSCALE = 0.088388347648318440f * 1.4426950408889634f;

constexpr unsigned W_MOD = 0;
constexpr unsigned W_FLAG = 32768;
constexpr unsigned W_P1 = 65536;
constexpr unsigned S_U = W_P1 + 8u * 64u * T * 16u;
constexpr unsigned S_H1 = S_U + 64u * T * 16u;
constexpr unsigned S_X1 = S_H1 + 64u * T * 16u;
constexpr unsigned S_HZ = S_X1 + 64u * T * 16u;
constexpr unsigned S_H3 = S_HZ + 256u * T * 16u;
constexpr unsigned W_END = S_H3 + 224u * T * 16u;
static_assert(W_FLAG + 256 * 64 <= W_P1, "ws");
constexpr unsigned OOB = 0xF0000000u;
#define K1MAP 0
#define FLAG_SLEEP 4
#define NPRE 20
#define ATHR 32.0f
constexpr float THR = ATHR;
constexpr unsigned SENT = 0xFFFFFFFFu;
constexpr unsigned SPIN_CAP = 1u << 16;

constexpr int LDS_X = 0;
constexpr int LDS_R = 65536;
constexpr int RING = 5, RINGB = RING * 2048;
constexpr int LDS_D = LDS_R + 8 * RINGB;
constexpr int LDS_TOTAL = LDS_D + 16384;
static_assert(LDS_TOTAL == 163840, "lds");

#define STAMP(kid, i) do {} while (0)
DI unsigned cvtpk(float lo, float hi) {
  f32x2 v = {lo, hi};
  bf16x2_t b = __builtin_convertvector(v, bf16x2_t);
  return __builtin_bit_cast(unsigned, b);
}
DI float bflo(unsigned u) { return U2F(u << 16); }
DI float bfhi(unsigned u) { return U2F(u & 0xffff0000u); }
DI unsigned rfl(unsigned x) { return __builtin_amdgcn_readfirstlane(x); }
DI __amdgpu_buffer_rsrc_t mksrd(const void* p, unsigned bytes) {
  return __builtin_amdgcn_make_buffer_rsrc((void*)p, (short)0, (int)bytes, 0x00020000);
}
DI void dma16(unsigned lds, unsigned voff, __amdgpu_buffer_rsrc_t srd, unsigned soff) {
  asm volatile("s_mov_b32 m0, %0\n\ts_nop 0\n\tbuffer_load_dwordx4 %1, %2, %3 offen lds"
               :: "s"(lds), "v"(voff), "s"(srd), "s"(soff) : "memory");
}
DI void gld16(u32x4& d, const void* p) { asm volatile("global_load_dwordx4 %0, %1, off" : "=v"(d) : "v"(p) : "memory"); }
DI void bld16_sc1(u32x4& d, unsigned voff, __amdgpu_buffer_rsrc_t srd, unsigned soff) {
  asm volatile("buffer_load_dwordx4 %0, %1, %2, %3 offen sc1" : "=v"(d) : "v"(voff), "s"(srd), "s"(soff) : "memory");
}
DI void dma16_sc1(unsigned lds, unsigned voff, __amdgpu_buffer_rsrc_t srd, unsigned soff) {
  asm volatile("s_mov_b32 m0, %0\n\ts_nop 0\n\tbuffer_load_dwordx4 %1, %2, %3 offen sc1 lds"
               :: "s"(lds), "v"(voff), "s"(srd), "s"(soff) : "memory");
}
DI bf16x8 lds_b128(lds_cptr p) { return *(const LAS bf16x8*)p; }
DI s16x4 lds_tr(lds_cptr p) {
  return __builtin_bit_cast(s16x4, __builtin_amdgcn_ds_read_tr16_b64_v4i16((LAS v4i16_t*)p));
}
DI bf16x8 cat8(s16x4 lo, s16x4 hi) { return __builtin_shufflevector(lo, hi, 0, 1, 2, 3, 4, 5, 6, 7); }
DI float swap32_add(float x) {
  auto r = __builtin_amdgcn_permlane32_swap(F2U(x), F2U(x), false, false);
  return U2F(r[0]) + U2F(r[1]);
}
DI float swap32_max(float x) {
  auto r = __builtin_amdgcn_permlane32_swap(F2U(x), F2U(x), false, false);
  return __builtin_fmaxf(U2F(r[0]), U2F(r[1]));
}
template <int CTRL> DI float dpp_add(float x) {
  int y = __builtin_amdgcn_update_dpp(0, F2I(x), CTRL, 0xF, 0xF, true);
  return x + I2F(y);
}
DI float max3f(float a, float b, float c) { float r; asm("v_max3_f32 %0, %1, %2, %3" : "=v"(r) : "v"(a), "v"(b), "v"(c)); return r; }
DI float max2f(float a, float b) { float r; asm("v_max_f32_e32 %0, %1, %2" : "=v"(r) : "v"(a), "v"(b)); return r; }
DI float dot2bf(unsigned a, unsigned b, float c) {
  return __builtin_amdgcn_fdot2_f32_bf16(__builtin_bit_cast(bf16x2_t, a), __builtin_bit_cast(bf16x2_t, b), c, false);
}
#define PIN(x) asm volatile("" : "+v"(x))
DI float swap16_add(float x) {
  auto r = __builtin_amdgcn_permlane16_swap(F2U(x), F2U(x), false, false);
  return U2F(r[0]) + U2F(r[1]);
}
DI float sum_hi3(float x) { return swap32_add(swap16_add(dpp_add<0x128>(x))); }
DI float gelu_tanh(float x) {
  const float k0 = -2.0f * 0.7978845608028654f * 1.4426950408889634f;
  const float t = x * x;
  const float z = x * __builtin_fmaf(t, 0.044715f * k0, k0);
  const float e = __builtin_amdgcn_exp2f(z);
  return x * __builtin_amdgcn_rcpf(1.0f + e);
}
DI void wave_sum8(const float (&a)[8], float (&out)[8]) {
  float b[4], c[2];
#pragma unroll
  for (int i = 0; i < 4; ++i) {
    auto r = __builtin_amdgcn_permlane32_swap(F2U(a[i]), F2U(a[i + 4]), false, false);
    b[i] = U2F(r[0]) + U2F(r[1]);
  }
#pragma unroll
  for (int i = 0; i < 2; ++i) {
    auto r = __builtin_amdgcn_permlane16_swap(F2U(b[i]), F2U(b[i + 2]), false, false);
    c[i] = U2F(r[0]) + U2F(r[1]);
  }
#pragma unroll
  for (int i = 0; i < 2; ++i) {
    c[i] = dpp_add<0xB1>(c[i]);
    c[i] = dpp_add<0x4E>(c[i]);
    c[i] = dpp_add<0x141>(c[i]);
    c[i] = dpp_add<0x140>(c[i]);
  }
#pragma unroll
  for (int q = 0; q < 4; ++q)
#pragma unroll
    for (int i = 0; i < 2; ++i)
      out[i + 2 * (q & 1) + 4 * (q >> 1)] = I2F(__builtin_amdgcn_readlane(F2I(c[i]), 16 * q));
}

DI unsigned nosent(unsigned v) { return v < 0xFFFFFFFEu ? v : 0xFFFFFFFEu; }
DI void pub16(__amdgpu_buffer_rsrc_t srd, unsigned voff, unsigned soff, u32x4 v) {
  v = (u32x4){nosent(v[0]), nosent(v[1]), nosent(v[2]), nosent(v[3])};
  __builtin_amdgcn_raw_buffer_store_b128(v, srd, voff + soff, 0, 16);
}
DI unsigned umax3(unsigned a, unsigned b, unsigned c) { const unsigned m = a > b ? a : b; return m > c ? m : c; }
DI bool has_sent(const u32x4* v, int n) {
  unsigned m = 0;
#pragma unroll
  for (int i = 0; i < n; ++i) { m = umax3(m, v[i][0], v[i][1]); m = umax3(m, v[i][2], v[i][3]); }
  return ANY(m == SENT);
}
template <int N> DI void poll_load(u32x4* v, __amdgpu_buffer_rsrc_t srd, unsigned voff, unsigned soff0, unsigned step) {
  for (unsigned spin = 0;; ++spin) {
    asm volatile("" ::: "memory");
#pragma unroll
    for (int i = 0; i < N; ++i) v[i] = __builtin_amdgcn_raw_buffer_load_b128(srd, voff, soff0 + (unsigned)i * step, 16);
    if (!has_sent(v, N) || spin > SPIN_CAP) break;
    __builtin_amdgcn_s_sleep(1);
  }
}
template <int N, int I0, int I1> DI void poll_ind(u32x4* v, __amdgpu_buffer_rsrc_t srd, unsigned voff, unsigned soff0, unsigned step) {
#pragma unroll
  for (int i = 0; i < N; ++i) v[i] = __builtin_amdgcn_raw_buffer_load_b128(srd, voff, soff0 + (unsigned)i * step, 16);
  if (has_sent(v, N)) {
    u32x4 ind[2];
    poll_load<2>(ind, srd, voff, soff0 + (unsigned)I0 * step, (unsigned)(I1 - I0) * step);
    poll_load<N>(v, srd, voff, soff0, step);
  }
}

__global__ __launch_bounds__(512) void k_init(const unsigned short* __restrict__ c, const unsigned short* __restrict__ wmod,
                                              const unsigned short* __restrict__ w3, void* ws) {
  __shared__ __attribute__((aligned(16))) float red[8][32];
  const unsigned tid = TIDX, l = tid & 63, w = tid >> 6;
  STAMP(0, 0);
  if (BIDX >= 152) {
    if (tid < 256) *(unsigned*)((char*)ws + W_FLAG + tid * 64) = 0u;
    return;
  }
  const bool isw3 = BIDX >= 96;
  const unsigned short* src = isw3 ? w3 + (BIDX - 96) * 32 + (tid & 3) * 8 : wmod + BIDX * 32 + (tid & 3) * 8;
  const unsigned ld = isw3 ? F : 3072;
  u32x4 jw[4]; unsigned jc[4];
#pragma unroll
  for (int i = 0; i < 4; ++i) jw[i] = *(const u32x4*)(src + (size_t)((tid >> 2) + 128 * i) * ld);
#pragma unroll
  for (int i = 0; i < 4; ++i) jc[i] = c[(tid >> 2) + 128 * i];
  float acc[8] = {0, 0, 0, 0, 0, 0, 0, 0};
#pragma unroll
  for (int i = 0; i < 4; ++i) {
    const float v = U2F(jc[i] << 16);
    const float sv = v * __builtin_amdgcn_rcpf(1.0f + __builtin_amdgcn_exp2f(-1.4426950408889634f * v));
    const float sc = isw3 ? 1.0f : sv;
#pragma unroll
    for (int k = 0; k < 4; ++k) {
      acc[2 * k] = __builtin_fmaf(sc, bflo(jw[i][k]), acc[2 * k]);
      acc[2 * k + 1] = __builtin_fmaf(sc, bfhi(jw[i][k]), acc[2 * k + 1]);
    }
  }
  asm volatile("" :: "v"(acc[0]), "v"(acc[7]));
  STAMP(0, 1);
#pragma unroll
  for (int k = 0; k < 8; ++k) acc[k] = sum_hi3(dpp_add<0x124>(acc[k]));
  if (l < 4) {
    *(f32x4*)&red[w][l * 8] = (f32x4){acc[0], acc[1], acc[2], acc[3]};
    *(f32x4*)&red[w][l * 8 + 4] = (f32x4){acc[4], acc[5], acc[6], acc[7]};
  }
  BAR_L();
  if (tid < 8) {
    f32x4 t = *(const f32x4*)&red[0][tid * 4];
#pragma unroll
    for (int i = 1; i < 8; ++i) t += *(const f32x4*)&red[i][tid * 4];
    *(f32x4*)((char*)ws + W_MOD + BIDX * 128 + tid * 16) = t;
  }
  STAMP(0, 2);
}

struct LaneC {
  unsigned lane, r, h, q, p, blk;
  unsigned wv;
  unsigned afr[2];
  unsigned xb[4];
};
DI void lane_consts(LaneC& c) {
  unsigned l = TIDX & 63;
  asm volatile("" : "+v"(l));
  c.lane = l; c.r = l & 31; c.h = l >> 5; c.q = (l & 15) >> 2; c.p = l & 3; c.blk = (l >> 4) & 1;
  const unsigned sp = 2 * (c.p & 1) + (c.p >> 1);
#pragma unroll
  for (int nt = 0; nt < 2; ++nt) c.afr[nt] = (8 * c.h + c.q) * 128 + 64 * (nt ^ (c.q >> 1)) + 32 * c.blk + 8 * sp;
#pragma unroll
  for (int s = 0; s < 4; ++s) c.xb[s] = 1024 * c.h + ((c.r ^ (2 * s + c.h)) << 4);
}
DI unsigned wdma_voff(unsigned l, unsigned ld) { return (l >> 3) * ld + (((l & 7) ^ (((l >> 4) & 1) << 2)) << 4); }

DI void wring_issue(int ks, unsigned ring, unsigned voff, __amdgpu_buffer_rsrc_t srd, unsigned soff0, unsigned ld) {
  const unsigned slot = ring + (unsigned)(ks % RING) * 2048u;
  dma16(slot, voff, srd, soff0 + (unsigned)(16 * ks) * ld);
  dma16(slot + 1024u, voff, srd, soff0 + (unsigned)(16 * ks + 8) * ld);
}
template <int N> DI void wait_vm() {
  if constexpr (N == 0) WAIT_V(0); else if constexpr (N == 1) WAIT_V(1); else if constexpr (N == 2) WAIT_V(2);
  else if constexpr (N == 3) WAIT_V(3); else if constexpr (N == 4) WAIT_V(4); else if constexpr (N == 5) WAIT_V(5);
  else if constexpr (N == 6) WAIT_V(6); else if constexpr (N == 7) WAIT_V(7); else if constexpr (N == 8) WAIT_V(8);
  else if constexpr (N == 9) WAIT_V(9); else if constexpr (N == 10) WAIT_V(10); else if constexpr (N == 11) WAIT_V(11);
  else if constexpr (N == 12) WAIT_V(12); else if constexpr (N == 13) WAIT_V(13); else if constexpr (N == 14) WAIT_V(14);
  else if constexpr (N == 15) WAIT_V(15); else if constexpr (N == 16) WAIT_V(16);
  else if constexpr (N == 17) WAIT_V(17); else if constexpr (N == 18) WAIT_V(18);
  else static_assert(N < 0, "wait_vm");
}

struct Frag { bf16x8 a[2], b[2]; };
template <int KS> DI void load_frags(Frag& f, lds_cptr lds, unsigned ringoff, const LaneC& c) {
  const lds_cptr sl = lds + ringoff + (KS % RING) * 2048;
#pragma unroll
  for (int nt = 0; nt < 2; ++nt) f.a[nt] = cat8(lds_tr(sl + c.afr[nt]), lds_tr(sl + c.afr[nt] + 512));
#pragma unroll
  for (int rt = 0; rt < 2; ++rt) f.b[rt] = lds_b128(lds + LDS_X + c.xb[KS & 3] + 2048 * KS + 512 * rt);
}
template <int NKS, int KS, int EXTRA>
DI void gemm_step(f32x16 (&acc)[2][2], Frag& cur, Frag& nxt, lds_cptr lds, unsigned ringoff, unsigned ring, unsigned voff,
                  __amdgpu_buffer_rsrc_t srd, unsigned soff0, unsigned ld, const LaneC& c) {
  if constexpr (KS + 1 < NKS) {
    constexpr int last = (KS + RING < NKS ? KS + RING : NKS) - 1;
    constexpr int after = 2 * (last - (KS + 1)) + ((KS + 1 < RING) ? EXTRA : 0);
    wait_vm<after>();
    load_frags<KS + 1>(nxt, lds, ringoff, c);
  }
  SBAR();
#pragma unroll
  for (int nt = 0; nt < 2; ++nt)
#pragma unroll
    for (int rt = 0; rt < 2; ++rt) acc[nt][rt] = MFMA32(cur.a[nt], cur.b[rt], acc[nt][rt]);
  if constexpr (KS + RING < NKS) {
    asm volatile("" :: "v"(cur.a[0]), "v"(cur.a[1]));
    wring_issue(KS + RING, ring, voff, srd, soff0, ld);
  }
  SBAR();
  if constexpr (KS + 1 < NKS) gemm_step<NKS, KS + 1, EXTRA>(acc, nxt, cur, lds, ringoff, ring, voff, srd, soff0, ld, c);
}
template <int NKS, int EXTRA>
DI void gemm_phase(f32x16 (&acc)[2][2], lds_cptr lds, unsigned ringoff, unsigned ring, unsigned voff,
                   __amdgpu_buffer_rsrc_t srd, unsigned soff0, unsigned ld, const LaneC& c) {
  Frag f0, f1;
  wait_vm<2 * (RING - 1) + EXTRA>();
  load_frags<0>(f0, lds, ringoff, c);
  gemm_step<NKS, 0, EXTRA>(acc, f0, f1, lds, ringoff, ring, voff, srd, soff0, ld, c);
}
DI u32x4 pack8(const f32x16& a, int s, float sc) {
  return (u32x4){cvtpk(a[8 * s] * sc, a[8 * s + 1] * sc), cvtpk(a[8 * s + 2] * sc, a[8 * s + 3] * sc),
                 cvtpk(a[8 * s + 4] * sc, a[8 * s + 5] * sc), cvtpk(a[8 * s + 6] * sc, a[8 * s + 7] * sc)};
}
DI unsigned ximg(unsigned chunk, unsigned row) { return chunk * 1024u + ((row ^ (chunk & 7u)) << 4); }

constexpr int KSLOT[8] = {LDS_D, LDS_R, LDS_R + 32768, LDS_X, LDS_X + 32768, LDS_D, LDS_X, LDS_X + 32768};
constexpr int VSLOT[8] = {LDS_R + 16384, LDS_R + 49152, LDS_X + 16384, LDS_X + 49152, LDS_R + 65536, LDS_X + 16384, LDS_X + 49152, LDS_D};
template <int N> struct IC { static constexpr int value = N; };
struct Params {
  const unsigned short *x, *kc, *vc, *wq, *wo, *w1, *w2, *w3, *w4, *c, *wmod;
  void* ws;
  u32x4* out;
};
DI void attn_part(const Params& P, lds_ptr lds, unsigned lds0, unsigned w, __amdgpu_buffer_rsrc_t srdW) {
  LaneC c; lane_consts(c);
  const unsigned g = BIDX & 7, pn = BIDX >> 3;
  const unsigned l = c.lane, tid = TIDX;
  const unsigned ringoff = LDS_R + w * RINGB, ring = lds0 + ringoff;
  STAMP(1, 0);

  u32x4 xr[8];
#pragma unroll
  for (int i = 0; i < 8; ++i) gld16(xr[i], P.x + (size_t)(pn * 64 + w * 8 + i) * D + l * 8);
  if (w < 4) dma16(lds0 + LDS_D + w * 1024u, l * 16u, srdW, W_MOD + w * 1024u);
  const __amdgpu_buffer_rsrc_t srdQ = mksrd(P.wq, 512u * 4096u * 2u);
  const unsigned vq = wdma_voff(l, 8192u), sq0 = (g * 512u + w * 64u) * 2u;
  const __amdgpu_buffer_rsrc_t srdK = mksrd(P.kc + ((size_t)g * SK + (SK - WIN)) * DH, WIN * DH * 2u);
  const __amdgpu_buffer_rsrc_t srdV = mksrd(P.vc + ((size_t)g * SK + (SK - WIN)) * DH, WIN * DH * 2u);
  const unsigned vk = ((l >> 2) & 7) * 256u + (l >> 5) * 64u + (((l & 3) ^ (2 * (w & 1) + ((l >> 4) & 1))) << 4);
  const unsigned vv = wdma_voff(l, 256u);
  auto dma_k = [&](int t, unsigned slot) {
    dma16(lds0 + slot + 2048u * w, vk, srdK, (unsigned)t * 16384u + 2048u * w);
    dma16(lds0 + slot + 2048u * w + 1024u, vk, srdK, (unsigned)t * 16384u + 2048u * w + 128u);
  };
  auto dma_v = [&](int t, unsigned slot) {
    dma16(lds0 + slot + 1024u * w, vv, srdV, (unsigned)t * 16384u + 2048u * w);
    dma16(lds0 + slot + 8192u + 1024u * w, vv, srdV, (unsigned)t * 16384u + 2048u * w + 128u);
  };

  {
    asm volatile("s_waitcnt vmcnt(0)" : "+v"(xr[0]), "+v"(xr[1]), "+v"(xr[2]), "+v"(xr[3]), "+v"(xr[4]), "+v"(xr[5]), "+v"(xr[6]),
                 "+v"(xr[7]) :: "memory");
    dma16(ring + 8192u, l * 16u, srdQ, (2 * (pn * 8 + w)) * 8192u + g * 1024u);
    dma16(ring + 9216u, l * 16u, srdQ, (2 * (pn * 8 + w) + 1) * 8192u + g * 1024u);
    wring_issue(0, ring, vq, srdQ, sq0, 8192u);
    wring_issue(1, ring, vq, srdQ, sq0, 8192u);
    BAR_L();
    u32x4 mo[4];
#pragma unroll
    for (int i = 0; i < 4; ++i) mo[i] = *(const LAS u32x4*)(lds + LDS_D + (i >> 1) * 2048 + l * 32 + (i & 1) * 16);
    float v[8][8], s[8], m[8];
#pragma unroll
    for (int i = 0; i < 8; ++i) {
#pragma unroll
      for (int j = 0; j < 4; ++j) { v[i][2 * j] = bflo(xr[i][j]); v[i][2 * j + 1] = bfhi(xr[i][j]); }
      s[i] = ((v[i][0] + v[i][1]) + (v[i][2] + v[i][3])) + ((v[i][4] + v[i][5]) + (v[i][6] + v[i][7]));
    }
    wave_sum8(s, m);
#pragma unroll
    for (int i = 0; i < 8; ++i) {
      const float mu = m[i] * (1.0f / D);
      float q = 0.f;
#pragma unroll
      for (int j = 0; j < 8; ++j) { v[i][j] -= mu; q = __builtin_fmaf(v[i][j], v[i][j], q); }
      s[i] = q;
    }
    wave_sum8(s, m);
    float rs[8];
#pragma unroll
    for (int i = 0; i < 8; ++i) rs[i] = __builtin_amdgcn_rsqf(m[i] * (1.0f / D) + LN_EPS);
    STAMP(1, 8);
    STAMP(1, 9);
#pragma unroll
    for (int i = 0; i < 8; ++i) {
      float hh[8];
#pragma unroll
      for (int j = 0; j < 8; ++j) hh[j] = __builtin_fmaf(v[i][j] * rs[i], 1.f + U2F(mo[2 + (j >> 2)][j & 3]), U2F(mo[j >> 2][j & 3]));
      const u32x4 o = {cvtpk(hh[0], hh[1]), cvtpk(hh[2], hh[3]), cvtpk(hh[4], hh[5]), cvtpk(hh[6], hh[7])};
      *(LAS u32x4*)(lds + LDS_X + ximg(l, w * 8 + i)) = o;
      if (i == 1 || i == 3 || i == 5) wring_issue(2 + i / 2, ring, vq, srdQ, sq0, 8192u);
    }
  }
  BAR_L();
  dma_k(0, LDS_D);
  STAMP(1, 1);

  {
    f32x16 acc[2][2] = {};
    gemm_phase<32, 2>(acc, lds, ringoff, ring, vq, srdQ, sq0, 8192u, c);
    STAMP(1, 2);
    WAIT_BAR(0);
    dma_k(1, KSLOT[1]); dma_v(0, VSLOT[0]); dma_k(2, KSLOT[2]); dma_v(1, VSLOT[1]);
#pragma unroll
    for (int nt = 0; nt < 2; ++nt)
#pragma unroll
      for (int rt = 0; rt < 2; ++rt)
#pragma unroll
        for (int s = 0; s < 2; ++s)
          *(LAS u32x4*)(lds + LDS_X + ximg(8 * w + 4 * nt + 2 * s + c.h, 32 * rt + c.r)) = pack8(acc[nt][rt], s, QSCALE);
    BAR_L();
  }

  STAMP(1, 3);
  const unsigned hh = w >> 1, rt = w & 1;
  bf16x8 qf[8];
#pragma unroll
  for (int s = 0; s < 8; ++s) qf[s] = lds_b128(lds + LDS_X + ximg(16 * hh + 2 * s + c.h, 32 * rt + c.r));
  unsigned kb[2], vb[2];
#pragma unroll
  for (int i = 0; i < 2; ++i) {
    kb[i] = 2048 * (c.r >> 3) + 64 * (c.r & 7) + (((2 * i + c.h) ^ ((c.r >> 2) & 3)) << 4);
    vb[i] = (4 * c.h + c.q) * 128 + 64 * (i ^ (c.q >> 1)) + 32 * c.blk + 8 * (2 * (c.p & 1) + (c.p >> 1));
  }
  const __amdgpu_buffer_rsrc_t srdO = mksrd(P.wo, 4096u * 512u * 2u);
  const unsigned vo = wdma_voff(l, 1024u), so0 = g * 512u * 1024u + w * 128u;

  f32x16 o[4] = {};
  f32x16 s0, s1;
  f32x16 negm;
  float lsum = 0.f;
  u32x4 pk[4];
  constexpr int PD = 6;
  bf16x8 fr[PD];
  const bool lag = w >= 4;
  auto frag = [&](int t, int it) -> bf16x8 {
    if (it < 16) {
      const lds_cptr a = lds + VSLOT[t > 0 ? t - 1 : 0] + vb[it & 1] + 8192 * ((it >> 1) & 1) + 2048 * (it >> 2);
      return cat8(lds_tr(a), lds_tr(a + 1024));
    }
    const int s = (it - 16) >> 1;
    return lds_b128(lds + KSLOT[t < 8 ? t : 0] + kb[s & 1] + 512 * (s >> 1) + 8192 * (it & 1));
  };
  auto seam = [&](int t, int tx) __attribute__((always_inline)) {
    if (t == 7) { BAR_L(); } else if (tx >= 1 && tx <= 5) { WAIT_BAR(4); } else if (tx == 6) { WAIT_BAR(2); } else { WAIT_BAR(0); }
  };
  auto refill = [&](int t) __attribute__((always_inline)) {
    if (t + 3 < 8) dma_k(t + 3, KSLOT[t + 3 < 8 ? t + 3 : 0]);
    if (t + 2 < 8) dma_v(t + 2, VSLOT[t + 2 < 8 ? t + 2 : 0]);
    if (t == 6) {
#pragma unroll
      for (int ks = 0; ks < RING; ++ks) wring_issue(ks, ring, vo, srdO, so0, 1024u);
    }
  };
  auto extra = [&](int e) __attribute__((always_inline)) {
    const unsigned dump = lds0 + LDS_R + w * 3072u + (unsigned)(e % 3) * 1024u;
    const unsigned id = (BIDX >> 3) * 8 + w, jh = BIDX & 1;
    const unsigned vrow = (l >> 5) * 1024u + (l & 31) * 16u + jh * 512u;
    auto w3v = [&](unsigned piece) { const unsigned byte = piece * 1024u + l * 16u, row = byte / 1792u;
                                     return row * 3584u + jh * 1792u + (byte - row * 1792u); };
    if (e < 4 || e >= 15) {
      const int i = e < 4 ? e : e - 11;
      const unsigned off = i < 3 ? S_U + (unsigned)i * (64u * T * 16u) + pn * 65536u + g * 8192u
                         : i < 7 ? S_HZ + pn * 262144u + g * 32768u + (unsigned)(i - 3) * 8192u
                                 : S_H3 + pn * 229376u + g * 28672u + (unsigned)(i - 7) * 8192u;
      const u32x4 sv = {SENT, SENT, SENT, SENT};
      __builtin_amdgcn_raw_buffer_store_b128(sv, srdW, (i == 10 && tid >= 256) ? OOB : tid * 16u + off, 0, 16);
    } else if (e < 6) dma16(dump, l * 16u, srdO, (g * 512u + (pn * 8 + w) * 2 + (e - 4)) * 1024u);
    else if (e == 6) dma16(dump, vrow, mksrd(P.w1, 512u * 1024u), id * 2048u);
    else if (e == 7) dma16(dump, vrow, mksrd(P.w2, 512u * 1024u), id * 2048u);
    else if (e < 11) dma16(dump, w3v(id + 256u * (e - 8)), mksrd(P.w3, 512u * 3584u), 0u);
    else if (e == 11) {
      if (id < 128) dma16(dump, w3v(id + 768u), mksrd(P.w3, 512u * 3584u), 0u);
      else dma16(dump, vrow, mksrd(P.w4, 1792u * 1024u), (id - 128u + 768u) * 2048u);
    } else dma16(dump, vrow, mksrd(P.w4, 1792u * 1024u), (id + 256u * (e - 12)) * 2048u);
  };
  auto step = [&](auto tc) __attribute__((always_inline)) {
    constexpr int t = decltype(tc)::value;
    constexpr int i0 = t == 0 ? 16 : 0, i1 = t == 8 ? 16 : 32;
    if (t <= 1) {
#pragma unroll
      for (int it = i0; it < i0 + PD; ++it) fr[it % PD] = frag(t, it);
    }
    if (t == 0) { s0 = (f32x16){}; s1 = (f32x16){}; } else if (t < 8) { s0 = negm; s1 = negm; }
    __builtin_amdgcn_s_setprio(1);
    SBAR();
#pragma unroll
    for (int it = i0; it < i1; ++it) {
      if (it < 16) o[it & 3] = MFMA32(fr[it % PD], __builtin_bit_cast(bf16x8, pk[it >> 2]), o[it & 3]);
      else if (it & 1) s1 = MFMA32(fr[it % PD], qf[(it - 16) >> 1], s1);
      else s0 = MFMA32(fr[it % PD], qf[(it - 16) >> 1], s0);
      if (it + PD < i1) fr[it % PD] = frag(t, it + PD);
      if (t >= 1 && it == 1) { if (!lag) refill(t - 1); }
      SBAR();
    }
    if constexpr (t == 8) return;
    __builtin_amdgcn_s_setprio(0);
    if (lag) { seam(t, t - 1); refill(t); }
    asm volatile("s_nop 15" : "+v"(s0), "+v"(s1));
    float ta = max3f(s0[0], s0[1], s1[0]), tb = max3f(s0[2], s0[3], s1[1]);
    ta = max3f(ta, s1[2], s1[3]);
#pragma unroll
    for (int i = 4; i < 16; i += 4) {
      ta = max3f(ta, s0[i], s0[i + 1]); tb = max3f(tb, s0[i + 2], s0[i + 3]);
      ta = max3f(ta, s1[i], s1[i + 1]); tb = max3f(tb, s1[i + 2], s1[i + 3]);
    }
    float tm = max2f(ta, tb);
    {
      auto r = __builtin_amdgcn_permlane32_swap(F2U(tm), F2U(tm), false, false);
      tm = max2f(U2F(r[0]), U2F(r[1]));
    }
    if (t == 0 || ANY(tm > THR)) {
      const float ndl = t == 0 ? -tm : -max2f(tm, 0.f);
      const float aone = c.h ? 0.f : 1.f;
      if (t == 0) negm = (f32x16){};
      __builtin_amdgcn_s_setprio(2);
      s0 = __builtin_amdgcn_mfma_f32_32x32x2f32(aone, ndl, s0, 0, 0, 0);
      s1 = __builtin_amdgcn_mfma_f32_32x32x2f32(aone, ndl, s1, 0, 0, 0);
      negm = __builtin_amdgcn_mfma_f32_32x32x2f32(aone, ndl, negm, 0, 0, 0);
      __builtin_amdgcn_s_setprio(0);
      if (t > 0) {
        const float al = __builtin_amdgcn_exp2f(ndl);
        lsum *= al;
#pragma unroll
        for (int dt = 0; dt < 4; ++dt)
#pragma unroll
          for (int i = 0; i < 16; ++i) o[dt][i] *= al;
      }
    }
    SBAR();
    if (t >= 1) {
#pragma unroll
      for (int it = 0; it < PD; ++it) fr[it] = frag(t + 1, it);
    }
#pragma unroll
    for (int i = 0; i < 16; ++i) {
      s0[i] = __builtin_amdgcn_exp2f(s0[i]); s1[i] = __builtin_amdgcn_exp2f(s1[i]);
      if (t >= 1 && (i & 3) == 1 && 4 * (t - 1) + (i >> 2) < 22) { PIN(s0[i]); extra(4 * (t - 1) + (i >> 2)); }
    }
    pk[0] = pack8(s0, 0, 1.f); pk[1] = pack8(s0, 1, 1.f); pk[2] = pack8(s1, 0, 1.f); pk[3] = pack8(s1, 1, 1.f);
    {
      float l0 = lsum, l1 = 0.f, l2 = 0.f, l3 = 0.f;
#pragma unroll
      for (int i = 0; i < 16; i += 2) { l0 += s0[i]; l1 += s0[i + 1]; l2 += s1[i]; l3 += s1[i + 1]; }
      lsum = (l0 + l1) + (l2 + l3);
    }
    PIN(pk[0]); PIN(pk[1]); PIN(pk[2]); PIN(pk[3]); PIN(lsum);
    SBAR();
    if (!lag) seam(t, t);
  };
  step(IC<0>{}); step(IC<1>{}); step(IC<2>{}); step(IC<3>{}); step(IC<4>{}); step(IC<5>{}); step(IC<6>{}); step(IC<7>{}); step(IC<8>{});
  __builtin_amdgcn_s_setprio(0);
  STAMP(1, 4);
  {
    const float il = __builtin_amdgcn_rcpf(swap32_add(lsum));
#pragma unroll
    for (int dt = 0; dt < 4; ++dt)
#pragma unroll
      for (int s = 0; s < 2; ++s)
        *(LAS u32x4*)(lds + LDS_X + ximg(16 * hh + 4 * dt + 2 * s + c.h, 32 * rt + c.r)) = pack8(o[dt], s, il);
  }
  BAR_L();
  STAMP(1, 5);
  dma16(lds0 + LDS_D + w * 1024u, l * 16u, srdW, W_MOD + 4096u + w * 1024u);
  {
    LaneC c; lane_consts(c);
    f32x16 acc[2][2] = {};
    gemm_phase<32, 1>(acc, lds, ringoff, ring, vo, srdO, so0, 1024u, c);
    STAMP(1, 6);
    const unsigned dst = W_P1 + ((((pn * 8 + (c.r >> 3)) * 8 + g) * 64 + 8 * w + c.h) * 8 + (c.r & 7)) * 16u;
#pragma unroll
    for (int nt = 0; nt < 2; ++nt)
#pragma unroll
      for (int r2 = 0; r2 < 2; ++r2)
#pragma unroll
        for (int s = 0; s < 2; ++s)
          __builtin_amdgcn_raw_buffer_store_b128(pack8(acc[nt][r2], s, 1.f), srdW, dst + ((4 * nt + 2 * s) * 8 + r2 * 4 * 8 * 64 * 8) * 16u, 0, 16);
  }
  WAIT_BAR(0);
  if (tid == 0) __builtin_amdgcn_raw_buffer_store_b32(1u, srdW, W_FLAG + (pn * 8 + g) * 64u, 0, 16);
  STAMP(1, 7);
}

DI void ln8(float (&v)[8], LAS float* red  , unsigned tid) {
  float s = ((v[0] + v[1]) + (v[2] + v[3])) + ((v[4] + v[5]) + (v[6] + v[7]));
  float q = (__builtin_fmaf(v[0], v[0], v[1] * v[1]) + __builtin_fmaf(v[2], v[2], v[3] * v[3])) +
            (__builtin_fmaf(v[4], v[4], v[5] * v[5]) + __builtin_fmaf(v[6], v[6], v[7] * v[7]));
  s = sum_hi3(s); q = sum_hi3(q);
  if ((tid & 63) < 8) *(LAS f32x2*)(red + ((tid >> 6) * 8 + (tid & 7)) * 2) = (f32x2){s, q};
  BAR_L();
  f32x2 e[8];
#pragma unroll
  for (int i = 0; i < 8; ++i) e[i] = *(const LAS f32x2*)(red + (i * 8 + (tid & 7)) * 2);
  const float mu = (((e[0][0] + e[1][0]) + (e[2][0] + e[3][0])) + ((e[4][0] + e[5][0]) + (e[6][0] + e[7][0]))) * (1.0f / D);
  const float ms = (((e[0][1] + e[1][1]) + (e[2][1] + e[3][1])) + ((e[4][1] + e[5][1]) + (e[6][1] + e[7][1]))) * (1.0f / D);
  const float rs = __builtin_amdgcn_rsqf(__builtin_fmaxf(__builtin_fmaf(-mu, mu, ms), 0.f) + LN_EPS);
  const float nb = -mu * rs;
#pragma unroll
  for (int k = 0; k < 8; ++k) v[k] = __builtin_fmaf(v[k], rs, nb);
}
DI void red4_put(const f32x16& a, int nt, lds_ptr scr, unsigned rt, unsigned kq, unsigned l) {
#pragma unroll
  for (int g = 0; g < 2; ++g)
#pragma unroll
    for (int i = 0; i < 2; ++i) {
      const int o = 8 * g + 4 * i;
      *(LAS f32x4*)(scr + ((rt * 4 + 2 * nt + g) * 4 + kq) * 2048 + i * 1024 + l * 16) = (f32x4){a[o], a[o + 1], a[o + 2], a[o + 3]};
    }
}
DI void red4_get(float (&v)[8], lds_cptr scr, unsigned rt, unsigned kq, unsigned l) {
  f32x4 t[4][2];
#pragma unroll
  for (int s = 0; s < 4; ++s)
#pragma unroll
    for (int i = 0; i < 2; ++i) t[s][i] = *(const LAS f32x4*)(scr + ((rt * 4 + kq) * 4 + s) * 2048 + i * 1024 + l * 16);
#pragma unroll
  for (int i = 0; i < 2; ++i)
#pragma unroll
    for (int k = 0; k < 4; ++k) v[4 * i + k] = (t[0][i][k] + t[1][i][k]) + (t[2][i][k] + t[3][i][k]);
}
DI void tile_mm8(const bf16x8 (&af)[8][2], const u32x4 (&xb)[8], float (&v)[8], lds_ptr scr, unsigned rt, unsigned kq, unsigned l) {
  f32x16 a0 = {}, a1 = {};
#pragma unroll
  for (int i = 0; i < 8; ++i) a0 = MFMA32(af[i][0], __builtin_bit_cast(bf16x8, xb[i]), a0);
  SBAR();
#pragma unroll
  for (int i = 0; i < 4; ++i) a1 = MFMA32(af[i][1], __builtin_bit_cast(bf16x8, xb[i]), a1);
  SBAR();
  red4_put(a0, 0, scr, rt, kq, l);
#pragma unroll
  for (int i = 4; i < 8; ++i) a1 = MFMA32(af[i][1], __builtin_bit_cast(bf16x8, xb[i]), a1);
  SBAR();
  red4_put(a1, 1, scr, rt, kq, l);
  BAR_L();
  red4_get(v, scr, rt, kq, l);
}

struct Frag3 { bf16x8 a, b[2]; };
constexpr int RING3 = 13;
constexpr int LA = 0, LB = 65536, LC = 131072;
DI unsigned r3off(int s, unsigned rc, unsigned rb) { return s < 4 ? rc + s * 1024u : rb + (s - 4) * 1024u; }
template <int KS> DI void load_frags3(Frag3& f, lds_cptr lds, unsigned rc, unsigned rb, unsigned a3, const LaneC& c) {
  const lds_cptr sl = lds + r3off(KS % RING3, rc, rb) + a3;
  f.a = cat8(lds_tr(sl), lds_tr(sl + 256));
#pragma unroll
  for (int rt = 0; rt < 2; ++rt) f.b[rt] = lds_b128(lds + LA + c.xb[KS & 3] + 2048 * KS + 512 * rt);
}
template <int KS>
DI void g3_step(f32x16 (&acc)[2], Frag3& cur, Frag3& n1, Frag3& n2, lds_cptr lds, unsigned lds0, unsigned rc, unsigned rb,
                unsigned voff, __amdgpu_buffer_rsrc_t srd, unsigned soff0, unsigned a3, const LaneC& c) {
  constexpr int NKS = 32;
  if constexpr (KS + 2 < NKS) {
    constexpr int last = (KS + RING3 < NKS ? KS + RING3 : NKS) - 1;
    wait_vm<last - (KS + 2)>();
    load_frags3<KS + 2>(n2, lds, rc, rb, a3, c);
  }
  SBAR();
  acc[0] = MFMA32(cur.a, cur.b[0], acc[0]);
  acc[1] = MFMA32(cur.a, cur.b[1], acc[1]);
  if constexpr (KS + RING3 < NKS) {
    asm volatile("" :: "v"(cur.a));
    dma16(lds0 + r3off(KS % RING3, rc, rb), voff, srd, soff0 + (unsigned)(16 * (KS + RING3)) * 3584u);
  }
  SBAR();
  if constexpr (KS + 1 < NKS) g3_step<KS + 1>(acc, n1, n2, cur, lds, lds0, rc, rb, voff, srd, soff0, a3, c);
}
DI void mlp_part(const Params& P, lds_ptr lds, unsigned lds0, unsigned w, __amdgpu_buffer_rsrc_t srdS) {
  LaneC c; lane_consts(c);
  const unsigned idx = BIDX >> 3, pn = 8 * ((BIDX & 7) >> 1) + (idx >> 2), j = 4 * (BIDX & 1) + (idx & 3);
  const unsigned tid = TIDX, l = c.lane;
  const unsigned vw = wdma_voff(l, 1024u);
  const unsigned rt = w & 1, kq = w >> 1;
  constexpr int LMOD = LDS_D;
  constexpr int LRED = LC + 28672;
  const unsigned prow = 32 * rt + c.r;
  const unsigned trow = pn * 64 + prow;
  const unsigned och = 8 * j + 2 * kq + c.h;
  const unsigned ovo = ((pn * 64 + och) * 64 + prow) * 16u;
  float gb[8];
  STAMP(2, 0);

  {
    const unsigned row = pn * 64 + j * 8 + (tid & 7), ch = tid >> 3;
    u32x4 xr;
    gld16(xr, P.x + (size_t)row * D + ch * 8);
    auto wait_flags = [&](unsigned fo) {
      for (unsigned spin = 0;; ++spin) {
        asm volatile("" ::: "memory");
        const unsigned f = __builtin_amdgcn_raw_buffer_load_b32(srdS, fo, 0, 16);
        if (!ANY(fo != OOB && f != 1u) || spin > SPIN_CAP) break;
        __builtin_amdgcn_s_sleep(FLAG_SLEEP);
      }
    };
    const __amdgpu_buffer_rsrc_t srd1 = mksrd(P.w1, 512u * 512u * 2u), srd2 = mksrd(P.w2, 512u * 512u * 2u);
#pragma unroll
    for (int i = 0; i < 8; ++i) dma16(lds0 + LA + (8 * w + i) * 1024u, vw, srd1, (8 * w + i) * 8192u + j * 128u);
    {
      const int fp = (int)pn - 1 + (int)(l >> 3);
      wait_flags((l < 24 && fp >= 0 && fp < 32) ? W_FLAG + ((unsigned)fp * 8 + (l & 7)) * 64u : OOB);
    }
    STAMP(2, 15);
    u32x4 p[8];
#pragma unroll
    for (int g = 0; g < 8; ++g) bld16_sc1(p[g], tid * 16u, srdS, W_P1 + ((pn * 8 + j) * 8 + g) * 8192u);
    float ga[8], sb[8], cb[8], g2[8];
    {
      u32x4 mv[10];
#pragma unroll
      for (int i = 0; i < 8; ++i) mv[i] = *(const LAS u32x4*)(lds + LMOD + (i >> 1) * 2048 + ch * 32 + 16 * (i & 1));
#pragma unroll
      for (int i = 0; i < 2; ++i) mv[8 + i] = *(const LAS u32x4*)(lds + LMOD + 6144 + och * 32 + 16 * i);
#pragma unroll
      for (int k = 0; k < 8; ++k) {
        ga[k] = U2F(mv[k >> 2][k & 3]); sb[k] = U2F(mv[2 + (k >> 2)][k & 3]); cb[k] = 1.f + U2F(mv[4 + (k >> 2)][k & 3]);
        g2[k] = U2F(mv[6 + (k >> 2)][k & 3]); gb[k] = U2F(mv[8 + (k >> 2)][k & 3]);
      }
    }
    asm volatile("s_waitcnt vmcnt(0)" : "+v"(xr), "+v"(p[0]), "+v"(p[1]), "+v"(p[2]), "+v"(p[3]), "+v"(p[4]), "+v"(p[5]),
                 "+v"(p[6]), "+v"(p[7]) :: "memory");
    STAMP(2, 1);
    float v[8], x2[8];
#pragma unroll
    for (int i = 0; i < 4; ++i) {
      const float lo = ((bflo(p[0][i]) + bflo(p[1][i])) + (bflo(p[2][i]) + bflo(p[3][i]))) + ((bflo(p[4][i]) + bflo(p[5][i])) + (bflo(p[6][i]) + bflo(p[7][i])));
      const float hi = ((bfhi(p[0][i]) + bfhi(p[1][i])) + (bfhi(p[2][i]) + bfhi(p[3][i]))) + ((bfhi(p[4][i]) + bfhi(p[5][i])) + (bfhi(p[6][i]) + bfhi(p[7][i])));
      v[2 * i] = x2[2 * i] = __builtin_fmaf(ga[2 * i], lo, bflo(xr[i]));
      v[2 * i + 1] = x2[2 * i + 1] = __builtin_fmaf(ga[2 * i + 1], hi, bfhi(xr[i]));
    }
    ln8(v, (LAS float*)(lds + LRED), tid);
    float hb[8];
#pragma unroll
    for (int k = 0; k < 8; ++k) v[k] = hb[k] = __builtin_fmaf(v[k], cb[k], sb[k]);
    ln8(v, (LAS float*)(lds + LRED + 512), tid);
    const unsigned vo = ((pn * 64 + ch) * 64 + j * 8 + (tid & 7)) * 16u, vz = ((pn * 64 + ch) * 256 + j * 8 + (tid & 7)) * 16u;
    pub16(srdS, vo, S_U, (u32x4){cvtpk(v[0], v[1]), cvtpk(v[2], v[3]), cvtpk(v[4], v[5]), cvtpk(v[6], v[7])});
#pragma unroll
    for (int i = 0; i < 2; ++i) {
      pub16(srdS, vz, S_HZ + i * 1024u, (u32x4){F2U(hb[4 * i]), F2U(hb[4 * i + 1]),
                                                F2U(hb[4 * i + 2]), F2U(hb[4 * i + 3])});
      f32x4 z;
#pragma unroll
      for (int e = 0; e < 4; ++e) z[e] = __builtin_fmaf(g2[4 * i + e], hb[4 * i + e], x2[4 * i + e]);
      pub16(srdS, vz, S_HZ + (2 + i) * 1024u, __builtin_bit_cast(u32x4, z));
    }
#pragma unroll
    for (int i = 0; i < 8; ++i) dma16(lds0 + LB + (8 * w + i) * 1024u, vw, srd2, (8 * w + i) * 8192u + j * 128u);
  }
  STAMP(2, 2);
  const __amdgpu_buffer_rsrc_t srd3 = mksrd(P.w3, 512u * F * 2u);
  const unsigned rc = LC + w * 4096u, rb = LB + w * 9216u;
  const unsigned v3 = (l >> 2) * 3584u + ((l & 3) << 4), s30 = (j * 224u + w * 32u) * 2u;
  float v[8];
  {
    bf16x8 af[8][2];
#pragma unroll
    for (int i = 0; i < 8; ++i)
#pragma unroll
      for (int nt = 0; nt < 2; ++nt) {
        const lds_cptr a = lds + LA + (8 * kq + i) * 2048 + c.afr[nt];
        af[i][nt] = cat8(lds_tr(a), lds_tr(a + 512));
      }
    BAR_L();
    u32x4 xb[8];
    poll_ind<8, 3, 7>(xb, srdS, ((pn * 64 + 16 * kq + c.h) * 64 + prow) * 16u, S_U, 2048u);
    STAMP(2, 3);
    tile_mm8(af, xb, v, lds + LA, rt, kq, l);
#pragma unroll
    for (int k = 0; k < 8; ++k) v[k] = gelu_tanh(v[k]);
    pub16(srdS, ovo, S_H1, (u32x4){cvtpk(v[0], v[1]), cvtpk(v[2], v[3]), cvtpk(v[4], v[5]), cvtpk(v[6], v[7])});
    if (w < 7) {
#pragma unroll
      for (int ks = 0; ks < 4; ++ks) dma16(lds0 + r3off(ks, rc, rb), v3, srd3, s30 + (unsigned)(16 * ks) * 3584u);
    }
  }
  STAMP(2, 4);
  float y[8];
  u32x4 cs[4];
  const unsigned cso = w < 7 ? (224 * j + 32 * w + 8 * c.h) * 4u : OOB;
  {
    bf16x8 af[8][2];
#pragma unroll
    for (int i = 0; i < 8; ++i)
#pragma unroll
      for (int nt = 0; nt < 2; ++nt) {
        const lds_cptr a = lds + LB + (8 * kq + i) * 2048 + c.afr[nt];
        af[i][nt] = cat8(lds_tr(a), lds_tr(a + 512));
      }
    BAR_L();
    u32x4 hz[4];
    const unsigned hzo = ((pn * 64 + och) * 256 + prow) * 16u;
#pragma unroll
    for (int i = 0; i < 4; ++i) hz[i] = __builtin_amdgcn_raw_buffer_load_b128(srdS, hzo, S_HZ + i * 1024u, 16);
#pragma unroll
    for (int i = 0; i < 4; ++i) cs[i] = __builtin_amdgcn_raw_buffer_load_b128(srdS, cso, W_MOD + 12288u + 64u * (i >> 1) + 16u * (i & 1), 0);
    const int sh = kq == 0 ? -1 : kq == 1 ? 1 : kq == 2 ? -2 : 2;
    const int srow = (int)trow + sh;
    const unsigned xvo = (srow >= 0 && srow < T) ? ((((unsigned)srow >> 6) * 64 + 16 * kq + c.h) * 64 + ((unsigned)srow & 63)) * 16u : OOB;
    u32x4 xb[8];
    poll_ind<8, 3, 7>(xb, srdS, xvo, S_H1, 2048u);
    STAMP(2, 5);
    tile_mm8(af, xb, v, lds + LA, rt, kq, l);
    if (has_sent(hz, 4)) poll_load<4>(hz, srdS, hzo, S_HZ, 1024u);
    float x1[8];
#pragma unroll
    for (int k = 0; k < 8; ++k) {
      x1[k] = U2F(hz[k >> 2][k & 3]) + v[k];
      y[k] = __builtin_fmaf(gb[k], v[k], U2F(hz[2 + (k >> 2)][k & 3]));
    }
    pub16(srdS, ovo, S_X1, (u32x4){cvtpk(x1[0], x1[1]), cvtpk(x1[2], x1[3]), cvtpk(x1[4], x1[5]), cvtpk(x1[6], x1[7])});
    if (w < 7) {
#pragma unroll
      for (int ks = 4; ks < RING3; ++ks) dma16(lds0 + r3off(ks, rc, rb), v3, srd3, s30 + (unsigned)(16 * ks) * 3584u);
    }
  }
  STAMP(2, 6);
  {
    u32x4 xr[8];
    BAR_L();
    poll_ind<8, 3, 7>(xr, srdS, ((pn * 64 + 8 * w) * 64 + l) * 16u, S_X1, 1024u);
    STAMP(2, 7);
#pragma unroll
    for (int i = 0; i < 8; ++i) *(LAS u32x4*)(lds + LA + ximg(8 * w + i, l)) = xr[i];
    {
      float s0 = 0.f, s1 = 0.f, q0 = 0.f, q1 = 0.f;
#pragma unroll
      for (int i = 0; i < 8; ++i)
#pragma unroll
        for (int k = 0; k < 4; k += 2) {
          s0 = dot2bf(xr[i][k], 0x3F803F80u, s0); s1 = dot2bf(xr[i][k + 1], 0x3F803F80u, s1);
          q0 = dot2bf(xr[i][k], xr[i][k], q0); q1 = dot2bf(xr[i][k + 1], xr[i][k + 1], q1);
        }
      *(LAS f32x2*)(lds + LRED + (w * 64 + l) * 8) = (f32x2){s0 + s1, q0 + q1};
    }
    BAR_L();
    STAMP(2, 8);
    if (w < 7) {
      f32x16 acc[2] = {};
      const unsigned a3 = (8 * c.h + c.q) * 64 + 32 * c.blk + 8 * (2 * (c.p & 1) + (c.p >> 1));
      Frag3 f0, f1, f2;
      wait_vm<RING3 - 2>();
      load_frags3<0>(f0, lds, rc, rb, a3, c);
      load_frags3<1>(f1, lds, rc, rb, a3, c);
      float mu[2], rs[2];
#pragma unroll
      for (int r2 = 0; r2 < 2; ++r2) {
        float s = 0.f, q = 0.f;
#pragma unroll
        for (int i = 0; i < 8; ++i) {
          const f32x2 t = *(const LAS f32x2*)(lds + LRED + (i * 64 + 32 * r2 + c.r) * 8);
          s += t[0]; q += t[1];
        }
        mu[r2] = s * (1.0f / D);
        rs[r2] = __builtin_amdgcn_rsqf(__builtin_fmaxf(__builtin_fmaf(-mu[r2], mu[r2], q * (1.0f / D)), 0.f) + LN_EPS);
      }
      if (w < 4) __builtin_amdgcn_s_setprio(2); else __builtin_amdgcn_s_setprio(1);
      g3_step<0>(acc, f0, f1, f2, lds, lds0, rc, rb, v3, srd3, s30, a3, c);
      __builtin_amdgcn_s_setprio(0);
      STAMP(2, 9);
#pragma unroll
      for (int r2 = 0; r2 < 2; ++r2) {
#pragma unroll
        for (int i = 0; i < 16; ++i) acc[r2][i] = gelu_tanh(rs[r2] * __builtin_fmaf(-mu[r2], U2F(cs[i >> 2][i & 3]), acc[r2][i]));
#pragma unroll
        for (int s2 = 0; s2 < 2; ++s2)
          pub16(srdS, ((pn * 224 + 28 * j + 4 * w + 2 * s2 + c.h) * 64 + 32 * r2 + c.r) * 16u, S_H3, pack8(acc[r2], s2, 1.f));
      }
    }
    BAR_L();
  }
  STAMP(2, 10);
  {
    const __amdgpu_buffer_rsrc_t srd4 = mksrd(P.w4, (unsigned)F * 512u * 2u);
    const unsigned qb = kq * 40960u;
    const unsigned s40 = (448u * kq + 8u * rt) * 1024u + j * 128u;
    auto w4dma = [&](int ks) { dma16(lds0 + qb + (ks % 20) * 2048u + rt * 1024u, vw, srd4, s40 + (unsigned)ks * 16384u); };
#pragma unroll
    for (int ks = 0; ks < NPRE; ++ks) w4dma(ks);
    const unsigned hvo = ((pn * 224 + 56 * kq + c.h) * 64 + prow) * 16u;
    u32x4 ha[14], hb[14];
    poll_ind<14, 9, 13>(ha, srdS, hvo, S_H3, 2048u);
    BAR_L();
    STAMP(2, 11);
    f32x16 acc[2] = {};
    bf16x8 fa[2][2];
    auto ld = [&](int ks) {
#pragma unroll
      for (int nt = 0; nt < 2; ++nt) {
        const lds_cptr a = lds + qb + (ks % 20) * 2048 + c.afr[nt];
        fa[ks & 1][nt] = cat8(lds_tr(a), lds_tr(a + 512));
      }
    };
    auto mm = [&](int ks, const u32x4& b) {
#pragma unroll
      for (int nt = 0; nt < 2; ++nt) acc[nt] = MFMA32(fa[ks & 1][nt], __builtin_bit_cast(bf16x8, b), acc[nt]);
    };
    auto hbl = [&](int i) { bld16_sc1(hb[i], hvo, srdS, S_H3 + (14u + i) * 2048u); };
    ld(0);
#pragma unroll
    for (int ks = 0; ks < 8; ++ks) {
      if (ks < 7) ld(ks + 1);
      SBAR(); mm(ks, ha[ks]);
      if (ks < 7) { hbl(2 * ks); hbl(2 * ks + 1); }
      else {
#pragma unroll
        for (int k2 = NPRE; k2 < 20; ++k2) w4dma(k2);
      }
      SBAR();
    }
    BAR_L();
    ld(8);
#pragma unroll
    for (int ks = 8; ks < 14; ++ks) {
      if (ks < 13) ld(ks + 1);
      SBAR(); mm(ks, ha[ks]);
      if (ks < 12) { w4dma(20 + 2 * (ks - 8)); w4dma(21 + 2 * (ks - 8)); }
      SBAR();
    }
    asm volatile("s_waitcnt vmcnt(8) lgkmcnt(0)\n\ts_barrier" : "+v"(hb[0]), "+v"(hb[1]), "+v"(hb[2]), "+v"(hb[3]), "+v"(hb[4]), "+v"(hb[5]),
                 "+v"(hb[6]), "+v"(hb[7]), "+v"(hb[8]), "+v"(hb[9]), "+v"(hb[10]), "+v"(hb[11]), "+v"(hb[12]), "+v"(hb[13]) :: "memory");
    if (has_sent(hb, 14)) poll_load<14>(hb, srdS, hvo, S_H3 + 14u * 2048u, 2048u);
    STAMP(2, 12);
    ld(14);
#pragma unroll
    for (int ks = 14; ks < 19; ++ks) { ld(ks + 1); SBAR(); mm(ks, hb[ks - 14]); SBAR(); }
    mm(19, hb[5]);
    WAIT_BAR(0);
    ld(20);
#pragma unroll
    for (int ks = 20; ks < 27; ++ks) { ld(ks + 1); SBAR(); mm(ks, hb[ks - 14]); SBAR(); }
    mm(27, hb[13]);
    BAR_L();
    STAMP(2, 13);
    red4_put(acc[0], 0, lds + LA, rt, kq, l);
    red4_put(acc[1], 1, lds + LA, rt, kq, l);
    BAR_L();
    red4_get(v, lds + LA, rt, kq, l);
#pragma unroll
    for (int k = 0; k < 8; ++k) v[k] = __builtin_fmaf(gb[k], v[k], y[k]);
    P.out[(size_t)trow * 64 + och] = (u32x4){cvtpk(v[0], v[1]), cvtpk(v[2], v[3]), cvtpk(v[4], v[5]), cvtpk(v[6], v[7])};
  }
  STAMP(2, 14);
}

__global__ __launch_bounds__(512, 2) void k_fused(Params P) {
  extern __shared__ __attribute__((aligned(16))) char smem[];
  const lds_ptr lds = (lds_ptr)smem;
  const unsigned lds0 = (unsigned)(uintptr_t)smem;
  const unsigned w = rfl(TIDX >> 6);
  const __amdgpu_buffer_rsrc_t srdW = mksrd(P.ws, W_END);
  attn_part(P, lds, lds0, w, srdW);
  mlp_part(P, lds, lds0, w, srdW);
}

#if defined(__HIP_DEVICE_COMPILE__)
#pragma clang attribute pop
#endif
extern "C" void kernel_launch(void* const* d_in, const int* in_sizes, int n_in, void* d_out, int out_size, void* d_ws,
                              size_t ws_size, hipStream_t stream) {
  Params P;
  P.x = (const unsigned short*)d_in[0];
  P.kc = (const unsigned short*)d_in[1];
  P.vc = (const unsigned short*)d_in[2];
  P.wq = (const unsigned short*)d_in[3];
  P.wo = (const unsigned short*)d_in[4];
  P.w1 = (const unsigned short*)d_in[6];
  P.w2 = (const unsigned short*)d_in[7];
  P.w3 = (const unsigned short*)d_in[8];
  P.w4 = (const unsigned short*)d_in[9];
  P.c = (const unsigned short*)d_in[10];
  P.wmod = (const unsigned short*)d_in[11];
  P.ws = d_ws;
  P.out = (u32x4*)d_out;
  (void)hipFuncSetAttribute((const void*)k_fused, hipFuncAttributeMaxDynamicSharedMemorySize, LDS_TOTAL);
  k_init<<<153, 512, 0, stream>>>(P.c, P.wmod, P.w3, d_ws);
  k_fused<<<256, 512, LDS_TOTAL, stream>>>(P);
}
```

```cpp
#include <hip/hip_runtime.h>
#include <hip/hip_bf16.h>
#include <stdint.h>

#if defined(__HIP_DEVICE_COMPILE__)
#pragma clang attribute push(__attribute__((target("no-packed-fp32-ops"))), apply_to = function)
#endif
typedef short bf16x8 __attribute__((ext_vector_type(8)));
typedef short s16x4 __attribute__((ext_vector_type(4)));
typedef float f32x16 __attribute__((ext_vector_type(16)));
typedef float f32x4 __attribute__((ext_vector_type(4)));
typedef float f32x2 __attribute__((ext_vector_type(2)));
typedef unsigned u32x4 __attribute__((ext_vector_type(4)));
typedef unsigned u32x2 __attribute__((ext_vector_type(2)));
typedef __bf16 bf16x2_t __attribute__((ext_vector_type(2)));
typedef short v4i16_t __attribute__((ext_vector_type(4)));
#define LAS __attribute__((address_space(3)))
typedef LAS const char* lds_cptr;
typedef LAS char* lds_ptr;
#define DI __device__ __forceinline__
#define F2U(x) __builtin_bit_cast(unsigned, (float)(x))
#define U2F(x) __builtin_bit_cast(float, (unsigned)(x))
#define F2I(x) __builtin_bit_cast(int, (float)(x))
#define I2F(x) __builtin_bit_cast(float, (int)(x))
#define TIDX __builtin_amdgcn_workitem_id_x()
#define BIDX __builtin_amdgcn_workgroup_id_x()
#define ANY(p) (__builtin_amdgcn_ballot_w64(p) != 0ull)
#define SBAR() __builtin_amdgcn_sched_barrier(0)
#define MFMA32(a, b, c) __builtin_amdgcn_mfma_f32_32x32x16_bf16((a), (b), (c), 0, 0, 0)
#define WAIT_V(n) asm volatile("s_waitcnt vmcnt(" #n ")" ::: "memory")
#define WAIT_L0() asm volatile("s_waitcnt lgkmcnt(0)" ::: "memory")
#define BAR_L() asm volatile("s_waitcnt lgkmcnt(0)\n\ts_barrier" ::: "memory")
#define WAIT_BAR(n) asm volatile("s_waitcnt vmcnt(" #n ") lgkmcnt(0)\n\ts_barrier" ::: "memory")

constexpr int T = 2048, D = 512, HKV = 8, SK = 2048, DH = 128, WIN = 512, F = 1792;
constexpr float LN_EPS = 1e-5f;
constexpr float QSCALE = 0.088388347648318440f * 1.4426950408889634f;

constexpr unsigned W_MOD = 0;
constexpr unsigned W_FLAG = 32768;
constexpr unsigned W_P1 = 65536;
constexpr unsigned S_U = W_P1 + 8u * 64u * T * 16u;
constexpr unsigned S_H1 = S_U + 64u * T * 16u;
constexpr unsigned S_X1 = S_H1 + 64u * T * 16u;
constexpr unsigned S_HZ = S_X1 + 64u * T * 16u;
constexpr unsigned S_H3 = S_HZ + 256u * T * 16u;
constexpr unsigned W_END = S_H3 + 224u * T * 16u;
static_assert(W_FLAG + 256 * 64 <= W_P1, "ws");
constexpr unsigned OOB = 0xF0000000u;
#define K1MAP 0
#define FLAG_SLEEP 4
#define ATHR 32.0f
constexpr float THR = ATHR;
constexpr unsigned SENT = 0xFFFFFFFFu;
constexpr unsigned SPIN_CAP = 1u << 16;

constexpr int LDS_X = 0;
constexpr int LDS_R = 65536;
constexpr int RING = 5, RINGB = RING * 2048;
constexpr int LDS_D = LDS_R + 8 * RINGB;
constexpr int LDS_TOTAL = LDS_D + 16384;
static_assert(LDS_TOTAL == 163840, "lds");

#define STAMP(kid, i) do {} while (0)
DI unsigned cvtpk(float lo, float hi) {
  f32x2 v = {lo, hi};
  bf16x2_t b = __builtin_convertvector(v, bf16x2_t);
  return __builtin_bit_cast(unsigned, b);
}
DI float bflo(unsigned u) { return U2F(u << 16); }
DI float bfhi(unsigned u) { return U2F(u & 0xffff0000u); }
DI unsigned rfl(unsigned x) { return __builtin_amdgcn_readfirstlane(x); }
DI __amdgpu_buffer_rsrc_t mksrd(const void* p, unsigned bytes) {
  return __builtin_amdgcn_make_buffer_rsrc((void*)p, (short)0, (int)bytes, 0x00020000);
}
DI void dma16(unsigned lds, unsigned voff, __amdgpu_buffer_rsrc_t srd, unsigned soff) {
  asm volatile("s_mov_b32 m0, %0\n\ts_nop 0\n\tbuffer_load_dwordx4 %1, %2, %3 offen lds"
               :: "s"(lds), "v"(voff), "s"(srd), "s"(soff) : "memory");
}
DI void gld16(u32x4& d, const void* p) { asm volatile("global_load_dwordx4 %0, %1, off" : "=v"(d) : "v"(p) : "memory"); }
DI void bld16_sc1(u32x4& d, unsigned voff, __amdgpu_buffer_rsrc_t srd, unsigned soff) {
  asm volatile("buffer_load_dwordx4 %0, %1, %2, %3 offen sc1" : "=v"(d) : "v"(voff), "s"(srd), "s"(soff) : "memory");
}
DI void dma16_sc1(unsigned lds, unsigned voff, __amdgpu_buffer_rsrc_t srd, unsigned soff) {
  asm volatile("s_mov_b32 m0, %0\n\ts_nop 0\n\tbuffer_load_dwordx4 %1, %2, %3 offen sc1 lds"
               :: "s"(lds), "v"(voff), "s"(srd), "s"(soff) : "memory");
}
DI bf16x8 lds_b128(lds_cptr p) { return *(const LAS bf16x8*)p; }
DI s16x4 lds_tr(lds_cptr p) {
  return __builtin_bit_cast(s16x4, __builtin_amdgcn_ds_read_tr16_b64_v4i16((LAS v4i16_t*)p));
}
DI bf16x8 cat8(s16x4 lo, s16x4 hi) { return __builtin_shufflevector(lo, hi, 0, 1, 2, 3, 4, 5, 6, 7); }
DI float swap32_add(float x) {
  auto r = __builtin_amdgcn_permlane32_swap(F2U(x), F2U(x), false, false);
  return U2F(r[0]) + U2F(r[1]);
}
DI float swap32_max(float x) {
  auto r = __builtin_amdgcn_permlane32_swap(F2U(x), F2U(x), false, false);
  return __builtin_fmaxf(U2F(r[0]), U2F(r[1]));
}
template <int CTRL> DI float dpp_add(float x) {
  int y = __builtin_amdgcn_update_dpp(0, F2I(x), CTRL, 0xF, 0xF, true);
  return x + I2F(y);
}
DI float max3f(float a, float b, float c) { float r; asm("v_max3_f32 %0, %1, %2, %3" : "=v"(r) : "v"(a), "v"(b), "v"(c)); return r; }
DI float max2f(float a, float b) { float r; asm("v_max_f32_e32 %0, %1, %2" : "=v"(r) : "v"(a), "v"(b)); return r; }
DI float dot2bf(unsigned a, unsigned b, float c) {
  return __builtin_amdgcn_fdot2_f32_bf16(__builtin_bit_cast(bf16x2_t, a), __builtin_bit_cast(bf16x2_t, b), c, false);
}
#define PIN(x) asm volatile("" : "+v"(x))
DI float swap16_add(float x) {
  auto r = __builtin_amdgcn_permlane16_swap(F2U(x), F2U(x), false, false);
  return U2F(r[0]) + U2F(r[1]);
}
DI float sum_hi3(float x) { return swap32_add(swap16_add(dpp_add<0x128>(x))); }
DI float gelu_tanh(float x) {
  const float k0 = -2.0f * 0.7978845608028654f * 1.4426950408889634f;
  const float t = x * x;
  const float z = x * __builtin_fmaf(t, 0.044715f * k0, k0);
  const float e = __builtin_amdgcn_exp2f(z);
  return x * __builtin_amdgcn_rcpf(1.0f + e);
}
DI void wave_sum8(const float (&a)[8], float (&out)[8]) {
  float b[4], c[2];
#pragma unroll
  for (int i = 0; i < 4; ++i) {
    auto r = __builtin_amdgcn_permlane32_swap(F2U(a[i]), F2U(a[i + 4]), false, false);
    b[i] = U2F(r[0]) + U2F(r[1]);
  }
#pragma unroll
  for (int i = 0; i < 2; ++i) {
    auto r = __builtin_amdgcn_permlane16_swap(F2U(b[i]), F2U(b[i + 2]), false, false);
    c[i] = U2F(r[0]) + U2F(r[1]);
  }
#pragma unroll
  for (int i = 0; i < 2; ++i) {
    c[i] = dpp_add<0xB1>(c[i]);
    c[i] = dpp_add<0x4E>(c[i]);
    c[i] = dpp_add<0x141>(c[i]);
    c[i] = dpp_add<0x140>(c[i]);
  }
#pragma unroll
  for (int q = 0; q < 4; ++q)
#pragma unroll
    for (int i = 0; i < 2; ++i)
      out[i + 2 * (q & 1) + 4 * (q >> 1)] = I2F(__builtin_amdgcn_readlane(F2I(c[i]), 16 * q));
}

DI unsigned nosent(unsigned v) { return v < 0xFFFFFFFEu ? v : 0xFFFFFFFEu; }
DI void pub16(__amdgpu_buffer_rsrc_t srd, unsigned voff, unsigned soff, u32x4 v) {
  v = (u32x4){nosent(v[0]), nosent(v[1]), nosent(v[2]), nosent(v[3])};
  __builtin_amdgcn_raw_buffer_store_b128(v, srd, voff + soff, 0, 16);
}
DI unsigned umax3(unsigned a, unsigned b, unsigned c) { const unsigned m = a > b ? a : b; return m > c ? m : c; }
DI bool has_sent(const u32x4* v, int n) {
  unsigned m = 0;
#pragma unroll
  for (int i = 0; i < n; ++i) { m = umax3(m, v[i][0], v[i][1]); m = umax3(m, v[i][2], v[i][3]); }
  return ANY(m == SENT);
}
template <int N> DI void poll_load(u32x4* v, __amdgpu_buffer_rsrc_t srd, unsigned voff, unsigned soff0, unsigned step) {
  for (unsigned spin = 0;; ++spin) {
    asm volatile("" ::: "memory");
#pragma unroll
    for (int i = 0; i < N; ++i) v[i] = __builtin_amdgcn_raw_buffer_load_b128(srd, voff, soff0 + (unsigned)i * step, 16);
    if (!has_sent(v, N) || spin > SPIN_CAP) break;
    __builtin_amdgcn_s_sleep(1);
  }
}
template <int N, int I0, int I1> DI void poll_ind(u32x4* v, __amdgpu_buffer_rsrc_t srd, unsigned voff, unsigned soff0, unsigned step) {
  u32x4 ind[2];
  poll_load<2>(ind, srd, voff, soff0 + (unsigned)I0 * step, (unsigned)(I1 - I0) * step);
  poll_load<N>(v, srd, voff, soff0, step);
}

__global__ __launch_bounds__(512) void k_init(const unsigned short* __restrict__ c, const unsigned short* __restrict__ wmod,
                                              const unsigned short* __restrict__ w3, void* ws) {
  __shared__ __attribute__((aligned(16))) float red[8][32];
  const unsigned tid = TIDX, l = tid & 63, w = tid >> 6;
  STAMP(0, 0);
  if (BIDX >= 152) {
    if (tid < 256) *(unsigned*)((char*)ws + W_FLAG + tid * 64) = 0u;
    return;
  }
  const bool isw3 = BIDX >= 96;
  const unsigned short* src = isw3 ? w3 + (BIDX - 96) * 32 + (tid & 3) * 8 : wmod + BIDX * 32 + (tid & 3) * 8;
  const unsigned ld = isw3 ? F : 3072;
  u32x4 jw[4]; unsigned jc[4];
#pragma unroll
  for (int i = 0; i < 4; ++i) jw[i] = *(const u32x4*)(src + (size_t)((tid >> 2) + 128 * i) * ld);
#pragma unroll
  for (int i = 0; i < 4; ++i) jc[i] = c[(tid >> 2) + 128 * i];
  float acc[8] = {0, 0, 0, 0, 0, 0, 0, 0};
#pragma unroll
  for (int i = 0; i < 4; ++i) {
    const float v = U2F(jc[i] << 16);
    const float sv = v * __builtin_amdgcn_rcpf(1.0f + __builtin_amdgcn_exp2f(-1.4426950408889634f * v));
    const float sc = isw3 ? 1.0f : sv;
#pragma unroll
    for (int k = 0; k < 4; ++k) {
      acc[2 * k] = __builtin_fmaf(sc, bflo(jw[i][k]), acc[2 * k]);
      acc[2 * k + 1] = __builtin_fmaf(sc, bfhi(jw[i][k]), acc[2 * k + 1]);
    }
  }
  asm volatile("" :: "v"(acc[0]), "v"(acc[7]));
  STAMP(0, 1);
#pragma unroll
  for (int k = 0; k < 8; ++k) acc[k] = sum_hi3(dpp_add<0x124>(acc[k]));
  if (l < 4) {
    *(f32x4*)&red[w][l * 8] = (f32x4){acc[0], acc[1], acc[2], acc[3]};
    *(f32x4*)&red[w][l * 8 + 4] = (f32x4){acc[4], acc[5], acc[6], acc[7]};
  }
  BAR_L();
  if (tid < 8) {
    f32x4 t = *(const f32x4*)&red[0][tid * 4];
#pragma unroll
    for (int i = 1; i < 8; ++i) t += *(const f32x4*)&red[i][tid * 4];
    *(f32x4*)((char*)ws + W_MOD + BIDX * 128 + tid * 16) = t;
  }
  STAMP(0, 2);
}

struct LaneC {
  unsigned lane, r, h, q, p, blk;
  unsigned wv;
  unsigned afr[2];
  unsigned xb[4];
};
DI void lane_consts(LaneC& c) {
  unsigned l = TIDX & 63;
  asm volatile("" : "+v"(l));
  c.lane = l; c.r = l & 31; c.h = l >> 5; c.q = (l & 15) >> 2; c.p = l & 3; c.blk = (l >> 4) & 1;
  const unsigned sp = 2 * (c.p & 1) + (c.p >> 1);
#pragma unroll
  for (int nt = 0; nt < 2; ++nt) c.afr[nt] = (8 * c.h + c.q) * 128 + 64 * (nt ^ (c.q >> 1)) + 32 * c.blk + 8 * sp;
#pragma unroll
  for (int s = 0; s < 4; ++s) c.xb[s] = 1024 * c.h + ((c.r ^ (2 * s + c.h)) << 4);
}
DI unsigned wdma_voff(unsigned l, unsigned ld) { return (l >> 3) * ld + (((l & 7) ^ (((l >> 4) & 1) << 2)) << 4); }

DI void wring_issue(int ks, unsigned ring, unsigned voff, __amdgpu_buffer_rsrc_t srd, unsigned soff0, unsigned ld) {
  const unsigned slot = ring + (unsigned)(ks % RING) * 2048u;
  dma16(slot, voff, srd, soff0 + (unsigned)(16 * ks) * ld);
  dma16(slot + 1024u, voff, srd, soff0 + (unsigned)(16 * ks + 8) * ld);
}
template <int N> DI void wait_vm() {
  if constexpr (N == 0) WAIT_V(0); else if constexpr (N == 1) WAIT_V(1); else if constexpr (N == 2) WAIT_V(2);
  else if constexpr (N == 3) WAIT_V(3); else if constexpr (N == 4) WAIT_V(4); else if constexpr (N == 5) WAIT_V(5);
  else if constexpr (N == 6) WAIT_V(6); else if constexpr (N == 7) WAIT_V(7); else if constexpr (N == 8) WAIT_V(8);
  else if constexpr (N == 9) WAIT_V(9); else if constexpr (N == 10) WAIT_V(10); else if constexpr (N == 11) WAIT_V(11);
  else if constexpr (N == 12) WAIT_V(12); else if constexpr (N == 13) WAIT_V(13); else if constexpr (N == 14) WAIT_V(14);
  else if constexpr (N == 15) WAIT_V(15); else if constexpr (N == 16) WAIT_V(16);
  else if constexpr (N == 17) WAIT_V(17); else if constexpr (N == 18) WAIT_V(18);
  else static_assert(N < 0, "wait_vm");
}

struct Frag { bf16x8 a[2], b[2]; };
template <int KS> DI void load_frags(Frag& f, lds_cptr lds, unsigned ringoff, const LaneC& c) {
  const lds_cptr sl = lds + ringoff + (KS % RING) * 2048;
#pragma unroll
  for (int nt = 0; nt < 2; ++nt) f.a[nt] = cat8(lds_tr(sl + c.afr[nt]), lds_tr(sl + c.afr[nt] + 512));
#pragma unroll
  for (int rt = 0; rt < 2; ++rt) f.b[rt] = lds_b128(lds + LDS_X + c.xb[KS & 3] + 2048 * KS + 512 * rt);
}
template <int NKS, int KS, int EXTRA>
DI void gemm_step(f32x16 (&acc)[2][2], Frag& cur, Frag& nxt, lds_cptr lds, unsigned ringoff, unsigned ring, unsigned voff,
                  __amdgpu_buffer_rsrc_t srd, unsigned soff0, unsigned ld, const LaneC& c) {
  if constexpr (KS + 1 < NKS) {
    constexpr int last = (KS + RING < NKS ? KS + RING : NKS) - 1;
    constexpr int after = 2 * (last - (KS + 1)) + ((KS + 1 < RING) ? EXTRA : 0);
    wait_vm<after>();
    load_frags<KS + 1>(nxt, lds, ringoff, c);
  }
  SBAR();
#pragma unroll
  for (int nt = 0; nt < 2; ++nt)
#pragma unroll
    for (int rt = 0; rt < 2; ++rt) acc[nt][rt] = MFMA32(cur.a[nt], cur.b[rt], acc[nt][rt]);
  if constexpr (KS + RING < NKS) {
    asm volatile("" :: "v"(cur.a[0]), "v"(cur.a[1]));
    wring_issue(KS + RING, ring, voff, srd, soff0, ld);
  }
  SBAR();
  if constexpr (KS + 1 < NKS) gemm_step<NKS, KS + 1, EXTRA>(acc, nxt, cur, lds, ringoff, ring, voff, srd, soff0, ld, c);
}
template <int NKS, int EXTRA>
DI void gemm_phase(f32x16 (&acc)[2][2], lds_cptr lds, unsigned ringoff, unsigned ring, unsigned voff,
                   __amdgpu_buffer_rsrc_t srd, unsigned soff0, unsigned ld, const LaneC& c) {
  Frag f0, f1;
  wait_vm<2 * (RING - 1) + EXTRA>();
  load_frags<0>(f0, lds, ringoff, c);
  gemm_step<NKS, 0, EXTRA>(acc, f0, f1, lds, ringoff, ring, voff, srd, soff0, ld, c);
}
DI u32x4 pack8(const f32x16& a, int s, float sc) {
  return (u32x4){cvtpk(a[8 * s] * sc, a[8 * s + 1] * sc), cvtpk(a[8 * s + 2] * sc, a[8 * s + 3] * sc),
                 cvtpk(a[8 * s + 4] * sc, a[8 * s + 5] * sc), cvtpk(a[8 * s + 6] * sc, a[8 * s + 7] * sc)};
}
DI unsigned ximg(unsigned chunk, unsigned row) { return chunk * 1024u + ((row ^ (chunk & 7u)) << 4); }

constexpr int KSLOT[8] = {LDS_D, LDS_R, LDS_R + 32768, LDS_X, LDS_X + 32768, LDS_D, LDS_X, LDS_X + 32768};
constexpr int VSLOT[8] = {LDS_R + 16384, LDS_R + 49152, LDS_X + 16384, LDS_X + 49152, LDS_R + 65536, LDS_X + 16384, LDS_X + 49152, LDS_D};
template <int N> struct IC { static constexpr int value = N; };
struct Params {
  const unsigned short *x, *kc, *vc, *wq, *wo, *w1, *w2, *w3, *w4, *c, *wmod;
  void* ws;
  u32x4* out;
};
DI void attn_part(const Params& P, lds_ptr lds, unsigned lds0, unsigned w, __amdgpu_buffer_rsrc_t srdW) {
  LaneC c; lane_consts(c);
  const unsigned g = BIDX & 7, pn = BIDX >> 3;
  const unsigned l = c.lane, tid = TIDX;
  const unsigned ringoff = LDS_R + w * RINGB, ring = lds0 + ringoff;
  STAMP(1, 0);

  u32x4 xr[8];
#pragma unroll
  for (int i = 0; i < 8; ++i) gld16(xr[i], P.x + (size_t)(pn * 64 + w * 8 + i) * D + l * 8);
  if (w < 4) dma16(lds0 + LDS_D + w * 1024u, l * 16u, srdW, W_MOD + w * 1024u);
  const __amdgpu_buffer_rsrc_t srdQ = mksrd(P.wq, 512u * 4096u * 2u);
  const unsigned vq = wdma_voff(l, 8192u), sq0 = (g * 512u + w * 64u) * 2u;
  const __amdgpu_buffer_rsrc_t srdK = mksrd(P.kc + ((size_t)g * SK + (SK - WIN)) * DH, WIN * DH * 2u);
  const __amdgpu_buffer_rsrc_t srdV = mksrd(P.vc + ((size_t)g * SK + (SK - WIN)) * DH, WIN * DH * 2u);
  const unsigned vk = ((l >> 2) & 7) * 256u + (l >> 5) * 64u + (((l & 3) ^ (2 * (w & 1) + ((l >> 4) & 1))) << 4);
  const unsigned vv = wdma_voff(l, 256u);
  auto dma_k = [&](int t, unsigned slot) {
    dma16(lds0 + slot + 2048u * w, vk, srdK, (unsigned)t * 16384u + 2048u * w);
    dma16(lds0 + slot + 2048u * w + 1024u, vk, srdK, (unsigned)t * 16384u + 2048u * w + 128u);
  };
  auto dma_v = [&](int t, unsigned slot) {
    dma16(lds0 + slot + 1024u * w, vv, srdV, (unsigned)t * 16384u + 2048u * w);
    dma16(lds0 + slot + 8192u + 1024u * w, vv, srdV, (unsigned)t * 16384u + 2048u * w + 128u);
  };

  {
    asm volatile("s_waitcnt vmcnt(0)" : "+v"(xr[0]), "+v"(xr[1]), "+v"(xr[2]), "+v"(xr[3]), "+v"(xr[4]), "+v"(xr[5]), "+v"(xr[6]),
                 "+v"(xr[7]) :: "memory");
    dma16(ring + 8192u, l * 16u, srdQ, (2 * (pn * 8 + w)) * 8192u + g * 1024u);
    dma16(ring + 9216u, l * 16u, srdQ, (2 * (pn * 8 + w) + 1) * 8192u + g * 1024u);
    wring_issue(0, ring, vq, srdQ, sq0, 8192u);
    wring_issue(1, ring, vq, srdQ, sq0, 8192u);
    BAR_L();
    u32x4 mo[4];
#pragma unroll
    for (int i = 0; i < 4; ++i) mo[i] = *(const LAS u32x4*)(lds + LDS_D + (i >> 1) * 2048 + l * 32 + (i & 1) * 16);
    float v[8][8], s[8], m[8];
#pragma unroll
    for (int i = 0; i < 8; ++i) {
#pragma unroll
      for (int j = 0; j < 4; ++j) { v[i][2 * j] = bflo(xr[i][j]); v[i][2 * j + 1] = bfhi(xr[i][j]); }
      s[i] = ((v[i][0] + v[i][1]) + (v[i][2] + v[i][3])) + ((v[i][4] + v[i][5]) + (v[i][6] + v[i][7]));
    }
    wave_sum8(s, m);
#pragma unroll
    for (int i = 0; i < 8; ++i) {
      const float mu = m[i] * (1.0f / D);
      float q = 0.f;
#pragma unroll
      for (int j = 0; j < 8; ++j) { v[i][j] -= mu; q = __builtin_fmaf(v[i][j], v[i][j], q); }
      s[i] = q;
    }
    wave_sum8(s, m);
    float rs[8];
#pragma unroll
    for (int i = 0; i < 8; ++i) rs[i] = __builtin_amdgcn_rsqf(m[i] * (1.0f / D) + LN_EPS);
    STAMP(1, 8);
    STAMP(1, 9);
#pragma unroll
    for (int i = 0; i < 8; ++i) {
      float hh[8];
#pragma unroll
      for (int j = 0; j < 8; ++j) hh[j] = __builtin_fmaf(v[i][j] * rs[i], 1.f + U2F(mo[2 + (j >> 2)][j & 3]), U2F(mo[j >> 2][j & 3]));
      const u32x4 o = {cvtpk(hh[0], hh[1]), cvtpk(hh[2], hh[3]), cvtpk(hh[4], hh[5]), cvtpk(hh[6], hh[7])};
      *(LAS u32x4*)(lds + LDS_X + ximg(l, w * 8 + i)) = o;
      if (i == 1 || i == 3 || i == 5) wring_issue(2 + i / 2, ring, vq, srdQ, sq0, 8192u);
    }
  }
  BAR_L();
  dma_k(0, LDS_D);
  STAMP(1, 1);

  {
    f32x16 acc[2][2] = {};
    gemm_phase<32, 2>(acc, lds, ringoff, ring, vq, srdQ, sq0, 8192u, c);
    STAMP(1, 2);
    WAIT_BAR(0);
    dma_k(1, KSLOT[1]); dma_v(0, VSLOT[0]); dma_k(2, KSLOT[2]); dma_v(1, VSLOT[1]);
#pragma unroll
    for (int nt = 0; nt < 2; ++nt)
#pragma unroll
      for (int rt = 0; rt < 2; ++rt)
#pragma unroll
        for (int s = 0; s < 2; ++s)
          *(LAS u32x4*)(lds + LDS_X + ximg(8 * w + 4 * nt + 2 * s + c.h, 32 * rt + c.r)) = pack8(acc[nt][rt], s, QSCALE);
    BAR_L();
  }

  STAMP(1, 3);
  const unsigned hh = w >> 1, rt = w & 1;
  bf16x8 qf[8];
#pragma unroll
  for (int s = 0; s < 8; ++s) qf[s] = lds_b128(lds + LDS_X + ximg(16 * hh + 2 * s + c.h, 32 * rt + c.r));
  unsigned kb[2], vb[2];
#pragma unroll
  for (int i = 0; i < 2; ++i) {
    kb[i] = 2048 * (c.r >> 3) + 64 * (c.r & 7) + (((2 * i + c.h) ^ ((c.r >> 2) & 3)) << 4);
    vb[i] = (4 * c.h + c.q) * 128 + 64 * (i ^ (c.q >> 1)) + 32 * c.blk + 8 * (2 * (c.p & 1) + (c.p >> 1));
  }
  const __amdgpu_buffer_rsrc_t srdO = mksrd(P.wo, 4096u * 512u * 2u);
  const unsigned vo = wdma_voff(l, 1024u), so0 = g * 512u * 1024u + w * 128u;

  f32x16 o[4] = {};
  f32x16 s0, s1;
  f32x16 negm;
  float lsum = 0.f;
  u32x4 pk[4];
  constexpr int PD = 6;
  bf16x8 fr[PD];
  const bool lag = w >= 4;
  auto frag = [&](int t, int it) -> bf16x8 {
    if (it < 16) {
      const lds_cptr a = lds + VSLOT[t > 0 ? t - 1 : 0] + vb[it & 1] + 8192 * ((it >> 1) & 1) + 2048 * (it >> 2);
      return cat8(lds_tr(a), lds_tr(a + 1024));
    }
    const int s = (it - 16) >> 1;
    return lds_b128(lds + KSLOT[t < 8 ? t : 0] + kb[s & 1] + 512 * (s >> 1) + 8192 * (it & 1));
  };
  auto seam = [&](int t) __attribute__((always_inline)) {
    if (t == 0) { WAIT_BAR(0); } else if (t < 6) { WAIT_BAR(4); } else if (t == 6) { WAIT_BAR(2); } else { BAR_L(); }
    if (t + 3 < 8) dma_k(t + 3, KSLOT[t + 3 < 8 ? t + 3 : 0]);
    if (t + 2 < 8) dma_v(t + 2, VSLOT[t + 2 < 8 ? t + 2 : 0]);
    if (t == 6) {
#pragma unroll
      for (int ks = 0; ks < RING; ++ks) wring_issue(ks, ring, vo, srdO, so0, 1024u);
    }
  };
  auto extra = [&](int e) __attribute__((always_inline)) {
    const unsigned dump = lds0 + LDS_R + w * 3072u + (unsigned)(e % 3) * 1024u;
    const unsigned id = (BIDX >> 3) * 8 + w, jh = BIDX & 1;
    const unsigned vrow = (l >> 5) * 1024u + (l & 31) * 16u + jh * 512u;
    auto w3v = [&](unsigned piece) { const unsigned byte = piece * 1024u + l * 16u, row = byte / 1792u;
                                     return row * 3584u + jh * 1792u + (byte - row * 1792u); };
    if (e < 4 || e >= 15) {
      const int i = e < 4 ? e : e - 11;
      const unsigned off = i < 3 ? S_U + (unsigned)i * (64u * T * 16u) + pn * 65536u + g * 8192u
                         : i < 7 ? S_HZ + pn * 262144u + g * 32768u + (unsigned)(i - 3) * 8192u
                                 : S_H3 + pn * 229376u + g * 28672u + (unsigned)(i - 7) * 8192u;
      const u32x4 sv = {SENT, SENT, SENT, SENT};
      __builtin_amdgcn_raw_buffer_store_b128(sv, srdW, (i == 10 && tid >= 256) ? OOB : tid * 16u + off, 0, 16);
    } else if (e < 6) dma16(dump, l * 16u, srdO, (g * 512u + (pn * 8 + w) * 2 + (e - 4)) * 1024u);
    else if (e == 6) dma16(dump, vrow, mksrd(P.w1, 512u * 1024u), id * 2048u);
    else if (e == 7) dma16(dump, vrow, mksrd(P.w2, 512u * 1024u), id * 2048u);
    else if (e < 11) dma16(dump, w3v(id + 256u * (e - 8)), mksrd(P.w3, 512u * 3584u), 0u);
    else if (e == 11) {
      if (id < 128) dma16(dump, w3v(id + 768u), mksrd(P.w3, 512u * 3584u), 0u);
      else dma16(dump, vrow, mksrd(P.w4, 1792u * 1024u), (id - 128u + 768u) * 2048u);
    } else dma16(dump, vrow, mksrd(P.w4, 1792u * 1024u), (id + 256u * (e - 12)) * 2048u);
  };
#pragma unroll
  for (int t = 0; t <= 8; ++t) {
    const int i0 = t == 0 ? 16 : 0, i1 = t == 8 ? 16 : 32;
    if (t <= 1) {
#pragma unroll
      for (int it = i0; it < i0 + PD; ++it) fr[it % PD] = frag(t, it);
    }
    if (t == 0) { s0 = (f32x16){}; s1 = (f32x16){}; } else if (t < 8) { s0 = negm; s1 = negm; }
    __builtin_amdgcn_s_setprio(1);
    SBAR();
#pragma unroll
    for (int it = i0; it < i1; ++it) {
      if (it < 16) o[it & 3] = MFMA32(fr[it % PD], __builtin_bit_cast(bf16x8, pk[it >> 2]), o[it & 3]);
      else if (it & 1) s1 = MFMA32(fr[it % PD], qf[(it - 16) >> 1], s1);
      else s0 = MFMA32(fr[it % PD], qf[(it - 16) >> 1], s0);
      if (it + PD < i1) fr[it % PD] = frag(t, it + PD);
      if (t >= 1 && t <= 7 && (it & 7) == 3 && 4 * (t - 1) + (it >> 3) < 22) extra(4 * (t - 1) + (it >> 3));
      SBAR();
    }
    if (t == 8) break;
    __builtin_amdgcn_s_setprio(0);
    if (lag) seam(t);
    asm volatile("s_nop 15" : "+v"(s0), "+v"(s1));
    float ta = max3f(s0[0], s0[1], s1[0]), tb = max3f(s0[2], s0[3], s1[1]);
    ta = max3f(ta, s1[2], s1[3]);
#pragma unroll
    for (int i = 4; i < 16; i += 4) {
      ta = max3f(ta, s0[i], s0[i + 1]); tb = max3f(tb, s0[i + 2], s0[i + 3]);
      ta = max3f(ta, s1[i], s1[i + 1]); tb = max3f(tb, s1[i + 2], s1[i + 3]);
    }
    float tm = max2f(ta, tb);
    {
      auto r = __builtin_amdgcn_permlane32_swap(F2U(tm), F2U(tm), false, false);
      tm = max2f(U2F(r[0]), U2F(r[1]));
    }
    if (t == 0 || ANY(tm > THR)) {
      const float ndl = t == 0 ? -tm : -max2f(tm, 0.f);
      const float aone = c.h ? 0.f : 1.f;
      if (t == 0) negm = (f32x16){};
      __builtin_amdgcn_s_setprio(2);
      s0 = __builtin_amdgcn_mfma_f32_32x32x2f32(aone, ndl, s0, 0, 0, 0);
      s1 = __builtin_amdgcn_mfma_f32_32x32x2f32(aone, ndl, s1, 0, 0, 0);
      negm = __builtin_amdgcn_mfma_f32_32x32x2f32(aone, ndl, negm, 0, 0, 0);
      __builtin_amdgcn_s_setprio(0);
      if (t > 0) {
        const float al = __builtin_amdgcn_exp2f(ndl);
        lsum *= al;
#pragma unroll
        for (int dt = 0; dt < 4; ++dt)
#pragma unroll
          for (int i = 0; i < 16; ++i) o[dt][i] *= al;
      }
    }
    SBAR();
    if (t >= 1) {
#pragma unroll
      for (int it = 0; it < PD; ++it) fr[it] = frag(t + 1, it);
    }
#pragma unroll
    for (int i = 0; i < 16; ++i) { s0[i] = __builtin_amdgcn_exp2f(s0[i]); s1[i] = __builtin_amdgcn_exp2f(s1[i]); }
    pk[0] = pack8(s0, 0, 1.f); pk[1] = pack8(s0, 1, 1.f); pk[2] = pack8(s1, 0, 1.f); pk[3] = pack8(s1, 1, 1.f);
    {
      float l0 = lsum, l1 = 0.f, l2 = 0.f, l3 = 0.f;
#pragma unroll
      for (int i = 0; i < 16; i += 2) { l0 += s0[i]; l1 += s0[i + 1]; l2 += s1[i]; l3 += s1[i + 1]; }
      lsum = (l0 + l1) + (l2 + l3);
    }
    PIN(pk[0]); PIN(pk[1]); PIN(pk[2]); PIN(pk[3]); PIN(lsum);
    SBAR();
    if (!lag) seam(t);
  }
  __builtin_amdgcn_s_setprio(0);
  STAMP(1, 4);
  {
    const float il = __builtin_amdgcn_rcpf(swap32_add(lsum));
#pragma unroll
    for (int dt = 0; dt < 4; ++dt)
#pragma unroll
      for (int s = 0; s < 2; ++s)
        *(LAS u32x4*)(lds + LDS_X + ximg(16 * hh + 4 * dt + 2 * s + c.h, 32 * rt + c.r)) = pack8(o[dt], s, il);
  }
  BAR_L();
  STAMP(1, 5);
  dma16(lds0 + LDS_D + w * 1024u, l * 16u, srdW, W_MOD + 4096u + w * 1024u);
  {
    LaneC c; lane_consts(c);
    f32x16 acc[2][2] = {};
    gemm_phase<32, 1>(acc, lds, ringoff, ring, vo, srdO, so0, 1024u, c);
    STAMP(1, 6);
    const unsigned dst = W_P1 + ((((pn * 8 + (c.r >> 3)) * 8 + g) * 64 + 8 * w + c.h) * 8 + (c.r & 7)) * 16u;
#pragma unroll
    for (int nt = 0; nt < 2; ++nt)
#pragma unroll
      for (int r2 = 0; r2 < 2; ++r2)
#pragma unroll
        for (int s = 0; s < 2; ++s)
          __builtin_amdgcn_raw_buffer_store_b128(pack8(acc[nt][r2], s, 1.f), srdW, dst + ((4 * nt + 2 * s) * 8 + r2 * 4 * 8 * 64 * 8) * 16u, 0, 16);
  }
  WAIT_BAR(0);
  if (tid == 0) __builtin_amdgcn_raw_buffer_store_b32(1u, srdW, W_FLAG + (pn * 8 + g) * 64u, 0, 16);
  STAMP(1, 7);
}

DI void ln8(float (&v)[8], LAS float* red  , unsigned tid) {
  float s = ((v[0] + v[1]) + (v[2] + v[3])) + ((v[4] + v[5]) + (v[6] + v[7]));
  float q = (__builtin_fmaf(v[0], v[0], v[1] * v[1]) + __builtin_fmaf(v[2], v[2], v[3] * v[3])) +
            (__builtin_fmaf(v[4], v[4], v[5] * v[5]) + __builtin_fmaf(v[6], v[6], v[7] * v[7]));
  s = sum_hi3(s); q = sum_hi3(q);
  if ((tid & 63) < 8) *(LAS f32x2*)(red + ((tid >> 6) * 8 + (tid & 7)) * 2) = (f32x2){s, q};
  BAR_L();
  f32x2 e[8];
#pragma unroll
  for (int i = 0; i < 8; ++i) e[i] = *(const LAS f32x2*)(red + (i * 8 + (tid & 7)) * 2);
  const float mu = (((e[0][0] + e[1][0]) + (e[2][0] + e[3][0])) + ((e[4][0] + e[5][0]) + (e[6][0] + e[7][0]))) * (1.0f / D);
  const float ms = (((e[0][1] + e[1][1]) + (e[2][1] + e[3][1])) + ((e[4][1] + e[5][1]) + (e[6][1] + e[7][1]))) * (1.0f / D);
  const float rs = __builtin_amdgcn_rsqf(__builtin_fmaxf(__builtin_fmaf(-mu, mu, ms), 0.f) + LN_EPS);
  const float nb = -mu * rs;
#pragma unroll
  for (int k = 0; k < 8; ++k) v[k] = __builtin_fmaf(v[k], rs, nb);
}
DI void red4_put(const f32x16& a, int nt, lds_ptr scr, unsigned rt, unsigned kq, unsigned l) {
#pragma unroll
  for (int g = 0; g < 2; ++g)
#pragma unroll
    for (int i = 0; i < 2; ++i) {
      const int o = 8 * g + 4 * i;
      *(LAS f32x4*)(scr + ((rt * 4 + 2 * nt + g) * 4 + kq) * 2048 + i * 1024 + l * 16) = (f32x4){a[o], a[o + 1], a[o + 2], a[o + 3]};
    }
}
DI void red4_get(float (&v)[8], lds_cptr scr, unsigned rt, unsigned kq, unsigned l) {
  f32x4 t[4][2];
#pragma unroll
  for (int s = 0; s < 4; ++s)
#pragma unroll
    for (int i = 0; i < 2; ++i) t[s][i] = *(const LAS f32x4*)(scr + ((rt * 4 + kq) * 4 + s) * 2048 + i * 1024 + l * 16);
#pragma unroll
  for (int i = 0; i < 2; ++i)
#pragma unroll
    for (int k = 0; k < 4; ++k) v[4 * i + k] = (t[0][i][k] + t[1][i][k]) + (t[2][i][k] + t[3][i][k]);
}
DI void tile_mm8(const bf16x8 (&af)[8][2], const u32x4 (&xb)[8], float (&v)[8], lds_ptr scr, unsigned rt, unsigned kq, unsigned l) {
  f32x16 a0 = {}, a1 = {};
#pragma unroll
  for (int i = 0; i < 8; ++i) a0 = MFMA32(af[i][0], __builtin_bit_cast(bf16x8, xb[i]), a0);
  SBAR();
#pragma unroll
  for (int i = 0; i < 4; ++i) a1 = MFMA32(af[i][1], __builtin_bit_cast(bf16x8, xb[i]), a1);
  SBAR();
  red4_put(a0, 0, scr, rt, kq, l);
#pragma unroll
  for (int i = 4; i < 8; ++i) a1 = MFMA32(af[i][1], __builtin_bit_cast(bf16x8, xb[i]), a1);
  SBAR();
  red4_put(a1, 1, scr, rt, kq, l);
  BAR_L();
  red4_get(v, scr, rt, kq, l);
}

struct Frag3 { bf16x8 a, b[2]; };
constexpr int RING3 = 13;
constexpr int LA = 0, LB = 65536, LC = 131072;
DI unsigned r3off(int s, unsigned rc, unsigned rb) { return s < 4 ? rc + s * 1024u : rb + (s - 4) * 1024u; }
template <int KS> DI void load_frags3(Frag3& f, lds_cptr lds, unsigned rc, unsigned rb, unsigned a3, const LaneC& c) {
  const lds_cptr sl = lds + r3off(KS % RING3, rc, rb) + a3;
  f.a = cat8(lds_tr(sl), lds_tr(sl + 256));
#pragma unroll
  for (int rt = 0; rt < 2; ++rt) f.b[rt] = lds_b128(lds + LA + c.xb[KS & 3] + 2048 * KS + 512 * rt);
}
template <int KS>
DI void g3_step(f32x16 (&acc)[2], Frag3& cur, Frag3& n1, Frag3& n2, lds_cptr lds, unsigned lds0, unsigned rc, unsigned rb,
                unsigned voff, __amdgpu_buffer_rsrc_t srd, unsigned soff0, unsigned a3, const LaneC& c) {
  constexpr int NKS = 32;
  if constexpr (KS + 2 < NKS) {
    constexpr int last = (KS + RING3 < NKS ? KS + RING3 : NKS) - 1;
    wait_vm<last - (KS + 2)>();
    load_frags3<KS + 2>(n2, lds, rc, rb, a3, c);
  }
  SBAR();
  acc[0] = MFMA32(cur.a, cur.b[0], acc[0]);
  acc[1] = MFMA32(cur.a, cur.b[1], acc[1]);
  if constexpr (KS + RING3 < NKS) {
    asm volatile("" :: "v"(cur.a));
    dma16(lds0 + r3off(KS % RING3, rc, rb), voff, srd, soff0 + (unsigned)(16 * (KS + RING3)) * 3584u);
  }
  SBAR();
  if constexpr (KS + 1 < NKS) g3_step<KS + 1>(acc, n1, n2, cur, lds, lds0, rc, rb, voff, srd, soff0, a3, c);
}
DI void mlp_part(const Params& P, lds_ptr lds, unsigned lds0, unsigned w, __amdgpu_buffer_rsrc_t srdS) {
  LaneC c; lane_consts(c);
  const unsigned idx = BIDX >> 3, pn = 8 * ((BIDX & 7) >> 1) + (idx >> 2), j = 4 * (BIDX & 1) + (idx & 3);
  const unsigned tid = TIDX, l = c.lane;
  const unsigned vw = wdma_voff(l, 1024u);
  const unsigned rt = w & 1, kq = w >> 1;
  constexpr int LMOD = LDS_D;
  constexpr int LRED = LC + 28672;
  const unsigned prow = 32 * rt + c.r;
  const unsigned trow = pn * 64 + prow;
  const unsigned och = 8 * j + 2 * kq + c.h;
  const unsigned ovo = ((pn * 64 + och) * 64 + prow) * 16u;
  float gb[8];
  STAMP(2, 0);

  {
    const unsigned row = pn * 64 + j * 8 + (tid & 7), ch = tid >> 3;
    u32x4 xr;
    gld16(xr, P.x + (size_t)row * D + ch * 8);
    auto wait_flags = [&](unsigned fo) {
      for (unsigned spin = 0;; ++spin) {
        asm volatile("" ::: "memory");
        const unsigned f = __builtin_amdgcn_raw_buffer_load_b32(srdS, fo, 0, 16);
        if (!ANY(fo != OOB && f != 1u) || spin > SPIN_CAP) break;
        __builtin_amdgcn_s_sleep(FLAG_SLEEP);
      }
    };
    const __amdgpu_buffer_rsrc_t srd1 = mksrd(P.w1, 512u * 512u * 2u), srd2 = mksrd(P.w2, 512u * 512u * 2u);
#pragma unroll
    for (int i = 0; i < 8; ++i) dma16(lds0 + LA + (8 * w + i) * 1024u, vw, srd1, (8 * w + i) * 8192u + j * 128u);
    {
      const int fp = (int)pn - 1 + (int)(l >> 3);
      wait_flags((l < 24 && fp >= 0 && fp < 32) ? W_FLAG + ((unsigned)fp * 8 + (l & 7)) * 64u : OOB);
    }
    STAMP(2, 15);
    u32x4 p[8];
#pragma unroll
    for (int g = 0; g < 8; ++g) bld16_sc1(p[g], tid * 16u, srdS, W_P1 + ((pn * 8 + j) * 8 + g) * 8192u);
    float ga[8], sb[8], cb[8], g2[8];
    {
      u32x4 mv[10];
#pragma unroll
      for (int i = 0; i < 8; ++i) mv[i] = *(const LAS u32x4*)(lds + LMOD + (i >> 1) * 2048 + ch * 32 + 16 * (i & 1));
#pragma unroll
      for (int i = 0; i < 2; ++i) mv[8 + i] = *(const LAS u32x4*)(lds + LMOD + 6144 + och * 32 + 16 * i);
#pragma unroll
      for (int k = 0; k < 8; ++k) {
        ga[k] = U2F(mv[k >> 2][k & 3]); sb[k] = U2F(mv[2 + (k >> 2)][k & 3]); cb[k] = 1.f + U2F(mv[4 + (k >> 2)][k & 3]);
        g2[k] = U2F(mv[6 + (k >> 2)][k & 3]); gb[k] = U2F(mv[8 + (k >> 2)][k & 3]);
      }
    }
    asm volatile("s_waitcnt vmcnt(0)" : "+v"(xr), "+v"(p[0]), "+v"(p[1]), "+v"(p[2]), "+v"(p[3]), "+v"(p[4]), "+v"(p[5]),
                 "+v"(p[6]), "+v"(p[7]) :: "memory");
    STAMP(2, 1);
    float v[8], x2[8];
#pragma unroll
    for (int i = 0; i < 4; ++i) {
      const float lo = ((bflo(p[0][i]) + bflo(p[1][i])) + (bflo(p[2][i]) + bflo(p[3][i]))) + ((bflo(p[4][i]) + bflo(p[5][i])) + (bflo(p[6][i]) + bflo(p[7][i])));
      const float hi = ((bfhi(p[0][i]) + bfhi(p[1][i])) + (bfhi(p[2][i]) + bfhi(p[3][i]))) + ((bfhi(p[4][i]) + bfhi(p[5][i])) + (bfhi(p[6][i]) + bfhi(p[7][i])));
      v[2 * i] = x2[2 * i] = __builtin_fmaf(ga[2 * i], lo, bflo(xr[i]));
      v[2 * i + 1] = x2[2 * i + 1] = __builtin_fmaf(ga[2 * i + 1], hi, bfhi(xr[i]));
    }
    ln8(v, (LAS float*)(lds + LRED), tid);
    float hb[8];
#pragma unroll
    for (int k = 0; k < 8; ++k) v[k] = hb[k] = __builtin_fmaf(v[k], cb[k], sb[k]);
    ln8(v, (LAS float*)(lds + LRED + 512), tid);
    const unsigned vo = ((pn * 64 + ch) * 64 + j * 8 + (tid & 7)) * 16u, vz = ((pn * 64 + ch) * 256 + j * 8 + (tid & 7)) * 16u;
    pub16(srdS, vo, S_U, (u32x4){cvtpk(v[0], v[1]), cvtpk(v[2], v[3]), cvtpk(v[4], v[5]), cvtpk(v[6], v[7])});
#pragma unroll
    for (int i = 0; i < 2; ++i) {
      pub16(srdS, vz, S_HZ + i * 1024u, (u32x4){F2U(hb[4 * i]), F2U(hb[4 * i + 1]),
                                                F2U(hb[4 * i + 2]), F2U(hb[4 * i + 3])});
      f32x4 z;
#pragma unroll
      for (int e = 0; e < 4; ++e) z[e] = __builtin_fmaf(g2[4 * i + e], hb[4 * i + e], x2[4 * i + e]);
      pub16(srdS, vz, S_HZ + (2 + i) * 1024u, __builtin_bit_cast(u32x4, z));
    }
#pragma unroll
    for (int i = 0; i < 8; ++i) dma16(lds0 + LB + (8 * w + i) * 1024u, vw, srd2, (8 * w + i) * 8192u + j * 128u);
  }
  STAMP(2, 2);
  const __amdgpu_buffer_rsrc_t srd3 = mksrd(P.w3, 512u * F * 2u);
  const unsigned rc = LC + w * 4096u, rb = LB + w * 9216u;
  const unsigned v3 = (l >> 2) * 3584u + ((l & 3) << 4), s30 = (j * 224u + w * 32u) * 2u;
  float v[8];
  {
    bf16x8 af[8][2];
#pragma unroll
    for (int i = 0; i < 8; ++i)
#pragma unroll
      for (int nt = 0; nt < 2; ++nt) {
        const lds_cptr a = lds + LA + (8 * kq + i) * 2048 + c.afr[nt];
        af[i][nt] = cat8(lds_tr(a), lds_tr(a + 512));
      }
    BAR_L();
    u32x4 xb[8];
    poll_ind<8, 3, 7>(xb, srdS, ((pn * 64 + 16 * kq + c.h) * 64 + prow) * 16u, S_U, 2048u);
    STAMP(2, 3);
    tile_mm8(af, xb, v, lds + LA, rt, kq, l);
#pragma unroll
    for (int k = 0; k < 8; ++k) v[k] = gelu_tanh(v[k]);
    pub16(srdS, ovo, S_H1, (u32x4){cvtpk(v[0], v[1]), cvtpk(v[2], v[3]), cvtpk(v[4], v[5]), cvtpk(v[6], v[7])});
    if (w < 7) {
#pragma unroll
      for (int ks = 0; ks < 4; ++ks) dma16(lds0 + r3off(ks, rc, rb), v3, srd3, s30 + (unsigned)(16 * ks) * 3584u);
    }
  }
  STAMP(2, 4);
  float y[8];
  u32x4 cs[4];
  const unsigned cso = w < 7 ? (224 * j + 32 * w + 8 * c.h) * 4u : OOB;
  {
    bf16x8 af[8][2];
#pragma unroll
    for (int i = 0; i < 8; ++i)
#pragma unroll
      for (int nt = 0; nt < 2; ++nt) {
        const lds_cptr a = lds + LB + (8 * kq + i) * 2048 + c.afr[nt];
        af[i][nt] = cat8(lds_tr(a), lds_tr(a + 512));
      }
    BAR_L();
    u32x4 hz[4];
    const unsigned hzo = ((pn * 64 + och) * 256 + prow) * 16u;
#pragma unroll
    for (int i = 0; i < 4; ++i) hz[i] = __builtin_amdgcn_raw_buffer_load_b128(srdS, hzo, S_HZ + i * 1024u, 16);
#pragma unroll
    for (int i = 0; i < 4; ++i) cs[i] = __builtin_amdgcn_raw_buffer_load_b128(srdS, cso, W_MOD + 12288u + 64u * (i >> 1) + 16u * (i & 1), 0);
    const int sh = kq == 0 ? -1 : kq == 1 ? 1 : kq == 2 ? -2 : 2;
    const int srow = (int)trow + sh;
    const unsigned xvo = (srow >= 0 && srow < T) ? ((((unsigned)srow >> 6) * 64 + 16 * kq + c.h) * 64 + ((unsigned)srow & 63)) * 16u : OOB;
    u32x4 xb[8];
    poll_ind<8, 3, 7>(xb, srdS, xvo, S_H1, 2048u);
    STAMP(2, 5);
    tile_mm8(af, xb, v, lds + LA, rt, kq, l);
    if (has_sent(hz, 4)) poll_load<4>(hz, srdS, hzo, S_HZ, 1024u);
    float x1[8];
#pragma unroll
    for (int k = 0; k < 8; ++k) {
      x1[k] = U2F(hz[k >> 2][k & 3]) + v[k];
      y[k] = __builtin_fmaf(gb[k], v[k], U2F(hz[2 + (k >> 2)][k & 3]));
    }
    pub16(srdS, ovo, S_X1, (u32x4){cvtpk(x1[0], x1[1]), cvtpk(x1[2], x1[3]), cvtpk(x1[4], x1[5]), cvtpk(x1[6], x1[7])});
    if (w < 7) {
#pragma unroll
      for (int ks = 4; ks < RING3; ++ks) dma16(lds0 + r3off(ks, rc, rb), v3, srd3, s30 + (unsigned)(16 * ks) * 3584u);
    }
  }
  STAMP(2, 6);
  {
    u32x4 xr[8];
    BAR_L();
    poll_ind<8, 3, 7>(xr, srdS, ((pn * 64 + 8 * w) * 64 + l) * 16u, S_X1, 1024u);
    STAMP(2, 7);
#pragma unroll
    for (int i = 0; i < 8; ++i) *(LAS u32x4*)(lds + LA + ximg(8 * w + i, l)) = xr[i];
    {
      float s0 = 0.f, s1 = 0.f, q0 = 0.f, q1 = 0.f;
#pragma unroll
      for (int i = 0; i < 8; ++i)
#pragma unroll
        for (int k = 0; k < 4; k += 2) {
          s0 = dot2bf(xr[i][k], 0x3F803F80u, s0); s1 = dot2bf(xr[i][k + 1], 0x3F803F80u, s1);
          q0 = dot2bf(xr[i][k], xr[i][k], q0); q1 = dot2bf(xr[i][k + 1], xr[i][k + 1], q1);
        }
      *(LAS f32x2*)(lds + LRED + (w * 64 + l) * 8) = (f32x2){s0 + s1, q0 + q1};
    }
    BAR_L();
    STAMP(2, 8);
    if (w < 7) {
      f32x16 acc[2] = {};
      const unsigned a3 = (8 * c.h + c.q) * 64 + 32 * c.blk + 8 * (2 * (c.p & 1) + (c.p >> 1));
      Frag3 f0, f1, f2;
      wait_vm<RING3 - 2>();
      load_frags3<0>(f0, lds, rc, rb, a3, c);
      load_frags3<1>(f1, lds, rc, rb, a3, c);
      float mu[2], rs[2];
#pragma unroll
      for (int r2 = 0; r2 < 2; ++r2) {
        float s = 0.f, q = 0.f;
#pragma unroll
        for (int i = 0; i < 8; ++i) {
          const f32x2 t = *(const LAS f32x2*)(lds + LRED + (i * 64 + 32 * r2 + c.r) * 8);
          s += t[0]; q += t[1];
        }
        mu[r2] = s * (1.0f / D);
        rs[r2] = __builtin_amdgcn_rsqf(__builtin_fmaxf(__builtin_fmaf(-mu[r2], mu[r2], q * (1.0f / D)), 0.f) + LN_EPS);
      }
      if (w < 4) __builtin_amdgcn_s_setprio(2); else __builtin_amdgcn_s_setprio(1);
      g3_step<0>(acc, f0, f1, f2, lds, lds0, rc, rb, v3, srd3, s30, a3, c);
      __builtin_amdgcn_s_setprio(0);
      STAMP(2, 9);
#pragma unroll
      for (int r2 = 0; r2 < 2; ++r2) {
#pragma unroll
        for (int i = 0; i < 16; ++i) acc[r2][i] = gelu_tanh(rs[r2] * __builtin_fmaf(-mu[r2], U2F(cs[i >> 2][i & 3]), acc[r2][i]));
#pragma unroll
        for (int s2 = 0; s2 < 2; ++s2)
          pub16(srdS, ((pn * 224 + 28 * j + 4 * w + 2 * s2 + c.h) * 64 + 32 * r2 + c.r) * 16u, S_H3, pack8(acc[r2], s2, 1.f));
      }
    }
    BAR_L();
  }
  STAMP(2, 10);
  {
    const __amdgpu_buffer_rsrc_t srd4 = mksrd(P.w4, (unsigned)F * 512u * 2u);
    const unsigned qb = kq * 40960u;
    const unsigned s40 = (448u * kq + 8u * rt) * 1024u + j * 128u;
    auto w4dma = [&](int ks) { dma16(lds0 + qb + (ks % 20) * 2048u + rt * 1024u, vw, srd4, s40 + (unsigned)ks * 16384u); };
#pragma unroll
    for (int ks = 0; ks < 8; ++ks) w4dma(ks);
    const unsigned hvo = ((pn * 224 + 56 * kq + c.h) * 64 + prow) * 16u;
    u32x4 ha[14], hb[14];
    poll_ind<14, 9, 13>(ha, srdS, hvo, S_H3, 2048u);
    BAR_L();
    STAMP(2, 11);
    f32x16 acc[2] = {};
    bf16x8 fa[2][2];
    auto ld = [&](int ks) {
#pragma unroll
      for (int nt = 0; nt < 2; ++nt) {
        const lds_cptr a = lds + qb + (ks % 20) * 2048 + c.afr[nt];
        fa[ks & 1][nt] = cat8(lds_tr(a), lds_tr(a + 512));
      }
    };
    auto mm = [&](int ks, const u32x4& b) {
#pragma unroll
      for (int nt = 0; nt < 2; ++nt) acc[nt] = MFMA32(fa[ks & 1][nt], __builtin_bit_cast(bf16x8, b), acc[nt]);
    };
    auto hbl = [&](int i) { bld16_sc1(hb[i], hvo, srdS, S_H3 + (14u + i) * 2048u); };
    ld(0);
#pragma unroll
    for (int ks = 0; ks < 8; ++ks) {
      if (ks < 7) ld(ks + 1);
      SBAR(); mm(ks, ha[ks]);
      if (ks < 6) { w4dma(8 + ks); hbl(2 * ks); hbl(2 * ks + 1); }
      else if (ks == 6) { hbl(12); hbl(13); w4dma(14); w4dma(15); w4dma(16); }
      else { w4dma(17); w4dma(18); w4dma(19); }
      SBAR();
    }
    WAIT_BAR(10);
    ld(8);
#pragma unroll
    for (int ks = 8; ks < 14; ++ks) {
      if (ks < 13) ld(ks + 1);
      SBAR(); mm(ks, ha[ks]);
      if (ks < 12) { w4dma(20 + 2 * (ks - 8)); w4dma(21 + 2 * (ks - 8)); }
      SBAR();
    }
    asm volatile("s_waitcnt vmcnt(8) lgkmcnt(0)\n\ts_barrier" : "+v"(hb[0]), "+v"(hb[1]), "+v"(hb[2]), "+v"(hb[3]), "+v"(hb[4]), "+v"(hb[5]),
                 "+v"(hb[6]), "+v"(hb[7]), "+v"(hb[8]), "+v"(hb[9]), "+v"(hb[10]), "+v"(hb[11]), "+v"(hb[12]), "+v"(hb[13]) :: "memory");
    if (has_sent(hb, 14)) poll_load<14>(hb, srdS, hvo, S_H3 + 14u * 2048u, 2048u);
    STAMP(2, 12);
    ld(14);
#pragma unroll
    for (int ks = 14; ks < 19; ++ks) { ld(ks + 1); SBAR(); mm(ks, hb[ks - 14]); SBAR(); }
    mm(19, hb[5]);
    WAIT_BAR(0);
    ld(20);
#pragma unroll
    for (int ks = 20; ks < 27; ++ks) { ld(ks + 1); SBAR(); mm(ks, hb[ks - 14]); SBAR(); }
    mm(27, hb[13]);
    BAR_L();
    STAMP(2, 13);
    red4_put(acc[0], 0, lds + LA, rt, kq, l);
    red4_put(acc[1], 1, lds + LA, rt, kq, l);
    BAR_L();
    red4_get(v, lds + LA, rt, kq, l);
#pragma unroll
    for (int k = 0; k < 8; ++k) v[k] = __builtin_fmaf(gb[k], v[k], y[k]);
    P.out[(size_t)trow * 64 + och] = (u32x4){cvtpk(v[0], v[1]), cvtpk(v[2], v[3]), cvtpk(v[4], v[5]), cvtpk(v[6], v[7])};
  }
  STAMP(2, 14);
}

__global__ __launch_bounds__(512, 2) void k_fused(Params P) {
  extern __shared__ __attribute__((aligned(16))) char smem[];
  const lds_ptr lds = (lds_ptr)smem;
  const unsigned lds0 = (unsigned)(uintptr_t)smem;
  const unsigned w = rfl(TIDX >> 6);
  const __amdgpu_buffer_rsrc_t srdW = mksrd(P.ws, W_END);
  attn_part(P, lds, lds0, w, srdW);
  mlp_part(P, lds, lds0, w, srdW);
}

#if defined(__HIP_DEVICE_COMPILE__)
#pragma clang attribute pop
#endif
extern "C" void kernel_launch(void* const* d_in, const int* in_sizes, int n_in, void* d_out, int out_size, void* d_ws,
                              size_t ws_size, hipStream_t stream) {
  Params P;
  P.x = (const unsigned short*)d_in[0];
  P.kc = (const unsigned short*)d_in[1];
  P.vc = (const unsigned short*)d_in[2];
  P.wq = (const unsigned short*)d_in[3];
  P.wo = (const unsigned short*)d_in[4];
  P.w1 = (const unsigned short*)d_in[6];
  P.w2 = (const unsigned short*)d_in[7];
  P.w3 = (const unsigned short*)d_in[8];
  P.w4 = (const unsigned short*)d_in[9];
  P.c = (const unsigned short*)d_in[10];
  P.wmod = (const unsigned short*)d_in[11];
  P.ws = d_ws;
  P.out = (u32x4*)d_out;
  (void)hipFuncSetAttribute((const void*)k_fused, hipFuncAttributeMaxDynamicSharedMemorySize, LDS_TOTAL);
  k_init<<<153, 512, 0, stream>>>(P.c, P.wmod, P.w3, d_ws);
  k_fused<<<256, 512, LDS_TOTAL, stream>>>(P);
}
```

```cpp
#include <hip/hip_runtime.h>
#include <hip/hip_bf16.h>
#include <stdint.h>

#if defined(__HIP_DEVICE_COMPILE__)
#pragma clang attribute push(__attribute__((target("no-packed-fp32-ops"))), apply_to = function)
#endif
typedef short bf16x8 __attribute__((ext_vector_type(8)));
typedef short s16x4 __attribute__((ext_vector_type(4)));
typedef float f32x16 __attribute__((ext_vector_type(16)));
typedef float f32x4 __attribute__((ext_vector_type(4)));
typedef float f32x2 __attribute__((ext_vector_type(2)));
typedef unsigned u32x4 __attribute__((ext_vector_type(4)));
typedef unsigned u32x2 __attribute__((ext_vector_type(2)));
typedef __bf16 bf16x2_t __attribute__((ext_vector_type(2)));
typedef short v4i16_t __attribute__((ext_vector_type(4)));
#define LAS __attribute__((address_space(3)))
typedef LAS const char* lds_cptr;
typedef LAS char* lds_ptr;
#define DI __device__ __forceinline__
#define F2U(x) __builtin_bit_cast(unsigned, (float)(x))
#define U2F(x) __builtin_bit_cast(float, (unsigned)(x))
#define F2I(x) __builtin_bit_cast(int, (float)(x))
#define I2F(x) __builtin_bit_cast(float, (int)(x))
#define TIDX __builtin_amdgcn_workitem_id_x()
#define BIDX __builtin_amdgcn_workgroup_id_x()
#define ANY(p) (__builtin_amdgcn_ballot_w64(p) != 0ull)
#define SBAR() __builtin_amdgcn_sched_barrier(0)
#define MFMA32(a, b, c) __builtin_amdgcn_mfma_f32_32x32x16_bf16((a), (b), (c), 0, 0, 0)
#define WAIT_V(n) asm volatile("s_waitcnt vmcnt(" #n ")" ::: "memory")
#define WAIT_L0() asm volatile("s_waitcnt lgkmcnt(0)" ::: "memory")
#define BAR_L() asm volatile("s_waitcnt lgkmcnt(0)\n\ts_barrier" ::: "memory")
#define WAIT_BAR(n) asm volatile("s_waitcnt vmcnt(" #n ") lgkmcnt(0)\n\ts_barrier" ::: "memory")

constexpr int T = 2048, D = 512, HKV = 8, SK = 2048, DH = 128, WIN = 512, F = 1792;
constexpr float LN_EPS = 1e-5f;
constexpr float QSCALE = 0.088388347648318440f * 1.4426950408889634f;

constexpr unsigned W_MOD = 0;
constexpr unsigned W_FLAG = 32768;
constexpr unsigned W_P1 = 65536;
constexpr unsigned S_U = W_P1 + 8u * 64u * T * 16u;
constexpr unsigned S_H1 = S_U + 64u * T * 16u;
constexpr unsigned S_X1 = S_H1 + 64u * T * 16u;
constexpr unsigned S_HZ = S_X1 + 64u * T * 16u;
constexpr unsigned S_H3 = S_HZ + 256u * T * 16u;
constexpr unsigned W_END = S_H3 + 224u * T * 16u;
static_assert(W_FLAG + 256 * 64 <= W_P1, "ws");
constexpr unsigned OOB = 0xF0000000u;
#define K1MAP 0
#define FLAG_SLEEP 4
#define NPRE 20
#define ATHR 32.0f
constexpr float THR = ATHR;
constexpr unsigned SENT = 0xFFFFFFFFu;
constexpr unsigned SPIN_CAP = 1u << 16;

constexpr int LDS_X = 0;
constexpr int LDS_R = 65536;
constexpr int RING = 5, RINGB = RING * 2048;
constexpr int LDS_D = LDS_R + 8 * RINGB;
constexpr int LDS_TOTAL = LDS_D + 16384;
static_assert(LDS_TOTAL == 163840, "lds");

#define STAMP(kid, i) do {} while (0)
DI unsigned cvtpk(float lo, float hi) {
  f32x2 v = {lo, hi};
  bf16x2_t b = __builtin_convertvector(v, bf16x2_t);
  return __builtin_bit_cast(unsigned, b);
}
DI float bflo(unsigned u) { return U2F(u << 16); }
DI float bfhi(unsigned u) { return U2F(u & 0xffff0000u); }
DI unsigned rfl(unsigned x) { return __builtin_amdgcn_readfirstlane(x); }
DI __amdgpu_buffer_rsrc_t mksrd(const void* p, unsigned bytes) {
  return __builtin_amdgcn_make_buffer_rsrc((void*)p, (short)0, (int)bytes, 0x00020000);
}
DI void dma16(unsigned lds, unsigned voff, __amdgpu_buffer_rsrc_t srd, unsigned soff) {
  asm volatile("s_mov_b32 m0, %0\n\ts_nop 0\n\tbuffer_load_dwordx4 %1, %2, %3 offen lds"
               :: "s"(lds), "v"(voff), "s"(srd), "s"(soff) : "memory");
}
DI void gld16(u32x4& d, const void* p) { asm volatile("global_load_dwordx4 %0, %1, off" : "=v"(d) : "v"(p) : "memory"); }
DI void bld16_sc1(u32x4& d, unsigned voff, __amdgpu_buffer_rsrc_t srd, unsigned soff) {
  asm volatile("buffer_load_dwordx4 %0, %1, %2, %3 offen sc1" : "=v"(d) : "v"(voff), "s"(srd), "s"(soff) : "memory");
}
DI void dma16_sc1(unsigned lds, unsigned voff, __amdgpu_buffer_rsrc_t srd, unsigned soff) {
  asm volatile("s_mov_b32 m0, %0\n\ts_nop 0\n\tbuffer_load_dwordx4 %1, %2, %3 offen sc1 lds"
               :: "s"(lds), "v"(voff), "s"(srd), "s"(soff) : "memory");
}
DI bf16x8 lds_b128(lds_cptr p) { return *(const LAS bf16x8*)p; }
DI s16x4 lds_tr(lds_cptr p) {
  return __builtin_bit_cast(s16x4, __builtin_amdgcn_ds_read_tr16_b64_v4i16((LAS v4i16_t*)p));
}
DI bf16x8 cat8(s16x4 lo, s16x4 hi) { return __builtin_shufflevector(lo, hi, 0, 1, 2, 3, 4, 5, 6, 7); }
DI float swap32_add(float x) {
  auto r = __builtin_amdgcn_permlane32_swap(F2U(x), F2U(x), false, false);
  return U2F(r[0]) + U2F(r[1]);
}
DI float swap32_max(float x) {
  auto r = __builtin_amdgcn_permlane32_swap(F2U(x), F2U(x), false, false);
  return __builtin_fmaxf(U2F(r[0]), U2F(r[1]));
}
template <int CTRL> DI float dpp_add(float x) {
  int y = __builtin_amdgcn_update_dpp(0, F2I(x), CTRL, 0xF, 0xF, true);
  return x + I2F(y);
}
DI float max3f(float a, float b, float c) { float r; asm("v_max3_f32 %0, %1, %2, %3" : "=v"(r) : "v"(a), "v"(b), "v"(c)); return r; }
DI float max2f(float a, float b) { float r; asm("v_max_f32_e32 %0, %1, %2" : "=v"(r) : "v"(a), "v"(b)); return r; }
DI float dot2bf(unsigned a, unsigned b, float c) {
  return __builtin_amdgcn_fdot2_f32_bf16(__builtin_bit_cast(bf16x2_t, a), __builtin_bit_cast(bf16x2_t, b), c, false);
}
#define PIN(x) asm volatile("" : "+v"(x))
DI float swap16_add(float x) {
  auto r = __builtin_amdgcn_permlane16_swap(F2U(x), F2U(x), false, false);
  return U2F(r[0]) + U2F(r[1]);
}
DI float sum_hi3(float x) { return swap32_add(swap16_add(dpp_add<0x128>(x))); }
DI float gelu_tanh(float x) {
  const float k0 = -2.0f * 0.7978845608028654f * 1.4426950408889634f;
  const float t = x * x;
  const float z = x * __builtin_fmaf(t, 0.044715f * k0, k0);
  const float e = __builtin_amdgcn_exp2f(z);
  return x * __builtin_amdgcn_rcpf(1.0f + e);
}
DI void wave_sum8(const float (&a)[8], float (&out)[8]) {
  float b[4], c[2];
#pragma unroll
  for (int i = 0; i < 4; ++i) {
    auto r = __builtin_amdgcn_permlane32_swap(F2U(a[i]), F2U(a[i + 4]), false, false);
    b[i] = U2F(r[0]) + U2F(r[1]);
  }
#pragma unroll
  for (int i = 0; i < 2; ++i) {
    auto r = __builtin_amdgcn_permlane16_swap(F2U(b[i]), F2U(b[i + 2]), false, false);
    c[i] = U2F(r[0]) + U2F(r[1]);
  }
#pragma unroll
  for (int i = 0; i < 2; ++i) {
    c[i] = dpp_add<0xB1>(c[i]);
    c[i] = dpp_add<0x4E>(c[i]);
    c[i] = dpp_add<0x141>(c[i]);
    c[i] = dpp_add<0x140>(c[i]);
  }
#pragma unroll
  for (int q = 0; q < 4; ++q)
#pragma unroll
    for (int i = 0; i < 2; ++i)
      out[i + 2 * (q & 1) + 4 * (q >> 1)] = I2F(__builtin_amdgcn_readlane(F2I(c[i]), 16 * q));
}

DI unsigned nosent(unsigned v) { return v < 0xFFFFFFFEu ? v : 0xFFFFFFFEu; }
DI void pub16(__amdgpu_buffer_rsrc_t srd, unsigned voff, unsigned soff, u32x4 v) {
  v = (u32x4){nosent(v[0]), nosent(v[1]), nosent(v[2]), nosent(v[3])};
  __builtin_amdgcn_raw_buffer_store_b128(v, srd, voff + soff, 0, 16);
}
DI unsigned umax3(unsigned a, unsigned b, unsigned c) { const unsigned m = a > b ? a : b; return m > c ? m : c; }
DI bool has_sent(const u32x4* v, int n) {
  unsigned m = 0;
#pragma unroll
  for (int i = 0; i < n; ++i) { m = umax3(m, v[i][0], v[i][1]); m = umax3(m, v[i][2], v[i][3]); }
  return ANY(m == SENT);
}
template <int N> DI void poll_load(u32x4* v, __amdgpu_buffer_rsrc_t srd, unsigned voff, unsigned soff0, unsigned step) {
  for (unsigned spin = 0;; ++spin) {
    asm volatile("" ::: "memory");
#pragma unroll
    for (int i = 0; i < N; ++i) v[i] = __builtin_amdgcn_raw_buffer_load_b128(srd, voff, soff0 + (unsigned)i * step, 16);
    if (!has_sent(v, N) || spin > SPIN_CAP) break;
    __builtin_amdgcn_s_sleep(1);
  }
}
template <int N, int I0, int I1> DI void poll_ind(u32x4* v, __amdgpu_buffer_rsrc_t srd, unsigned voff, unsigned soff0, unsigned step) {
#pragma unroll
  for (int i = 0; i < N; ++i) v[i] = __builtin_amdgcn_raw_buffer_load_b128(srd, voff, soff0 + (unsigned)i * step, 16);
  if (has_sent(v, N)) {
    u32x4 ind[2];
    poll_load<2>(ind, srd, voff, soff0 + (unsigned)I0 * step, (unsigned)(I1 - I0) * step);
    poll_load<N>(v, srd, voff, soff0, step);
  }
}

__global__ __launch_bounds__(512) void k_init(const unsigned short* __restrict__ c, const unsigned short* __restrict__ wmod,
                                              const unsigned short* __restrict__ w3, void* ws) {
  __shared__ __attribute__((aligned(16))) float red[8][32];
  const unsigned tid = TIDX, l = tid & 63, w = tid >> 6;
  STAMP(0, 0);
  if (BIDX >= 152) {
    if (tid < 256) *(unsigned*)((char*)ws + W_FLAG + tid * 64) = 0u;
    return;
  }
  const bool isw3 = BIDX >= 96;
  const unsigned short* src = isw3 ? w3 + (BIDX - 96) * 32 + (tid & 3) * 8 : wmod + BIDX * 32 + (tid & 3) * 8;
  const unsigned ld = isw3 ? F : 3072;
  u32x4 jw[4]; unsigned jc[4];
#pragma unroll
  for (int i = 0; i < 4; ++i) jw[i] = *(const u32x4*)(src + (size_t)((tid >> 2) + 128 * i) * ld);
#pragma unroll
  for (int i = 0; i < 4; ++i) jc[i] = c[(tid >> 2) + 128 * i];
  float acc[8] = {0, 0, 0, 0, 0, 0, 0, 0};
#pragma unroll
  for (int i = 0; i < 4; ++i) {
    const float v = U2F(jc[i] << 16);
    const float sv = v * __builtin_amdgcn_rcpf(1.0f + __builtin_amdgcn_exp2f(-1.4426950408889634f * v));
    const float sc = isw3 ? 1.0f : sv;
#pragma unroll
    for (int k = 0; k < 4; ++k) {
      acc[2 * k] = __builtin_fmaf(sc, bflo(jw[i][k]), acc[2 * k]);
      acc[2 * k + 1] = __builtin_fmaf(sc, bfhi(jw[i][k]), acc[2 * k + 1]);
    }
  }
  asm volatile("" :: "v"(acc[0]), "v"(acc[7]));
  STAMP(0, 1);
#pragma unroll
  for (int k = 0; k < 8; ++k) acc[k] = sum_hi3(dpp_add<0x124>(acc[k]));
  if (l < 4) {
    *(f32x4*)&red[w][l * 8] = (f32x4){acc[0], acc[1], acc[2], acc[3]};
    *(f32x4*)&red[w][l * 8 + 4] = (f32x4){acc[4], acc[5], acc[6], acc[7]};
  }
  BAR_L();
  if (tid < 8) {
    f32x4 t = *(const f32x4*)&red[0][tid * 4];
#pragma unroll
    for (int i = 1; i < 8; ++i) t += *(const f32x4*)&red[i][tid * 4];
    *(f32x4*)((char*)ws + W_MOD + BIDX * 128 + tid * 16) = t;
  }
  STAMP(0, 2);
}

struct LaneC {
  unsigned lane, r, h, q, p, blk;
  unsigned wv;
  unsigned afr[2];
  unsigned xb[4];
};
DI void lane_consts(LaneC& c) {
  unsigned l = TIDX & 63;
  asm volatile("" : "+v"(l));
  c.lane = l; c.r = l & 31; c.h = l >> 5; c.q = (l & 15) >> 2; c.p = l & 3; c.blk = (l >> 4) & 1;
  const unsigned sp = 2 * (c.p & 1) + (c.p >> 1);
#pragma unroll
  for (int nt = 0; nt < 2; ++nt) c.afr[nt] = (8 * c.h + c.q) * 128 + 64 * (nt ^ (c.q >> 1)) + 32 * c.blk + 8 * sp;
#pragma unroll
  for (int s = 0; s < 4; ++s) c.xb[s] = 1024 * c.h + ((c.r ^ (2 * s + c.h)) << 4);
}
DI unsigned wdma_voff(unsigned l, unsigned ld) { return (l >> 3) * ld + (((l & 7) ^ (((l >> 4) & 1) << 2)) << 4); }

DI void wring_issue(int ks, unsigned ring, unsigned voff, __amdgpu_buffer_rsrc_t srd, unsigned soff0, unsigned ld) {
  const unsigned slot = ring + (unsigned)(ks % RING) * 2048u;
  dma16(slot, voff, srd, soff0 + (unsigned)(16 * ks) * ld);
  dma16(slot + 1024u, voff ^ 32u, srd, soff0 + (unsigned)(16 * ks + 8) * ld);
}
template <int N> DI void wait_vm() {
  if constexpr (N == 0) WAIT_V(0); else if constexpr (N == 1) WAIT_V(1); else if constexpr (N == 2) WAIT_V(2);
  else if constexpr (N == 3) WAIT_V(3); else if constexpr (N == 4) WAIT_V(4); else if constexpr (N == 5) WAIT_V(5);
  else if constexpr (N == 6) WAIT_V(6); else if constexpr (N == 7) WAIT_V(7); else if constexpr (N == 8) WAIT_V(8);
  else if constexpr (N == 9) WAIT_V(9); else if constexpr (N == 10) WAIT_V(10); else if constexpr (N == 11) WAIT_V(11);
  else if constexpr (N == 12) WAIT_V(12); else if constexpr (N == 13) WAIT_V(13); else if constexpr (N == 14) WAIT_V(14);
  else if constexpr (N == 15) WAIT_V(15); else if constexpr (N == 16) WAIT_V(16);
  else if constexpr (N == 17) WAIT_V(17); else if constexpr (N == 18) WAIT_V(18);
  else static_assert(N < 0, "wait_vm");
}

#define MFMA16(a, b, c) __builtin_amdgcn_mfma_f32_16x16x32_bf16((a), (b), (c), 0, 0, 0)
struct G16 { unsigned a[4], hi, x[2]; };
DI void g16_consts(G16& k, unsigned l, unsigned ringoff) {
  const unsigned i = l & 15, g = l >> 4, q = i >> 2, p = l & 3, x = 2 * (q >> 1) + (g & 1);
#pragma unroll
  for (int m = 0; m < 4; ++m) k.a[m] = ringoff + (8 * (g & 1) + q) * 128 + ((2 * (m ^ x) + (p >> 1)) << 4) + 8 * (p & 1);
  k.hi = g >> 1;
#pragma unroll
  for (int e = 0; e < 2; ++e) k.x[e] = LDS_X + 1024 * g + ((i ^ (4 * e + g)) << 4);
}
struct Frag { bf16x8 a[4], b[4]; };
template <int KK> DI void load_frags(Frag& f, lds_cptr lds, const G16& k) {
  constexpr int sA = (2 * KK) % RING;
  const unsigned d = sA == RING - 1 ? (1u - k.hi) * (unsigned)((RING - 1) * 2048) : k.hi * 2048u;
  constexpr int o = sA == RING - 1 ? 0 : sA * 2048;
#pragma unroll
  for (int m = 0; m < 4; ++m) f.a[m] = cat8(lds_tr(lds + (k.a[m] + d) + o), lds_tr(lds + (k.a[m] + d) + o + 512));
#pragma unroll
  for (int r = 0; r < 4; ++r) f.b[r] = lds_b128(lds + k.x[KK & 1] + 4096 * KK + 256 * r);
}
template <int KK, int EXTRA>
DI void gemm_step(f32x4 (&acc)[4][4], Frag& cur, Frag& nxt, lds_cptr lds, unsigned ring, unsigned voff,
                  __amdgpu_buffer_rsrc_t srd, unsigned soff0, unsigned ld, const G16& k) {
  if constexpr (2 * KK + RING < 32) {
    asm volatile("" :: "v"(cur.a[0]), "v"(cur.a[1]), "v"(cur.a[2]), "v"(cur.a[3]));
    wring_issue(2 * KK + RING, ring, voff, srd, soff0, ld);
    if constexpr (2 * KK + RING + 1 < 32) wring_issue(2 * KK + RING + 1, ring, voff, srd, soff0, ld);
  }
  if constexpr (KK + 1 < 16) {
    constexpr int last = 2 * KK + RING + 1 < 32 ? 2 * KK + RING + 1 : 31;
    wait_vm<2 * (last - (2 * KK + 3)) + ((2 * KK + 3 < RING) ? EXTRA : 0)>();
    load_frags<KK + 1>(nxt, lds, k);
  }
  SBAR();
#pragma unroll
  for (int m = 0; m < 4; ++m)
#pragma unroll
    for (int r = 0; r < 4; ++r) acc[m][r] = MFMA16(cur.a[m], cur.b[r], acc[m][r]);
  SBAR();
  if constexpr (KK + 1 < 16) gemm_step<KK + 1, EXTRA>(acc, nxt, cur, lds, ring, voff, srd, soff0, ld, k);
}
template <int EXTRA>
DI void gemm_phase(f32x4 (&acc)[4][4], lds_cptr lds, unsigned ringoff, unsigned ring, unsigned voff,
                   __amdgpu_buffer_rsrc_t srd, unsigned soff0, unsigned ld, unsigned l) {
  G16 k; g16_consts(k, l, ringoff);
  Frag f0, f1;
  wait_vm<2 * (RING - 2) + EXTRA>();
  load_frags<0>(f0, lds, k);
  gemm_step<0, EXTRA>(acc, f0, f1, lds, ring, voff, srd, soff0, ld, k);
}
DI u32x4 pack16(const f32x4& a, const f32x4& b, float sc) {
  auto r0 = __builtin_amdgcn_permlane16_swap(cvtpk(a[0] * sc, a[1] * sc), cvtpk(b[0] * sc, b[1] * sc), false, false);
  auto r1 = __builtin_amdgcn_permlane16_swap(cvtpk(a[2] * sc, a[3] * sc), cvtpk(b[2] * sc, b[3] * sc), false, false);
  return (u32x4){r0[0], r1[0], r0[1], r1[1]};
}
DI u32x4 pack8(const f32x16& a, int s, float sc) {
  return (u32x4){cvtpk(a[8 * s] * sc, a[8 * s + 1] * sc), cvtpk(a[8 * s + 2] * sc, a[8 * s + 3] * sc),
                 cvtpk(a[8 * s + 4] * sc, a[8 * s + 5] * sc), cvtpk(a[8 * s + 6] * sc, a[8 * s + 7] * sc)};
}
DI unsigned ximg(unsigned chunk, unsigned row) { return chunk * 1024u + ((row ^ (chunk & 7u)) << 4); }

constexpr int KSLOT[8] = {LDS_D, LDS_R, LDS_R + 32768, LDS_X, LDS_X + 32768, LDS_D, LDS_X, LDS_X + 32768};
constexpr int VSLOT[8] = {LDS_R + 16384, LDS_R + 49152, LDS_X + 16384, LDS_X + 49152, LDS_R + 65536, LDS_X + 16384, LDS_X + 49152, LDS_D};
template <int N> struct IC { static constexpr int value = N; };
struct Params {
  const unsigned short *x, *kc, *vc, *wq, *wo, *w1, *w2, *w3, *w4, *c, *wmod;
  void* ws;
  u32x4* out;
};
DI void attn_part(const Params& P, lds_ptr lds, unsigned lds0, unsigned w, __amdgpu_buffer_rsrc_t srdW) {
  LaneC c; lane_consts(c);
  const unsigned g = BIDX & 7, pn = BIDX >> 3;
  const unsigned l = c.lane, tid = TIDX;
  const unsigned ringoff = LDS_R + w * RINGB, ring = lds0 + ringoff;
  STAMP(1, 0);

  u32x4 xr[8];
#pragma unroll
  for (int i = 0; i < 8; ++i) gld16(xr[i], P.x + (size_t)(pn * 64 + w * 8 + i) * D + l * 8);
  if (w < 4) dma16(lds0 + LDS_D + w * 1024u, l * 16u, srdW, W_MOD + w * 1024u);
  const __amdgpu_buffer_rsrc_t srdQ = mksrd(P.wq, 512u * 4096u * 2u);
  const unsigned vq = wdma_voff(l, 8192u), sq0 = (g * 512u + w * 64u) * 2u;
  const __amdgpu_buffer_rsrc_t srdK = mksrd(P.kc + ((size_t)g * SK + (SK - WIN)) * DH, WIN * DH * 2u);
  const __amdgpu_buffer_rsrc_t srdV = mksrd(P.vc + ((size_t)g * SK + (SK - WIN)) * DH, WIN * DH * 2u);
  const unsigned vk = ((l >> 2) & 7) * 256u + (l >> 5) * 64u + (((l & 3) ^ (2 * (w & 1) + ((l >> 4) & 1))) << 4);
  const unsigned vv = wdma_voff(l, 256u);
  auto dma_k = [&](int t, unsigned slot) {
    dma16(lds0 + slot + 2048u * w, vk, srdK, (unsigned)t * 16384u + 2048u * w);
    dma16(lds0 + slot + 2048u * w + 1024u, vk, srdK, (unsigned)t * 16384u + 2048u * w + 128u);
  };
  auto dma_v = [&](int t, unsigned slot) {
    dma16(lds0 + slot + 1024u * w, vv, srdV, (unsigned)t * 16384u + 2048u * w);
    dma16(lds0 + slot + 8192u + 1024u * w, vv, srdV, (unsigned)t * 16384u + 2048u * w + 128u);
  };

  {
    asm volatile("s_waitcnt vmcnt(0)" : "+v"(xr[0]), "+v"(xr[1]), "+v"(xr[2]), "+v"(xr[3]), "+v"(xr[4]), "+v"(xr[5]), "+v"(xr[6]),
                 "+v"(xr[7]) :: "memory");
    dma16(ring + 8192u, l * 16u, srdQ, (2 * (pn * 8 + w)) * 8192u + g * 1024u);
    dma16(ring + 9216u, l * 16u, srdQ, (2 * (pn * 8 + w) + 1) * 8192u + g * 1024u);
    wring_issue(0, ring, vq, srdQ, sq0, 8192u);
    wring_issue(1, ring, vq, srdQ, sq0, 8192u);
    BAR_L();
    u32x4 mo[4];
#pragma unroll
    for (int i = 0; i < 4; ++i) mo[i] = *(const LAS u32x4*)(lds + LDS_D + (i >> 1) * 2048 + l * 32 + (i & 1) * 16);
    float v[8][8], s[8], m[8];
#pragma unroll
    for (int i = 0; i < 8; ++i) {
#pragma unroll
      for (int j = 0; j < 4; ++j) { v[i][2 * j] = bflo(xr[i][j]); v[i][2 * j + 1] = bfhi(xr[i][j]); }
      s[i] = ((v[i][0] + v[i][1]) + (v[i][2] + v[i][3])) + ((v[i][4] + v[i][5]) + (v[i][6] + v[i][7]));
    }
    wave_sum8(s, m);
#pragma unroll
    for (int i = 0; i < 8; ++i) {
      const float mu = m[i] * (1.0f / D);
      float q = 0.f;
#pragma unroll
      for (int j = 0; j < 8; ++j) { v[i][j] -= mu; q = __builtin_fmaf(v[i][j], v[i][j], q); }
      s[i] = q;
    }
    wave_sum8(s, m);
    float rs[8];
#pragma unroll
    for (int i = 0; i < 8; ++i) rs[i] = __builtin_amdgcn_rsqf(m[i] * (1.0f / D) + LN_EPS);
    STAMP(1, 8);
    STAMP(1, 9);
#pragma unroll
    for (int i = 0; i < 8; ++i) {
      float hh[8];
#pragma unroll
      for (int j = 0; j < 8; ++j) hh[j] = __builtin_fmaf(v[i][j] * rs[i], 1.f + U2F(mo[2 + (j >> 2)][j & 3]), U2F(mo[j >> 2][j & 3]));
      const u32x4 o = {cvtpk(hh[0], hh[1]), cvtpk(hh[2], hh[3]), cvtpk(hh[4], hh[5]), cvtpk(hh[6], hh[7])};
      *(LAS u32x4*)(lds + LDS_X + ximg(l, w * 8 + i)) = o;
      if (i == 1 || i == 3 || i == 5) wring_issue(2 + i / 2, ring, vq, srdQ, sq0, 8192u);
    }
  }
  BAR_L();
  dma_k(0, LDS_D);
  STAMP(1, 1);

  {
    f32x4 acc[4][4] = {};
    gemm_phase<2>(acc, lds, ringoff, ring, vq, srdQ, sq0, 8192u, l);
    STAMP(1, 2);
    WAIT_BAR(0);
    dma_k(1, KSLOT[1]); dma_v(0, VSLOT[0]); dma_k(2, KSLOT[2]); dma_v(1, VSLOT[1]);
    const unsigned cl = 8 * w + 2 * ((l >> 4) & 1) + (l >> 5);
#pragma unroll
    for (int r = 0; r < 4; ++r)
#pragma unroll
      for (int mp = 0; mp < 2; ++mp)
        *(LAS u32x4*)(lds + LDS_X + ximg(cl + 4 * mp, 16 * r + (l & 15))) = pack16(acc[2 * mp][r], acc[2 * mp + 1][r], QSCALE);
    BAR_L();
  }

  STAMP(1, 3);
  const unsigned hh = w >> 1, rt = w & 1;
  bf16x8 qf[8];
#pragma unroll
  for (int s = 0; s < 8; ++s) qf[s] = lds_b128(lds + LDS_X + ximg(16 * hh + 2 * s + c.h, 32 * rt + c.r));
  unsigned kb[2], vb[2];
#pragma unroll
  for (int i = 0; i < 2; ++i) {
    kb[i] = 2048 * (c.r >> 3) + 64 * (c.r & 7) + (((2 * i + c.h) ^ ((c.r >> 2) & 3)) << 4);
    vb[i] = (4 * c.h + c.q) * 128 + 64 * (i ^ (c.q >> 1)) + 32 * c.blk + 8 * (2 * (c.p & 1) + (c.p >> 1));
  }
  const __amdgpu_buffer_rsrc_t srdO = mksrd(P.wo, 4096u * 512u * 2u);
  const unsigned vo = wdma_voff(l, 1024u), so0 = g * 512u * 1024u + w * 128u;

  f32x16 o[4] = {};
  f32x16 s0, s1;
  f32x16 negm;
  float lsum = 0.f;
  u32x4 pk[4];
  constexpr int PD = 6;
  bf16x8 fr[PD];
  const bool lag = w >= 4;
  auto frag = [&](int t, int it) -> bf16x8 {
    if (it < 16) {
      const lds_cptr a = lds + VSLOT[t > 0 ? t - 1 : 0] + vb[it & 1] + 8192 * ((it >> 1) & 1) + 2048 * (it >> 2);
      return cat8(lds_tr(a), lds_tr(a + 1024));
    }
    const int s = (it - 16) >> 1;
    return lds_b128(lds + KSLOT[t < 8 ? t : 0] + kb[s & 1] + 512 * (s >> 1) + 8192 * (it & 1));
  };
  auto seam = [&](int t, int tx) __attribute__((always_inline)) {
    if (t == 7) { BAR_L(); } else if (tx >= 1 && tx <= 5) { WAIT_BAR(4); } else if (tx == 6) { WAIT_BAR(2); } else { WAIT_BAR(0); }
  };
  auto refill = [&](int t) __attribute__((always_inline)) {
    if (t + 3 < 8) dma_k(t + 3, KSLOT[t + 3 < 8 ? t + 3 : 0]);
    if (t + 2 < 8) dma_v(t + 2, VSLOT[t + 2 < 8 ? t + 2 : 0]);
    if (t == 6) {
#pragma unroll
      for (int ks = 0; ks < RING; ++ks) wring_issue(ks, ring, vo, srdO, so0, 1024u);
    }
  };
  auto extra = [&](int e) __attribute__((always_inline)) {
    const unsigned dump = lds0 + LDS_R + w * 3072u + (unsigned)(e % 3) * 1024u;
    const unsigned id = (BIDX >> 3) * 8 + w, jh = BIDX & 1;
    const unsigned vrow = (l >> 5) * 1024u + (l & 31) * 16u + jh * 512u;
    auto w3v = [&](unsigned piece) { const unsigned byte = piece * 1024u + l * 16u, row = byte / 1792u;
                                     return row * 3584u + jh * 1792u + (byte - row * 1792u); };
    if (e < 4 || e >= 15) {
      const int i = e < 4 ? e : e - 11;
      const unsigned off = i < 3 ? S_U + (unsigned)i * (64u * T * 16u) + pn * 65536u + g * 8192u
                         : i < 7 ? S_HZ + pn * 262144u + g * 32768u + (unsigned)(i - 3) * 8192u
                                 : S_H3 + pn * 229376u + g * 28672u + (unsigned)(i - 7) * 8192u;
      const u32x4 sv = {SENT, SENT, SENT, SENT};
      __builtin_amdgcn_raw_buffer_store_b128(sv, srdW, (i == 10 && tid >= 256) ? OOB : tid * 16u + off, 0, 16);
    } else if (e < 6) dma16(dump, l * 16u, srdO, (g * 512u + (pn * 8 + w) * 2 + (e - 4)) * 1024u);
    else if (e == 6) dma16(dump, vrow, mksrd(P.w1, 512u * 1024u), id * 2048u);
    else if (e == 7) dma16(dump, vrow, mksrd(P.w2, 512u * 1024u), id * 2048u);
    else if (e < 11) dma16(dump, w3v(id + 256u * (e - 8)), mksrd(P.w3, 512u * 3584u), 0u);
    else if (e == 11) {
      if (id < 128) dma16(dump, w3v(id + 768u), mksrd(P.w3, 512u * 3584u), 0u);
      else dma16(dump, vrow, mksrd(P.w4, 1792u * 1024u), (id - 128u + 768u) * 2048u);
    } else dma16(dump, vrow, mksrd(P.w4, 1792u * 1024u), (id + 256u * (e - 12)) * 2048u);
  };
  auto step = [&](auto tc) __attribute__((always_inline)) {
    constexpr int t = decltype(tc)::value;
    constexpr int i0 = t == 0 ? 16 : 0, i1 = t == 8 ? 16 : 32;
    if (t <= 1) {
#pragma unroll
      for (int it = i0; it < i0 + PD; ++it) fr[it % PD] = frag(t, it);
    }
    if (t == 0) { s0 = (f32x16){}; s1 = (f32x16){}; } else if (t < 8) { s0 = negm; s1 = negm; }
    __builtin_amdgcn_s_setprio(1);
    SBAR();
#pragma unroll
    for (int it = i0; it < i1; ++it) {
      if (it < 16) o[it & 3] = MFMA32(fr[it % PD], __builtin_bit_cast(bf16x8, pk[it >> 2]), o[it & 3]);
      else if (it & 1) s1 = MFMA32(fr[it % PD], qf[(it - 16) >> 1], s1);
      else s0 = MFMA32(fr[it % PD], qf[(it - 16) >> 1], s0);
      if (it + PD < i1) fr[it % PD] = frag(t, it + PD);
      if (t >= 1 && it == 1) { if (!lag) refill(t - 1); }
      SBAR();
    }
    if constexpr (t == 8) return;
    __builtin_amdgcn_s_setprio(0);
    if (lag) { seam(t, t - 1); refill(t); }
    asm volatile("s_nop 15" : "+v"(s0), "+v"(s1));
    float ta = max3f(s0[0], s0[1], s1[0]), tb = max3f(s0[2], s0[3], s1[1]);
    ta = max3f(ta, s1[2], s1[3]);
#pragma unroll
    for (int i = 4; i < 16; i += 4) {
      ta = max3f(ta, s0[i], s0[i + 1]); tb = max3f(tb, s0[i + 2], s0[i + 3]);
      ta = max3f(ta, s1[i], s1[i + 1]); tb = max3f(tb, s1[i + 2], s1[i + 3]);
    }
    float tm = max2f(ta, tb);
    {
      auto r = __builtin_amdgcn_permlane32_swap(F2U(tm), F2U(tm), false, false);
      tm = max2f(U2F(r[0]), U2F(r[1]));
    }
    if (t == 0 || ANY(tm > THR)) {
      const float ndl = t == 0 ? -tm : -max2f(tm, 0.f);
      const float aone = c.h ? 0.f : 1.f;
      if (t == 0) negm = (f32x16){};
      __builtin_amdgcn_s_setprio(2);
      s0 = __builtin_amdgcn_mfma_f32_32x32x2f32(aone, ndl, s0, 0, 0, 0);
      s1 = __builtin_amdgcn_mfma_f32_32x32x2f32(aone, ndl, s1, 0, 0, 0);
      negm = __builtin_amdgcn_mfma_f32_32x32x2f32(aone, ndl, negm, 0, 0, 0);
      __builtin_amdgcn_s_setprio(0);
      if (t > 0) {
        const float al = __builtin_amdgcn_exp2f(ndl);
        lsum *= al;
#pragma unroll
        for (int dt = 0; dt < 4; ++dt)
#pragma unroll
          for (int i = 0; i < 16; ++i) o[dt][i] *= al;
      }
    }
    SBAR();
    if (t >= 1) {
#pragma unroll
      for (int it = 0; it < PD; ++it) fr[it] = frag(t + 1, it);
    }
#pragma unroll
    for (int i = 0; i < 16; ++i) {
      s0[i] = __builtin_amdgcn_exp2f(s0[i]); s1[i] = __builtin_amdgcn_exp2f(s1[i]);
      if (t >= 1 && (i & 3) == 1 && 4 * (t - 1) + (i >> 2) < 22) { PIN(s0[i]); extra(4 * (t - 1) + (i >> 2)); }
    }
    pk[0] = pack8(s0, 0, 1.f); pk[1] = pack8(s0, 1, 1.f); pk[2] = pack8(s1, 0, 1.f); pk[3] = pack8(s1, 1, 1.f);
    {
      float l0 = lsum, l1 = 0.f, l2 = 0.f, l3 = 0.f;
#pragma unroll
      for (int i = 0; i < 16; i += 2) { l0 += s0[i]; l1 += s0[i + 1]; l2 += s1[i]; l3 += s1[i + 1]; }
      lsum = (l0 + l1) + (l2 + l3);
    }
    PIN(pk[0]); PIN(pk[1]); PIN(pk[2]); PIN(pk[3]); PIN(lsum);
    SBAR();
    if (!lag) seam(t, t);
  };
  step(IC<0>{}); step(IC<1>{}); step(IC<2>{}); step(IC<3>{}); step(IC<4>{}); step(IC<5>{}); step(IC<6>{}); step(IC<7>{}); step(IC<8>{});
  __builtin_amdgcn_s_setprio(0);
  STAMP(1, 4);
  {
    const float il = __builtin_amdgcn_rcpf(swap32_add(lsum));
#pragma unroll
    for (int dt = 0; dt < 4; ++dt)
#pragma unroll
      for (int s = 0; s < 2; ++s)
        *(LAS u32x4*)(lds + LDS_X + ximg(16 * hh + 4 * dt + 2 * s + c.h, 32 * rt + c.r)) = pack8(o[dt], s, il);
  }
  BAR_L();
  STAMP(1, 5);
  dma16(lds0 + LDS_D + w * 1024u, l * 16u, srdW, W_MOD + 4096u + w * 1024u);
  {
    unsigned l2 = TIDX & 63;
    asm volatile("" : "+v"(l2));
    f32x4 acc[4][4] = {};
    gemm_phase<1>(acc, lds, ringoff, ring, vo, srdO, so0, 1024u, l2);
    STAMP(1, 6);
    const unsigned dst = W_P1 + ((((pn * 8 + ((l2 >> 3) & 1)) * 8 + g) * 64 + 8 * w + 2 * ((l2 >> 4) & 1) + (l2 >> 5)) * 8 + (l2 & 7)) * 16u;
#pragma unroll
    for (int r = 0; r < 4; ++r)
#pragma unroll
      for (int mp = 0; mp < 2; ++mp)
        __builtin_amdgcn_raw_buffer_store_b128(pack16(acc[2 * mp][r], acc[2 * mp + 1][r], 1.f), srdW,
                                               dst + (4 * mp * 8 + r * 2 * 8 * 64 * 8) * 16u, 0, 16);
  }
  WAIT_BAR(0);
  if (tid == 0) __builtin_amdgcn_raw_buffer_store_b32(1u, srdW, W_FLAG + (pn * 8 + g) * 64u, 0, 16);
  STAMP(1, 7);
}

DI void ln8(float (&v)[8], LAS float* red  , unsigned tid) {
  float s = ((v[0] + v[1]) + (v[2] + v[3])) + ((v[4] + v[5]) + (v[6] + v[7]));
  float q = (__builtin_fmaf(v[0], v[0], v[1] * v[1]) + __builtin_fmaf(v[2], v[2], v[3] * v[3])) +
            (__builtin_fmaf(v[4], v[4], v[5] * v[5]) + __builtin_fmaf(v[6], v[6], v[7] * v[7]));
  s = sum_hi3(s); q = sum_hi3(q);
  if ((tid & 63) < 8) *(LAS f32x2*)(red + ((tid >> 6) * 8 + (tid & 7)) * 2) = (f32x2){s, q};
  BAR_L();
  f32x2 e[8];
#pragma unroll
  for (int i = 0; i < 8; ++i) e[i] = *(const LAS f32x2*)(red + (i * 8 + (tid & 7)) * 2);
  const float mu = (((e[0][0] + e[1][0]) + (e[2][0] + e[3][0])) + ((e[4][0] + e[5][0]) + (e[6][0] + e[7][0]))) * (1.0f / D);
  const float ms = (((e[0][1] + e[1][1]) + (e[2][1] + e[3][1])) + ((e[4][1] + e[5][1]) + (e[6][1] + e[7][1]))) * (1.0f / D);
  const float rs = __builtin_amdgcn_rsqf(__builtin_fmaxf(__builtin_fmaf(-mu, mu, ms), 0.f) + LN_EPS);
  const float nb = -mu * rs;
#pragma unroll
  for (int k = 0; k < 8; ++k) v[k] = __builtin_fmaf(v[k], rs, nb);
}
DI void red4_put(const f32x16& a, int nt, lds_ptr scr, unsigned rt, unsigned kq, unsigned l) {
#pragma unroll
  for (int g = 0; g < 2; ++g)
#pragma unroll
    for (int i = 0; i < 2; ++i) {
      const int o = 8 * g + 4 * i;
      *(LAS f32x4*)(scr + ((rt * 4 + 2 * nt + g) * 4 + kq) * 2048 + i * 1024 + l * 16) = (f32x4){a[o], a[o + 1], a[o + 2], a[o + 3]};
    }
}
DI void red4_get(float (&v)[8], lds_cptr scr, unsigned rt, unsigned kq, unsigned l) {
  f32x4 t[4][2];
#pragma unroll
  for (int s = 0; s < 4; ++s)
#pragma unroll
    for (int i = 0; i < 2; ++i) t[s][i] = *(const LAS f32x4*)(scr + ((rt * 4 + kq) * 4 + s) * 2048 + i * 1024 + l * 16);
#pragma unroll
  for (int i = 0; i < 2; ++i)
#pragma unroll
    for (int k = 0; k < 4; ++k) v[4 * i + k] = (t[0][i][k] + t[1][i][k]) + (t[2][i][k] + t[3][i][k]);
}
DI void tile_mm8(const bf16x8 (&af)[8][2], const u32x4 (&xb)[8], float (&v)[8], lds_ptr scr, unsigned rt, unsigned kq, unsigned l) {
  f32x16 a0 = {}, a1 = {};
#pragma unroll
  for (int i = 0; i < 8; ++i) a0 = MFMA32(af[i][0], __builtin_bit_cast(bf16x8, xb[i]), a0);
  SBAR();
#pragma unroll
  for (int i = 0; i < 4; ++i) a1 = MFMA32(af[i][1], __builtin_bit_cast(bf16x8, xb[i]), a1);
  SBAR();
  red4_put(a0, 0, scr, rt, kq, l);
#pragma unroll
  for (int i = 4; i < 8; ++i) a1 = MFMA32(af[i][1], __builtin_bit_cast(bf16x8, xb[i]), a1);
  SBAR();
  red4_put(a1, 1, scr, rt, kq, l);
  BAR_L();
  red4_get(v, scr, rt, kq, l);
}

struct Frag3 { bf16x8 a, b[2]; };
constexpr int RING3 = 13;
constexpr int LA = 0, LB = 65536, LC = 131072;
DI unsigned r3off(int s, unsigned rc, unsigned rb) { return s < 4 ? rc + s * 1024u : rb + (s - 4) * 1024u; }
template <int KS> DI void load_frags3(Frag3& f, lds_cptr lds, unsigned rc, unsigned rb, unsigned a3, const LaneC& c) {
  const lds_cptr sl = lds + r3off(KS % RING3, rc, rb) + a3;
  f.a = cat8(lds_tr(sl), lds_tr(sl + 256));
#pragma unroll
  for (int rt = 0; rt < 2; ++rt) f.b[rt] = lds_b128(lds + LA + c.xb[KS & 3] + 2048 * KS + 512 * rt);
}
template <int KS>
DI void g3_step(f32x16 (&acc)[2], Frag3& cur, Frag3& n1, Frag3& n2, lds_cptr lds, unsigned lds0, unsigned rc, unsigned rb,
                unsigned voff, __amdgpu_buffer_rsrc_t srd, unsigned soff0, unsigned a3, const LaneC& c) {
  constexpr int NKS = 32;
  if constexpr (KS + 2 < NKS) {
    constexpr int last = (KS + RING3 < NKS ? KS + RING3 : NKS) - 1;
    wait_vm<last - (KS + 2)>();
    load_frags3<KS + 2>(n2, lds, rc, rb, a3, c);
  }
  SBAR();
  acc[0] = MFMA32(cur.a, cur.b[0], acc[0]);
  acc[1] = MFMA32(cur.a, cur.b[1], acc[1]);
  if constexpr (KS + RING3 < NKS) {
    asm volatile("" :: "v"(cur.a));
    dma16(lds0 + r3off(KS % RING3, rc, rb), voff, srd, soff0 + (unsigned)(16 * (KS + RING3)) * 3584u);
  }
  SBAR();
  if constexpr (KS + 1 < NKS) g3_step<KS + 1>(acc, n1, n2, cur, lds, lds0, rc, rb, voff, srd, soff0, a3, c);
}
DI void mlp_part(const Params& P, lds_ptr lds, unsigned lds0, unsigned w, __amdgpu_buffer_rsrc_t srdS) {
  LaneC c; lane_consts(c);
  const unsigned idx = BIDX >> 3, pn = 8 * ((BIDX & 7) >> 1) + (idx >> 2), j = 4 * (BIDX & 1) + (idx & 3);
  const unsigned tid = TIDX, l = c.lane;
  const unsigned vw = wdma_voff(l, 1024u);
  const unsigned rt = w & 1, kq = w >> 1;
  constexpr int LMOD = LDS_D;
  constexpr int LRED = LC + 28672;
  const unsigned prow = 32 * rt + c.r;
  const unsigned trow = pn * 64 + prow;
  const unsigned och = 8 * j + 2 * kq + c.h;
  const unsigned ovo = ((pn * 64 + och) * 64 + prow) * 16u;
  float gb[8];
  STAMP(2, 0);

  {
    const unsigned row = pn * 64 + j * 8 + (tid & 7), ch = tid >> 3;
    u32x4 xr;
    gld16(xr, P.x + (size_t)row * D + ch * 8);
    auto wait_flags = [&](unsigned fo) {
      for (unsigned spin = 0;; ++spin) {
        asm volatile("" ::: "memory");
        const unsigned f = __builtin_amdgcn_raw_buffer_load_b32(srdS, fo, 0, 16);
        if (!ANY(fo != OOB && f != 1u) || spin > SPIN_CAP) break;
        __builtin_amdgcn_s_sleep(FLAG_SLEEP);
      }
    };
    const __amdgpu_buffer_rsrc_t srd1 = mksrd(P.w1, 512u * 512u * 2u), srd2 = mksrd(P.w2, 512u * 512u * 2u);
#pragma unroll
    for (int i = 0; i < 8; ++i) dma16(lds0 + LA + (8 * w + i) * 1024u, vw, srd1, (8 * w + i) * 8192u + j * 128u);
    {
      const int fp = (int)pn - 1 + (int)(l >> 3);
      if (w == 0) wait_flags((l < 24 && fp >= 0 && fp < 32) ? W_FLAG + ((unsigned)fp * 8 + (l & 7)) * 64u : OOB);
      BAR_L();
    }
    STAMP(2, 15);
    u32x4 p[8];
#pragma unroll
    for (int g = 0; g < 8; ++g) bld16_sc1(p[g], tid * 16u, srdS, W_P1 + ((pn * 8 + j) * 8 + g) * 8192u);
    float ga[8], sb[8], cb[8], g2[8];
    {
      u32x4 mv[10];
#pragma unroll
      for (int i = 0; i < 8; ++i) mv[i] = *(const LAS u32x4*)(lds + LMOD + (i >> 1) * 2048 + ch * 32 + 16 * (i & 1));
#pragma unroll
      for (int i = 0; i < 2; ++i) mv[8 + i] = *(const LAS u32x4*)(lds + LMOD + 6144 + och * 32 + 16 * i);
#pragma unroll
      for (int k = 0; k < 8; ++k) {
        ga[k] = U2F(mv[k >> 2][k & 3]); sb[k] = U2F(mv[2 + (k >> 2)][k & 3]); cb[k] = 1.f + U2F(mv[4 + (k >> 2)][k & 3]);
        g2[k] = U2F(mv[6 + (k >> 2)][k & 3]); gb[k] = U2F(mv[8 + (k >> 2)][k & 3]);
      }
    }
    asm volatile("s_waitcnt vmcnt(0)" : "+v"(xr), "+v"(p[0]), "+v"(p[1]), "+v"(p[2]), "+v"(p[3]), "+v"(p[4]), "+v"(p[5]),
                 "+v"(p[6]), "+v"(p[7]) :: "memory");
    STAMP(2, 1);
    float v[8], x2[8];
#pragma unroll
    for (int i = 0; i < 4; ++i) {
      const float lo = ((bflo(p[0][i]) + bflo(p[1][i])) + (bflo(p[2][i]) + bflo(p[3][i]))) + ((bflo(p[4][i]) + bflo(p[5][i])) + (bflo(p[6][i]) + bflo(p[7][i])));
      const float hi = ((bfhi(p[0][i]) + bfhi(p[1][i])) + (bfhi(p[2][i]) + bfhi(p[3][i]))) + ((bfhi(p[4][i]) + bfhi(p[5][i])) + (bfhi(p[6][i]) + bfhi(p[7][i])));
      v[2 * i] = x2[2 * i] = __builtin_fmaf(ga[2 * i], lo, bflo(xr[i]));
      v[2 * i + 1] = x2[2 * i + 1] = __builtin_fmaf(ga[2 * i + 1], hi, bfhi(xr[i]));
    }
    ln8(v, (LAS float*)(lds + LRED), tid);
    float hb[8];
#pragma unroll
    for (int k = 0; k < 8; ++k) v[k] = hb[k] = __builtin_fmaf(v[k], cb[k], sb[k]);
    ln8(v, (LAS float*)(lds + LRED + 512), tid);
    const unsigned vo = ((pn * 64 + ch) * 64 + j * 8 + (tid & 7)) * 16u, vz = ((pn * 64 + ch) * 256 + j * 8 + (tid & 7)) * 16u;
    pub16(srdS, vo, S_U, (u32x4){cvtpk(v[0], v[1]), cvtpk(v[2], v[3]), cvtpk(v[4], v[5]), cvtpk(v[6], v[7])});
#pragma unroll
    for (int i = 0; i < 2; ++i) {
      pub16(srdS, vz, S_HZ + i * 1024u, (u32x4){F2U(hb[4 * i]), F2U(hb[4 * i + 1]),
                                                F2U(hb[4 * i + 2]), F2U(hb[4 * i + 3])});
      f32x4 z;
#pragma unroll
      for (int e = 0; e < 4; ++e) z[e] = __builtin_fmaf(g2[4 * i + e], hb[4 * i + e], x2[4 * i + e]);
      pub16(srdS, vz, S_HZ + (2 + i) * 1024u, __builtin_bit_cast(u32x4, z));
    }
#pragma unroll
    for (int i = 0; i < 8; ++i) dma16(lds0 + LB + (8 * w + i) * 1024u, vw, srd2, (8 * w + i) * 8192u + j * 128u);
  }
  STAMP(2, 2);
  const __amdgpu_buffer_rsrc_t srd3 = mksrd(P.w3, 512u * F * 2u);
  const unsigned rc = LC + w * 4096u, rb = LB + w * 9216u;
  const unsigned v3 = (l >> 2) * 3584u + ((l & 3) << 4), s30 = (j * 224u + w * 32u) * 2u;
  float v[8];
  {
    bf16x8 af[8][2];
#pragma unroll
    for (int i = 0; i < 8; ++i)
#pragma unroll
      for (int nt = 0; nt < 2; ++nt) {
        const lds_cptr a = lds + LA + (8 * kq + i) * 2048 + c.afr[nt];
        af[i][nt] = cat8(lds_tr(a), lds_tr(a + 512));
      }
    BAR_L();
    u32x4 xb[8];
    poll_ind<8, 3, 7>(xb, srdS, ((pn * 64 + 16 * kq + c.h) * 64 + prow) * 16u, S_U, 2048u);
    STAMP(2, 3);
    tile_mm8(af, xb, v, lds + LA, rt, kq, l);
#pragma unroll
    for (int k = 0; k < 8; ++k) v[k] = gelu_tanh(v[k]);
    pub16(srdS, ovo, S_H1, (u32x4){cvtpk(v[0], v[1]), cvtpk(v[2], v[3]), cvtpk(v[4], v[5]), cvtpk(v[6], v[7])});
    if (w < 7) {
#pragma unroll
      for (int ks = 0; ks < 4; ++ks) dma16(lds0 + r3off(ks, rc, rb), v3, srd3, s30 + (unsigned)(16 * ks) * 3584u);
    }
  }
  STAMP(2, 4);
  float y[8];
  u32x4 cs[4];
  const unsigned cso = w < 7 ? (224 * j + 32 * w + 8 * c.h) * 4u : OOB;
  {
    bf16x8 af[8][2];
#pragma unroll
    for (int i = 0; i < 8; ++i)
#pragma unroll
      for (int nt = 0; nt < 2; ++nt) {
        const lds_cptr a = lds + LB + (8 * kq + i) * 2048 + c.afr[nt];
        af[i][nt] = cat8(lds_tr(a), lds_tr(a + 512));
      }
    BAR_L();
    u32x4 hz[4];
    const unsigned hzo = ((pn * 64 + och) * 256 + prow) * 16u;
#pragma unroll
    for (int i = 0; i < 4; ++i) hz[i] = __builtin_amdgcn_raw_buffer_load_b128(srdS, hzo, S_HZ + i * 1024u, 16);
#pragma unroll
    for (int i = 0; i < 4; ++i) cs[i] = __builtin_amdgcn_raw_buffer_load_b128(srdS, cso, W_MOD + 12288u + 64u * (i >> 1) + 16u * (i & 1), 0);
    const int sh = kq == 0 ? -1 : kq == 1 ? 1 : kq == 2 ? -2 : 2;
    const int srow = (int)trow + sh;
    const unsigned xvo = (srow >= 0 && srow < T) ? ((((unsigned)srow >> 6) * 64 + 16 * kq + c.h) * 64 + ((unsigned)srow & 63)) * 16u : OOB;
    u32x4 xb[8];
    poll_ind<8, 3, 7>(xb, srdS, xvo, S_H1, 2048u);
    STAMP(2, 5);
    tile_mm8(af, xb, v, lds + LA, rt, kq, l);
    if (has_sent(hz, 4)) poll_load<4>(hz, srdS, hzo, S_HZ, 1024u);
    float x1[8];
#pragma unroll
    for (int k = 0; k < 8; ++k) {
      x1[k] = U2F(hz[k >> 2][k & 3]) + v[k];
      y[k] = __builtin_fmaf(gb[k], v[k], U2F(hz[2 + (k >> 2)][k & 3]));
    }
    pub16(srdS, ovo, S_X1, (u32x4){cvtpk(x1[0], x1[1]), cvtpk(x1[2], x1[3]), cvtpk(x1[4], x1[5]), cvtpk(x1[6], x1[7])});
    if (w < 7) {
#pragma unroll
      for (int ks = 4; ks < RING3; ++ks) dma16(lds0 + r3off(ks, rc, rb), v3, srd3, s30 + (unsigned)(16 * ks) * 3584u);
    }
  }
  STAMP(2, 6);
  {
    u32x4 xr[8];
    BAR_L();
    poll_ind<8, 3, 7>(xr, srdS, ((pn * 64 + 8 * w) * 64 + l) * 16u, S_X1, 1024u);
    STAMP(2, 7);
#pragma unroll
    for (int i = 0; i < 8; ++i) *(LAS u32x4*)(lds + LA + ximg(8 * w + i, l)) = xr[i];
    {
      float s0 = 0.f, s1 = 0.f, q0 = 0.f, q1 = 0.f;
#pragma unroll
      for (int i = 0; i < 8; ++i)
#pragma unroll
        for (int k = 0; k < 4; k += 2) {
          s0 = dot2bf(xr[i][k], 0x3F803F80u, s0); s1 = dot2bf(xr[i][k + 1], 0x3F803F80u, s1);
          q0 = dot2bf(xr[i][k], xr[i][k], q0); q1 = dot2bf(xr[i][k + 1], xr[i][k + 1], q1);
        }
      *(LAS f32x2*)(lds + LRED + (w * 64 + l) * 8) = (f32x2){s0 + s1, q0 + q1};
    }
    BAR_L();
    STAMP(2, 8);
    if (w < 7) {
      f32x16 acc[2] = {};
      const unsigned a3 = (8 * c.h + c.q) * 64 + 32 * c.blk + 8 * (2 * (c.p & 1) + (c.p >> 1));
      Frag3 f0, f1, f2;
      wait_vm<RING3 - 2>();
      load_frags3<0>(f0, lds, rc, rb, a3, c);
      load_frags3<1>(f1, lds, rc, rb, a3, c);
      float mu[2], rs[2];
#pragma unroll
      for (int r2 = 0; r2 < 2; ++r2) {
        float s = 0.f, q = 0.f;
#pragma unroll
        for (int i = 0; i < 8; ++i) {
          const f32x2 t = *(const LAS f32x2*)(lds + LRED + (i * 64 + 32 * r2 + c.r) * 8);
          s += t[0]; q += t[1];
        }
        mu[r2] = s * (1.0f / D);
        rs[r2] = __builtin_amdgcn_rsqf(__builtin_fmaxf(__builtin_fmaf(-mu[r2], mu[r2], q * (1.0f / D)), 0.f) + LN_EPS);
      }
      if (w < 4) __builtin_amdgcn_s_setprio(2); else __builtin_amdgcn_s_setprio(1);
      g3_step<0>(acc, f0, f1, f2, lds, lds0, rc, rb, v3, srd3, s30, a3, c);
      __builtin_amdgcn_s_setprio(0);
      STAMP(2, 9);
#pragma unroll
      for (int r2 = 0; r2 < 2; ++r2) {
#pragma unroll
        for (int i = 0; i < 16; ++i) acc[r2][i] = gelu_tanh(rs[r2] * __builtin_fmaf(-mu[r2], U2F(cs[i >> 2][i & 3]), acc[r2][i]));
#pragma unroll
        for (int s2 = 0; s2 < 2; ++s2)
          pub16(srdS, ((pn * 224 + 28 * j + 4 * w + 2 * s2 + c.h) * 64 + 32 * r2 + c.r) * 16u, S_H3, pack8(acc[r2], s2, 1.f));
      }
    }
    BAR_L();
  }
  STAMP(2, 10);
  {
    const __amdgpu_buffer_rsrc_t srd4 = mksrd(P.w4, (unsigned)F * 512u * 2u);
    const unsigned qb = kq * 40960u;
    const unsigned s40 = (448u * kq + 8u * rt) * 1024u + j * 128u;
    auto w4dma = [&](int ks) { dma16(lds0 + qb + (ks % 20) * 2048u + rt * 1024u, vw, srd4, s40 + (unsigned)ks * 16384u); };
#pragma unroll
    for (int ks = 0; ks < NPRE; ++ks) w4dma(ks);
    const unsigned hvo = ((pn * 224 + 56 * kq + c.h) * 64 + prow) * 16u;
    u32x4 ha[14], hb[14];
    poll_ind<14, 9, 13>(ha, srdS, hvo, S_H3, 2048u);
    BAR_L();
    STAMP(2, 11);
    f32x16 acc[2] = {};
    bf16x8 fa[2][2];
    auto ld = [&](int ks) {
#pragma unroll
      for (int nt = 0; nt < 2; ++nt) {
        const lds_cptr a = lds + qb + (ks % 20) * 2048 + c.afr[nt];
        fa[ks & 1][nt] = cat8(lds_tr(a), lds_tr(a + 512));
      }
    };
    auto mm = [&](int ks, const u32x4& b) {
#pragma unroll
      for (int nt = 0; nt < 2; ++nt) acc[nt] = MFMA32(fa[ks & 1][nt], __builtin_bit_cast(bf16x8, b), acc[nt]);
    };
    auto hbl = [&](int i) { bld16_sc1(hb[i], hvo, srdS, S_H3 + (14u + i) * 2048u); };
    ld(0);
#pragma unroll
    for (int ks = 0; ks < 8; ++ks) {
      if (ks < 7) ld(ks + 1);
      SBAR(); mm(ks, ha[ks]);
      if (ks < 7) { hbl(2 * ks); hbl(2 * ks + 1); }
      else {
#pragma unroll
        for (int k2 = NPRE; k2 < 20; ++k2) w4dma(k2);
      }
      SBAR();
    }
    BAR_L();
    ld(8);
#pragma unroll
    for (int ks = 8; ks < 14; ++ks) {
      if (ks < 13) ld(ks + 1);
      SBAR(); mm(ks, ha[ks]);
      if (ks < 12) { w4dma(20 + 2 * (ks - 8)); w4dma(21 + 2 * (ks - 8)); }
      SBAR();
    }
    asm volatile("s_waitcnt vmcnt(8) lgkmcnt(0)\n\ts_barrier" : "+v"(hb[0]), "+v"(hb[1]), "+v"(hb[2]), "+v"(hb[3]), "+v"(hb[4]), "+v"(hb[5]),
                 "+v"(hb[6]), "+v"(hb[7]), "+v"(hb[8]), "+v"(hb[9]), "+v"(hb[10]), "+v"(hb[11]), "+v"(hb[12]), "+v"(hb[13]) :: "memory");
    if (has_sent(hb, 14)) poll_load<14>(hb, srdS, hvo, S_H3 + 14u * 2048u, 2048u);
    STAMP(2, 12);
    ld(14);
#pragma unroll
    for (int ks = 14; ks < 19; ++ks) { ld(ks + 1); SBAR(); mm(ks, hb[ks - 14]); SBAR(); }
    mm(19, hb[5]);
    WAIT_BAR(0);
    ld(20);
#pragma unroll
    for (int ks = 20; ks < 27; ++ks) { ld(ks + 1); SBAR(); mm(ks, hb[ks - 14]); SBAR(); }
    mm(27, hb[13]);
    BAR_L();
    STAMP(2, 13);
    red4_put(acc[0], 0, lds + LA, rt, kq, l);
    red4_put(acc[1], 1, lds + LA, rt, kq, l);
    BAR_L();
    red4_get(v, lds + LA, rt, kq, l);
#pragma unroll
    for (int k = 0; k < 8; ++k) v[k] = __builtin_fmaf(gb[k], v[k], y[k]);
    P.out[(size_t)trow * 64 + och] = (u32x4){cvtpk(v[0], v[1]), cvtpk(v[2], v[3]), cvtpk(v[4], v[5]), cvtpk(v[6], v[7])};
  }
  STAMP(2, 14);
}

__global__ __launch_bounds__(512, 2) void k_fused(Params P) {
  extern __shared__ __attribute__((aligned(16))) char smem[];
  const lds_ptr lds = (lds_ptr)smem;
  const unsigned lds0 = (unsigned)(uintptr_t)smem;
  const unsigned w = rfl(TIDX >> 6);
  const __amdgpu_buffer_rsrc_t srdW = mksrd(P.ws, W_END);
  attn_part(P, lds, lds0, w, srdW);
  mlp_part(P, lds, lds0, w, srdW);
}

#if defined(__HIP_DEVICE_COMPILE__)
#pragma clang attribute pop
#endif
extern "C" void kernel_launch(void* const* d_in, const int* in_sizes, int n_in, void* d_out, int out_size, void* d_ws,
                              size_t ws_size, hipStream_t stream) {
  Params P;
  P.x = (const unsigned short*)d_in[0];
  P.kc = (const unsigned short*)d_in[1];
  P.vc = (const unsigned short*)d_in[2];
  P.wq = (const unsigned short*)d_in[3];
  P.wo = (const unsigned short*)d_in[4];
  P.w1 = (const unsigned short*)d_in[6];
  P.w2 = (const unsigned short*)d_in[7];
  P.w3 = (const unsigned short*)d_in[8];
  P.w4 = (const unsigned short*)d_in[9];
  P.c = (const unsigned short*)d_in[10];
  P.wmod = (const unsigned short*)d_in[11];
  P.ws = d_ws;
  P.out = (u32x4*)d_out;
  (void)hipFuncSetAttribute((const void*)k_fused, hipFuncAttributeMaxDynamicSharedMemorySize, LDS_TOTAL);
  k_init<<<153, 512, 0, stream>>>(P.c, P.wmod, P.w3, d_ws);
  k_fused<<<256, 512, LDS_TOTAL, stream>>>(P);
}
```

```cpp
#include <hip/hip_runtime.h>
#include <hip/hip_bf16.h>
#include <stdint.h>

#if defined(__HIP_DEVICE_COMPILE__)
#pragma clang attribute push(__attribute__((target("no-packed-fp32-ops"))), apply_to = function)
#endif
typedef short bf16x8 __attribute__((ext_vector_type(8)));
typedef short s16x4 __attribute__((ext_vector_type(4)));
typedef float f32x16 __attribute__((ext_vector_type(16)));
typedef float f32x4 __attribute__((ext_vector_type(4)));
typedef float f32x2 __attribute__((ext_vector_type(2)));
typedef unsigned u32x4 __attribute__((ext_vector_type(4)));
typedef unsigned u32x2 __attribute__((ext_vector_type(2)));
typedef __bf16 bf16x2_t __attribute__((ext_vector_type(2)));
typedef short v4i16_t __attribute__((ext_vector_type(4)));
#define LAS __attribute__((address_space(3)))
typedef LAS const char* lds_cptr;
typedef LAS char* lds_ptr;
#define DI __device__ __forceinline__
#define F2U(x) __builtin_bit_cast(unsigned, (float)(x))
#define U2F(x) __builtin_bit_cast(float, (unsigned)(x))
#define F2I(x) __builtin_bit_cast(int, (float)(x))
#define I2F(x) __builtin_bit_cast(float, (int)(x))
#define TIDX __builtin_amdgcn_workitem_id_x()
#define BIDX __builtin_amdgcn_workgroup_id_x()
#define ANY(p) (__builtin_amdgcn_ballot_w64(p) != 0ull)
#define SBAR() __builtin_amdgcn_sched_barrier(0)
#define MFMA32(a, b, c) __builtin_amdgcn_mfma_f32_32x32x16_bf16((a), (b), (c), 0, 0, 0)
#define WAIT_V(n) asm volatile("s_waitcnt vmcnt(" #n ")" ::: "memory")
#define WAIT_L0() asm volatile("s_waitcnt lgkmcnt(0)" ::: "memory")
#define BAR_L() asm volatile("s_waitcnt lgkmcnt(0)\n\ts_barrier" ::: "memory")
#define WAIT_BAR(n) asm volatile("s_waitcnt vmcnt(" #n ") lgkmcnt(0)\n\ts_barrier" ::: "memory")

constexpr int T = 2048, D = 512, HKV = 8, SK = 2048, DH = 128, WIN = 512, F = 1792;
constexpr float LN_EPS = 1e-5f;
constexpr float QSCALE = 0.088388347648318440f * 1.4426950408889634f;

constexpr unsigned W_EPOCH = 0;
constexpr unsigned W_FLAG = 4096;
constexpr unsigned W_MOD = 32768;
constexpr unsigned W_MP = W_MOD + 40960;
constexpr unsigned W_P1 = 393216;
constexpr unsigned S_U = W_P1 + 8u * 64u * T * 16u;
constexpr unsigned S_H1 = S_U + 64u * T * 16u;
constexpr unsigned S_X1 = S_H1 + 64u * T * 16u;
constexpr unsigned S_HZ = S_X1 + 64u * T * 16u;
constexpr unsigned S_H3 = S_HZ + 256u * T * 16u;
constexpr unsigned W_END = S_H3 + 224u * T * 16u;
static_assert(W_MP + 8u * 4864u * 8u <= W_P1, "ws");
constexpr unsigned OOB = 0xF0000000u;
#define K1MAP 0
#define FLAG_SLEEP 4
#define NPRE 20
#define ATHR 32.0f
constexpr float THR = ATHR;
constexpr unsigned SENT = 0xFFFFFFFFu;
constexpr unsigned SPIN_CAP = 1u << 16;

constexpr int LDS_X = 0;
constexpr int LDS_R = 65536;
constexpr int RING = 5, RINGB = RING * 2048;
constexpr int LDS_D = LDS_R + 8 * RINGB;
constexpr int LDS_TOTAL = LDS_D + 16384;
static_assert(LDS_TOTAL == 163840, "lds");

#define STAMP(kid, i) do {} while (0)
DI unsigned cvtpk(float lo, float hi) {
  f32x2 v = {lo, hi};
  bf16x2_t b = __builtin_convertvector(v, bf16x2_t);
  return __builtin_bit_cast(unsigned, b);
}
DI float bflo(unsigned u) { return U2F(u << 16); }
DI float bfhi(unsigned u) { return U2F(u & 0xffff0000u); }
DI unsigned rfl(unsigned x) { return __builtin_amdgcn_readfirstlane(x); }
DI __amdgpu_buffer_rsrc_t mksrd(const void* p, unsigned bytes) {
  return __builtin_amdgcn_make_buffer_rsrc((void*)p, (short)0, (int)bytes, 0x00020000);
}
DI void dma16(unsigned lds, unsigned voff, __amdgpu_buffer_rsrc_t srd, unsigned soff) {
  asm volatile("s_mov_b32 m0, %0\n\ts_nop 0\n\tbuffer_load_dwordx4 %1, %2, %3 offen lds"
               :: "s"(lds), "v"(voff), "s"(srd), "s"(soff) : "memory");
}
DI void gld16(u32x4& d, const void* p) { asm volatile("global_load_dwordx4 %0, %1, off" : "=v"(d) : "v"(p) : "memory"); }
DI void bld16_sc1(u32x4& d, unsigned voff, __amdgpu_buffer_rsrc_t srd, unsigned soff) {
  asm volatile("buffer_load_dwordx4 %0, %1, %2, %3 offen sc1" : "=v"(d) : "v"(voff), "s"(srd), "s"(soff) : "memory");
}
DI void dma16_sc1(unsigned lds, unsigned voff, __amdgpu_buffer_rsrc_t srd, unsigned soff) {
  asm volatile("s_mov_b32 m0, %0\n\ts_nop 0\n\tbuffer_load_dwordx4 %1, %2, %3 offen sc1 lds"
               :: "s"(lds), "v"(voff), "s"(srd), "s"(soff) : "memory");
}
DI bf16x8 lds_b128(lds_cptr p) { return *(const LAS bf16x8*)p; }
DI s16x4 lds_tr(lds_cptr p) {
  return __builtin_bit_cast(s16x4, __builtin_amdgcn_ds_read_tr16_b64_v4i16((LAS v4i16_t*)p));
}
DI bf16x8 cat8(s16x4 lo, s16x4 hi) { return __builtin_shufflevector(lo, hi, 0, 1, 2, 3, 4, 5, 6, 7); }
DI float swap32_add(float x) {
  auto r = __builtin_amdgcn_permlane32_swap(F2U(x), F2U(x), false, false);
  return U2F(r[0]) + U2F(r[1]);
}
DI float swap32_max(float x) {
  auto r = __builtin_amdgcn_permlane32_swap(F2U(x), F2U(x), false, false);
  return __builtin_fmaxf(U2F(r[0]), U2F(r[1]));
}
template <int CTRL> DI float dpp_add(float x) {
  int y = __builtin_amdgcn_update_dpp(0, F2I(x), CTRL, 0xF, 0xF, true);
  return x + I2F(y);
}
DI float max3f(float a, float b, float c) { float r; asm("v_max3_f32 %0, %1, %2, %3" : "=v"(r) : "v"(a), "v"(b), "v"(c)); return r; }
DI float max2f(float a, float b) { float r; asm("v_max_f32_e32 %0, %1, %2" : "=v"(r) : "v"(a), "v"(b)); return r; }
DI float dot2bf(unsigned a, unsigned b, float c) {
  return __builtin_amdgcn_fdot2_f32_bf16(__builtin_bit_cast(bf16x2_t, a), __builtin_bit_cast(bf16x2_t, b), c, false);
}
#define PIN(x) asm volatile("" : "+v"(x))
DI float swap16_add(float x) {
  auto r = __builtin_amdgcn_permlane16_swap(F2U(x), F2U(x), false, false);
  return U2F(r[0]) + U2F(r[1]);
}
DI float sum_hi3(float x) { return swap32_add(swap16_add(dpp_add<0x128>(x))); }
DI float gelu_tanh(float x) {
  const float k0 = -2.0f * 0.7978845608028654f * 1.4426950408889634f;
  const float t = x * x;
  const float z = x * __builtin_fmaf(t, 0.044715f * k0, k0);
  const float e = __builtin_amdgcn_exp2f(z);
  return x * __builtin_amdgcn_rcpf(1.0f + e);
}
DI void wave_sum8(const float (&a)[8], float (&out)[8]) {
  float b[4], c[2];
#pragma unroll
  for (int i = 0; i < 4; ++i) {
    auto r = __builtin_amdgcn_permlane32_swap(F2U(a[i]), F2U(a[i + 4]), false, false);
    b[i] = U2F(r[0]) + U2F(r[1]);
  }
#pragma unroll
  for (int i = 0; i < 2; ++i) {
    auto r = __builtin_amdgcn_permlane16_swap(F2U(b[i]), F2U(b[i + 2]), false, false);
    c[i] = U2F(r[0]) + U2F(r[1]);
  }
#pragma unroll
  for (int i = 0; i < 2; ++i) {
    c[i] = dpp_add<0xB1>(c[i]);
    c[i] = dpp_add<0x4E>(c[i]);
    c[i] = dpp_add<0x141>(c[i]);
    c[i] = dpp_add<0x140>(c[i]);
  }
#pragma unroll
  for (int q = 0; q < 4; ++q)
#pragma unroll
    for (int i = 0; i < 2; ++i)
      out[i + 2 * (q & 1) + 4 * (q >> 1)] = I2F(__builtin_amdgcn_readlane(F2I(c[i]), 16 * q));
}

DI unsigned nosent(unsigned v) { return v < 0xFFFFFFFEu ? v : 0xFFFFFFFEu; }
DI void pub16(__amdgpu_buffer_rsrc_t srd, unsigned voff, unsigned soff, u32x4 v) {
  v = (u32x4){nosent(v[0]), nosent(v[1]), nosent(v[2]), nosent(v[3])};
  __builtin_amdgcn_raw_buffer_store_b128(v, srd, voff + soff, 0, 16);
}
DI unsigned umax3(unsigned a, unsigned b, unsigned c) { const unsigned m = a > b ? a : b; return m > c ? m : c; }
DI bool has_sent(const u32x4* v, int n) {
  unsigned m = 0;
#pragma unroll
  for (int i = 0; i < n; ++i) { m = umax3(m, v[i][0], v[i][1]); m = umax3(m, v[i][2], v[i][3]); }
  return ANY(m == SENT);
}
template <int N> DI void poll_load(u32x4* v, __amdgpu_buffer_rsrc_t srd, unsigned voff, unsigned soff0, unsigned step) {
  for (unsigned spin = 0;; ++spin) {
    asm volatile("" ::: "memory");
#pragma unroll
    for (int i = 0; i < N; ++i) v[i] = __builtin_amdgcn_raw_buffer_load_b128(srd, voff, soff0 + (unsigned)i * step, 16);
    if (!has_sent(v, N) || spin > SPIN_CAP) break;
    __builtin_amdgcn_s_sleep(1);
  }
}
template <int N, int I0, int I1> DI void poll_ind(u32x4* v, __amdgpu_buffer_rsrc_t srd, unsigned voff, unsigned soff0, unsigned step) {
#pragma unroll
  for (int i = 0; i < N; ++i) v[i] = __builtin_amdgcn_raw_buffer_load_b128(srd, voff, soff0 + (unsigned)i * step, 16);
  if (has_sent(v, N)) {
    u32x4 ind[2];
    poll_load<2>(ind, srd, voff, soff0 + (unsigned)I0 * step, (unsigned)(I1 - I0) * step);
    poll_load<N>(v, srd, voff, soff0, step);
  }
}

DI bool bad_tag(const u32x4* v, int n, unsigned tag) {
  unsigned m = 0;
#pragma unroll
  for (int i = 0; i < n; ++i) m |= (v[i][1] ^ tag) | (v[i][3] ^ tag);
  return m != 0;
}
template <int N> DI void poll_tag(u32x4* v, __amdgpu_buffer_rsrc_t srd, unsigned voff, unsigned soff0, unsigned step, unsigned tag) {
  for (unsigned spin = 0;; ++spin) {
    asm volatile("" ::: "memory");
#pragma unroll
    for (int i = 0; i < N; ++i) v[i] = __builtin_amdgcn_raw_buffer_load_b128(srd, voff, soff0 + (unsigned)i * step, 16);
    if (!ANY(voff < OOB && bad_tag(v, N, tag)) || spin > SPIN_CAP) break;
    __builtin_amdgcn_s_sleep(1);
  }
}

struct LaneC {
  unsigned lane, r, h, q, p, blk;
  unsigned wv;
  unsigned afr[2];
  unsigned xb[4];
};
DI void lane_consts(LaneC& c) {
  unsigned l = TIDX & 63;
  asm volatile("" : "+v"(l));
  c.lane = l; c.r = l & 31; c.h = l >> 5; c.q = (l & 15) >> 2; c.p = l & 3; c.blk = (l >> 4) & 1;
  const unsigned sp = 2 * (c.p & 1) + (c.p >> 1);
#pragma unroll
  for (int nt = 0; nt < 2; ++nt) c.afr[nt] = (8 * c.h + c.q) * 128 + 64 * (nt ^ (c.q >> 1)) + 32 * c.blk + 8 * sp;
#pragma unroll
  for (int s = 0; s < 4; ++s) c.xb[s] = 1024 * c.h + ((c.r ^ (2 * s + c.h)) << 4);
}
DI unsigned wdma_voff(unsigned l, unsigned ld) { return (l >> 3) * ld + (((l & 7) ^ (((l >> 4) & 1) << 2)) << 4); }

DI void wring_issue(int ks, unsigned ring, unsigned voff, __amdgpu_buffer_rsrc_t srd, unsigned soff0, unsigned ld) {
  const unsigned slot = ring + (unsigned)(ks % RING) * 2048u;
  dma16(slot, voff, srd, soff0 + (unsigned)(16 * ks) * ld);
  dma16(slot + 1024u, voff ^ 32u, srd, soff0 + (unsigned)(16 * ks + 8) * ld);
}
template <int N> DI void wait_vm() {
  if constexpr (N == 0) WAIT_V(0); else if constexpr (N == 1) WAIT_V(1); else if constexpr (N == 2) WAIT_V(2);
  else if constexpr (N == 3) WAIT_V(3); else if constexpr (N == 4) WAIT_V(4); else if constexpr (N == 5) WAIT_V(5);
  else if constexpr (N == 6) WAIT_V(6); else if constexpr (N == 7) WAIT_V(7); else if constexpr (N == 8) WAIT_V(8);
  else if constexpr (N == 9) WAIT_V(9); else if constexpr (N == 10) WAIT_V(10); else if constexpr (N == 11) WAIT_V(11);
  else if constexpr (N == 12) WAIT_V(12); else if constexpr (N == 13) WAIT_V(13); else if constexpr (N == 14) WAIT_V(14);
  else if constexpr (N == 15) WAIT_V(15); else if constexpr (N == 16) WAIT_V(16);
  else if constexpr (N == 17) WAIT_V(17); else if constexpr (N == 18) WAIT_V(18);
  else static_assert(N < 0, "wait_vm");
}

#define MFMA16(a, b, c) __builtin_amdgcn_mfma_f32_16x16x32_bf16((a), (b), (c), 0, 0, 0)
struct G16 { unsigned a[4], hi, x[2]; };
DI void g16_consts(G16& k, unsigned l, unsigned ringoff) {
  const unsigned i = l & 15, g = l >> 4, q = i >> 2, p = l & 3, x = 2 * (q >> 1) + (g & 1);
#pragma unroll
  for (int m = 0; m < 4; ++m) k.a[m] = ringoff + (8 * (g & 1) + q) * 128 + ((2 * (m ^ x) + (p >> 1)) << 4) + 8 * (p & 1);
  k.hi = g >> 1;
#pragma unroll
  for (int e = 0; e < 2; ++e) k.x[e] = LDS_X + 1024 * g + ((i ^ (4 * e + g)) << 4);
}
struct Frag { bf16x8 a[4], b[4]; };
template <int KK> DI void load_frags(Frag& f, lds_cptr lds, const G16& k) {
  constexpr int sA = (2 * KK) % RING;
  const unsigned d = sA == RING - 1 ? (1u - k.hi) * (unsigned)((RING - 1) * 2048) : k.hi * 2048u;
  constexpr int o = sA == RING - 1 ? 0 : sA * 2048;
#pragma unroll
  for (int m = 0; m < 4; ++m) f.a[m] = cat8(lds_tr(lds + (k.a[m] + d) + o), lds_tr(lds + (k.a[m] + d) + o + 512));
#pragma unroll
  for (int r = 0; r < 4; ++r) f.b[r] = lds_b128(lds + k.x[KK & 1] + 4096 * KK + 256 * r);
}
template <int KK, int EXTRA>
DI void gemm_step(f32x4 (&acc)[4][4], Frag& cur, Frag& nxt, lds_cptr lds, unsigned ring, unsigned voff,
                  __amdgpu_buffer_rsrc_t srd, unsigned soff0, unsigned ld, const G16& k) {
  if constexpr (2 * KK + RING < 32) {
    asm volatile("" :: "v"(cur.a[0]), "v"(cur.a[1]), "v"(cur.a[2]), "v"(cur.a[3]));
    wring_issue(2 * KK + RING, ring, voff, srd, soff0, ld);
    if constexpr (2 * KK + RING + 1 < 32) wring_issue(2 * KK + RING + 1, ring, voff, srd, soff0, ld);
  }
  if constexpr (KK + 1 < 16) {
    constexpr int last = 2 * KK + RING + 1 < 32 ? 2 * KK + RING + 1 : 31;
    wait_vm<2 * (last - (2 * KK + 3)) + ((2 * KK + 3 < RING) ? EXTRA : 0)>();
    load_frags<KK + 1>(nxt, lds, k);
  }
  SBAR();
#pragma unroll
  for (int m = 0; m < 4; ++m)
#pragma unroll
    for (int r = 0; r < 4; ++r) acc[m][r] = MFMA16(cur.a[m], cur.b[r], acc[m][r]);
  SBAR();
  if constexpr (KK + 1 < 16) gemm_step<KK + 1, EXTRA>(acc, nxt, cur, lds, ring, voff, srd, soff0, ld, k);
}
template <int EXTRA>
DI void gemm_phase(f32x4 (&acc)[4][4], lds_cptr lds, unsigned ringoff, unsigned ring, unsigned voff,
                   __amdgpu_buffer_rsrc_t srd, unsigned soff0, unsigned ld, unsigned l) {
  G16 k; g16_consts(k, l, ringoff);
  Frag f0, f1;
  wait_vm<2 * (RING - 2) + EXTRA>();
  load_frags<0>(f0, lds, k);
  gemm_step<0, EXTRA>(acc, f0, f1, lds, ring, voff, srd, soff0, ld, k);
}
DI u32x4 pack16(const f32x4& a, const f32x4& b, float sc) {
  auto r0 = __builtin_amdgcn_permlane16_swap(cvtpk(a[0] * sc, a[1] * sc), cvtpk(b[0] * sc, b[1] * sc), false, false);
  auto r1 = __builtin_amdgcn_permlane16_swap(cvtpk(a[2] * sc, a[3] * sc), cvtpk(b[2] * sc, b[3] * sc), false, false);
  return (u32x4){r0[0], r1[0], r0[1], r1[1]};
}
DI u32x4 pack8(const f32x16& a, int s, float sc) {
  return (u32x4){cvtpk(a[8 * s] * sc, a[8 * s + 1] * sc), cvtpk(a[8 * s + 2] * sc, a[8 * s + 3] * sc),
                 cvtpk(a[8 * s + 4] * sc, a[8 * s + 5] * sc), cvtpk(a[8 * s + 6] * sc, a[8 * s + 7] * sc)};
}
DI unsigned ximg(unsigned chunk, unsigned row) { return chunk * 1024u + ((row ^ (chunk & 7u)) << 4); }

constexpr int KSLOT[8] = {LDS_D, LDS_R, LDS_R + 32768, LDS_X, LDS_X + 32768, LDS_D, LDS_X, LDS_X + 32768};
constexpr int VSLOT[8] = {LDS_R + 16384, LDS_R + 49152, LDS_X + 16384, LDS_X + 49152, LDS_R + 65536, LDS_X + 16384, LDS_X + 49152, LDS_D};
template <int N> struct IC { static constexpr int value = N; };
struct Params {
  const unsigned short *x, *kc, *vc, *wq, *wo, *w1, *w2, *w3, *w4, *c, *wmod;
  void* ws;
  u32x4* out;
};
DI unsigned attn_part(const Params& P, lds_ptr lds, unsigned lds0, unsigned w, __amdgpu_buffer_rsrc_t srdW) {
  LaneC c; lane_consts(c);
  const unsigned g = BIDX & 7, pn = BIDX >> 3;
  const unsigned l = c.lane, tid = TIDX;
  const unsigned ringoff = LDS_R + w * RINGB, ring = lds0 + ringoff;
  STAMP(1, 0);

  unsigned ep;
  asm volatile("buffer_load_dword %0, off, %1, 0 sc1" : "=v"(ep) : "s"(srdW) : "memory");
  u32x4 jw[4]; unsigned jc[4];
  auto job_load = [&](unsigned jcol, unsigned jks) __attribute__((always_inline)) {
    const bool isw3 = jcol >= 96u;
    const unsigned short* src = isw3 ? P.w3 + (jcol - 96u) * 32 + (l & 3) * 8 : P.wmod + jcol * 32 + (l & 3) * 8;
    const unsigned ld = isw3 ? F : 3072, r0 = jks * 64 + (l >> 2);
#pragma unroll
    for (int i = 0; i < 4; ++i) gld16(jw[i], src + (size_t)(r0 + 16 * i) * ld);
#pragma unroll
    for (int i = 0; i < 4; ++i) asm volatile("global_load_ushort %0, %1, off" : "=v"(jc[i]) : "v"(P.c + r0 + 16 * i) : "memory");
  };
  auto job_run = [&](unsigned jcol, unsigned jks, unsigned tag) __attribute__((always_inline)) {
    asm volatile("s_waitcnt vmcnt(0)" : "+v"(jw[0]), "+v"(jw[1]), "+v"(jw[2]), "+v"(jw[3]), "+v"(jc[0]), "+v"(jc[1]), "+v"(jc[2]), "+v"(jc[3]) :: "memory");
    float acc[8] = {0, 0, 0, 0, 0, 0, 0, 0};
#pragma unroll
    for (int i = 0; i < 4; ++i) {
      const float v = U2F(jc[i] << 16);
      const float sv = v * __builtin_amdgcn_rcpf(1.0f + __builtin_amdgcn_exp2f(-1.4426950408889634f * v));
      const float sc = jcol >= 96u ? 1.0f : sv;
#pragma unroll
      for (int k = 0; k < 4; ++k) {
        acc[2 * k] = __builtin_fmaf(sc, bflo(jw[i][k]), acc[2 * k]);
        acc[2 * k + 1] = __builtin_fmaf(sc, bfhi(jw[i][k]), acc[2 * k + 1]);
      }
    }
#pragma unroll
    for (int k = 0; k < 8; ++k) acc[k] = sum_hi3(dpp_add<0x124>(acc[k]));
    const unsigned e = (l >> 2) & 3;
    const float v0 = e == 0 ? acc[0] : e == 1 ? acc[2] : e == 2 ? acc[4] : acc[6], v1 = e == 0 ? acc[1] : e == 1 ? acc[3] : e == 2 ? acc[5] : acc[7];
    __builtin_amdgcn_raw_buffer_store_b128((u32x4){F2U(v0), tag, F2U(v1), tag}, srdW,
                                           l < 16 ? W_MP + (jks * 4864u + jcol * 32u + (l & 3) * 8u + e * 2u) * 8u : OOB, 0, 16);
  };
  u32x4 xr[8];
  auto x_load = [&]() __attribute__((always_inline)) {
#pragma unroll
    for (int i = 0; i < 8; ++i) gld16(xr[i], P.x + (size_t)(pn * 64 + w * 8 + i) * D + l * 8);
  };
  if (w == 0) job_load(BIDX & 31u, BIDX >> 5); else x_load();
  asm volatile("s_waitcnt vmcnt(8)\n\ts_barrier" : "+v"(ep) :: "memory");
  const unsigned tag = rfl(ep) + 1u;
  if (w == 0) { job_run(BIDX & 31u, BIDX >> 5, tag); x_load(); }
  const __amdgpu_buffer_rsrc_t srdQ = mksrd(P.wq, 512u * 4096u * 2u);
  const unsigned vq = wdma_voff(l, 8192u), sq0 = (g * 512u + w * 64u) * 2u;
  const __amdgpu_buffer_rsrc_t srdK = mksrd(P.kc + ((size_t)g * SK + (SK - WIN)) * DH, WIN * DH * 2u);
  const __amdgpu_buffer_rsrc_t srdV = mksrd(P.vc + ((size_t)g * SK + (SK - WIN)) * DH, WIN * DH * 2u);
  const unsigned vk = ((l >> 2) & 7) * 256u + (l >> 5) * 64u + (((l & 3) ^ (2 * (w & 1) + ((l >> 4) & 1))) << 4);
  const unsigned vv = wdma_voff(l, 256u);
  auto dma_k = [&](int t, unsigned slot) {
    dma16(lds0 + slot + 2048u * w, vk, srdK, (unsigned)t * 16384u + 2048u * w);
    dma16(lds0 + slot + 2048u * w + 1024u, vk, srdK, (unsigned)t * 16384u + 2048u * w + 128u);
  };
  auto dma_v = [&](int t, unsigned slot) {
    dma16(lds0 + slot + 1024u * w, vv, srdV, (unsigned)t * 16384u + 2048u * w);
    dma16(lds0 + slot + 8192u + 1024u * w, vv, srdV, (unsigned)t * 16384u + 2048u * w + 128u);
  };

  {
    asm volatile("s_waitcnt vmcnt(0)" : "+v"(xr[0]), "+v"(xr[1]), "+v"(xr[2]), "+v"(xr[3]), "+v"(xr[4]), "+v"(xr[5]), "+v"(xr[6]),
                 "+v"(xr[7]) :: "memory");
    dma16(ring + 8192u, l * 16u, srdQ, (2 * (pn * 8 + w)) * 8192u + g * 1024u);
    dma16(ring + 9216u, l * 16u, srdQ, (2 * (pn * 8 + w) + 1) * 8192u + g * 1024u);
    wring_issue(0, ring, vq, srdQ, sq0, 8192u);
    wring_issue(1, ring, vq, srdQ, sq0, 8192u);
    float v[8][8], s[8], m[8];
#pragma unroll
    for (int i = 0; i < 8; ++i) {
#pragma unroll
      for (int j = 0; j < 4; ++j) { v[i][2 * j] = bflo(xr[i][j]); v[i][2 * j + 1] = bfhi(xr[i][j]); }
      s[i] = ((v[i][0] + v[i][1]) + (v[i][2] + v[i][3])) + ((v[i][4] + v[i][5]) + (v[i][6] + v[i][7]));
    }
    wave_sum8(s, m);
#pragma unroll
    for (int i = 0; i < 8; ++i) {
      const float mu = m[i] * (1.0f / D);
      float q = 0.f;
#pragma unroll
      for (int j = 0; j < 8; ++j) { v[i][j] -= mu; q = __builtin_fmaf(v[i][j], v[i][j], q); }
      s[i] = q;
    }
    wave_sum8(s, m);
    float rs[8];
#pragma unroll
    for (int i = 0; i < 8; ++i) rs[i] = __builtin_amdgcn_rsqf(m[i] * (1.0f / D) + LN_EPS);
    STAMP(1, 8);
    {
      u32x4 mp[8];
      poll_tag<8>(mp, srdW, tid * 16u, W_MP, 4864u * 8u, tag);
      f32x2 t;
#pragma unroll
      for (int k = 0; k < 2; ++k)
        t[k] = ((U2F(mp[0][2 * k]) + U2F(mp[1][2 * k])) + (U2F(mp[2][2 * k]) + U2F(mp[3][2 * k]))) +
               ((U2F(mp[4][2 * k]) + U2F(mp[5][2 * k])) + (U2F(mp[6][2 * k]) + U2F(mp[7][2 * k])));
      *(LAS f32x2*)(lds + LDS_D + tid * 8) = t;
    }
    BAR_L();
    if (BIDX == 0 && tid == 0) __builtin_amdgcn_raw_buffer_store_b32(tag, srdW, W_EPOCH, 0, 16);
    u32x4 mo[4];
#pragma unroll
    for (int i = 0; i < 4; ++i) mo[i] = *(const LAS u32x4*)(lds + LDS_D + (i >> 1) * 2048 + l * 32 + (i & 1) * 16);
    STAMP(1, 9);
#pragma unroll
    for (int i = 0; i < 8; ++i) {
      float hh[8];
#pragma unroll
      for (int j = 0; j < 8; ++j) hh[j] = __builtin_fmaf(v[i][j] * rs[i], 1.f + U2F(mo[2 + (j >> 2)][j & 3]), U2F(mo[j >> 2][j & 3]));
      const u32x4 o = {cvtpk(hh[0], hh[1]), cvtpk(hh[2], hh[3]), cvtpk(hh[4], hh[5]), cvtpk(hh[6], hh[7])};
      *(LAS u32x4*)(lds + LDS_X + ximg(l, w * 8 + i)) = o;
      if (i == 1 || i == 3 || i == 5) wring_issue(2 + i / 2, ring, vq, srdQ, sq0, 8192u);
    }
  }
  BAR_L();
  dma_k(0, LDS_D);
  STAMP(1, 1);

  {
    f32x4 acc[4][4] = {};
    gemm_phase<2>(acc, lds, ringoff, ring, vq, srdQ, sq0, 8192u, l);
    STAMP(1, 2);
    if (w < 4 && w * 256u + BIDX < 960u) {
      const unsigned jid = w * 256u + BIDX;
      job_load(32u + jid % 120u, jid / 120u);
      job_run(32u + jid % 120u, jid / 120u, tag);
    }
    WAIT_BAR(0);
    dma_k(1, KSLOT[1]); dma_v(0, VSLOT[0]); dma_k(2, KSLOT[2]); dma_v(1, VSLOT[1]);
    const unsigned cl = 8 * w + 2 * ((l >> 4) & 1) + (l >> 5);
#pragma unroll
    for (int r = 0; r < 4; ++r)
#pragma unroll
      for (int mp = 0; mp < 2; ++mp)
        *(LAS u32x4*)(lds + LDS_X + ximg(cl + 4 * mp, 16 * r + (l & 15))) = pack16(acc[2 * mp][r], acc[2 * mp + 1][r], QSCALE);
    BAR_L();
  }

  STAMP(1, 3);
  const unsigned hh = w >> 1, rt = w & 1;
  bf16x8 qf[8];
#pragma unroll
  for (int s = 0; s < 8; ++s) qf[s] = lds_b128(lds + LDS_X + ximg(16 * hh + 2 * s + c.h, 32 * rt + c.r));
  unsigned kb[2], vb[2];
#pragma unroll
  for (int i = 0; i < 2; ++i) {
    kb[i] = 2048 * (c.r >> 3) + 64 * (c.r & 7) + (((2 * i + c.h) ^ ((c.r >> 2) & 3)) << 4);
    vb[i] = (4 * c.h + c.q) * 128 + 64 * (i ^ (c.q >> 1)) + 32 * c.blk + 8 * (2 * (c.p & 1) + (c.p >> 1));
  }
  const __amdgpu_buffer_rsrc_t srdO = mksrd(P.wo, 4096u * 512u * 2u);
  const unsigned vo = wdma_voff(l, 1024u), so0 = g * 512u * 1024u + w * 128u;

  f32x16 o[4] = {};
  f32x16 s0, s1;
  f32x16 negm;
  float lsum = 0.f;
  u32x4 pk[4];
  constexpr int PD = 6;
  bf16x8 fr[PD];
  const bool lag = w >= 4;
  auto frag = [&](int t, int it) -> bf16x8 {
    if (it < 16) {
      const lds_cptr a = lds + VSLOT[t > 0 ? t - 1 : 0] + vb[it & 1] + 8192 * ((it >> 1) & 1) + 2048 * (it >> 2);
      return cat8(lds_tr(a), lds_tr(a + 1024));
    }
    const int s = (it - 16) >> 1;
    return lds_b128(lds + KSLOT[t < 8 ? t : 0] + kb[s & 1] + 512 * (s >> 1) + 8192 * (it & 1));
  };
  auto seam = [&](int t, int tx) __attribute__((always_inline)) {
    if (t == 7) { BAR_L(); } else if (tx >= 1 && tx <= 5) { WAIT_BAR(4); } else if (tx == 6) { WAIT_BAR(2); } else { WAIT_BAR(0); }
  };
  auto refill = [&](int t) __attribute__((always_inline)) {
    if (t + 3 < 8) dma_k(t + 3, KSLOT[t + 3 < 8 ? t + 3 : 0]);
    if (t + 2 < 8) dma_v(t + 2, VSLOT[t + 2 < 8 ? t + 2 : 0]);
    if (t == 6) {
#pragma unroll
      for (int ks = 0; ks < RING; ++ks) wring_issue(ks, ring, vo, srdO, so0, 1024u);
    }
  };
  auto extra = [&](int e) __attribute__((always_inline)) {
    const unsigned dump = lds0 + LDS_R + w * 3072u + (unsigned)(e % 3) * 1024u;
    const unsigned id = (BIDX >> 3) * 8 + w, jh = BIDX & 1;
    const unsigned vrow = (l >> 5) * 1024u + (l & 31) * 16u + jh * 512u;
    auto w3v = [&](unsigned piece) { const unsigned byte = piece * 1024u + l * 16u, row = byte / 1792u;
                                     return row * 3584u + jh * 1792u + (byte - row * 1792u); };
    if (e < 4 || e >= 15) {
      const int i = e < 4 ? e : e - 11;
      const unsigned off = i < 3 ? S_U + (unsigned)i * (64u * T * 16u) + pn * 65536u + g * 8192u
                         : i < 7 ? S_HZ + pn * 262144u + g * 32768u + (unsigned)(i - 3) * 8192u
                                 : S_H3 + pn * 229376u + g * 28672u + (unsigned)(i - 7) * 8192u;
      const u32x4 sv = {SENT, SENT, SENT, SENT};
      __builtin_amdgcn_raw_buffer_store_b128(sv, srdW, (i == 10 && tid >= 256) ? OOB : tid * 16u + off, 0, 16);
    } else if (e < 6) dma16(dump, l * 16u, srdO, (g * 512u + (pn * 8 + w) * 2 + (e - 4)) * 1024u);
    else if (e == 6) dma16(dump, vrow, mksrd(P.w1, 512u * 1024u), id * 2048u);
    else if (e == 7) dma16(dump, vrow, mksrd(P.w2, 512u * 1024u), id * 2048u);
    else if (e < 11) dma16(dump, w3v(id + 256u * (e - 8)), mksrd(P.w3, 512u * 3584u), 0u);
    else if (e == 11) {
      if (id < 128) dma16(dump, w3v(id + 768u), mksrd(P.w3, 512u * 3584u), 0u);
      else dma16(dump, vrow, mksrd(P.w4, 1792u * 1024u), (id - 128u + 768u) * 2048u);
    } else dma16(dump, vrow, mksrd(P.w4, 1792u * 1024u), (id + 256u * (e - 12)) * 2048u);
  };
  auto step = [&](auto tc) __attribute__((always_inline)) {
    constexpr int t = decltype(tc)::value;
    constexpr int i0 = t == 0 ? 16 : 0, i1 = t == 8 ? 16 : 32;
    if (t <= 1) {
#pragma unroll
      for (int it = i0; it < i0 + PD; ++it) fr[it % PD] = frag(t, it);
    }
    if (t == 0) { s0 = (f32x16){}; s1 = (f32x16){}; } else if (t < 8) { s0 = negm; s1 = negm; }
    __builtin_amdgcn_s_setprio(1);
    SBAR();
#pragma unroll
    for (int it = i0; it < i1; ++it) {
      if (it < 16) o[it & 3] = MFMA32(fr[it % PD], __builtin_bit_cast(bf16x8, pk[it >> 2]), o[it & 3]);
      else if (it & 1) s1 = MFMA32(fr[it % PD], qf[(it - 16) >> 1], s1);
      else s0 = MFMA32(fr[it % PD], qf[(it - 16) >> 1], s0);
      if (it + PD < i1) fr[it % PD] = frag(t, it + PD);
      if (t >= 1 && it == 1) { if (!lag) refill(t - 1); }
      SBAR();
    }
    if constexpr (t == 8) return;
    __builtin_amdgcn_s_setprio(0);
    if (lag) { seam(t, t - 1); refill(t); }
    asm volatile("s_nop 15" : "+v"(s0), "+v"(s1));
    float ta = max3f(s0[0], s0[1], s1[0]), tb = max3f(s0[2], s0[3], s1[1]);
    ta = max3f(ta, s1[2], s1[3]);
#pragma unroll
    for (int i = 4; i < 16; i += 4) {
      ta = max3f(ta, s0[i], s0[i + 1]); tb = max3f(tb, s0[i + 2], s0[i + 3]);
      ta = max3f(ta, s1[i], s1[i + 1]); tb = max3f(tb, s1[i + 2], s1[i + 3]);
    }
    float tm = max2f(ta, tb);
    {
      auto r = __builtin_amdgcn_permlane32_swap(F2U(tm), F2U(tm), false, false);
      tm = max2f(U2F(r[0]), U2F(r[1]));
    }
    if (t == 0 || ANY(tm > THR)) {
      const float ndl = t == 0 ? -tm : -max2f(tm, 0.f);
      const float aone = c.h ? 0.f : 1.f;
      if (t == 0) negm = (f32x16){};
      __builtin_amdgcn_s_setprio(2);
      s0 = __builtin_amdgcn_mfma_f32_32x32x2f32(aone, ndl, s0, 0, 0, 0);
      s1 = __builtin_amdgcn_mfma_f32_32x32x2f32(aone, ndl, s1, 0, 0, 0);
      negm = __builtin_amdgcn_mfma_f32_32x32x2f32(aone, ndl, negm, 0, 0, 0);
      __builtin_amdgcn_s_setprio(0);
      if (t > 0) {
        const float al = __builtin_amdgcn_exp2f(ndl);
        lsum *= al;
#pragma unroll
        for (int dt = 0; dt < 4; ++dt)
#pragma unroll
          for (int i = 0; i < 16; ++i) o[dt][i] *= al;
      }
    }
    SBAR();
    if (t >= 1) {
#pragma unroll
      for (int it = 0; it < PD; ++it) fr[it] = frag(t + 1, it);
    }
#pragma unroll
    for (int i = 0; i < 16; ++i) {
      s0[i] = __builtin_amdgcn_exp2f(s0[i]); s1[i] = __builtin_amdgcn_exp2f(s1[i]);
      if (t >= 1 && (i & 3) == 1 && 4 * (t - 1) + (i >> 2) < 22) { PIN(s0[i]); extra(4 * (t - 1) + (i >> 2)); }
    }
    pk[0] = pack8(s0, 0, 1.f); pk[1] = pack8(s0, 1, 1.f); pk[2] = pack8(s1, 0, 1.f); pk[3] = pack8(s1, 1, 1.f);
    {
      float l0 = lsum, l1 = 0.f, l2 = 0.f, l3 = 0.f;
#pragma unroll
      for (int i = 0; i < 16; i += 2) { l0 += s0[i]; l1 += s0[i + 1]; l2 += s1[i]; l3 += s1[i + 1]; }
      lsum = (l0 + l1) + (l2 + l3);
    }
    PIN(pk[0]); PIN(pk[1]); PIN(pk[2]); PIN(pk[3]); PIN(lsum);
    if constexpr (t == 3) {
      if (BIDX < 120 && w == 0) {
        u32x4 mp[2];
        poll_tag<2>(mp, srdW, ((l >> 3) * 4864u + (32u + BIDX) * 32u + (l & 7) * 2u) * 8u, W_MP, 128u, tag);
        const float a0 = sum_hi3(U2F(mp[0][0])), a1 = sum_hi3(U2F(mp[0][2])), b0 = sum_hi3(U2F(mp[1][0])), b1 = sum_hi3(U2F(mp[1][2]));
        const bool hi = (l >> 3) & 1;
        __builtin_amdgcn_raw_buffer_store_b128((u32x4){F2U(hi ? b0 : a0), tag, F2U(hi ? b1 : a1), tag}, srdW,
                                               l < 16 ? W_MOD + ((32u + BIDX) * 32u + l * 2u) * 8u : OOB, 0, 16);
      }
    }
    SBAR();
    if (!lag) seam(t, t);
  };
  step(IC<0>{}); step(IC<1>{}); step(IC<2>{}); step(IC<3>{}); step(IC<4>{}); step(IC<5>{}); step(IC<6>{}); step(IC<7>{}); step(IC<8>{});
  __builtin_amdgcn_s_setprio(0);
  STAMP(1, 4);
  {
    const float il = __builtin_amdgcn_rcpf(swap32_add(lsum));
#pragma unroll
    for (int dt = 0; dt < 4; ++dt)
#pragma unroll
      for (int s = 0; s < 2; ++s)
        *(LAS u32x4*)(lds + LDS_X + ximg(16 * hh + 4 * dt + 2 * s + c.h, 32 * rt + c.r)) = pack8(o[dt], s, il);
  }
  BAR_L();
  STAMP(1, 5);
  dma16_sc1(lds0 + LDS_D + w * 2048u, l * 16u, srdW, W_MOD + 8192u + w * 2048u);
  dma16_sc1(lds0 + LDS_D + w * 2048u + 1024u, l * 16u, srdW, W_MOD + 8192u + w * 2048u + 1024u);
  {
    unsigned l2 = TIDX & 63;
    asm volatile("" : "+v"(l2));
    f32x4 acc[4][4] = {};
    gemm_phase<2>(acc, lds, ringoff, ring, vo, srdO, so0, 1024u, l2);
    STAMP(1, 6);
    const unsigned dst = W_P1 + ((((pn * 8 + ((l2 >> 3) & 1)) * 8 + g) * 64 + 8 * w + 2 * ((l2 >> 4) & 1) + (l2 >> 5)) * 8 + (l2 & 7)) * 16u;
#pragma unroll
    for (int r = 0; r < 4; ++r)
#pragma unroll
      for (int mp = 0; mp < 2; ++mp)
        __builtin_amdgcn_raw_buffer_store_b128(pack16(acc[2 * mp][r], acc[2 * mp + 1][r], 1.f), srdW,
                                               dst + (4 * mp * 8 + r * 2 * 8 * 64 * 8) * 16u, 0, 16);
  }
  WAIT_BAR(0);
  if (tid == 0) __builtin_amdgcn_raw_buffer_store_b32(tag, srdW, W_FLAG + (pn * 8 + g) * 64u, 0, 16);
  STAMP(1, 7);
  return tag;
}

DI void ln8(float (&v)[8], LAS float* red  , unsigned tid) {
  float s = ((v[0] + v[1]) + (v[2] + v[3])) + ((v[4] + v[5]) + (v[6] + v[7]));
  float q = (__builtin_fmaf(v[0], v[0], v[1] * v[1]) + __builtin_fmaf(v[2], v[2], v[3] * v[3])) +
            (__builtin_fmaf(v[4], v[4], v[5] * v[5]) + __builtin_fmaf(v[6], v[6], v[7] * v[7]));
  s = sum_hi3(s); q = sum_hi3(q);
  if ((tid & 63) < 8) *(LAS f32x2*)(red + ((tid >> 6) * 8 + (tid & 7)) * 2) = (f32x2){s, q};
  BAR_L();
  f32x2 e[8];
#pragma unroll
  for (int i = 0; i < 8; ++i) e[i] = *(const LAS f32x2*)(red + (i * 8 + (tid & 7)) * 2);
  const float mu = (((e[0][0] + e[1][0]) + (e[2][0] + e[3][0])) + ((e[4][0] + e[5][0]) + (e[6][0] + e[7][0]))) * (1.0f / D);
  const float ms = (((e[0][1] + e[1][1]) + (e[2][1] + e[3][1])) + ((e[4][1] + e[5][1]) + (e[6][1] + e[7][1]))) * (1.0f / D);
  const float rs = __builtin_amdgcn_rsqf(__builtin_fmaxf(__builtin_fmaf(-mu, mu, ms), 0.f) + LN_EPS);
  const float nb = -mu * rs;
#pragma unroll
  for (int k = 0; k < 8; ++k) v[k] = __builtin_fmaf(v[k], rs, nb);
}
DI void red4_put(const f32x16& a, int nt, lds_ptr scr, unsigned rt, unsigned kq, unsigned l) {
#pragma unroll
  for (int g = 0; g < 2; ++g)
#pragma unroll
    for (int i = 0; i < 2; ++i) {
      const int o = 8 * g + 4 * i;
      *(LAS f32x4*)(scr + ((rt * 4 + 2 * nt + g) * 4 + kq) * 2048 + i * 1024 + l * 16) = (f32x4){a[o], a[o + 1], a[o + 2], a[o + 3]};
    }
}
DI void red4_get(float (&v)[8], lds_cptr scr, unsigned rt, unsigned kq, unsigned l) {
  f32x4 t[4][2];
#pragma unroll
  for (int s = 0; s < 4; ++s)
#pragma unroll
    for (int i = 0; i < 2; ++i) t[s][i] = *(const LAS f32x4*)(scr + ((rt * 4 + kq) * 4 + s) * 2048 + i * 1024 + l * 16);
#pragma unroll
  for (int i = 0; i < 2; ++i)
#pragma unroll
    for (int k = 0; k < 4; ++k) v[4 * i + k] = (t[0][i][k] + t[1][i][k]) + (t[2][i][k] + t[3][i][k]);
}
DI void tile_mm8(const bf16x8 (&af)[8][2], const u32x4 (&xb)[8], float (&v)[8], lds_ptr scr, unsigned rt, unsigned kq, unsigned l) {
  f32x16 a0 = {}, a1 = {};
#pragma unroll
  for (int i = 0; i < 8; ++i) a0 = MFMA32(af[i][0], __builtin_bit_cast(bf16x8, xb[i]), a0);
  SBAR();
#pragma unroll
  for (int i = 0; i < 4; ++i) a1 = MFMA32(af[i][1], __builtin_bit_cast(bf16x8, xb[i]), a1);
  SBAR();
  red4_put(a0, 0, scr, rt, kq, l);
#pragma unroll
  for (int i = 4; i < 8; ++i) a1 = MFMA32(af[i][1], __builtin_bit_cast(bf16x8, xb[i]), a1);
  SBAR();
  red4_put(a1, 1, scr, rt, kq, l);
  BAR_L();
  red4_get(v, scr, rt, kq, l);
}

struct Frag3 { bf16x8 a, b[2]; };
constexpr int RING3 = 13;
constexpr int LA = 0, LB = 65536, LC = 131072;
DI unsigned r3off(int s, unsigned rc, unsigned rb) { return s < 4 ? rc + s * 1024u : rb + (s - 4) * 1024u; }
template <int KS> DI void load_frags3(Frag3& f, lds_cptr lds, unsigned rc, unsigned rb, unsigned a3, const LaneC& c) {
  const lds_cptr sl = lds + r3off(KS % RING3, rc, rb) + a3;
  f.a = cat8(lds_tr(sl), lds_tr(sl + 256));
#pragma unroll
  for (int rt = 0; rt < 2; ++rt) f.b[rt] = lds_b128(lds + LA + c.xb[KS & 3] + 2048 * KS + 512 * rt);
}
template <int KS>
DI void g3_step(f32x16 (&acc)[2], Frag3& cur, Frag3& n1, Frag3& n2, lds_cptr lds, unsigned lds0, unsigned rc, unsigned rb,
                unsigned voff, __amdgpu_buffer_rsrc_t srd, unsigned soff0, unsigned a3, const LaneC& c) {
  constexpr int NKS = 32;
  if constexpr (KS + 2 < NKS) {
    constexpr int last = (KS + RING3 < NKS ? KS + RING3 : NKS) - 1;
    wait_vm<last - (KS + 2)>();
    load_frags3<KS + 2>(n2, lds, rc, rb, a3, c);
  }
  SBAR();
  acc[0] = MFMA32(cur.a, cur.b[0], acc[0]);
  acc[1] = MFMA32(cur.a, cur.b[1], acc[1]);
  if constexpr (KS + RING3 < NKS) {
    asm volatile("" :: "v"(cur.a));
    dma16(lds0 + r3off(KS % RING3, rc, rb), voff, srd, soff0 + (unsigned)(16 * (KS + RING3)) * 3584u);
  }
  SBAR();
  if constexpr (KS + 1 < NKS) g3_step<KS + 1>(acc, n1, n2, cur, lds, lds0, rc, rb, voff, srd, soff0, a3, c);
}
DI void mlp_part(const Params& P, lds_ptr lds, unsigned lds0, unsigned w, __amdgpu_buffer_rsrc_t srdS, unsigned tag) {
  LaneC c; lane_consts(c);
  const unsigned idx = BIDX >> 3, pn = 8 * ((BIDX & 7) >> 1) + (idx >> 2), j = 4 * (BIDX & 1) + (idx & 3);
  const unsigned tid = TIDX, l = c.lane;
  const unsigned vw = wdma_voff(l, 1024u);
  const unsigned rt = w & 1, kq = w >> 1;
  constexpr int LMOD = LDS_D;
  constexpr int LRED = LC + 12288;
  constexpr int LST = LC + 28672;
  const unsigned prow = 32 * rt + c.r;
  const unsigned trow = pn * 64 + prow;
  const unsigned och = 8 * j + 2 * kq + c.h;
  const unsigned ovo = ((pn * 64 + och) * 64 + prow) * 16u;
  float gb[8];
  STAMP(2, 0);

  {
    const unsigned row = pn * 64 + j * 8 + (tid & 7), ch = tid >> 3;
    u32x4 xr;
    gld16(xr, P.x + (size_t)row * D + ch * 8);
    auto wait_flags = [&](unsigned fo) {
      for (unsigned spin = 0;; ++spin) {
        asm volatile("" ::: "memory");
        const unsigned f = __builtin_amdgcn_raw_buffer_load_b32(srdS, fo, 0, 16);
        if (!ANY(fo != OOB && f != tag) || spin > SPIN_CAP) break;
        __builtin_amdgcn_s_sleep(FLAG_SLEEP);
      }
    };
    const __amdgpu_buffer_rsrc_t srd1 = mksrd(P.w1, 512u * 512u * 2u), srd2 = mksrd(P.w2, 512u * 512u * 2u);
#pragma unroll
    for (int i = 0; i < 8; ++i) dma16(lds0 + LA + (8 * w + i) * 1024u, vw, srd1, (8 * w + i) * 8192u + j * 128u);
    {
      const int fp = (int)pn - 1 + (int)(l >> 3);
      if (w == 0) wait_flags((l < 24 && fp >= 0 && fp < 32) ? W_FLAG + ((unsigned)fp * 8 + (l & 7)) * 64u : OOB);
      BAR_L();
    }
    STAMP(2, 15);
    u32x4 p[8];
#pragma unroll
    for (int g = 0; g < 8; ++g) bld16_sc1(p[g], tid * 16u, srdS, W_P1 + ((pn * 8 + j) * 8 + g) * 8192u);
    float ga[8], sb[8], cb[8], g2[8];
    {
      u32x4 mv[20];
#pragma unroll
      for (int i = 0; i < 16; ++i) mv[i] = *(const LAS u32x4*)(lds + LMOD + (i >> 2) * 4096 + ch * 64 + 16 * (i & 3));
#pragma unroll
      for (int i = 0; i < 4; ++i) mv[16 + i] = *(const LAS u32x4*)(lds + LMOD + 12288 + och * 64 + 16 * i);
      if (ANY(bad_tag(mv, 20, tag))) {
#pragma unroll
        for (int i = 0; i < 4; ++i) poll_tag<4>(mv + 4 * i, srdS, ch * 64u, W_MOD + 8192u + i * 4096u, 16u, tag);
        poll_tag<4>(mv + 16, srdS, och * 64u, W_MOD + 20480u, 16u, tag);
      }
#pragma unroll
      for (int k = 0; k < 8; ++k) {
        ga[k] = U2F(mv[k >> 1][2 * (k & 1)]); sb[k] = U2F(mv[4 + (k >> 1)][2 * (k & 1)]); cb[k] = 1.f + U2F(mv[8 + (k >> 1)][2 * (k & 1)]);
        g2[k] = U2F(mv[12 + (k >> 1)][2 * (k & 1)]); gb[k] = U2F(mv[16 + (k >> 1)][2 * (k & 1)]);
      }
    }
    asm volatile("s_waitcnt vmcnt(0)" : "+v"(xr), "+v"(p[0]), "+v"(p[1]), "+v"(p[2]), "+v"(p[3]), "+v"(p[4]), "+v"(p[5]),
                 "+v"(p[6]), "+v"(p[7]) :: "memory");
    STAMP(2, 1);
    float v[8], x2[8];
#pragma unroll
    for (int i = 0; i < 4; ++i) {
      const float lo = ((bflo(p[0][i]) + bflo(p[1][i])) + (bflo(p[2][i]) + bflo(p[3][i]))) + ((bflo(p[4][i]) + bflo(p[5][i])) + (bflo(p[6][i]) + bflo(p[7][i])));
      const float hi = ((bfhi(p[0][i]) + bfhi(p[1][i])) + (bfhi(p[2][i]) + bfhi(p[3][i]))) + ((bfhi(p[4][i]) + bfhi(p[5][i])) + (bfhi(p[6][i]) + bfhi(p[7][i])));
      v[2 * i] = x2[2 * i] = __builtin_fmaf(ga[2 * i], lo, bflo(xr[i]));
      v[2 * i + 1] = x2[2 * i + 1] = __builtin_fmaf(ga[2 * i + 1], hi, bfhi(xr[i]));
    }
    ln8(v, (LAS float*)(lds + LRED), tid);
    float hb[8];
#pragma unroll
    for (int k = 0; k < 8; ++k) v[k] = hb[k] = __builtin_fmaf(v[k], cb[k], sb[k]);
    ln8(v, (LAS float*)(lds + LRED + 512), tid);
    const unsigned vo = ((pn * 64 + ch) * 64 + j * 8 + (tid & 7)) * 16u, vz = ((pn * 64 + ch) * 256 + j * 8 + (tid & 7)) * 16u;
    pub16(srdS, vo, S_U, (u32x4){cvtpk(v[0], v[1]), cvtpk(v[2], v[3]), cvtpk(v[4], v[5]), cvtpk(v[6], v[7])});
#pragma unroll
    for (int i = 0; i < 2; ++i) {
      pub16(srdS, vz, S_HZ + i * 1024u, (u32x4){F2U(hb[4 * i]), F2U(hb[4 * i + 1]),
                                                F2U(hb[4 * i + 2]), F2U(hb[4 * i + 3])});
      f32x4 z;
#pragma unroll
      for (int e = 0; e < 4; ++e) z[e] = __builtin_fmaf(g2[4 * i + e], hb[4 * i + e], x2[4 * i + e]);
      pub16(srdS, vz, S_HZ + (2 + i) * 1024u, __builtin_bit_cast(u32x4, z));
    }
#pragma unroll
    for (int i = 0; i < 8; ++i) dma16(lds0 + LB + (8 * w + i) * 1024u, vw, srd2, (8 * w + i) * 8192u + j * 128u);
  }
  STAMP(2, 2);
  const __amdgpu_buffer_rsrc_t srd3 = mksrd(P.w3, 512u * F * 2u);
  const unsigned rc = LC + w * 4096u, rb = LB + w * 9216u;
  const unsigned v3 = (l >> 2) * 3584u + ((l & 3) << 4), s30 = (j * 224u + w * 32u) * 2u;
  float v[8];
  {
    bf16x8 af[8][2];
#pragma unroll
    for (int i = 0; i < 8; ++i)
#pragma unroll
      for (int nt = 0; nt < 2; ++nt) {
        const lds_cptr a = lds + LA + (8 * kq + i) * 2048 + c.afr[nt];
        af[i][nt] = cat8(lds_tr(a), lds_tr(a + 512));
      }
    BAR_L();
    u32x4 xb[8];
    poll_ind<8, 3, 7>(xb, srdS, ((pn * 64 + 16 * kq + c.h) * 64 + prow) * 16u, S_U, 2048u);
    STAMP(2, 3);
    tile_mm8(af, xb, v, lds + LA, rt, kq, l);
#pragma unroll
    for (int k = 0; k < 8; ++k) v[k] = gelu_tanh(v[k]);
    pub16(srdS, ovo, S_H1, (u32x4){cvtpk(v[0], v[1]), cvtpk(v[2], v[3]), cvtpk(v[4], v[5]), cvtpk(v[6], v[7])});
    if (w < 7) {
#pragma unroll
      for (int ks = 0; ks < 4; ++ks) dma16(lds0 + r3off(ks, rc, rb), v3, srd3, s30 + (unsigned)(16 * ks) * 3584u);
    }
  }
  STAMP(2, 4);
  float y[8];
  float cs[16];
  const unsigned cso = w < 7 ? (3072 + 224 * j + 32 * w + 8 * c.h) * 8u : OOB;
  {
    bf16x8 af[8][2];
#pragma unroll
    for (int i = 0; i < 8; ++i)
#pragma unroll
      for (int nt = 0; nt < 2; ++nt) {
        const lds_cptr a = lds + LB + (8 * kq + i) * 2048 + c.afr[nt];
        af[i][nt] = cat8(lds_tr(a), lds_tr(a + 512));
      }
    BAR_L();
    u32x4 hz[4];
    const unsigned hzo = ((pn * 64 + och) * 256 + prow) * 16u;
#pragma unroll
    for (int i = 0; i < 4; ++i) hz[i] = __builtin_amdgcn_raw_buffer_load_b128(srdS, hzo, S_HZ + i * 1024u, 16);
    {
      u32x4 cg[8];
#pragma unroll
      for (int i = 0; i < 8; ++i) cg[i] = __builtin_amdgcn_raw_buffer_load_b128(srdS, cso, W_MOD + 128u * (i >> 2) + 16u * (i & 3), 16);
      if (ANY(cso < OOB && bad_tag(cg, 8, tag))) {
        poll_tag<4>(cg, srdS, cso, W_MOD, 16u, tag);
        poll_tag<4>(cg + 4, srdS, cso, W_MOD + 128u, 16u, tag);
      }
#pragma unroll
      for (int i = 0; i < 16; ++i) cs[i] = U2F(cg[i >> 1][2 * (i & 1)]);
    }
    const int sh = kq == 0 ? -1 : kq == 1 ? 1 : kq == 2 ? -2 : 2;
    const int srow = (int)trow + sh;
    const unsigned xvo = (srow >= 0 && srow < T) ? ((((unsigned)srow >> 6) * 64 + 16 * kq + c.h) * 64 + ((unsigned)srow & 63)) * 16u : OOB;
    u32x4 xb[8];
    poll_ind<8, 3, 7>(xb, srdS, xvo, S_H1, 2048u);
    STAMP(2, 5);
    tile_mm8(af, xb, v, lds + LA, rt, kq, l);
    if (has_sent(hz, 4)) poll_load<4>(hz, srdS, hzo, S_HZ, 1024u);
    float x1[8];
#pragma unroll
    for (int k = 0; k < 8; ++k) {
      x1[k] = U2F(hz[k >> 2][k & 3]) + v[k];
      y[k] = __builtin_fmaf(gb[k], v[k], U2F(hz[2 + (k >> 2)][k & 3]));
    }
    pub16(srdS, ovo, S_X1, (u32x4){cvtpk(x1[0], x1[1]), cvtpk(x1[2], x1[3]), cvtpk(x1[4], x1[5]), cvtpk(x1[6], x1[7])});
    if (w < 7) {
#pragma unroll
      for (int ks = 4; ks < RING3; ++ks) dma16(lds0 + r3off(ks, rc, rb), v3, srd3, s30 + (unsigned)(16 * ks) * 3584u);
    }
  }
  STAMP(2, 6);
  {
    u32x4 xr[8];
    BAR_L();
    poll_ind<8, 3, 7>(xr, srdS, ((pn * 64 + 8 * w) * 64 + l) * 16u, S_X1, 1024u);
    STAMP(2, 7);
#pragma unroll
    for (int i = 0; i < 8; ++i) *(LAS u32x4*)(lds + LA + ximg(8 * w + i, l)) = xr[i];
    {
      float s0 = 0.f, s1 = 0.f, q0 = 0.f, q1 = 0.f;
#pragma unroll
      for (int i = 0; i < 8; ++i)
#pragma unroll
        for (int k = 0; k < 4; k += 2) {
          s0 = dot2bf(xr[i][k], 0x3F803F80u, s0); s1 = dot2bf(xr[i][k + 1], 0x3F803F80u, s1);
          q0 = dot2bf(xr[i][k], xr[i][k], q0); q1 = dot2bf(xr[i][k + 1], xr[i][k + 1], q1);
        }
      *(LAS f32x2*)(lds + LST + (w * 64 + l) * 8) = (f32x2){s0 + s1, q0 + q1};
    }
    BAR_L();
    STAMP(2, 8);
    if (w < 7) {
      f32x16 acc[2] = {};
      const unsigned a3 = (8 * c.h + c.q) * 64 + 32 * c.blk + 8 * (2 * (c.p & 1) + (c.p >> 1));
      Frag3 f0, f1, f2;
      wait_vm<RING3 - 2>();
      load_frags3<0>(f0, lds, rc, rb, a3, c);
      load_frags3<1>(f1, lds, rc, rb, a3, c);
      float mu[2], rs[2];
#pragma unroll
      for (int r2 = 0; r2 < 2; ++r2) {
        float s = 0.f, q = 0.f;
#pragma unroll
        for (int i = 0; i < 8; ++i) {
          const f32x2 t = *(const LAS f32x2*)(lds + LST + (i * 64 + 32 * r2 + c.r) * 8);
          s += t[0]; q += t[1];
        }
        mu[r2] = s * (1.0f / D);
        rs[r2] = __builtin_amdgcn_rsqf(__builtin_fmaxf(__builtin_fmaf(-mu[r2], mu[r2], q * (1.0f / D)), 0.f) + LN_EPS);
      }
      if (w < 4) __builtin_amdgcn_s_setprio(2); else __builtin_amdgcn_s_setprio(1);
      g3_step<0>(acc, f0, f1, f2, lds, lds0, rc, rb, v3, srd3, s30, a3, c);
      __builtin_amdgcn_s_setprio(0);
      STAMP(2, 9);
#pragma unroll
      for (int r2 = 0; r2 < 2; ++r2) {
#pragma unroll
        for (int i = 0; i < 16; ++i) acc[r2][i] = gelu_tanh(rs[r2] * __builtin_fmaf(-mu[r2], cs[i], acc[r2][i]));
#pragma unroll
        for (int s2 = 0; s2 < 2; ++s2)
          pub16(srdS, ((pn * 224 + 28 * j + 4 * w + 2 * s2 + c.h) * 64 + 32 * r2 + c.r) * 16u, S_H3, pack8(acc[r2], s2, 1.f));
      }
    }
    BAR_L();
  }
  STAMP(2, 10);
  {
    const __amdgpu_buffer_rsrc_t srd4 = mksrd(P.w4, (unsigned)F * 512u * 2u);
    const unsigned qb = kq * 40960u;
    const unsigned s40 = (448u * kq + 8u * rt) * 1024u + j * 128u;
    auto w4dma = [&](int ks) { dma16(lds0 + qb + (ks % 20) * 2048u + rt * 1024u, vw, srd4, s40 + (unsigned)ks * 16384u); };
#pragma unroll
    for (int ks = 0; ks < NPRE; ++ks) w4dma(ks);
    const unsigned hvo = ((pn * 224 + 56 * kq + c.h) * 64 + prow) * 16u;
    u32x4 ha[14], hb[14];
    poll_ind<14, 9, 13>(ha, srdS, hvo, S_H3, 2048u);
    BAR_L();
    STAMP(2, 11);
    f32x16 acc[2] = {};
    bf16x8 fa[2][2];
    auto ld = [&](int ks) {
#pragma unroll
      for (int nt = 0; nt < 2; ++nt) {
        const lds_cptr a = lds + qb + (ks % 20) * 2048 + c.afr[nt];
        fa[ks & 1][nt] = cat8(lds_tr(a), lds_tr(a + 512));
      }
    };
    auto mm = [&](int ks, const u32x4& b) {
#pragma unroll
      for (int nt = 0; nt < 2; ++nt) acc[nt] = MFMA32(fa[ks & 1][nt], __builtin_bit_cast(bf16x8, b), acc[nt]);
    };
    auto hbl = [&](int i) { bld16_sc1(hb[i], hvo, srdS, S_H3 + (14u + i) * 2048u); };
    ld(0);
#pragma unroll
    for (int ks = 0; ks < 8; ++ks) {
      if (ks < 7) ld(ks + 1);
      SBAR(); mm(ks, ha[ks]);
      if (ks < 7) { hbl(2 * ks); hbl(2 * ks + 1); }
      else {
#pragma unroll
        for (int k2 = NPRE; k2 < 20; ++k2) w4dma(k2);
      }
      SBAR();
    }
    BAR_L();
    ld(8);
#pragma unroll
    for (int ks = 8; ks < 14; ++ks) {
      if (ks < 13) ld(ks + 1);
      SBAR(); mm(ks, ha[ks]);
      if (ks < 12) { w4dma(20 + 2 * (ks - 8)); w4dma(21 + 2 * (ks - 8)); }
      SBAR();
    }
    asm volatile("s_waitcnt vmcnt(8) lgkmcnt(0)\n\ts_barrier" : "+v"(hb[0]), "+v"(hb[1]), "+v"(hb[2]), "+v"(hb[3]), "+v"(hb[4]), "+v"(hb[5]),
                 "+v"(hb[6]), "+v"(hb[7]), "+v"(hb[8]), "+v"(hb[9]), "+v"(hb[10]), "+v"(hb[11]), "+v"(hb[12]), "+v"(hb[13]) :: "memory");
    if (has_sent(hb, 14)) poll_load<14>(hb, srdS, hvo, S_H3 + 14u * 2048u, 2048u);
    STAMP(2, 12);
    ld(14);
#pragma unroll
    for (int ks = 14; ks < 19; ++ks) { ld(ks + 1); SBAR(); mm(ks, hb[ks - 14]); SBAR(); }
    mm(19, hb[5]);
    WAIT_BAR(0);
    ld(20);
#pragma unroll
    for (int ks = 20; ks < 27; ++ks) { ld(ks + 1); SBAR(); mm(ks, hb[ks - 14]); SBAR(); }
    mm(27, hb[13]);
    BAR_L();
    STAMP(2, 13);
    red4_put(acc[0], 0, lds + LA, rt, kq, l);
    red4_put(acc[1], 1, lds + LA, rt, kq, l);
    BAR_L();
    red4_get(v, lds + LA, rt, kq, l);
#pragma unroll
    for (int k = 0; k < 8; ++k) v[k] = __builtin_fmaf(gb[k], v[k], y[k]);
    P.out[(size_t)trow * 64 + och] = (u32x4){cvtpk(v[0], v[1]), cvtpk(v[2], v[3]), cvtpk(v[4], v[5]), cvtpk(v[6], v[7])};
  }
  STAMP(2, 14);
}

__global__ __launch_bounds__(512, 2) void k_fused(Params P) {
  extern __shared__ __attribute__((aligned(16))) char smem[];
  const lds_ptr lds = (lds_ptr)smem;
  const unsigned lds0 = (unsigned)(uintptr_t)smem;
  const unsigned w = rfl(TIDX >> 6);
  const __amdgpu_buffer_rsrc_t srdW = mksrd(P.ws, W_END);
  const unsigned tag = attn_part(P, lds, lds0, w, srdW);
  mlp_part(P, lds, lds0, w, srdW, tag);
}

#if defined(__HIP_DEVICE_COMPILE__)
#pragma clang attribute pop
#endif
extern "C" void kernel_launch(void* const* d_in, const int* in_sizes, int n_in, void* d_out, int out_size, void* d_ws,
                              size_t ws_size, hipStream_t stream) {
  Params P;
  P.x = (const unsigned short*)d_in[0];
  P.kc = (const unsigned short*)d_in[1];
  P.vc = (const unsigned short*)d_in[2];
  P.wq = (const unsigned short*)d_in[3];
  P.wo = (const unsigned short*)d_in[4];
  P.w1 = (const unsigned short*)d_in[6];
  P.w2 = (const unsigned short*)d_in[7];
  P.w3 = (const unsigned short*)d_in[8];
  P.w4 = (const unsigned short*)d_in[9];
  P.c = (const unsigned short*)d_in[10];
  P.wmod = (const unsigned short*)d_in[11];
  P.ws = d_ws;
  P.out = (u32x4*)d_out;
  (void)hipFuncSetAttribute((const void*)k_fused, hipFuncAttributeMaxDynamicSharedMemorySize, LDS_TOTAL);
  k_fused<<<256, 512, LDS_TOTAL, stream>>>(P);
}
```

```cpp
#include <hip/hip_runtime.h>
#include <hip/hip_bf16.h>
#include <stdint.h>

#if defined(__HIP_DEVICE_COMPILE__)
#pragma clang attribute push(__attribute__((target("no-packed-fp32-ops"))), apply_to = function)
#endif
typedef short bf16x8 __attribute__((ext_vector_type(8)));
typedef short s16x4 __attribute__((ext_vector_type(4)));
typedef float f32x16 __attribute__((ext_vector_type(16)));
typedef float f32x4 __attribute__((ext_vector_type(4)));
typedef float f32x2 __attribute__((ext_vector_type(2)));
typedef unsigned u32x4 __attribute__((ext_vector_type(4)));
typedef unsigned u32x2 __attribute__((ext_vector_type(2)));
typedef __bf16 bf16x2_t __attribute__((ext_vector_type(2)));
typedef short v4i16_t __attribute__((ext_vector_type(4)));
#define LAS __attribute__((address_space(3)))
typedef LAS const char* lds_cptr;
typedef LAS char* lds_ptr;
#define DI __device__ __forceinline__
#define F2U(x) __builtin_bit_cast(unsigned, (float)(x))
#define U2F(x) __builtin_bit_cast(float, (unsigned)(x))
#define F2I(x) __builtin_bit_cast(int, (float)(x))
#define I2F(x) __builtin_bit_cast(float, (int)(x))
#define TIDX __builtin_amdgcn_workitem_id_x()
#define BIDX __builtin_amdgcn_workgroup_id_x()
#define ANY(p) (__builtin_amdgcn_ballot_w64(p) != 0ull)
#define SBAR() __builtin_amdgcn_sched_barrier(0)
#define MFMA32(a, b, c) __builtin_amdgcn_mfma_f32_32x32x16_bf16((a), (b), (c), 0, 0, 0)
#define WAIT_V(n) asm volatile("s_waitcnt vmcnt(" #n ")" ::: "memory")
#define WAIT_L0() asm volatile("s_waitcnt lgkmcnt(0)" ::: "memory")
#define BAR_L() asm volatile("s_waitcnt lgkmcnt(0)\n\ts_barrier" ::: "memory")
#define WAIT_BAR(n) asm volatile("s_waitcnt vmcnt(" #n ") lgkmcnt(0)\n\ts_barrier" ::: "memory")

constexpr int T = 2048, D = 512, HKV = 8, SK = 2048, DH = 128, WIN = 512, F = 1792;
constexpr float LN_EPS = 1e-5f;
constexpr float QSCALE = 0.088388347648318440f * 1.4426950408889634f;

constexpr unsigned W_EPOCH = 0;
constexpr unsigned W_FLAG = 4096;
constexpr unsigned W_MOD = 32768;
constexpr unsigned W_MP = W_MOD + 40960;
constexpr unsigned W_P1 = 393216;
constexpr unsigned S_U = W_P1 + 8u * 64u * T * 16u;
constexpr unsigned S_H1 = S_U + 64u * T * 16u;
constexpr unsigned S_X1 = S_H1 + 64u * T * 16u;
constexpr unsigned S_HZ = S_X1 + 64u * T * 16u;
constexpr unsigned S_H3 = S_HZ + 256u * T * 16u;
constexpr unsigned W_END = S_H3 + 224u * T * 16u;
static_assert(W_MP + 8u * 4864u * 8u <= W_P1, "ws");
constexpr unsigned OOB = 0xF0000000u;
#define K1MAP 0
#define FLAG_SLEEP 4
#define NPRE 20
#define ATHR 32.0f
constexpr float THR = ATHR;
constexpr unsigned SENT = 0xFFFFFFFFu;
constexpr unsigned SPIN_CAP = 1u << 16;

constexpr int LDS_X = 0;
constexpr int LDS_R = 65536;
constexpr int RING = 5, RINGB = RING * 2048;
constexpr int LDS_D = LDS_R + 8 * RINGB;
constexpr int LDS_TOTAL = LDS_D + 16384;
static_assert(LDS_TOTAL == 163840, "lds");

#define STAMP(kid, i) do {} while (0)
DI unsigned cvtpk(float lo, float hi) {
  f32x2 v = {lo, hi};
  bf16x2_t b = __builtin_convertvector(v, bf16x2_t);
  return __builtin_bit_cast(unsigned, b);
}
DI float bflo(unsigned u) { return U2F(u << 16); }
DI float bfhi(unsigned u) { return U2F(u & 0xffff0000u); }
DI unsigned rfl(unsigned x) { return __builtin_amdgcn_readfirstlane(x); }
DI __amdgpu_buffer_rsrc_t mksrd(const void* p, unsigned bytes) {
  return __builtin_amdgcn_make_buffer_rsrc((void*)p, (short)0, (int)bytes, 0x00020000);
}
DI void dma16(unsigned lds, unsigned voff, __amdgpu_buffer_rsrc_t srd, unsigned soff) {
  asm volatile("s_mov_b32 m0, %0\n\ts_nop 0\n\tbuffer_load_dwordx4 %1, %2, %3 offen lds"
               :: "s"(lds), "v"(voff), "s"(srd), "s"(soff) : "memory");
}
DI void gld16(u32x4& d, const void* p) { asm volatile("global_load_dwordx4 %0, %1, off" : "=v"(d) : "v"(p) : "memory"); }
DI void bld16_sc1(u32x4& d, unsigned voff, __amdgpu_buffer_rsrc_t srd, unsigned soff) {
  asm volatile("buffer_load_dwordx4 %0, %1, %2, %3 offen sc1" : "=v"(d) : "v"(voff), "s"(srd), "s"(soff) : "memory");
}
DI void dma16_sc1(unsigned lds, unsigned voff, __amdgpu_buffer_rsrc_t srd, unsigned soff) {
  asm volatile("s_mov_b32 m0, %0\n\ts_nop 0\n\tbuffer_load_dwordx4 %1, %2, %3 offen sc1 lds"
               :: "s"(lds), "v"(voff), "s"(srd), "s"(soff) : "memory");
}
DI bf16x8 lds_b128(lds_cptr p) { return *(const LAS bf16x8*)p; }
DI s16x4 lds_tr(lds_cptr p) {
  return __builtin_bit_cast(s16x4, __builtin_amdgcn_ds_read_tr16_b64_v4i16((LAS v4i16_t*)p));
}
DI bf16x8 cat8(s16x4 lo, s16x4 hi) { return __builtin_shufflevector(lo, hi, 0, 1, 2, 3, 4, 5, 6, 7); }
DI float swap32_add(float x) {
  auto r = __builtin_amdgcn_permlane32_swap(F2U(x), F2U(x), false, false);
  return U2F(r[0]) + U2F(r[1]);
}
DI float swap32_max(float x) {
  auto r = __builtin_amdgcn_permlane32_swap(F2U(x), F2U(x), false, false);
  return __builtin_fmaxf(U2F(r[0]), U2F(r[1]));
}
template <int CTRL> DI float dpp_add(float x) {
  int y = __builtin_amdgcn_update_dpp(0, F2I(x), CTRL, 0xF, 0xF, true);
  return x + I2F(y);
}
DI float max3f(float a, float b, float c) { float r; asm("v_max3_f32 %0, %1, %2, %3" : "=v"(r) : "v"(a), "v"(b), "v"(c)); return r; }
DI float max2f(float a, float b) { float r; asm("v_max_f32_e32 %0, %1, %2" : "=v"(r) : "v"(a), "v"(b)); return r; }
DI float dot2bf(unsigned a, unsigned b, float c) {
  return __builtin_amdgcn_fdot2_f32_bf16(__builtin_bit_cast(bf16x2_t, a), __builtin_bit_cast(bf16x2_t, b), c, false);
}
#define PIN(x) asm volatile("" : "+v"(x))
DI float swap16_add(float x) {
  auto r = __builtin_amdgcn_permlane16_swap(F2U(x), F2U(x), false, false);
  return U2F(r[0]) + U2F(r[1]);
}
DI float sum_hi3(float x) { return swap32_add(swap16_add(dpp_add<0x128>(x))); }
DI float gelu_tanh(float x) {
  const float k0 = -2.0f * 0.7978845608028654f * 1.4426950408889634f;
  const float t = x * x;
  const float z = x * __builtin_fmaf(t, 0.044715f * k0, k0);
  const float e = __builtin_amdgcn_exp2f(z);
  return x * __builtin_amdgcn_rcpf(1.0f + e);
}
DI void wave_sum8(const float (&a)[8], float (&out)[8]) {
  float b[4], c[2];
#pragma unroll
  for (int i = 0; i < 4; ++i) {
    auto r = __builtin_amdgcn_permlane32_swap(F2U(a[i]), F2U(a[i + 4]), false, false);
    b[i] = U2F(r[0]) + U2F(r[1]);
  }
#pragma unroll
  for (int i = 0; i < 2; ++i) {
    auto r = __builtin_amdgcn_permlane16_swap(F2U(b[i]), F2U(b[i + 2]), false, false);
    c[i] = U2F(r[0]) + U2F(r[1]);
  }
#pragma unroll
  for (int i = 0; i < 2; ++i) {
    c[i] = dpp_add<0xB1>(c[i]);
    c[i] = dpp_add<0x4E>(c[i]);
    c[i] = dpp_add<0x141>(c[i]);
    c[i] = dpp_add<0x140>(c[i]);
  }
#pragma unroll
  for (int q = 0; q < 4; ++q)
#pragma unroll
    for (int i = 0; i < 2; ++i)
      out[i + 2 * (q & 1) + 4 * (q >> 1)] = I2F(__builtin_amdgcn_readlane(F2I(c[i]), 16 * q));
}

DI unsigned nosent(unsigned v) { return v < 0xFFFFFFFEu ? v : 0xFFFFFFFEu; }
DI void pub16(__amdgpu_buffer_rsrc_t srd, unsigned voff, unsigned soff, u32x4 v) {
  v = (u32x4){nosent(v[0]), nosent(v[1]), nosent(v[2]), nosent(v[3])};
  __builtin_amdgcn_raw_buffer_store_b128(v, srd, voff + soff, 0, 16);
}
DI unsigned umax3(unsigned a, unsigned b, unsigned c) { const unsigned m = a > b ? a : b; return m > c ? m : c; }
DI bool has_sent(const u32x4* v, int n) {
  unsigned m = 0;
#pragma unroll
  for (int i = 0; i < n; ++i) { m = umax3(m, v[i][0], v[i][1]); m = umax3(m, v[i][2], v[i][3]); }
  return ANY(m == SENT);
}
template <int N> DI void poll_load(u32x4* v, __amdgpu_buffer_rsrc_t srd, unsigned voff, unsigned soff0, unsigned step) {
  for (unsigned spin = 0;; ++spin) {
    asm volatile("" ::: "memory");
#pragma unroll
    for (int i = 0; i < N; ++i) v[i] = __builtin_amdgcn_raw_buffer_load_b128(srd, voff, soff0 + (unsigned)i * step, 16);
    if (!has_sent(v, N) || spin > SPIN_CAP) break;
    __builtin_amdgcn_s_sleep(1);
  }
}
template <int N, int I0, int I1> DI void poll_ind(u32x4* v, __amdgpu_buffer_rsrc_t srd, unsigned voff, unsigned soff0, unsigned step) {
#pragma unroll
  for (int i = 0; i < N; ++i) v[i] = __builtin_amdgcn_raw_buffer_load_b128(srd, voff, soff0 + (unsigned)i * step, 16);
  if (has_sent(v, N)) {
    u32x4 ind[2];
    poll_load<2>(ind, srd, voff, soff0 + (unsigned)I0 * step, (unsigned)(I1 - I0) * step);
    poll_load<N>(v, srd, voff, soff0, step);
  }
}

DI bool bad_tag(const u32x4* v, int n, unsigned tag) {
  unsigned m = 0;
#pragma unroll
  for (int i = 0; i < n; ++i) m |= (v[i][1] ^ tag) | (v[i][3] ^ tag);
  return m != 0;
}
template <int N> DI void poll_tag(u32x4* v, __amdgpu_buffer_rsrc_t srd, unsigned voff, unsigned soff0, unsigned step, unsigned tag) {
  for (unsigned spin = 0;; ++spin) {
    asm volatile("" ::: "memory");
#pragma unroll
    for (int i = 0; i < N; ++i) v[i] = __builtin_amdgcn_raw_buffer_load_b128(srd, voff, soff0 + (unsigned)i * step, 16);
    if (!ANY(voff < OOB && bad_tag(v, N, tag)) || spin > SPIN_CAP) break;
    __builtin_amdgcn_s_sleep(1);
  }
}

struct LaneC {
  unsigned lane, r, h, q, p, blk;
  unsigned wv;
  unsigned afr[2];
  unsigned xb[4];
};
DI void lane_consts(LaneC& c) {
  unsigned l = TIDX & 63;
  asm volatile("" : "+v"(l));
  c.lane = l; c.r = l & 31; c.h = l >> 5; c.q = (l & 15) >> 2; c.p = l & 3; c.blk = (l >> 4) & 1;
  const unsigned sp = 2 * (c.p & 1) + (c.p >> 1);
#pragma unroll
  for (int nt = 0; nt < 2; ++nt) c.afr[nt] = (8 * c.h + c.q) * 128 + 64 * (nt ^ (c.q >> 1)) + 32 * c.blk + 8 * sp;
#pragma unroll
  for (int s = 0; s < 4; ++s) c.xb[s] = 1024 * c.h + ((c.r ^ (2 * s + c.h)) << 4);
}
DI unsigned wdma_voff(unsigned l, unsigned ld) { return (l >> 3) * ld + (((l & 7) ^ (((l >> 4) & 1) << 2)) << 4); }

DI void wring_issue(int ks, unsigned ring, unsigned voff, __amdgpu_buffer_rsrc_t srd, unsigned soff0, unsigned ld) {
  const unsigned slot = ring + (unsigned)(ks % RING) * 2048u;
  dma16(slot, voff, srd, soff0 + (unsigned)(16 * ks) * ld);
  dma16(slot + 1024u, voff ^ 32u, srd, soff0 + (unsigned)(16 * ks + 8) * ld);
}
template <int N> DI void wait_vm() {
  if constexpr (N == 0) WAIT_V(0); else if constexpr (N == 1) WAIT_V(1); else if constexpr (N == 2) WAIT_V(2);
  else if constexpr (N == 3) WAIT_V(3); else if constexpr (N == 4) WAIT_V(4); else if constexpr (N == 5) WAIT_V(5);
  else if constexpr (N == 6) WAIT_V(6); else if constexpr (N == 7) WAIT_V(7); else if constexpr (N == 8) WAIT_V(8);
  else if constexpr (N == 9) WAIT_V(9); else if constexpr (N == 10) WAIT_V(10); else if constexpr (N == 11) WAIT_V(11);
  else if constexpr (N == 12) WAIT_V(12); else if constexpr (N == 13) WAIT_V(13); else if constexpr (N == 14) WAIT_V(14);
  else if constexpr (N == 15) WAIT_V(15); else if constexpr (N == 16) WAIT_V(16);
  else if constexpr (N == 17) WAIT_V(17); else if constexpr (N == 18) WAIT_V(18);
  else static_assert(N < 0, "wait_vm");
}

#define MFMA16(a, b, c) __builtin_amdgcn_mfma_f32_16x16x32_bf16((a), (b), (c), 0, 0, 0)
struct G16 { unsigned a[4], hi, x[2]; };
DI void g16_consts(G16& k, unsigned l, unsigned ringoff) {
  const unsigned i = l & 15, g = l >> 4, q = i >> 2, p = l & 3, x = 2 * (q >> 1) + (g & 1);
#pragma unroll
  for (int m = 0; m < 4; ++m) k.a[m] = ringoff + (8 * (g & 1) + q) * 128 + ((2 * (m ^ x) + (p >> 1)) << 4) + 8 * (p & 1);
  k.hi = g >> 1;
#pragma unroll
  for (int e = 0; e < 2; ++e) k.x[e] = LDS_X + 1024 * g + ((i ^ (4 * e + g)) << 4);
}
struct Frag { bf16x8 a[4], b[4]; };
template <int KK> DI void load_frags(Frag& f, lds_cptr lds, const G16& k) {
  constexpr int sA = (2 * KK) % RING;
  const unsigned d = sA == RING - 1 ? (1u - k.hi) * (unsigned)((RING - 1) * 2048) : k.hi * 2048u;
  constexpr int o = sA == RING - 1 ? 0 : sA * 2048;
#pragma unroll
  for (int m = 0; m < 4; ++m) f.a[m] = cat8(lds_tr(lds + (k.a[m] + d) + o), lds_tr(lds + (k.a[m] + d) + o + 512));
#pragma unroll
  for (int r = 0; r < 4; ++r) f.b[r] = lds_b128(lds + k.x[KK & 1] + 4096 * KK + 256 * r);
}
template <int KK, int EXTRA>
DI void gemm_step(f32x4 (&acc)[4][4], Frag& cur, Frag& nxt, lds_cptr lds, unsigned ring, unsigned voff,
                  __amdgpu_buffer_rsrc_t srd, unsigned soff0, unsigned ld, const G16& k) {
  if constexpr (2 * KK + RING < 32) {
    asm volatile("" :: "v"(cur.a[0]), "v"(cur.a[1]), "v"(cur.a[2]), "v"(cur.a[3]));
    wring_issue(2 * KK + RING, ring, voff, srd, soff0, ld);
    if constexpr (2 * KK + RING + 1 < 32) wring_issue(2 * KK + RING + 1, ring, voff, srd, soff0, ld);
  }
  if constexpr (KK + 1 < 16) {
    constexpr int last = 2 * KK + RING + 1 < 32 ? 2 * KK + RING + 1 : 31;
    wait_vm<2 * (last - (2 * KK + 3)) + ((2 * KK + 3 < RING) ? EXTRA : 0)>();
    load_frags<KK + 1>(nxt, lds, k);
  }
  SBAR();
#pragma unroll
  for (int m = 0; m < 4; ++m)
#pragma unroll
    for (int r = 0; r < 4; ++r) acc[m][r] = MFMA16(cur.a[m], cur.b[r], acc[m][r]);
  SBAR();
  if constexpr (KK + 1 < 16) gemm_step<KK + 1, EXTRA>(acc, nxt, cur, lds, ring, voff, srd, soff0, ld, k);
}
template <int EXTRA>
DI void gemm_phase(f32x4 (&acc)[4][4], lds_cptr lds, unsigned ringoff, unsigned ring, unsigned voff,
                   __amdgpu_buffer_rsrc_t srd, unsigned soff0, unsigned ld, unsigned l) {
  G16 k; g16_consts(k, l, ringoff);
  Frag f0, f1;
  wait_vm<2 * (RING - 2) + EXTRA>();
  load_frags<0>(f0, lds, k);
  gemm_step<0, EXTRA>(acc, f0, f1, lds, ring, voff, srd, soff0, ld, k);
}
DI u32x4 pack16(const f32x4& a, const f32x4& b, float sc) {
  auto r0 = __builtin_amdgcn_permlane16_swap(cvtpk(a[0] * sc, a[1] * sc), cvtpk(b[0] * sc, b[1] * sc), false, false);
  auto r1 = __builtin_amdgcn_permlane16_swap(cvtpk(a[2] * sc, a[3] * sc), cvtpk(b[2] * sc, b[3] * sc), false, false);
  return (u32x4){r0[0], r1[0], r0[1], r1[1]};
}
DI u32x4 pack8(const f32x16& a, int s, float sc) {
  return (u32x4){cvtpk(a[8 * s] * sc, a[8 * s + 1] * sc), cvtpk(a[8 * s + 2] * sc, a[8 * s + 3] * sc),
                 cvtpk(a[8 * s + 4] * sc, a[8 * s + 5] * sc), cvtpk(a[8 * s + 6] * sc, a[8 * s + 7] * sc)};
}
DI unsigned ximg(unsigned chunk, unsigned row) { return chunk * 1024u + ((row ^ (chunk & 7u)) << 4); }

constexpr int KSLOT[8] = {LDS_D, LDS_R, LDS_R + 32768, LDS_X, LDS_X + 32768, LDS_D, LDS_X, LDS_X + 32768};
constexpr int VSLOT[8] = {LDS_R + 16384, LDS_R + 49152, LDS_X + 16384, LDS_X + 49152, LDS_R + 65536, LDS_X + 16384, LDS_X + 49152, LDS_D};
template <int N> struct IC { static constexpr int value = N; };
struct Params {
  const unsigned short *x, *kc, *vc, *wq, *wo, *w1, *w2, *w3, *w4, *c, *wmod;
  void* ws;
  u32x4* out;
};
DI unsigned attn_part(const Params& P, lds_ptr lds, unsigned lds0, unsigned w, __amdgpu_buffer_rsrc_t srdW) {
  LaneC c; lane_consts(c);
  const unsigned g = BIDX & 7, pn = BIDX >> 3;
  const unsigned l = c.lane, tid = TIDX;
  const unsigned ringoff = LDS_R + w * RINGB, ring = lds0 + ringoff;
  STAMP(1, 0);

  unsigned ep;
  asm volatile("buffer_load_dword %0, off, %1, 0 sc1" : "=v"(ep) : "s"(srdW) : "memory");
  u32x4 jw[4]; unsigned jc[4];
  auto job_load = [&](unsigned jcol, unsigned jks) __attribute__((always_inline)) {
    const bool isw3 = jcol >= 96u;
    const unsigned short* src = isw3 ? P.w3 + (jcol - 96u) * 32 + (l & 3) * 8 : P.wmod + jcol * 32 + (l & 3) * 8;
    const unsigned ld = isw3 ? F : 3072, r0 = jks * 64 + (l >> 2);
#pragma unroll
    for (int i = 0; i < 4; ++i) gld16(jw[i], src + (size_t)(r0 + 16 * i) * ld);
#pragma unroll
    for (int i = 0; i < 4; ++i) asm volatile("global_load_ushort %0, %1, off" : "=v"(jc[i]) : "v"(P.c + r0 + 16 * i) : "memory");
  };
  auto job_run = [&](unsigned jcol, unsigned jks, unsigned tag) __attribute__((always_inline)) {
    asm volatile("s_waitcnt vmcnt(0)" : "+v"(jw[0]), "+v"(jw[1]), "+v"(jw[2]), "+v"(jw[3]), "+v"(jc[0]), "+v"(jc[1]), "+v"(jc[2]), "+v"(jc[3]) :: "memory");
    float acc[8] = {0, 0, 0, 0, 0, 0, 0, 0};
#pragma unroll
    for (int i = 0; i < 4; ++i) {
      const float v = U2F(jc[i] << 16);
      const float sv = v * __builtin_amdgcn_rcpf(1.0f + __builtin_amdgcn_exp2f(-1.4426950408889634f * v));
      const float sc = jcol >= 96u ? 1.0f : sv;
#pragma unroll
      for (int k = 0; k < 4; ++k) {
        acc[2 * k] = __builtin_fmaf(sc, bflo(jw[i][k]), acc[2 * k]);
        acc[2 * k + 1] = __builtin_fmaf(sc, bfhi(jw[i][k]), acc[2 * k + 1]);
      }
    }
#pragma unroll
    for (int k = 0; k < 8; ++k) acc[k] = sum_hi3(dpp_add<0x124>(acc[k]));
    const unsigned e = (l >> 2) & 3;
    const float v0 = e == 0 ? acc[0] : e == 1 ? acc[2] : e == 2 ? acc[4] : acc[6], v1 = e == 0 ? acc[1] : e == 1 ? acc[3] : e == 2 ? acc[5] : acc[7];
    __builtin_amdgcn_raw_buffer_store_b128((u32x4){F2U(v0), tag, F2U(v1), tag}, srdW,
                                           l < 16 ? W_MP + (jks * 4864u + jcol * 32u + (l & 3) * 8u + e * 2u) * 8u : OOB, 0, 16);
  };
  u32x4 xr[8];
  auto x_load = [&]() __attribute__((always_inline)) {
#pragma unroll
    for (int i = 0; i < 8; ++i) gld16(xr[i], P.x + (size_t)(pn * 64 + w * 8 + i) * D + l * 8);
  };
  if (w == 0) job_load(BIDX & 31u, BIDX >> 5); else x_load();
  asm volatile("s_waitcnt vmcnt(8)\n\ts_barrier" : "+v"(ep) :: "memory");
  const unsigned tag = rfl(ep) + 1u;
  if (w == 0) { job_run(BIDX & 31u, BIDX >> 5, tag); x_load(); }
  const __amdgpu_buffer_rsrc_t srdQ = mksrd(P.wq, 512u * 4096u * 2u);
  const unsigned vq = wdma_voff(l, 8192u), sq0 = (g * 512u + w * 64u) * 2u;
  const __amdgpu_buffer_rsrc_t srdK = mksrd(P.kc + ((size_t)g * SK + (SK - WIN)) * DH, WIN * DH * 2u);
  const __amdgpu_buffer_rsrc_t srdV = mksrd(P.vc + ((size_t)g * SK + (SK - WIN)) * DH, WIN * DH * 2u);
  const unsigned vk = ((l >> 2) & 7) * 256u + (l >> 5) * 64u + (((l & 3) ^ (2 * (w & 1) + ((l >> 4) & 1))) << 4);
  const unsigned vv = wdma_voff(l, 256u);
  auto dma_k = [&](int t, unsigned slot) {
    dma16(lds0 + slot + 2048u * w, vk, srdK, (unsigned)t * 16384u + 2048u * w);
    dma16(lds0 + slot + 2048u * w + 1024u, vk, srdK, (unsigned)t * 16384u + 2048u * w + 128u);
  };
  auto dma_v = [&](int t, unsigned slot) {
    dma16(lds0 + slot + 1024u * w, vv, srdV, (unsigned)t * 16384u + 2048u * w);
    dma16(lds0 + slot + 8192u + 1024u * w, vv, srdV, (unsigned)t * 16384u + 2048u * w + 128u);
  };

  {
    asm volatile("s_waitcnt vmcnt(0)" : "+v"(xr[0]), "+v"(xr[1]), "+v"(xr[2]), "+v"(xr[3]), "+v"(xr[4]), "+v"(xr[5]), "+v"(xr[6]),
                 "+v"(xr[7]) :: "memory");
    dma16(ring + 8192u, l * 16u, srdQ, (2 * (pn * 8 + w)) * 8192u + g * 1024u);
    dma16(ring + 9216u, l * 16u, srdQ, (2 * (pn * 8 + w) + 1) * 8192u + g * 1024u);
    wring_issue(0, ring, vq, srdQ, sq0, 8192u);
    wring_issue(1, ring, vq, srdQ, sq0, 8192u);
    float v[8][8], s[8], m[8];
#pragma unroll
    for (int i = 0; i < 8; ++i) {
#pragma unroll
      for (int j = 0; j < 4; ++j) { v[i][2 * j] = bflo(xr[i][j]); v[i][2 * j + 1] = bfhi(xr[i][j]); }
      s[i] = ((v[i][0] + v[i][1]) + (v[i][2] + v[i][3])) + ((v[i][4] + v[i][5]) + (v[i][6] + v[i][7]));
    }
    wave_sum8(s, m);
#pragma unroll
    for (int i = 0; i < 8; ++i) {
      const float mu = m[i] * (1.0f / D);
      float q = 0.f;
#pragma unroll
      for (int j = 0; j < 8; ++j) { v[i][j] -= mu; q = __builtin_fmaf(v[i][j], v[i][j], q); }
      s[i] = q;
    }
    wave_sum8(s, m);
    float rs[8];
#pragma unroll
    for (int i = 0; i < 8; ++i) rs[i] = __builtin_amdgcn_rsqf(m[i] * (1.0f / D) + LN_EPS);
    STAMP(1, 8);
    {
      u32x4 mp[8];
      poll_tag<8>(mp, srdW, tid * 16u, W_MP, 4864u * 8u, tag);
      f32x2 t;
#pragma unroll
      for (int k = 0; k < 2; ++k)
        t[k] = ((U2F(mp[0][2 * k]) + U2F(mp[1][2 * k])) + (U2F(mp[2][2 * k]) + U2F(mp[3][2 * k]))) +
               ((U2F(mp[4][2 * k]) + U2F(mp[5][2 * k])) + (U2F(mp[6][2 * k]) + U2F(mp[7][2 * k])));
      *(LAS f32x2*)(lds + LDS_D + tid * 8) = t;
    }
    BAR_L();
    if (BIDX == 0 && tid == 0) __builtin_amdgcn_raw_buffer_store_b32(tag, srdW, W_EPOCH, 0, 16);
    u32x4 mo[4];
#pragma unroll
    for (int i = 0; i < 4; ++i) mo[i] = *(const LAS u32x4*)(lds + LDS_D + (i >> 1) * 2048 + l * 32 + (i & 1) * 16);
    STAMP(1, 9);
#pragma unroll
    for (int i = 0; i < 8; ++i) {
      float hh[8];
#pragma unroll
      for (int j = 0; j < 8; ++j) hh[j] = __builtin_fmaf(v[i][j] * rs[i], 1.f + U2F(mo[2 + (j >> 2)][j & 3]), U2F(mo[j >> 2][j & 3]));
      const u32x4 o = {cvtpk(hh[0], hh[1]), cvtpk(hh[2], hh[3]), cvtpk(hh[4], hh[5]), cvtpk(hh[6], hh[7])};
      *(LAS u32x4*)(lds + LDS_X + ximg(l, w * 8 + i)) = o;
      if (i == 1 || i == 3 || i == 5) wring_issue(2 + i / 2, ring, vq, srdQ, sq0, 8192u);
    }
  }
  BAR_L();
  dma_k(0, LDS_D);
  STAMP(1, 1);

  {
    f32x4 acc[4][4] = {};
    gemm_phase<2>(acc, lds, ringoff, ring, vq, srdQ, sq0, 8192u, l);
    STAMP(1, 2);
    if (w < 4 && w * 256u + BIDX < 960u) {
      const unsigned jid = w * 256u + BIDX;
      job_load(32u + jid % 120u, jid / 120u);
      job_run(32u + jid % 120u, jid / 120u, tag);
    }
    WAIT_BAR(0);
    dma_k(1, KSLOT[1]); dma_v(0, VSLOT[0]); dma_k(2, KSLOT[2]); dma_v(1, VSLOT[1]);
    const unsigned cl = 8 * w + 2 * ((l >> 4) & 1) + (l >> 5);
#pragma unroll
    for (int r = 0; r < 4; ++r)
#pragma unroll
      for (int mp = 0; mp < 2; ++mp)
        *(LAS u32x4*)(lds + LDS_X + ximg(cl + 4 * mp, 16 * r + (l & 15))) = pack16(acc[2 * mp][r], acc[2 * mp + 1][r], QSCALE);
    BAR_L();
  }

  STAMP(1, 3);
  const unsigned hh = w >> 1, rt = w & 1;
  bf16x8 qf[8];
#pragma unroll
  for (int s = 0; s < 8; ++s) qf[s] = lds_b128(lds + LDS_X + ximg(16 * hh + 2 * s + c.h, 32 * rt + c.r));
  unsigned kb[2], vb[2];
#pragma unroll
  for (int i = 0; i < 2; ++i) {
    kb[i] = 2048 * (c.r >> 3) + 64 * (c.r & 7) + (((2 * i + c.h) ^ ((c.r >> 2) & 3)) << 4);
    vb[i] = (4 * c.h + c.q) * 128 + 64 * (i ^ (c.q >> 1)) + 32 * c.blk + 8 * (2 * (c.p & 1) + (c.p >> 1));
  }
  const __amdgpu_buffer_rsrc_t srdO = mksrd(P.wo, 4096u * 512u * 2u);
  const unsigned vo = wdma_voff(l, 1024u), so0 = g * 512u * 1024u + w * 128u;

  f32x16 o[4] = {};
  f32x16 s0, s1;
  f32x16 negm;
  float lsum = 0.f;
  u32x4 pk[4];
  constexpr int PD = 6;
  bf16x8 fr[PD];
  const bool lag = w >= 4;
  auto frag = [&](int t, int it) -> bf16x8 {
    if (it < 16) {
      const lds_cptr a = lds + VSLOT[t > 0 ? t - 1 : 0] + vb[it & 1] + 8192 * ((it >> 1) & 1) + 2048 * (it >> 2);
      return cat8(lds_tr(a), lds_tr(a + 1024));
    }
    const int s = (it - 16) >> 1;
    return lds_b128(lds + KSLOT[t < 8 ? t : 0] + kb[s & 1] + 512 * (s >> 1) + 8192 * (it & 1));
  };
  auto seam = [&](int t, int tx) __attribute__((always_inline)) {
    if (t == 7) { BAR_L(); } else if (tx >= 1 && tx <= 5) { WAIT_BAR(4); } else if (tx == 6) { WAIT_BAR(2); } else { WAIT_BAR(0); }
  };
  auto refill = [&](int t) __attribute__((always_inline)) {
    if (t + 3 < 8) dma_k(t + 3, KSLOT[t + 3 < 8 ? t + 3 : 0]);
    if (t + 2 < 8) dma_v(t + 2, VSLOT[t + 2 < 8 ? t + 2 : 0]);
    if (t == 6) {
#pragma unroll
      for (int ks = 0; ks < RING; ++ks) wring_issue(ks, ring, vo, srdO, so0, 1024u);
    }
  };
  auto extra = [&](int e) __attribute__((always_inline)) {
    const unsigned dump = lds0 + LDS_R + w * 3072u + (unsigned)(e % 3) * 1024u;
    const unsigned id = (BIDX >> 3) * 8 + w, jh = BIDX & 1;
    const unsigned vrow = (l >> 5) * 1024u + (l & 31) * 16u + jh * 512u;
    auto w3v = [&](unsigned piece) { const unsigned byte = piece * 1024u + l * 16u, row = byte / 1792u;
                                     return row * 3584u + jh * 1792u + (byte - row * 1792u); };
    if (e < 4 || e >= 15) {
      const int i = e < 4 ? e : e - 11;
      const unsigned off = i < 3 ? S_U + (unsigned)i * (64u * T * 16u) + pn * 65536u + g * 8192u
                         : i < 7 ? S_HZ + pn * 262144u + g * 32768u + (unsigned)(i - 3) * 8192u
                                 : S_H3 + pn * 229376u + g * 28672u + (unsigned)(i - 7) * 8192u;
      const u32x4 sv = {SENT, SENT, SENT, SENT};
      __builtin_amdgcn_raw_buffer_store_b128(sv, srdW, (i == 10 && tid >= 256) ? OOB : tid * 16u + off, 0, 16);
    } else if (e < 6) dma16(dump, l * 16u, srdO, (g * 512u + (pn * 8 + w) * 2 + (e - 4)) * 1024u);
    else if (e == 6) dma16(dump, vrow, mksrd(P.w1, 512u * 1024u), id * 2048u);
    else if (e == 7) dma16(dump, vrow, mksrd(P.w2, 512u * 1024u), id * 2048u);
    else if (e < 11) dma16(dump, w3v(id + 256u * (e - 8)), mksrd(P.w3, 512u * 3584u), 0u);
    else if (e == 11) {
      if (id < 128) dma16(dump, w3v(id + 768u), mksrd(P.w3, 512u * 3584u), 0u);
      else dma16(dump, vrow, mksrd(P.w4, 1792u * 1024u), (id - 128u + 768u) * 2048u);
    } else dma16(dump, vrow, mksrd(P.w4, 1792u * 1024u), (id + 256u * (e - 12)) * 2048u);
  };
  auto step = [&](auto tc) __attribute__((always_inline)) {
    constexpr int t = decltype(tc)::value;
    constexpr int i0 = t == 0 ? 16 : 0, i1 = t == 8 ? 16 : 32;
    if (t <= 1) {
#pragma unroll
      for (int it = i0; it < i0 + PD; ++it) fr[it % PD] = frag(t, it);
    }
    if (t == 0) { s0 = (f32x16){}; s1 = (f32x16){}; } else if (t < 8) { s0 = negm; s1 = negm; }
    __builtin_amdgcn_s_setprio(1);
    SBAR();
#pragma unroll
    for (int it = i0; it < i1; ++it) {
      if (it < 16) o[it & 3] = MFMA32(fr[it % PD], __builtin_bit_cast(bf16x8, pk[it >> 2]), o[it & 3]);
      else if (it & 1) s1 = MFMA32(fr[it % PD], qf[(it - 16) >> 1], s1);
      else s0 = MFMA32(fr[it % PD], qf[(it - 16) >> 1], s0);
      if (it + PD < i1) fr[it % PD] = frag(t, it + PD);
      if (t >= 1 && it == 1) { if (!lag) refill(t - 1); }
      SBAR();
    }
    if constexpr (t == 8) return;
    __builtin_amdgcn_s_setprio(0);
    if (lag) { seam(t, t - 1); refill(t); }
    asm volatile("s_nop 15" : "+v"(s0), "+v"(s1));
    float ta = max3f(s0[0], s0[1], s1[0]), tb = max3f(s0[2], s0[3], s1[1]);
    ta = max3f(ta, s1[2], s1[3]);
#pragma unroll
    for (int i = 4; i < 16; i += 4) {
      ta = max3f(ta, s0[i], s0[i + 1]); tb = max3f(tb, s0[i + 2], s0[i + 3]);
      ta = max3f(ta, s1[i], s1[i + 1]); tb = max3f(tb, s1[i + 2], s1[i + 3]);
    }
    float tm = max2f(ta, tb);
    {
      auto r = __builtin_amdgcn_permlane32_swap(F2U(tm), F2U(tm), false, false);
      tm = max2f(U2F(r[0]), U2F(r[1]));
    }
    if (t == 0 || ANY(tm > THR)) {
      const float ndl = t == 0 ? -tm : -max2f(tm, 0.f);
      const float aone = c.h ? 0.f : 1.f;
      if (t == 0) negm = (f32x16){};
      __builtin_amdgcn_s_setprio(2);
      s0 = __builtin_amdgcn_mfma_f32_32x32x2f32(aone, ndl, s0, 0, 0, 0);
      s1 = __builtin_amdgcn_mfma_f32_32x32x2f32(aone, ndl, s1, 0, 0, 0);
      negm = __builtin_amdgcn_mfma_f32_32x32x2f32(aone, ndl, negm, 0, 0, 0);
      __builtin_amdgcn_s_setprio(0);
      if (t > 0) {
        const float al = __builtin_amdgcn_exp2f(ndl);
        lsum *= al;
#pragma unroll
        for (int dt = 0; dt < 4; ++dt)
#pragma unroll
          for (int i = 0; i < 16; ++i) o[dt][i] *= al;
      }
    }
    SBAR();
    if (t >= 1) {
#pragma unroll
      for (int it = 0; it < PD; ++it) fr[it] = frag(t + 1, it);
    }
#pragma unroll
    for (int i = 0; i < 16; ++i) {
      s0[i] = __builtin_amdgcn_exp2f(s0[i]); s1[i] = __builtin_amdgcn_exp2f(s1[i]);
      if (t >= 1 && (i & 3) == 1 && 4 * (t - 1) + (i >> 2) < 22) { PIN(s0[i]); extra(4 * (t - 1) + (i >> 2)); }
    }
    pk[0] = pack8(s0, 0, 1.f); pk[1] = pack8(s0, 1, 1.f); pk[2] = pack8(s1, 0, 1.f); pk[3] = pack8(s1, 1, 1.f);
    {
      float l0 = lsum, l1 = 0.f, l2 = 0.f, l3 = 0.f;
#pragma unroll
      for (int i = 0; i < 16; i += 2) { l0 += s0[i]; l1 += s0[i + 1]; l2 += s1[i]; l3 += s1[i + 1]; }
      lsum = (l0 + l1) + (l2 + l3);
    }
    PIN(pk[0]); PIN(pk[1]); PIN(pk[2]); PIN(pk[3]); PIN(lsum);
    if constexpr (t == 3) {
      if (BIDX < 120 && w == 0) {
        u32x4 mp[2];
        poll_tag<2>(mp, srdW, ((l >> 3) * 4864u + (32u + BIDX) * 32u + (l & 7) * 2u) * 8u, W_MP, 128u, tag);
        const float a0 = sum_hi3(U2F(mp[0][0])), a1 = sum_hi3(U2F(mp[0][2])), b0 = sum_hi3(U2F(mp[1][0])), b1 = sum_hi3(U2F(mp[1][2]));
        const bool hi = (l >> 3) & 1;
        __builtin_amdgcn_raw_buffer_store_b128((u32x4){F2U(hi ? b0 : a0), tag, F2U(hi ? b1 : a1), tag}, srdW,
                                               l < 16 ? W_MOD + ((32u + BIDX) * 32u + l * 2u) * 8u : OOB, 0, 16);
      }
    }
    SBAR();
    if (!lag) seam(t, t);
  };
  step(IC<0>{}); step(IC<1>{}); step(IC<2>{}); step(IC<3>{}); step(IC<4>{}); step(IC<5>{}); step(IC<6>{}); step(IC<7>{}); step(IC<8>{});
  __builtin_amdgcn_s_setprio(0);
  STAMP(1, 4);
  {
    const float il = __builtin_amdgcn_rcpf(swap32_add(lsum));
#pragma unroll
    for (int dt = 0; dt < 4; ++dt)
#pragma unroll
      for (int s = 0; s < 2; ++s)
        *(LAS u32x4*)(lds + LDS_X + ximg(16 * hh + 4 * dt + 2 * s + c.h, 32 * rt + c.r)) = pack8(o[dt], s, il);
  }
  BAR_L();
  STAMP(1, 5);
  dma16_sc1(lds0 + LDS_D + w * 2048u, l * 16u, srdW, W_MOD + 8192u + w * 2048u);
  dma16_sc1(lds0 + LDS_D + w * 2048u + 1024u, l * 16u, srdW, W_MOD + 8192u + w * 2048u + 1024u);
  {
    unsigned l2 = TIDX & 63;
    asm volatile("" : "+v"(l2));
    f32x4 acc[4][4] = {};
    gemm_phase<2>(acc, lds, ringoff, ring, vo, srdO, so0, 1024u, l2);
    STAMP(1, 6);
    const unsigned dst = W_P1 + ((((pn * 8 + ((l2 >> 3) & 1)) * 8 + g) * 64 + 8 * w + 2 * ((l2 >> 4) & 1) + (l2 >> 5)) * 8 + (l2 & 7)) * 16u;
#pragma unroll
    for (int r = 0; r < 4; ++r)
#pragma unroll
      for (int mp = 0; mp < 2; ++mp)
        __builtin_amdgcn_raw_buffer_store_b128(pack16(acc[2 * mp][r], acc[2 * mp + 1][r], 1.f), srdW,
                                               dst + (4 * mp * 8 + r * 2 * 8 * 64 * 8) * 16u, 0, 16);
  }
  WAIT_BAR(0);
  if (tid == 0) __builtin_amdgcn_raw_buffer_store_b32(tag, srdW, W_FLAG + (pn * 8 + g) * 64u, 0, 16);
  STAMP(1, 7);
  return tag;
}

DI void ln8(float (&v)[8], LAS float* red  , unsigned tid) {
  float s = ((v[0] + v[1]) + (v[2] + v[3])) + ((v[4] + v[5]) + (v[6] + v[7]));
  float q = (__builtin_fmaf(v[0], v[0], v[1] * v[1]) + __builtin_fmaf(v[2], v[2], v[3] * v[3])) +
            (__builtin_fmaf(v[4], v[4], v[5] * v[5]) + __builtin_fmaf(v[6], v[6], v[7] * v[7]));
  s = sum_hi3(s); q = sum_hi3(q);
  if ((tid & 63) < 8) *(LAS f32x2*)(red + ((tid >> 6) * 8 + (tid & 7)) * 2) = (f32x2){s, q};
  BAR_L();
  f32x2 e[8];
#pragma unroll
  for (int i = 0; i < 8; ++i) e[i] = *(const LAS f32x2*)(red + (i * 8 + (tid & 7)) * 2);
  const float mu = (((e[0][0] + e[1][0]) + (e[2][0] + e[3][0])) + ((e[4][0] + e[5][0]) + (e[6][0] + e[7][0]))) * (1.0f / D);
  const float ms = (((e[0][1] + e[1][1]) + (e[2][1] + e[3][1])) + ((e[4][1] + e[5][1]) + (e[6][1] + e[7][1]))) * (1.0f / D);
  const float rs = __builtin_amdgcn_rsqf(__builtin_fmaxf(__builtin_fmaf(-mu, mu, ms), 0.f) + LN_EPS);
  const float nb = -mu * rs;
#pragma unroll
  for (int k = 0; k < 8; ++k) v[k] = __builtin_fmaf(v[k], rs, nb);
}
DI void red4_put(const f32x16& a, int nt, lds_ptr scr, unsigned rt, unsigned kq, unsigned l) {
#pragma unroll
  for (int g = 0; g < 2; ++g)
#pragma unroll
    for (int i = 0; i < 2; ++i) {
      const int o = 8 * g + 4 * i;
      *(LAS f32x4*)(scr + ((rt * 4 + 2 * nt + g) * 4 + kq) * 2048 + i * 1024 + l * 16) = (f32x4){a[o], a[o + 1], a[o + 2], a[o + 3]};
    }
}
DI void red4_get(float (&v)[8], lds_cptr scr, unsigned rt, unsigned kq, unsigned l) {
  f32x4 t[4][2];
#pragma unroll
  for (int s = 0; s < 4; ++s)
#pragma unroll
    for (int i = 0; i < 2; ++i) t[s][i] = *(const LAS f32x4*)(scr + ((rt * 4 + kq) * 4 + s) * 2048 + i * 1024 + l * 16);
#pragma unroll
  for (int i = 0; i < 2; ++i)
#pragma unroll
    for (int k = 0; k < 4; ++k) v[4 * i + k] = (t[0][i][k] + t[1][i][k]) + (t[2][i][k] + t[3][i][k]);
}
DI void tile_mm8(const bf16x8 (&af)[8][2], const u32x4 (&xb)[8], float (&v)[8], lds_ptr scr, unsigned rt, unsigned kq, unsigned l) {
  f32x16 a0 = {}, a1 = {};
#pragma unroll
  for (int i = 0; i < 8; ++i) a0 = MFMA32(af[i][0], __builtin_bit_cast(bf16x8, xb[i]), a0);
  SBAR();
#pragma unroll
  for (int i = 0; i < 4; ++i) a1 = MFMA32(af[i][1], __builtin_bit_cast(bf16x8, xb[i]), a1);
  SBAR();
  red4_put(a0, 0, scr, rt, kq, l);
#pragma unroll
  for (int i = 4; i < 8; ++i) a1 = MFMA32(af[i][1], __builtin_bit_cast(bf16x8, xb[i]), a1);
  SBAR();
  red4_put(a1, 1, scr, rt, kq, l);
  BAR_L();
  red4_get(v, scr, rt, kq, l);
}

struct Frag3 { bf16x8 a, b[2]; };
constexpr int RING3 = 13;
constexpr int LA = 0, LB = 65536, LC = 131072;
DI unsigned r3off(int s, unsigned rc, unsigned rb) { return s < 4 ? rc + s * 1024u : rb + (s - 4) * 1024u; }
template <int KS> DI void load_frags3(Frag3& f, lds_cptr lds, unsigned rc, unsigned rb, unsigned a3, const LaneC& c) {
  const lds_cptr sl = lds + r3off(KS % RING3, rc, rb) + a3;
  f.a = cat8(lds_tr(sl), lds_tr(sl + 256));
#pragma unroll
  for (int rt = 0; rt < 2; ++rt) f.b[rt] = lds_b128(lds + LA + c.xb[KS & 3] + 2048 * KS + 512 * rt);
}
template <int KS>
DI void g3_step(f32x16 (&acc)[2], Frag3& cur, Frag3& n1, Frag3& n2, lds_cptr lds, unsigned lds0, unsigned rc, unsigned rb,
                unsigned voff, __amdgpu_buffer_rsrc_t srd, unsigned soff0, unsigned a3, const LaneC& c) {
  constexpr int NKS = 32;
  if constexpr (KS + 2 < NKS) {
    constexpr int last = (KS + RING3 < NKS ? KS + RING3 : NKS) - 1;
    wait_vm<last - (KS + 2)>();
    load_frags3<KS + 2>(n2, lds, rc, rb, a3, c);
  }
  SBAR();
  acc[0] = MFMA32(cur.a, cur.b[0], acc[0]);
  acc[1] = MFMA32(cur.a, cur.b[1], acc[1]);
  if constexpr (KS + RING3 < NKS) {
    asm volatile("" :: "v"(cur.a));
    dma16(lds0 + r3off(KS % RING3, rc, rb), voff, srd, soff0 + (unsigned)(16 * (KS + RING3)) * 3584u);
  }
  SBAR();
  if constexpr (KS + 1 < NKS) g3_step<KS + 1>(acc, n1, n2, cur, lds, lds0, rc, rb, voff, srd, soff0, a3, c);
}
DI void mlp_part(const Params& P, lds_ptr lds, unsigned lds0, unsigned w, __amdgpu_buffer_rsrc_t srdS, unsigned tag) {
  LaneC c; lane_consts(c);
  const unsigned idx = BIDX >> 3, pn = 8 * ((BIDX & 7) >> 1) + (idx >> 2), j = 4 * (BIDX & 1) + (idx & 3);
  const unsigned tid = TIDX, l = c.lane;
  const unsigned vw = wdma_voff(l, 1024u);
  const unsigned rt = w & 1, kq = w >> 1;
  constexpr int LMOD = LDS_D;
  constexpr int LRED = LC + 12288;
  constexpr int LST = LC + 28672;
  const unsigned prow = 32 * rt + c.r;
  const unsigned trow = pn * 64 + prow;
  const unsigned och = 8 * j + 2 * kq + c.h;
  const unsigned ovo = ((pn * 64 + och) * 64 + prow) * 16u;
  float gb[8];
  STAMP(2, 0);

  {
    const unsigned row = pn * 64 + j * 8 + (tid & 7), ch = tid >> 3;
    u32x4 xr;
    gld16(xr, P.x + (size_t)row * D + ch * 8);
    auto wait_flags = [&](unsigned fo) {
      for (unsigned spin = 0;; ++spin) {
        asm volatile("" ::: "memory");
        const unsigned f = __builtin_amdgcn_raw_buffer_load_b32(srdS, fo, 0, 16);
        if (!ANY(fo != OOB && f != tag) || spin > SPIN_CAP) break;
        __builtin_amdgcn_s_sleep(FLAG_SLEEP);
      }
    };
    const __amdgpu_buffer_rsrc_t srd1 = mksrd(P.w1, 512u * 512u * 2u), srd2 = mksrd(P.w2, 512u * 512u * 2u);
#pragma unroll
    for (int i = 0; i < 8; ++i) dma16(lds0 + LA + (8 * w + i) * 1024u, vw, srd1, (8 * w + i) * 8192u + j * 128u);
    {
      const int fp = (int)pn - 1 + (int)(l >> 3);
      if (w == 0) wait_flags((l < 24 && fp >= 0 && fp < 32) ? W_FLAG + ((unsigned)fp * 8 + (l & 7)) * 64u : OOB);
      BAR_L();
    }
    STAMP(2, 15);
    u32x4 p[8];
#pragma unroll
    for (int g = 0; g < 8; ++g) bld16_sc1(p[g], tid * 16u, srdS, W_P1 + ((pn * 8 + j) * 8 + g) * 8192u);
    float ga[8], sb[8], cb[8], g2[8];
    {
      u32x4 mv[20];
#pragma unroll
      for (int i = 0; i < 16; ++i) mv[i] = *(const LAS u32x4*)(lds + LMOD + (i >> 2) * 4096 + ch * 64 + 16 * (i & 3));
#pragma unroll
      for (int i = 0; i < 4; ++i) mv[16 + i] = *(const LAS u32x4*)(lds + LMOD + 12288 + och * 64 + 16 * i);
      if (ANY(bad_tag(mv, 20, tag))) {
#pragma unroll
        for (int i = 0; i < 4; ++i) poll_tag<4>(mv + 4 * i, srdS, ch * 64u, W_MOD + 8192u + i * 4096u, 16u, tag);
        poll_tag<4>(mv + 16, srdS, och * 64u, W_MOD + 20480u, 16u, tag);
      }
#pragma unroll
      for (int k = 0; k < 8; ++k) {
        ga[k] = U2F(mv[k >> 1][2 * (k & 1)]); sb[k] = U2F(mv[4 + (k >> 1)][2 * (k & 1)]); cb[k] = 1.f + U2F(mv[8 + (k >> 1)][2 * (k & 1)]);
        g2[k] = U2F(mv[12 + (k >> 1)][2 * (k & 1)]); gb[k] = U2F(mv[16 + (k >> 1)][2 * (k & 1)]);
      }
    }
    asm volatile("s_waitcnt vmcnt(0)" : "+v"(xr), "+v"(p[0]), "+v"(p[1]), "+v"(p[2]), "+v"(p[3]), "+v"(p[4]), "+v"(p[5]),
                 "+v"(p[6]), "+v"(p[7]) :: "memory");
    STAMP(2, 1);
    float v[8], x2[8];
#pragma unroll
    for (int i = 0; i < 4; ++i) {
      const float lo = ((bflo(p[0][i]) + bflo(p[1][i])) + (bflo(p[2][i]) + bflo(p[3][i]))) + ((bflo(p[4][i]) + bflo(p[5][i])) + (bflo(p[6][i]) + bflo(p[7][i])));
      const float hi = ((bfhi(p[0][i]) + bfhi(p[1][i])) + (bfhi(p[2][i]) + bfhi(p[3][i]))) + ((bfhi(p[4][i]) + bfhi(p[5][i])) + (bfhi(p[6][i]) + bfhi(p[7][i])));
      v[2 * i] = x2[2 * i] = __builtin_fmaf(ga[2 * i], lo, bflo(xr[i]));
      v[2 * i + 1] = x2[2 * i + 1] = __builtin_fmaf(ga[2 * i + 1], hi, bfhi(xr[i]));
    }
    ln8(v, (LAS float*)(lds + LRED), tid);
    float hb[8];
#pragma unroll
    for (int k = 0; k < 8; ++k) v[k] = hb[k] = __builtin_fmaf(v[k], cb[k], sb[k]);
    ln8(v, (LAS float*)(lds + LRED + 512), tid);
    const unsigned vo = ((pn * 64 + ch) * 64 + j * 8 + (tid & 7)) * 16u, vz = ((pn * 64 + ch) * 256 + j * 8 + (tid & 7)) * 16u;
    pub16(srdS, vo, S_U, (u32x4){cvtpk(v[0], v[1]), cvtpk(v[2], v[3]), cvtpk(v[4], v[5]), cvtpk(v[6], v[7])});
#pragma unroll
    for (int i = 0; i < 2; ++i) {
      pub16(srdS, vz, S_HZ + i * 1024u, (u32x4){F2U(hb[4 * i]), F2U(hb[4 * i + 1]),
                                                F2U(hb[4 * i + 2]), F2U(hb[4 * i + 3])});
      f32x4 z;
#pragma unroll
      for (int e = 0; e < 4; ++e) z[e] = __builtin_fmaf(g2[4 * i + e], hb[4 * i + e], x2[4 * i + e]);
      pub16(srdS, vz, S_HZ + (2 + i) * 1024u, __builtin_bit_cast(u32x4, z));
    }
#pragma unroll
    for (int i = 0; i < 8; ++i) dma16(lds0 + LB + (8 * w + i) * 1024u, vw, srd2, (8 * w + i) * 8192u + j * 128u);
  }
  STAMP(2, 2);
  const __amdgpu_buffer_rsrc_t srd3 = mksrd(P.w3, 512u * F * 2u);
  const unsigned rc = LC + w * 4096u, rb = LB + w * 9216u;
  const unsigned v3 = (l >> 2) * 3584u + ((l & 3) << 4), s30 = (j * 224u + w * 32u) * 2u;
  float v[8];
  {
    bf16x8 af[8][2];
#pragma unroll
    for (int i = 0; i < 8; ++i)
#pragma unroll
      for (int nt = 0; nt < 2; ++nt) {
        const lds_cptr a = lds + LA + (8 * kq + i) * 2048 + c.afr[nt];
        af[i][nt] = cat8(lds_tr(a), lds_tr(a + 512));
      }
    BAR_L();
    u32x4 xb[8];
    poll_ind<8, 3, 7>(xb, srdS, ((pn * 64 + 16 * kq + c.h) * 64 + prow) * 16u, S_U, 2048u);
    STAMP(2, 3);
    tile_mm8(af, xb, v, lds + LA, rt, kq, l);
#pragma unroll
    for (int k = 0; k < 8; ++k) v[k] = gelu_tanh(v[k]);
    pub16(srdS, ovo, S_H1, (u32x4){cvtpk(v[0], v[1]), cvtpk(v[2], v[3]), cvtpk(v[4], v[5]), cvtpk(v[6], v[7])});
    if (w < 7) {
#pragma unroll
      for (int ks = 0; ks < 4; ++ks) dma16(lds0 + r3off(ks, rc, rb), v3, srd3, s30 + (unsigned)(16 * ks) * 3584u);
    }
  }
  STAMP(2, 4);
  float y[8];
  float cs[16];
  const unsigned cso = w < 7 ? (3072 + 224 * j + 32 * w + 8 * c.h) * 8u : OOB;
  {
    bf16x8 af[8][2];
#pragma unroll
    for (int i = 0; i < 8; ++i)
#pragma unroll
      for (int nt = 0; nt < 2; ++nt) {
        const lds_cptr a = lds + LB + (8 * kq + i) * 2048 + c.afr[nt];
        af[i][nt] = cat8(lds_tr(a), lds_tr(a + 512));
      }
    BAR_L();
    u32x4 hz[4];
    const unsigned hzo = ((pn * 64 + och) * 256 + prow) * 16u;
#pragma unroll
    for (int i = 0; i < 4; ++i) hz[i] = __builtin_amdgcn_raw_buffer_load_b128(srdS, hzo, S_HZ + i * 1024u, 16);
    {
      u32x4 cg[8];
#pragma unroll
      for (int i = 0; i < 8; ++i) cg[i] = __builtin_amdgcn_raw_buffer_load_b128(srdS, cso, W_MOD + 128u * (i >> 2) + 16u * (i & 3), 16);
      if (ANY(cso < OOB && bad_tag(cg, 8, tag))) {
        poll_tag<4>(cg, srdS, cso, W_MOD, 16u, tag);
        poll_tag<4>(cg + 4, srdS, cso, W_MOD + 128u, 16u, tag);
      }
#pragma unroll
      for (int i = 0; i < 16; ++i) cs[i] = U2F(cg[i >> 1][2 * (i & 1)]);
    }
    const int sh = kq == 0 ? -1 : kq == 1 ? 1 : kq == 2 ? -2 : 2;
    const int srow = (int)trow + sh;
    const unsigned xvo = (srow >= 0 && srow < T) ? ((((unsigned)srow >> 6) * 64 + 16 * kq + c.h) * 64 + ((unsigned)srow & 63)) * 16u : OOB;
    u32x4 xb[8];
    poll_ind<8, 3, 7>(xb, srdS, xvo, S_H1, 2048u);
    STAMP(2, 5);
    tile_mm8(af, xb, v, lds + LA, rt, kq, l);
    if (has_sent(hz, 4)) poll_load<4>(hz, srdS, hzo, S_HZ, 1024u);
    float x1[8];
#pragma unroll
    for (int k = 0; k < 8; ++k) {
      x1[k] = U2F(hz[k >> 2][k & 3]) + v[k];
      y[k] = __builtin_fmaf(gb[k], v[k], U2F(hz[2 + (k >> 2)][k & 3]));
    }
    pub16(srdS, ovo, S_X1, (u32x4){cvtpk(x1[0], x1[1]), cvtpk(x1[2], x1[3]), cvtpk(x1[4], x1[5]), cvtpk(x1[6], x1[7])});
    if (w < 7) {
#pragma unroll
      for (int ks = 4; ks < RING3; ++ks) dma16(lds0 + r3off(ks, rc, rb), v3, srd3, s30 + (unsigned)(16 * ks) * 3584u);
    }
  }
  STAMP(2, 6);
  {
    u32x4 xr[8];
    BAR_L();
    poll_ind<8, 3, 7>(xr, srdS, ((pn * 64 + 8 * w) * 64 + l) * 16u, S_X1, 1024u);
    STAMP(2, 7);
#pragma unroll
    for (int i = 0; i < 8; ++i) *(LAS u32x4*)(lds + LA + ximg(8 * w + i, l)) = xr[i];
    {
      float s0 = 0.f, s1 = 0.f, q0 = 0.f, q1 = 0.f;
#pragma unroll
      for (int i = 0; i < 8; ++i)
#pragma unroll
        for (int k = 0; k < 4; k += 2) {
          s0 = dot2bf(xr[i][k], 0x3F803F80u, s0); s1 = dot2bf(xr[i][k + 1], 0x3F803F80u, s1);
          q0 = dot2bf(xr[i][k], xr[i][k], q0); q1 = dot2bf(xr[i][k + 1], xr[i][k + 1], q1);
        }
      *(LAS f32x2*)(lds + LST + (w * 64 + l) * 8) = (f32x2){s0 + s1, q0 + q1};
    }
    BAR_L();
    STAMP(2, 8);
    if (w < 7) {
      f32x16 acc[2] = {};
      const unsigned a3 = (8 * c.h + c.q) * 64 + 32 * c.blk + 8 * (2 * (c.p & 1) + (c.p >> 1));
      Frag3 f0, f1, f2;
      wait_vm<RING3 - 2>();
      load_frags3<0>(f0, lds, rc, rb, a3, c);
      load_frags3<1>(f1, lds, rc, rb, a3, c);
      float mu[2], rs[2];
#pragma unroll
      for (int r2 = 0; r2 < 2; ++r2) {
        float s = 0.f, q = 0.f;
#pragma unroll
        for (int i = 0; i < 8; ++i) {
          const f32x2 t = *(const LAS f32x2*)(lds + LST + (i * 64 + 32 * r2 + c.r) * 8);
          s += t[0]; q += t[1];
        }
        mu[r2] = s * (1.0f / D);
        rs[r2] = __builtin_amdgcn_rsqf(__builtin_fmaxf(__builtin_fmaf(-mu[r2], mu[r2], q * (1.0f / D)), 0.f) + LN_EPS);
      }
      if (w < 4) __builtin_amdgcn_s_setprio(2); else __builtin_amdgcn_s_setprio(1);
      g3_step<0>(acc, f0, f1, f2, lds, lds0, rc, rb, v3, srd3, s30, a3, c);
      __builtin_amdgcn_s_setprio(0);
      STAMP(2, 9);
#pragma unroll
      for (int r2 = 0; r2 < 2; ++r2) {
#pragma unroll
        for (int i = 0; i < 16; ++i) acc[r2][i] = gelu_tanh(rs[r2] * __builtin_fmaf(-mu[r2], cs[i], acc[r2][i]));
#pragma unroll
        for (int s2 = 0; s2 < 2; ++s2)
          pub16(srdS, ((pn * 224 + 28 * j + 4 * w + 2 * s2 + c.h) * 64 + 32 * r2 + c.r) * 16u, S_H3, pack8(acc[r2], s2, 1.f));
      }
    }
    BAR_L();
  }
  STAMP(2, 10);
  {
    const __amdgpu_buffer_rsrc_t srd4 = mksrd(P.w4, (unsigned)F * 512u * 2u);
    const unsigned qb = kq * 40960u;
    const unsigned s40 = (448u * kq + 8u * rt) * 1024u + j * 128u;
    auto w4dma = [&](int ks) { dma16(lds0 + qb + (ks % 20) * 2048u + rt * 1024u, vw, srd4, s40 + (unsigned)ks * 16384u); };
#pragma unroll
    for (int ks = 0; ks < NPRE; ++ks) w4dma(ks);
    const unsigned hvo = ((pn * 224 + 56 * kq + c.h) * 64 + prow) * 16u;
    u32x4 ha[14], hb[14];
    poll_ind<14, 9, 13>(ha, srdS, hvo, S_H3, 2048u);
    BAR_L();
    STAMP(2, 11);
    f32x16 acc[2] = {};
    bf16x8 fa[2][2];
    auto ld = [&](int ks) {
#pragma unroll
      for (int nt = 0; nt < 2; ++nt) {
        const lds_cptr a = lds + qb + (ks % 20) * 2048 + c.afr[nt];
        fa[ks & 1][nt] = cat8(lds_tr(a), lds_tr(a + 512));
      }
    };
    auto mm = [&](int ks, const u32x4& b) {
#pragma unroll
      for (int nt = 0; nt < 2; ++nt) acc[nt] = MFMA32(fa[ks & 1][nt], __builtin_bit_cast(bf16x8, b), acc[nt]);
    };
    auto hbl = [&](int i) { bld16_sc1(hb[i], hvo, srdS, S_H3 + (14u + i) * 2048u); };
    ld(0);
#pragma unroll
    for (int ks = 0; ks < 8; ++ks) {
      if (ks < 7) ld(ks + 1);
      SBAR(); mm(ks, ha[ks]);
      if (ks < 7) { hbl(2 * ks); hbl(2 * ks + 1); }
      else {
#pragma unroll
        for (int k2 = NPRE; k2 < 20; ++k2) w4dma(k2);
      }
      SBAR();
    }
    BAR_L();
    ld(8);
#pragma unroll
    for (int ks = 8; ks < 14; ++ks) {
      if (ks < 13) ld(ks + 1);
      SBAR(); mm(ks, ha[ks]);
      if (ks < 12) { w4dma(20 + 2 * (ks - 8)); w4dma(21 + 2 * (ks - 8)); }
      SBAR();
    }
    asm volatile("s_waitcnt vmcnt(8) lgkmcnt(0)\n\ts_barrier" : "+v"(hb[0]), "+v"(hb[1]), "+v"(hb[2]), "+v"(hb[3]), "+v"(hb[4]), "+v"(hb[5]),
                 "+v"(hb[6]), "+v"(hb[7]), "+v"(hb[8]), "+v"(hb[9]), "+v"(hb[10]), "+v"(hb[11]), "+v"(hb[12]), "+v"(hb[13]) :: "memory");
    if (has_sent(hb, 14)) poll_load<14>(hb, srdS, hvo, S_H3 + 14u * 2048u, 2048u);
    STAMP(2, 12);
    ld(14);
#pragma unroll
    for (int ks = 14; ks < 19; ++ks) { ld(ks + 1); SBAR(); mm(ks, hb[ks - 14]); SBAR(); }
    mm(19, hb[5]);
    WAIT_BAR(0);
    ld(20);
#pragma unroll
    for (int ks = 20; ks < 27; ++ks) { ld(ks + 1); SBAR(); mm(ks, hb[ks - 14]); SBAR(); }
    mm(27, hb[13]);
    BAR_L();
    STAMP(2, 13);
    red4_put(acc[0], 0, lds + LA, rt, kq, l);
    red4_put(acc[1], 1, lds + LA, rt, kq, l);
    BAR_L();
    red4_get(v, lds + LA, rt, kq, l);
#pragma unroll
    for (int k = 0; k < 8; ++k) v[k] = __builtin_fmaf(gb[k], v[k], y[k]);
    P.out[(size_t)trow * 64 + och] = (u32x4){cvtpk(v[0], v[1]), cvtpk(v[2], v[3]), cvtpk(v[4], v[5]), cvtpk(v[6], v[7])};
  }
  STAMP(2, 14);
}

__global__ __launch_bounds__(512, 2) void k_fused(Params P) {
  extern __shared__ __attribute__((aligned(16))) char smem[];
  const lds_ptr lds = (lds_ptr)smem;
  const unsigned lds0 = (unsigned)(uintptr_t)smem;
  const unsigned w = rfl(TIDX >> 6);
  const __amdgpu_buffer_rsrc_t srdW = mksrd(P.ws, W_END);
  {
    unsigned long long pc; unsigned len;
    asm volatile("s_getpc_b64 %0\n.Lkf_here:\n\ts_mov_b32 %1, .Lkf_end-.Lkf_here" : "=s"(pc), "=s"(len));
    dma16(lds0 + LDS_R + w * RINGB + 8192u, (TIDX & 63) * 16u, mksrd((const void*)(pc & ~15ull), len & ~15u), ((BIDX >> 3) * 8 + w) * 1024u);
  }
  const unsigned tag = attn_part(P, lds, lds0, w, srdW);
  mlp_part(P, lds, lds0, w, srdW, tag);
  asm volatile(".Lkf_end:");
}

#if defined(__HIP_DEVICE_COMPILE__)
#pragma clang attribute pop
#endif
extern "C" void kernel_launch(void* const* d_in, const int* in_sizes, int n_in, void* d_out, int out_size, void* d_ws,
                              size_t ws_size, hipStream_t stream) {
  Params P;
  P.x = (const unsigned short*)d_in[0];
  P.kc = (const unsigned short*)d_in[1];
  P.vc = (const unsigned short*)d_in[2];
  P.wq = (const unsigned short*)d_in[3];
  P.wo = (const unsigned short*)d_in[4];
  P.w1 = (const unsigned short*)d_in[6];
  P.w2 = (const unsigned short*)d_in[7];
  P.w3 = (const unsigned short*)d_in[8];
  P.w4 = (const unsigned short*)d_in[9];
  P.c = (const unsigned short*)d_in[10];
  P.wmod = (const unsigned short*)d_in[11];
  P.ws = d_ws;
  P.out = (u32x4*)d_out;
  (void)hipFuncSetAttribute((const void*)k_fused, hipFuncAttributeMaxDynamicSharedMemorySize, LDS_TOTAL);
  k_fused<<<256, 512, LDS_TOTAL, stream>>>(P);
}
```

```cpp
#include <hip/hip_runtime.h>
#include <hip/hip_bf16.h>
#include <stdint.h>

#if defined(__HIP_DEVICE_COMPILE__)
#pragma clang attribute push(__attribute__((target("no-packed-fp32-ops"))), apply_to = function)
#endif
typedef short bf16x8 __attribute__((ext_vector_type(8)));
typedef short s16x4 __attribute__((ext_vector_type(4)));
typedef float f32x16 __attribute__((ext_vector_type(16)));
typedef float f32x4 __attribute__((ext_vector_type(4)));
typedef float f32x2 __attribute__((ext_vector_type(2)));
typedef unsigned u32x4 __attribute__((ext_vector_type(4)));
typedef unsigned u32x2 __attribute__((ext_vector_type(2)));
typedef __bf16 bf16x2_t __attribute__((ext_vector_type(2)));
typedef short v4i16_t __attribute__((ext_vector_type(4)));
#define LAS __attribute__((address_space(3)))
typedef LAS const char* lds_cptr;
typedef LAS char* lds_ptr;
#define DI __device__ __forceinline__
#define F2U(x) __builtin_bit_cast(unsigned, (float)(x))
#define U2F(x) __builtin_bit_cast(float, (unsigned)(x))
#define F2I(x) __builtin_bit_cast(int, (float)(x))
#define I2F(x) __builtin_bit_cast(float, (int)(x))
#define TIDX __builtin_amdgcn_workitem_id_x()
#define BIDX __builtin_amdgcn_workgroup_id_x()
#define ANY(p) (__builtin_amdgcn_ballot_w64(p) != 0ull)
#define SBAR() __builtin_amdgcn_sched_barrier(0)
#define MFMA32(a, b, c) __builtin_amdgcn_mfma_f32_32x32x16_bf16((a), (b), (c), 0, 0, 0)
#define WAIT_V(n) asm volatile("s_waitcnt vmcnt(" #n ")" ::: "memory")
#define WAIT_L0() asm volatile("s_waitcnt lgkmcnt(0)" ::: "memory")
#define BAR_L() asm volatile("s_waitcnt lgkmcnt(0)\n\ts_barrier" ::: "memory")
#define WAIT_BAR(n) asm volatile("s_waitcnt vmcnt(" #n ") lgkmcnt(0)\n\ts_barrier" ::: "memory")

constexpr int T = 2048, D = 512, HKV = 8, SK = 2048, DH = 128, WIN = 512, F = 1792;
constexpr float LN_EPS = 1e-5f;
constexpr float QSCALE = 0.088388347648318440f * 1.4426950408889634f;

constexpr unsigned W_EPOCH = 0;
constexpr unsigned W_FLAG = 4096;
constexpr unsigned W_MOD = 32768;
constexpr unsigned W_MP = W_MOD + 40960;
constexpr unsigned W_P1 = 393216;
constexpr unsigned S_U = W_P1 + 8u * 64u * T * 16u;
constexpr unsigned S_H1 = S_U + 64u * T * 16u;
constexpr unsigned S_X1 = S_H1 + 64u * T * 16u;
constexpr unsigned S_HZ = S_X1 + 64u * T * 16u;
constexpr unsigned S_H3 = S_HZ + 256u * T * 16u;
constexpr unsigned W_END = S_H3 + 224u * T * 16u;
static_assert(W_MP + 8u * 4864u * 8u <= W_P1, "ws");
constexpr unsigned OOB = 0xF0000000u;
#define K1MAP 0
#define FLAG_SLEEP 4
#define GSPLIT 2
#define ATHR 32.0f
constexpr float THR = ATHR;
constexpr unsigned SENT = 0xFFFFFFFFu;
constexpr unsigned SPIN_CAP = 1u << 16;

constexpr int LDS_X = 0;
constexpr int LDS_R = 65536;
constexpr int RING = 5, RINGB = RING * 2048;
constexpr int LDS_D = LDS_R + 8 * RINGB;
constexpr int LDS_TOTAL = LDS_D + 16384;
static_assert(LDS_TOTAL == 163840, "lds");

#define STAMP(kid, i) do {} while (0)
DI unsigned cvtpk(float lo, float hi) {
  f32x2 v = {lo, hi};
  bf16x2_t b = __builtin_convertvector(v, bf16x2_t);
  return __builtin_bit_cast(unsigned, b);
}
DI float bflo(unsigned u) { return U2F(u << 16); }
DI float bfhi(unsigned u) { return U2F(u & 0xffff0000u); }
DI unsigned rfl(unsigned x) { return __builtin_amdgcn_readfirstlane(x); }
DI __amdgpu_buffer_rsrc_t mksrd(const void* p, unsigned bytes) {
  return __builtin_amdgcn_make_buffer_rsrc((void*)p, (short)0, (int)bytes, 0x00020000);
}
DI void dma16(unsigned lds, unsigned voff, __amdgpu_buffer_rsrc_t srd, unsigned soff) {
  asm volatile("s_mov_b32 m0, %0\n\ts_nop 0\n\tbuffer_load_dwordx4 %1, %2, %3 offen lds"
               :: "s"(lds), "v"(voff), "s"(srd), "s"(soff) : "memory");
}
DI void gld16(u32x4& d, const void* p) { asm volatile("global_load_dwordx4 %0, %1, off" : "=v"(d) : "v"(p) : "memory"); }
DI void bld16_sc1(u32x4& d, unsigned voff, __amdgpu_buffer_rsrc_t srd, unsigned soff) {
  asm volatile("buffer_load_dwordx4 %0, %1, %2, %3 offen sc1" : "=v"(d) : "v"(voff), "s"(srd), "s"(soff) : "memory");
}
DI void dma16_sc1(unsigned lds, unsigned voff, __amdgpu_buffer_rsrc_t srd, unsigned soff) {
  asm volatile("s_mov_b32 m0, %0\n\ts_nop 0\n\tbuffer_load_dwordx4 %1, %2, %3 offen sc1 lds"
               :: "s"(lds), "v"(voff), "s"(srd), "s"(soff) : "memory");
}
DI bf16x8 lds_b128(lds_cptr p) { return *(const LAS bf16x8*)p; }
DI s16x4 lds_tr(lds_cptr p) {
  return __builtin_bit_cast(s16x4, __builtin_amdgcn_ds_read_tr16_b64_v4i16((LAS v4i16_t*)p));
}
DI bf16x8 cat8(s16x4 lo, s16x4 hi) { return __builtin_shufflevector(lo, hi, 0, 1, 2, 3, 4, 5, 6, 7); }
DI float swap32_add(float x) {
  auto r = __builtin_amdgcn_permlane32_swap(F2U(x), F2U(x), false, false);
  return U2F(r[0]) + U2F(r[1]);
}
DI float swap32_max(float x) {
  auto r = __builtin_amdgcn_permlane32_swap(F2U(x), F2U(x), false, false);
  return __builtin_fmaxf(U2F(r[0]), U2F(r[1]));
}
template <int CTRL> DI float dpp_add(float x) {
  int y = __builtin_amdgcn_update_dpp(0, F2I(x), CTRL, 0xF, 0xF, true);
  return x + I2F(y);
}
DI float max3f(float a, float b, float c) { float r; asm("v_max3_f32 %0, %1, %2, %3" : "=v"(r) : "v"(a), "v"(b), "v"(c)); return r; }
DI float max2f(float a, float b) { float r; asm("v_max_f32_e32 %0, %1, %2" : "=v"(r) : "v"(a), "v"(b)); return r; }
DI float dot2bf(unsigned a, unsigned b, float c) {
  return __builtin_amdgcn_fdot2_f32_bf16(__builtin_bit_cast(bf16x2_t, a), __builtin_bit_cast(bf16x2_t, b), c, false);
}
#define PIN(x) asm volatile("" : "+v"(x))
DI float swap16_add(float x) {
  auto r = __builtin_amdgcn_permlane16_swap(F2U(x), F2U(x), false, false);
  return U2F(r[0]) + U2F(r[1]);
}
DI float sum_hi3(float x) { return swap32_add(swap16_add(dpp_add<0x128>(x))); }
DI float gelu_tanh(float x) {
  const float k0 = -2.0f * 0.7978845608028654f * 1.4426950408889634f;
  const float t = x * x;
  const float z = x * __builtin_fmaf(t, 0.044715f * k0, k0);
  const float e = __builtin_amdgcn_exp2f(z);
  return x * __builtin_amdgcn_rcpf(1.0f + e);
}
DI void wave_sum8(const float (&a)[8], float (&out)[8]) {
  float b[4], c[2];
#pragma unroll
  for (int i = 0; i < 4; ++i) {
    auto r = __builtin_amdgcn_permlane32_swap(F2U(a[i]), F2U(a[i + 4]), false, false);
    b[i] = U2F(r[0]) + U2F(r[1]);
  }
#pragma unroll
  for (int i = 0; i < 2; ++i) {
    auto r = __builtin_amdgcn_permlane16_swap(F2U(b[i]), F2U(b[i + 2]), false, false);
    c[i] = U2F(r[0]) + U2F(r[1]);
  }
#pragma unroll
  for (int i = 0; i < 2; ++i) {
    c[i] = dpp_add<0xB1>(c[i]);
    c[i] = dpp_add<0x4E>(c[i]);
    c[i] = dpp_add<0x141>(c[i]);
    c[i] = dpp_add<0x140>(c[i]);
  }
#pragma unroll
  for (int q = 0; q < 4; ++q)
#pragma unroll
    for (int i = 0; i < 2; ++i)
      out[i + 2 * (q & 1) + 4 * (q >> 1)] = I2F(__builtin_amdgcn_readlane(F2I(c[i]), 16 * q));
}

DI unsigned nosent(unsigned v) { return v < 0xFFFFFFFEu ? v : 0xFFFFFFFEu; }
DI void pub16(__amdgpu_buffer_rsrc_t srd, unsigned voff, unsigned soff, u32x4 v) {
  v = (u32x4){nosent(v[0]), nosent(v[1]), nosent(v[2]), nosent(v[3])};
  __builtin_amdgcn_raw_buffer_store_b128(v, srd, voff + soff, 0, 16);
}
DI unsigned umax3(unsigned a, unsigned b, unsigned c) { const unsigned m = a > b ? a : b; return m > c ? m : c; }
DI bool has_sent(const u32x4* v, int n) {
  unsigned m = 0;
#pragma unroll
  for (int i = 0; i < n; ++i) { m = umax3(m, v[i][0], v[i][1]); m = umax3(m, v[i][2], v[i][3]); }
  return ANY(m == SENT);
}
template <int N> DI void poll_load(u32x4* v, __amdgpu_buffer_rsrc_t srd, unsigned voff, unsigned soff0, unsigned step) {
  for (unsigned spin = 0;; ++spin) {
    asm volatile("" ::: "memory");
#pragma unroll
    for (int i = 0; i < N; ++i) v[i] = __builtin_amdgcn_raw_buffer_load_b128(srd, voff, soff0 + (unsigned)i * step, 16);
    if (!has_sent(v, N) || spin > SPIN_CAP) break;
    __builtin_amdgcn_s_sleep(1);
  }
}
template <int N, int I0, int I1> DI void poll_ind(u32x4* v, __amdgpu_buffer_rsrc_t srd, unsigned voff, unsigned soff0, unsigned step) {
#pragma unroll
  for (int i = 0; i < N; ++i) v[i] = __builtin_amdgcn_raw_buffer_load_b128(srd, voff, soff0 + (unsigned)i * step, 16);
  if (has_sent(v, N)) {
    u32x4 ind[2];
    poll_load<2>(ind, srd, voff, soff0 + (unsigned)I0 * step, (unsigned)(I1 - I0) * step);
    poll_load<N>(v, srd, voff, soff0, step);
  }
}

DI bool bad_tag(const u32x4* v, int n, unsigned tag) {
  unsigned m = 0;
#pragma unroll
  for (int i = 0; i < n; ++i) m |= (v[i][1] ^ tag) | (v[i][3] ^ tag);
  return m != 0;
}
template <int N> DI void poll_tag(u32x4* v, __amdgpu_buffer_rsrc_t srd, unsigned voff, unsigned soff0, unsigned step, unsigned tag) {
  for (unsigned spin = 0;; ++spin) {
    asm volatile("" ::: "memory");
#pragma unroll
    for (int i = 0; i < N; ++i) v[i] = __builtin_amdgcn_raw_buffer_load_b128(srd, voff, soff0 + (unsigned)i * step, 16);
    if (!ANY(voff < OOB && bad_tag(v, N, tag)) || spin > SPIN_CAP) break;
    __builtin_amdgcn_s_sleep(1);
  }
}

struct LaneC {
  unsigned lane, r, h, q, p, blk;
  unsigned wv;
  unsigned afr[2];
  unsigned xb[4];
};
DI void lane_consts(LaneC& c) {
  unsigned l = TIDX & 63;
  asm volatile("" : "+v"(l));
  c.lane = l; c.r = l & 31; c.h = l >> 5; c.q = (l & 15) >> 2; c.p = l & 3; c.blk = (l >> 4) & 1;
  const unsigned sp = 2 * (c.p & 1) + (c.p >> 1);
#pragma unroll
  for (int nt = 0; nt < 2; ++nt) c.afr[nt] = (8 * c.h + c.q) * 128 + 64 * (nt ^ (c.q >> 1)) + 32 * c.blk + 8 * sp;
#pragma unroll
  for (int s = 0; s < 4; ++s) c.xb[s] = 1024 * c.h + ((c.r ^ (2 * s + c.h)) << 4);
}
DI unsigned wdma_voff(unsigned l, unsigned ld) { return (l >> 3) * ld + (((l & 7) ^ (((l >> 4) & 1) << 2)) << 4); }

DI void wring_issue(int ks, unsigned ring, unsigned voff, __amdgpu_buffer_rsrc_t srd, unsigned soff0, unsigned ld) {
  const unsigned slot = ring + (unsigned)(ks % RING) * 2048u;
  dma16(slot, voff, srd, soff0 + (unsigned)(16 * ks) * ld);
  dma16(slot + 1024u, voff ^ 32u, srd, soff0 + (unsigned)(16 * ks + 8) * ld);
}
template <int N> DI void wait_vm() {
  if constexpr (N == 0) WAIT_V(0); else if constexpr (N == 1) WAIT_V(1); else if constexpr (N == 2) WAIT_V(2);
  else if constexpr (N == 3) WAIT_V(3); else if constexpr (N == 4) WAIT_V(4); else if constexpr (N == 5) WAIT_V(5);
  else if constexpr (N == 6) WAIT_V(6); else if constexpr (N == 7) WAIT_V(7); else if constexpr (N == 8) WAIT_V(8);
  else if constexpr (N == 9) WAIT_V(9); else if constexpr (N == 10) WAIT_V(10); else if constexpr (N == 11) WAIT_V(11);
  else if constexpr (N == 12) WAIT_V(12); else if constexpr (N == 13) WAIT_V(13); else if constexpr (N == 14) WAIT_V(14);
  else if constexpr (N == 15) WAIT_V(15); else if constexpr (N == 16) WAIT_V(16);
  else if constexpr (N == 17) WAIT_V(17); else if constexpr (N == 18) WAIT_V(18);
  else static_assert(N < 0, "wait_vm");
}

#define MFMA16(a, b, c) __builtin_amdgcn_mfma_f32_16x16x32_bf16((a), (b), (c), 0, 0, 0)
struct G16 { unsigned a[4], hi, x[2]; };
DI void g16_consts(G16& k, unsigned l, unsigned ringoff) {
  const unsigned i = l & 15, g = l >> 4, q = i >> 2, p = l & 3, x = 2 * (q >> 1) + (g & 1);
#pragma unroll
  for (int m = 0; m < 4; ++m) k.a[m] = ringoff + (8 * (g & 1) + q) * 128 + ((2 * (m ^ x) + (p >> 1)) << 4) + 8 * (p & 1);
  k.hi = g >> 1;
#pragma unroll
  for (int e = 0; e < 2; ++e) k.x[e] = LDS_X + 1024 * g + ((i ^ (4 * e + g)) << 4);
}
struct Frag { bf16x8 a[4], b[4]; };
template <int KK> DI void load_frags(Frag& f, lds_cptr lds, const G16& k) {
  constexpr int sA = (2 * KK) % RING;
  const unsigned d = sA == RING - 1 ? (1u - k.hi) * (unsigned)((RING - 1) * 2048) : k.hi * 2048u;
  constexpr int o = sA == RING - 1 ? 0 : sA * 2048;
#pragma unroll
  for (int m = 0; m < 4; ++m) f.a[m] = cat8(lds_tr(lds + (k.a[m] + d) + o), lds_tr(lds + (k.a[m] + d) + o + 512));
#pragma unroll
  for (int r = 0; r < 4; ++r) f.b[r] = lds_b128(lds + k.x[KK & 1] + 4096 * KK + 256 * r);
}
template <int KK, int EXTRA>
DI void gemm_step(f32x4 (&acc)[4][4], Frag& cur, Frag& nxt, lds_cptr lds, unsigned ring, unsigned voff,
                  __amdgpu_buffer_rsrc_t srd, unsigned soff0, unsigned ld, const G16& k) {
  if constexpr (2 * KK + RING < 32) {
    asm volatile("" :: "v"(cur.a[0]), "v"(cur.a[1]), "v"(cur.a[2]), "v"(cur.a[3]));
    wring_issue(2 * KK + RING, ring, voff, srd, soff0, ld);
    if constexpr (2 * KK + RING + 1 < 32) wring_issue(2 * KK + RING + 1, ring, voff, srd, soff0, ld);
  }
  SBAR();
#pragma unroll
  for (int m = 0; m < GSPLIT; ++m)
#pragma unroll
    for (int r = 0; r < 4; ++r) acc[m][r] = MFMA16(cur.a[m], cur.b[r], acc[m][r]);
  SBAR();
  if constexpr (KK + 1 < 16) {
    constexpr int last = 2 * KK + RING + 1 < 32 ? 2 * KK + RING + 1 : 31;
    wait_vm<2 * (last - (2 * KK + 3)) + ((2 * KK + 3 < RING) ? EXTRA : 0)>();
    load_frags<KK + 1>(nxt, lds, k);
  }
  SBAR();
#pragma unroll
  for (int m = GSPLIT; m < 4; ++m)
#pragma unroll
    for (int r = 0; r < 4; ++r) acc[m][r] = MFMA16(cur.a[m], cur.b[r], acc[m][r]);
  SBAR();
  if constexpr (KK + 1 < 16) gemm_step<KK + 1, EXTRA>(acc, nxt, cur, lds, ring, voff, srd, soff0, ld, k);
}
template <int EXTRA>
DI void gemm_phase(f32x4 (&acc)[4][4], lds_cptr lds, unsigned ringoff, unsigned ring, unsigned voff,
                   __amdgpu_buffer_rsrc_t srd, unsigned soff0, unsigned ld, unsigned l) {
  G16 k; g16_consts(k, l, ringoff);
  Frag f0, f1;
  wait_vm<2 * (RING - 2) + EXTRA>();
  load_frags<0>(f0, lds, k);
  gemm_step<0, EXTRA>(acc, f0, f1, lds, ring, voff, srd, soff0, ld, k);
}
DI u32x4 pack16(const f32x4& a, const f32x4& b, float sc) {
  auto r0 = __builtin_amdgcn_permlane16_swap(cvtpk(a[0] * sc, a[1] * sc), cvtpk(b[0] * sc, b[1] * sc), false, false);
  auto r1 = __builtin_amdgcn_permlane16_swap(cvtpk(a[2] * sc, a[3] * sc), cvtpk(b[2] * sc, b[3] * sc), false, false);
  return (u32x4){r0[0], r1[0], r0[1], r1[1]};
}
DI u32x4 pack8(const f32x16& a, int s, float sc) {
  return (u32x4){cvtpk(a[8 * s] * sc, a[8 * s + 1] * sc), cvtpk(a[8 * s + 2] * sc, a[8 * s + 3] * sc),
                 cvtpk(a[8 * s + 4] * sc, a[8 * s + 5] * sc), cvtpk(a[8 * s + 6] * sc, a[8 * s + 7] * sc)};
}
DI unsigned ximg(unsigned chunk, unsigned row) { return chunk * 1024u + ((row ^ (chunk & 7u)) << 4); }

constexpr int KSLOT[8] = {LDS_D, LDS_R, LDS_R + 32768, LDS_X, LDS_X + 32768, LDS_D, LDS_X, LDS_X + 32768};
constexpr int VSLOT[8] = {LDS_R + 16384, LDS_R + 49152, LDS_X + 16384, LDS_X + 49152, LDS_R + 65536, LDS_X + 16384, LDS_X + 49152, LDS_D};
template <int N> struct IC { static constexpr int value = N; };
struct Params {
  const unsigned short *x, *kc, *vc, *wq, *wo, *w1, *w2, *w3, *w4, *c, *wmod;
  void* ws;
  u32x4* out;
};
DI unsigned attn_part(const Params& P, lds_ptr lds, unsigned lds0, unsigned w, __amdgpu_buffer_rsrc_t srdW) {
  LaneC c; lane_consts(c);
  const unsigned g = BIDX & 7, pn = BIDX >> 3;
  const unsigned l = c.lane, tid = TIDX;
  const unsigned ringoff = LDS_R + w * RINGB, ring = lds0 + ringoff;
  STAMP(1, 0);

  unsigned ep;
  asm volatile("buffer_load_dword %0, off, %1, 0 sc1" : "=v"(ep) : "s"(srdW) : "memory");
  u32x4 jw[4]; unsigned jc[4];
  auto c_load = [&](unsigned jks) __attribute__((always_inline)) {
#pragma unroll
    for (int i = 0; i < 4; ++i) asm volatile("global_load_ushort %0, %1, off" : "=v"(jc[i]) : "v"(P.c + jks * 64 + (l >> 2) + 16 * i) : "memory");
  };
  auto job_run = [&](unsigned jcol, unsigned jks, unsigned tag) __attribute__((always_inline)) {
    float acc[8] = {0, 0, 0, 0, 0, 0, 0, 0};
#pragma unroll
    for (int i = 0; i < 4; ++i) {
      const float v = U2F(jc[i] << 16);
      const float sv = v * __builtin_amdgcn_rcpf(1.0f + __builtin_amdgcn_exp2f(-1.4426950408889634f * v));
      const float sc = jcol >= 96u ? 1.0f : sv;
#pragma unroll
      for (int k = 0; k < 4; ++k) {
        acc[2 * k] = __builtin_fmaf(sc, bflo(jw[i][k]), acc[2 * k]);
        acc[2 * k + 1] = __builtin_fmaf(sc, bfhi(jw[i][k]), acc[2 * k + 1]);
      }
    }
#pragma unroll
    for (int k = 0; k < 8; ++k) acc[k] = sum_hi3(dpp_add<0x124>(acc[k]));
    const unsigned e = (l >> 2) & 3;
    const float v0 = e == 0 ? acc[0] : e == 1 ? acc[2] : e == 2 ? acc[4] : acc[6], v1 = e == 0 ? acc[1] : e == 1 ? acc[3] : e == 2 ? acc[5] : acc[7];
    __builtin_amdgcn_raw_buffer_store_b128((u32x4){F2U(v0), tag, F2U(v1), tag}, srdW,
                                           l < 16 ? W_MP + (jks * 4864u + jcol * 32u + (l & 3) * 8u + e * 2u) * 8u : OOB, 0, 16);
  };
  u32x4 xr[8];
  auto x_load = [&]() __attribute__((always_inline)) {
#pragma unroll
    for (int i = 0; i < 8; ++i) gld16(xr[i], P.x + (size_t)(pn * 64 + w * 8 + i) * D + l * 8);
  };
  if (w == 0) {
#pragma unroll
    for (int i = 0; i < 4; ++i) gld16(jw[i], P.wmod + (BIDX & 31u) * 32 + (l & 3) * 8 + (size_t)((BIDX >> 5) * 64 + (l >> 2) + 16 * i) * 3072);
    c_load(BIDX >> 5);
  } else x_load();
  asm volatile("s_waitcnt vmcnt(8)\n\ts_barrier" : "+v"(ep) :: "memory");
  const unsigned tag = rfl(ep) + 1u;
  if (w == 0) {
    asm volatile("s_waitcnt vmcnt(0)" : "+v"(jw[0]), "+v"(jw[1]), "+v"(jw[2]), "+v"(jw[3]), "+v"(jc[0]), "+v"(jc[1]), "+v"(jc[2]), "+v"(jc[3]) :: "memory");
    job_run(BIDX & 31u, BIDX >> 5, tag); x_load();
  }
  const unsigned jid = w * 256u + BIDX, jcol2 = 32u + jid % 120u, jks2 = jid / 120u;
  const bool job2 = w < 4 && jid < 960u;
  const __amdgpu_buffer_rsrc_t srdQ = mksrd(P.wq, 512u * 4096u * 2u);
  const unsigned vq = wdma_voff(l, 8192u), sq0 = (g * 512u + w * 64u) * 2u;
  const __amdgpu_buffer_rsrc_t srdK = mksrd(P.kc + ((size_t)g * SK + (SK - WIN)) * DH, WIN * DH * 2u);
  const __amdgpu_buffer_rsrc_t srdV = mksrd(P.vc + ((size_t)g * SK + (SK - WIN)) * DH, WIN * DH * 2u);
  const unsigned vk = ((l >> 2) & 7) * 256u + (l >> 5) * 64u + (((l & 3) ^ (2 * (w & 1) + ((l >> 4) & 1))) << 4);
  const unsigned vv = wdma_voff(l, 256u);
  auto dma_k = [&](int t, unsigned slot) {
    dma16(lds0 + slot + 2048u * w, vk, srdK, (unsigned)t * 16384u + 2048u * w);
    dma16(lds0 + slot + 2048u * w + 1024u, vk, srdK, (unsigned)t * 16384u + 2048u * w + 128u);
  };
  auto dma_v = [&](int t, unsigned slot) {
    dma16(lds0 + slot + 1024u * w, vv, srdV, (unsigned)t * 16384u + 2048u * w);
    dma16(lds0 + slot + 8192u + 1024u * w, vv, srdV, (unsigned)t * 16384u + 2048u * w + 128u);
  };

  {
    asm volatile("s_waitcnt vmcnt(0)" : "+v"(xr[0]), "+v"(xr[1]), "+v"(xr[2]), "+v"(xr[3]), "+v"(xr[4]), "+v"(xr[5]), "+v"(xr[6]),
                 "+v"(xr[7]) :: "memory");
    dma16(ring + 8192u, l * 16u, srdQ, (2 * (pn * 8 + w)) * 8192u + g * 1024u);
    dma16(ring + 9216u, l * 16u, srdQ, (2 * (pn * 8 + w) + 1) * 8192u + g * 1024u);
    wring_issue(0, ring, vq, srdQ, sq0, 8192u);
    wring_issue(1, ring, vq, srdQ, sq0, 8192u);
    float v[8][8], s[8], m[8];
#pragma unroll
    for (int i = 0; i < 8; ++i) {
#pragma unroll
      for (int j = 0; j < 4; ++j) { v[i][2 * j] = bflo(xr[i][j]); v[i][2 * j + 1] = bfhi(xr[i][j]); }
      s[i] = ((v[i][0] + v[i][1]) + (v[i][2] + v[i][3])) + ((v[i][4] + v[i][5]) + (v[i][6] + v[i][7]));
    }
    wave_sum8(s, m);
#pragma unroll
    for (int i = 0; i < 8; ++i) {
      const float mu = m[i] * (1.0f / D);
      float q = 0.f;
#pragma unroll
      for (int j = 0; j < 8; ++j) { v[i][j] -= mu; q = __builtin_fmaf(v[i][j], v[i][j], q); }
      s[i] = q;
    }
    wave_sum8(s, m);
    float rs[8];
#pragma unroll
    for (int i = 0; i < 8; ++i) rs[i] = __builtin_amdgcn_rsqf(m[i] * (1.0f / D) + LN_EPS);
    STAMP(1, 8);
    {
      u32x4 mp[8];
      poll_tag<8>(mp, srdW, tid * 16u, W_MP, 4864u * 8u, tag);
      f32x2 t;
#pragma unroll
      for (int k = 0; k < 2; ++k)
        t[k] = ((U2F(mp[0][2 * k]) + U2F(mp[1][2 * k])) + (U2F(mp[2][2 * k]) + U2F(mp[3][2 * k]))) +
               ((U2F(mp[4][2 * k]) + U2F(mp[5][2 * k])) + (U2F(mp[6][2 * k]) + U2F(mp[7][2 * k])));
      *(LAS f32x2*)(lds + LDS_D + tid * 8) = t;
    }
    BAR_L();
    if (BIDX == 0 && tid == 0) __builtin_amdgcn_raw_buffer_store_b32(tag, srdW, W_EPOCH, 0, 16);
    u32x4 mo[4];
#pragma unroll
    for (int i = 0; i < 4; ++i) mo[i] = *(const LAS u32x4*)(lds + LDS_D + (i >> 1) * 2048 + l * 32 + (i & 1) * 16);
    STAMP(1, 9);
#pragma unroll
    for (int i = 0; i < 8; ++i) {
      float hh[8];
#pragma unroll
      for (int j = 0; j < 8; ++j) hh[j] = __builtin_fmaf(v[i][j] * rs[i], 1.f + U2F(mo[2 + (j >> 2)][j & 3]), U2F(mo[j >> 2][j & 3]));
      const u32x4 o = {cvtpk(hh[0], hh[1]), cvtpk(hh[2], hh[3]), cvtpk(hh[4], hh[5]), cvtpk(hh[6], hh[7])};
      *(LAS u32x4*)(lds + LDS_X + ximg(l, w * 8 + i)) = o;
      if (i == 1 || i == 3 || i == 5) wring_issue(2 + i / 2, ring, vq, srdQ, sq0, 8192u);
    }
  }
  BAR_L();
  dma_k(0, LDS_D);
  STAMP(1, 1);

  {
    f32x4 acc[4][4] = {};
    gemm_phase<2>(acc, lds, ringoff, ring, vq, srdQ, sq0, 8192u, l);
    STAMP(1, 2);
    WAIT_BAR(0);
    dma_k(1, KSLOT[1]); dma_v(0, VSLOT[0]); dma_k(2, KSLOT[2]); dma_v(1, VSLOT[1]);
    if (job2) {
      const bool isw3 = jcol2 >= 96u;
      const unsigned ldb = isw3 ? 3584u : 6144u;
      const __amdgpu_buffer_rsrc_t srdJ = isw3 ? mksrd(P.w3, 512u * 3584u) : mksrd(P.wmod, 512u * 6144u);
#pragma unroll
      for (int i = 0; i < 4; ++i)
        dma16(lds0 + LDS_R + 65536u + w * 4096u + i * 1024u, (l >> 2) * ldb + (l & 3) * 16u, srdJ, (jks2 * 64 + 16 * i) * ldb + (isw3 ? jcol2 - 96u : jcol2) * 64u);
      c_load(jks2);
    }
    const unsigned cl = 8 * w + 2 * ((l >> 4) & 1) + (l >> 5);
#pragma unroll
    for (int r = 0; r < 4; ++r)
#pragma unroll
      for (int mp = 0; mp < 2; ++mp)
        *(LAS u32x4*)(lds + LDS_X + ximg(cl + 4 * mp, 16 * r + (l & 15))) = pack16(acc[2 * mp][r], acc[2 * mp + 1][r], QSCALE);
    BAR_L();
  }

  STAMP(1, 3);
  const unsigned hh = w >> 1, rt = w & 1;
  bf16x8 qf[8];
#pragma unroll
  for (int s = 0; s < 8; ++s) qf[s] = lds_b128(lds + LDS_X + ximg(16 * hh + 2 * s + c.h, 32 * rt + c.r));
  unsigned kb[2], vb[2];
#pragma unroll
  for (int i = 0; i < 2; ++i) {
    kb[i] = 2048 * (c.r >> 3) + 64 * (c.r & 7) + (((2 * i + c.h) ^ ((c.r >> 2) & 3)) << 4);
    vb[i] = (4 * c.h + c.q) * 128 + 64 * (i ^ (c.q >> 1)) + 32 * c.blk + 8 * (2 * (c.p & 1) + (c.p >> 1));
  }
  const __amdgpu_buffer_rsrc_t srdO = mksrd(P.wo, 4096u * 512u * 2u);
  const unsigned vo = wdma_voff(l, 1024u), so0 = g * 512u * 1024u + w * 128u;

  f32x16 o[4] = {};
  f32x16 s0, s1;
  f32x16 negm;
  float lsum = 0.f;
  u32x4 pk[4];
  constexpr int PD = 6;
  bf16x8 fr[PD];
  const bool lag = w >= 4;
  auto frag = [&](int t, int it) -> bf16x8 {
    if (it < 16) {
      const lds_cptr a = lds + VSLOT[t > 0 ? t - 1 : 0] + vb[it & 1] + 8192 * ((it >> 1) & 1) + 2048 * (it >> 2);
      return cat8(lds_tr(a), lds_tr(a + 1024));
    }
    const int s = (it - 16) >> 1;
    return lds_b128(lds + KSLOT[t < 8 ? t : 0] + kb[s & 1] + 512 * (s >> 1) + 8192 * (it & 1));
  };
  auto seam = [&](int t, int tx) __attribute__((always_inline)) {
    if (t == 7) { BAR_L(); } else if (tx >= 1 && tx <= 5) { WAIT_BAR(4); } else if (tx == 6) { WAIT_BAR(2); } else { WAIT_BAR(0); }
  };
  auto refill = [&](int t) __attribute__((always_inline)) {
    if (t + 3 < 8) dma_k(t + 3, KSLOT[t + 3 < 8 ? t + 3 : 0]);
    if (t + 2 < 8) dma_v(t + 2, VSLOT[t + 2 < 8 ? t + 2 : 0]);
    if (t == 6) {
#pragma unroll
      for (int ks = 0; ks < RING; ++ks) wring_issue(ks, ring, vo, srdO, so0, 1024u);
    }
  };
  auto extra = [&](int e) __attribute__((always_inline)) {
    const unsigned dump = lds0 + LDS_R + w * 3072u + (unsigned)(e % 3) * 1024u;
    const unsigned id = (BIDX >> 3) * 8 + w, jh = BIDX & 1;
    const unsigned vrow = (l >> 5) * 1024u + (l & 31) * 16u + jh * 512u;
    auto w3v = [&](unsigned piece) { const unsigned byte = piece * 1024u + l * 16u, row = byte / 1792u;
                                     return row * 3584u + jh * 1792u + (byte - row * 1792u); };
    if (e < 4 || e >= 15) {
      const int i = e < 4 ? e : e - 11;
      const unsigned off = i < 3 ? S_U + (unsigned)i * (64u * T * 16u) + pn * 65536u + g * 8192u
                         : i < 7 ? S_HZ + pn * 262144u + g * 32768u + (unsigned)(i - 3) * 8192u
                                 : S_H3 + pn * 229376u + g * 28672u + (unsigned)(i - 7) * 8192u;
      const u32x4 sv = {SENT, SENT, SENT, SENT};
      __builtin_amdgcn_raw_buffer_store_b128(sv, srdW, (i == 10 && tid >= 256) ? OOB : tid * 16u + off, 0, 16);
    } else if (e < 6) dma16(dump, l * 16u, srdO, (g * 512u + (pn * 8 + w) * 2 + (e - 4)) * 1024u);
    else if (e == 6) dma16(dump, vrow, mksrd(P.w1, 512u * 1024u), id * 2048u);
    else if (e == 7) dma16(dump, vrow, mksrd(P.w2, 512u * 1024u), id * 2048u);
    else if (e < 11) dma16(dump, w3v(id + 256u * (e - 8)), mksrd(P.w3, 512u * 3584u), 0u);
    else if (e == 11) {
      if (id < 128) dma16(dump, w3v(id + 768u), mksrd(P.w3, 512u * 3584u), 0u);
      else dma16(dump, vrow, mksrd(P.w4, 1792u * 1024u), (id - 128u + 768u) * 2048u);
    } else dma16(dump, vrow, mksrd(P.w4, 1792u * 1024u), (id + 256u * (e - 12)) * 2048u);
  };
  auto step = [&](auto tc) __attribute__((always_inline)) {
    constexpr int t = decltype(tc)::value;
    constexpr int i0 = t == 0 ? 16 : 0, i1 = t == 8 ? 16 : 32;
    if (t <= 1) {
#pragma unroll
      for (int it = i0; it < i0 + PD; ++it) fr[it % PD] = frag(t, it);
    }
    if (t == 0) { s0 = (f32x16){}; s1 = (f32x16){}; } else if (t < 8) { s0 = negm; s1 = negm; }
    __builtin_amdgcn_s_setprio(1);
    SBAR();
#pragma unroll
    for (int it = i0; it < i1; ++it) {
      if (it < 16) o[it & 3] = MFMA32(fr[it % PD], __builtin_bit_cast(bf16x8, pk[it >> 2]), o[it & 3]);
      else if (it & 1) s1 = MFMA32(fr[it % PD], qf[(it - 16) >> 1], s1);
      else s0 = MFMA32(fr[it % PD], qf[(it - 16) >> 1], s0);
      if (it + PD < i1) fr[it % PD] = frag(t, it + PD);
      if (t >= 1 && it == 1) { if (!lag) refill(t - 1); }
      SBAR();
    }
    if constexpr (t == 8) return;
    __builtin_amdgcn_s_setprio(0);
    if (lag) { seam(t, t - 1); refill(t); }
    asm volatile("s_nop 15" : "+v"(s0), "+v"(s1));
    float ta = max3f(s0[0], s0[1], s1[0]), tb = max3f(s0[2], s0[3], s1[1]);
    ta = max3f(ta, s1[2], s1[3]);
#pragma unroll
    for (int i = 4; i < 16; i += 4) {
      ta = max3f(ta, s0[i], s0[i + 1]); tb = max3f(tb, s0[i + 2], s0[i + 3]);
      ta = max3f(ta, s1[i], s1[i + 1]); tb = max3f(tb, s1[i + 2], s1[i + 3]);
    }
    float tm = max2f(ta, tb);
    {
      auto r = __builtin_amdgcn_permlane32_swap(F2U(tm), F2U(tm), false, false);
      tm = max2f(U2F(r[0]), U2F(r[1]));
    }
    if (t == 0 || ANY(tm > THR)) {
      const float ndl = t == 0 ? -tm : -max2f(tm, 0.f);
      const float aone = c.h ? 0.f : 1.f;
      if (t == 0) negm = (f32x16){};
      __builtin_amdgcn_s_setprio(2);
      s0 = __builtin_amdgcn_mfma_f32_32x32x2f32(aone, ndl, s0, 0, 0, 0);
      s1 = __builtin_amdgcn_mfma_f32_32x32x2f32(aone, ndl, s1, 0, 0, 0);
      negm = __builtin_amdgcn_mfma_f32_32x32x2f32(aone, ndl, negm, 0, 0, 0);
      __builtin_amdgcn_s_setprio(0);
      if (t > 0) {
        const float al = __builtin_amdgcn_exp2f(ndl);
        lsum *= al;
#pragma unroll
        for (int dt = 0; dt < 4; ++dt)
#pragma unroll
          for (int i = 0; i < 16; ++i) o[dt][i] *= al;
      }
    }
    SBAR();
    if (t >= 1) {
#pragma unroll
      for (int it = 0; it < PD; ++it) fr[it] = frag(t + 1, it);
    }
#pragma unroll
    for (int i = 0; i < 16; ++i) {
      s0[i] = __builtin_amdgcn_exp2f(s0[i]); s1[i] = __builtin_amdgcn_exp2f(s1[i]);
      if (t >= 1 && (i & 3) == 1 && 4 * (t - 1) + (i >> 2) < 22) { PIN(s0[i]); extra(4 * (t - 1) + (i >> 2)); }
    }
    pk[0] = pack8(s0, 0, 1.f); pk[1] = pack8(s0, 1, 1.f); pk[2] = pack8(s1, 0, 1.f); pk[3] = pack8(s1, 1, 1.f);
    {
      float l0 = lsum, l1 = 0.f, l2 = 0.f, l3 = 0.f;
#pragma unroll
      for (int i = 0; i < 16; i += 2) { l0 += s0[i]; l1 += s0[i + 1]; l2 += s1[i]; l3 += s1[i + 1]; }
      lsum = (l0 + l1) + (l2 + l3);
    }
    PIN(pk[0]); PIN(pk[1]); PIN(pk[2]); PIN(pk[3]); PIN(lsum);
    if constexpr (t == 1) {
      if (job2) {
        asm volatile("" : "+v"(jc[0]), "+v"(jc[1]), "+v"(jc[2]), "+v"(jc[3]));
#pragma unroll
        for (int i = 0; i < 4; ++i) jw[i] = *(const LAS u32x4*)(lds + LDS_R + 65536 + w * 4096 + i * 1024 + l * 16);
        job_run(jcol2, jks2, tag);
      }
    }
    if constexpr (t == 4) {
      if (BIDX < 120 && w == 0) {
        u32x4 mp[2];
        poll_tag<2>(mp, srdW, ((l >> 3) * 4864u + (32u + BIDX) * 32u + (l & 7) * 2u) * 8u, W_MP, 128u, tag);
        const float a0 = sum_hi3(U2F(mp[0][0])), a1 = sum_hi3(U2F(mp[0][2])), b0 = sum_hi3(U2F(mp[1][0])), b1 = sum_hi3(U2F(mp[1][2]));
        const bool hi = (l >> 3) & 1;
        __builtin_amdgcn_raw_buffer_store_b128((u32x4){F2U(hi ? b0 : a0), tag, F2U(hi ? b1 : a1), tag}, srdW,
                                               l < 16 ? W_MOD + ((32u + BIDX) * 32u + l * 2u) * 8u : OOB, 0, 16);
      }
    }
    SBAR();
    if (!lag) seam(t, t);
  };
  step(IC<0>{}); step(IC<1>{}); step(IC<2>{}); step(IC<3>{}); step(IC<4>{}); step(IC<5>{}); step(IC<6>{}); step(IC<7>{}); step(IC<8>{});
  __builtin_amdgcn_s_setprio(0);
  STAMP(1, 4);
  {
    const float il = __builtin_amdgcn_rcpf(swap32_add(lsum));
#pragma unroll
    for (int dt = 0; dt < 4; ++dt)
#pragma unroll
      for (int s = 0; s < 2; ++s)
        *(LAS u32x4*)(lds + LDS_X + ximg(16 * hh + 4 * dt + 2 * s + c.h, 32 * rt + c.r)) = pack8(o[dt], s, il);
  }
  BAR_L();
  STAMP(1, 5);
  dma16_sc1(lds0 + LDS_D + w * 2048u, l * 16u, srdW, W_MOD + 8192u + w * 2048u);
  dma16_sc1(lds0 + LDS_D + w * 2048u + 1024u, l * 16u, srdW, W_MOD + 8192u + w * 2048u + 1024u);
  {
    unsigned l2 = TIDX & 63;
    asm volatile("" : "+v"(l2));
    f32x4 acc[4][4] = {};
    gemm_phase<2>(acc, lds, ringoff, ring, vo, srdO, so0, 1024u, l2);
    STAMP(1, 6);
    const unsigned dst = W_P1 + ((((pn * 8 + ((l2 >> 3) & 1)) * 8 + g) * 64 + 8 * w + 2 * ((l2 >> 4) & 1) + (l2 >> 5)) * 8 + (l2 & 7)) * 16u;
#pragma unroll
    for (int r = 0; r < 4; ++r)
#pragma unroll
      for (int mp = 0; mp < 2; ++mp)
        __builtin_amdgcn_raw_buffer_store_b128(pack16(acc[2 * mp][r], acc[2 * mp + 1][r], 1.f), srdW,
                                               dst + (4 * mp * 8 + r * 2 * 8 * 64 * 8) * 16u, 0, 16);
  }
  WAIT_BAR(0);
  if (tid == 0) __builtin_amdgcn_raw_buffer_store_b32(tag, srdW, W_FLAG + (pn * 8 + g) * 64u, 0, 16);
  STAMP(1, 7);
  return tag;
}

DI void ln8(float (&v)[8], LAS float* red  , unsigned tid) {
  float s = ((v[0] + v[1]) + (v[2] + v[3])) + ((v[4] + v[5]) + (v[6] + v[7]));
  float q = (__builtin_fmaf(v[0], v[0], v[1] * v[1]) + __builtin_fmaf(v[2], v[2], v[3] * v[3])) +
            (__builtin_fmaf(v[4], v[4], v[5] * v[5]) + __builtin_fmaf(v[6], v[6], v[7] * v[7]));
  s = sum_hi3(s); q = sum_hi3(q);
  if ((tid & 63) < 8) *(LAS f32x2*)(red + ((tid >> 6) * 8 + (tid & 7)) * 2) = (f32x2){s, q};
  BAR_L();
  f32x2 e[8];
#pragma unroll
  for (int i = 0; i < 8; ++i) e[i] = *(const LAS f32x2*)(red + (i * 8 + (tid & 7)) * 2);
  const float mu = (((e[0][0] + e[1][0]) + (e[2][0] + e[3][0])) + ((e[4][0] + e[5][0]) + (e[6][0] + e[7][0]))) * (1.0f / D);
  const float ms = (((e[0][1] + e[1][1]) + (e[2][1] + e[3][1])) + ((e[4][1] + e[5][1]) + (e[6][1] + e[7][1]))) * (1.0f / D);
  const float rs = __builtin_amdgcn_rsqf(__builtin_fmaxf(__builtin_fmaf(-mu, mu, ms), 0.f) + LN_EPS);
  const float nb = -mu * rs;
#pragma unroll
  for (int k = 0; k < 8; ++k) v[k] = __builtin_fmaf(v[k], rs, nb);
}
DI void red4_put(const f32x16& a, int nt, lds_ptr scr, unsigned rt, unsigned kq, unsigned l) {
#pragma unroll
  for (int g = 0; g < 2; ++g)
#pragma unroll
    for (int i = 0; i < 2; ++i) {
      const int o = 8 * g + 4 * i;
      *(LAS f32x4*)(scr + ((rt * 4 + 2 * nt + g) * 4 + kq) * 2048 + i * 1024 + l * 16) = (f32x4){a[o], a[o + 1], a[o + 2], a[o + 3]};
    }
}
DI void red4_get(float (&v)[8], lds_cptr scr, unsigned rt, unsigned kq, unsigned l) {
  f32x4 t[4][2];
#pragma unroll
  for (int s = 0; s < 4; ++s)
#pragma unroll
    for (int i = 0; i < 2; ++i) t[s][i] = *(const LAS f32x4*)(scr + ((rt * 4 + kq) * 4 + s) * 2048 + i * 1024 + l * 16);
#pragma unroll
  for (int i = 0; i < 2; ++i)
#pragma unroll
    for (int k = 0; k < 4; ++k) v[4 * i + k] = (t[0][i][k] + t[1][i][k]) + (t[2][i][k] + t[3][i][k]);
}
DI void tile_mm8(const bf16x8 (&af)[8][2], const u32x4 (&xb)[8], float (&v)[8], lds_ptr scr, unsigned rt, unsigned kq, unsigned l) {
  f32x16 a0 = {}, a1 = {};
#pragma unroll
  for (int i = 0; i < 8; ++i) a0 = MFMA32(af[i][0], __builtin_bit_cast(bf16x8, xb[i]), a0);
  SBAR();
#pragma unroll
  for (int i = 0; i < 4; ++i) a1 = MFMA32(af[i][1], __builtin_bit_cast(bf16x8, xb[i]), a1);
  SBAR();
  red4_put(a0, 0, scr, rt, kq, l);
#pragma unroll
  for (int i = 4; i < 8; ++i) a1 = MFMA32(af[i][1], __builtin_bit_cast(bf16x8, xb[i]), a1);
  SBAR();
  red4_put(a1, 1, scr, rt, kq, l);
  BAR_L();
  red4_get(v, scr, rt, kq, l);
}

struct Frag3 { bf16x8 a, b[2]; };
constexpr int RING3 = 12;
constexpr int LA = 0, LB = 65536, LC = 131072;
DI unsigned r3off(int s, unsigned rc, unsigned rb) { return s < 4 ? rc + s * 1024u : rb + (s - 4) * 1024u; }
template <int KS> DI void load_frags3(Frag3& f, lds_cptr lds, unsigned rc, unsigned rb, unsigned a3, const LaneC& c) {
  const lds_cptr sl = lds + r3off(KS % RING3, rc, rb) + a3;
  f.a = cat8(lds_tr(sl), lds_tr(sl + 256));
#pragma unroll
  for (int rt = 0; rt < 2; ++rt) f.b[rt] = lds_b128(lds + LA + c.xb[KS & 3] + 2048 * KS + 512 * rt);
}
constexpr int w4slot(int n) { return (8 + n) % RING3; }
template <int KS>
DI void g3_step(f32x16 (&acc)[2], Frag3& cur, Frag3& n1, Frag3& n2, lds_cptr lds, unsigned lds0, unsigned rc, unsigned rb,
                unsigned voff, __amdgpu_buffer_rsrc_t srd, unsigned soff0, unsigned a3, const LaneC& c,
                unsigned v4, __amdgpu_buffer_rsrc_t srd4, unsigned s40) {
  constexpr int NKS = 32;
  if constexpr (KS + 2 < NKS) {
    constexpr int last = (KS + RING3 < NKS ? KS + RING3 : NKS) - 1;
    wait_vm<last - (KS + 2) + (KS > NKS - RING3 ? KS - (NKS - RING3) : 0)>();
    load_frags3<KS + 2>(n2, lds, rc, rb, a3, c);
  }
  SBAR();
  acc[0] = MFMA32(cur.a, cur.b[0], acc[0]);
  acc[1] = MFMA32(cur.a, cur.b[1], acc[1]);
  asm volatile("" :: "v"(cur.a));
  if constexpr (KS + RING3 < NKS) dma16(lds0 + r3off(KS % RING3, rc, rb), voff, srd, soff0 + (unsigned)(16 * (KS + RING3)) * 3584u);
  else dma16(lds0 + r3off(KS % RING3, rc, rb), v4, srd4, s40 + (unsigned)(KS - (NKS - RING3)) * 16384u);
  SBAR();
  if constexpr (KS + 1 < NKS) g3_step<KS + 1>(acc, n1, n2, cur, lds, lds0, rc, rb, voff, srd, soff0, a3, c, v4, srd4, s40);
}

DI void mlp_part(const Params& P, lds_ptr lds, unsigned lds0, unsigned w, __amdgpu_buffer_rsrc_t srdS, unsigned tag) {
  LaneC c; lane_consts(c);
  const unsigned idx = BIDX >> 3, pn = 8 * ((BIDX & 7) >> 1) + (idx >> 2), j = 4 * (BIDX & 1) + (idx & 3);
  const unsigned tid = TIDX, l = c.lane;
  const unsigned vw = wdma_voff(l, 1024u);
  const unsigned rt = w & 1, kq = w >> 1;
  constexpr int LMOD = LDS_D;
  constexpr int LRED = LC + 12288;
  constexpr int LST = LB + 7 * 8192;
  const unsigned prow = 32 * rt + c.r;
  const unsigned trow = pn * 64 + prow;
  const unsigned och = 8 * j + 2 * kq + c.h;
  const unsigned ovo = ((pn * 64 + och) * 64 + prow) * 16u;
  float gb[8];
  STAMP(2, 0);

  {
    const unsigned row = pn * 64 + j * 8 + (tid & 7), ch = tid >> 3;
    u32x4 xr;
    gld16(xr, P.x + (size_t)row * D + ch * 8);
    auto wait_flags = [&](unsigned fo) {
      for (unsigned spin = 0;; ++spin) {
        asm volatile("" ::: "memory");
        const unsigned f = __builtin_amdgcn_raw_buffer_load_b32(srdS, fo, 0, 16);
        if (!ANY(fo != OOB && f != tag) || spin > SPIN_CAP) break;
        __builtin_amdgcn_s_sleep(FLAG_SLEEP);
      }
    };
    const __amdgpu_buffer_rsrc_t srd1 = mksrd(P.w1, 512u * 512u * 2u), srd2 = mksrd(P.w2, 512u * 512u * 2u);
#pragma unroll
    for (int i = 0; i < 8; ++i) dma16(lds0 + LA + (8 * w + i) * 1024u, vw, srd1, (8 * w + i) * 8192u + j * 128u);
    {
      const int fp = (int)pn - 1 + (int)(l >> 3);
      if (w == 0) wait_flags((l < 24 && fp >= 0 && fp < 32) ? W_FLAG + ((unsigned)fp * 8 + (l & 7)) * 64u : OOB);
      BAR_L();
    }
    STAMP(2, 15);
    u32x4 p[8];
#pragma unroll
    for (int g = 0; g < 8; ++g) bld16_sc1(p[g], tid * 16u, srdS, W_P1 + ((pn * 8 + j) * 8 + g) * 8192u);
    float ga[8], sb[8], cb[8], g2[8];
    {
      u32x4 mv[20];
#pragma unroll
      for (int i = 0; i < 16; ++i) mv[i] = *(const LAS u32x4*)(lds + LMOD + (i >> 2) * 4096 + ch * 64 + 16 * (i & 3));
#pragma unroll
      for (int i = 0; i < 4; ++i) mv[16 + i] = *(const LAS u32x4*)(lds + LMOD + 12288 + och * 64 + 16 * i);
      if (ANY(bad_tag(mv, 20, tag))) {
#pragma unroll
        for (int i = 0; i < 4; ++i) poll_tag<4>(mv + 4 * i, srdS, ch * 64u, W_MOD + 8192u + i * 4096u, 16u, tag);
        poll_tag<4>(mv + 16, srdS, och * 64u, W_MOD + 20480u, 16u, tag);
      }
#pragma unroll
      for (int k = 0; k < 8; ++k) {
        ga[k] = U2F(mv[k >> 1][2 * (k & 1)]); sb[k] = U2F(mv[4 + (k >> 1)][2 * (k & 1)]); cb[k] = 1.f + U2F(mv[8 + (k >> 1)][2 * (k & 1)]);
        g2[k] = U2F(mv[12 + (k >> 1)][2 * (k & 1)]); gb[k] = U2F(mv[16 + (k >> 1)][2 * (k & 1)]);
      }
    }
    asm volatile("s_waitcnt vmcnt(0)" : "+v"(xr), "+v"(p[0]), "+v"(p[1]), "+v"(p[2]), "+v"(p[3]), "+v"(p[4]), "+v"(p[5]),
                 "+v"(p[6]), "+v"(p[7]) :: "memory");
    STAMP(2, 1);
    float v[8], x2[8];
#pragma unroll
    for (int i = 0; i < 4; ++i) {
      const float lo = ((bflo(p[0][i]) + bflo(p[1][i])) + (bflo(p[2][i]) + bflo(p[3][i]))) + ((bflo(p[4][i]) + bflo(p[5][i])) + (bflo(p[6][i]) + bflo(p[7][i])));
      const float hi = ((bfhi(p[0][i]) + bfhi(p[1][i])) + (bfhi(p[2][i]) + bfhi(p[3][i]))) + ((bfhi(p[4][i]) + bfhi(p[5][i])) + (bfhi(p[6][i]) + bfhi(p[7][i])));
      v[2 * i] = x2[2 * i] = __builtin_fmaf(ga[2 * i], lo, bflo(xr[i]));
      v[2 * i + 1] = x2[2 * i + 1] = __builtin_fmaf(ga[2 * i + 1], hi, bfhi(xr[i]));
    }
    ln8(v, (LAS float*)(lds + LRED), tid);
    float hb[8];
#pragma unroll
    for (int k = 0; k < 8; ++k) v[k] = hb[k] = __builtin_fmaf(v[k], cb[k], sb[k]);
    ln8(v, (LAS float*)(lds + LRED + 512), tid);
    const unsigned vo = ((pn * 64 + ch) * 64 + j * 8 + (tid & 7)) * 16u, vz = ((pn * 64 + ch) * 256 + j * 8 + (tid & 7)) * 16u;
    pub16(srdS, vo, S_U, (u32x4){cvtpk(v[0], v[1]), cvtpk(v[2], v[3]), cvtpk(v[4], v[5]), cvtpk(v[6], v[7])});
#pragma unroll
    for (int i = 0; i < 2; ++i) {
      pub16(srdS, vz, S_HZ + i * 1024u, (u32x4){F2U(hb[4 * i]), F2U(hb[4 * i + 1]),
                                                F2U(hb[4 * i + 2]), F2U(hb[4 * i + 3])});
      f32x4 z;
#pragma unroll
      for (int e = 0; e < 4; ++e) z[e] = __builtin_fmaf(g2[4 * i + e], hb[4 * i + e], x2[4 * i + e]);
      pub16(srdS, vz, S_HZ + (2 + i) * 1024u, __builtin_bit_cast(u32x4, z));
    }
#pragma unroll
    for (int i = 0; i < 8; ++i) dma16(lds0 + LB + (8 * w + i) * 1024u, vw, srd2, (8 * w + i) * 8192u + j * 128u);
  }
  STAMP(2, 2);
  const __amdgpu_buffer_rsrc_t srd3 = mksrd(P.w3, 512u * F * 2u);
  const unsigned rc = LC + w * 4096u, rb = LB + w * 8192u;
  const unsigned v3 = (l >> 2) * 3584u + ((l & 3) << 4), s30 = (j * 224u + w * 32u) * 2u;
  float v[8];
  {
    bf16x8 af[8][2];
#pragma unroll
    for (int i = 0; i < 8; ++i)
#pragma unroll
      for (int nt = 0; nt < 2; ++nt) {
        const lds_cptr a = lds + LA + (8 * kq + i) * 2048 + c.afr[nt];
        af[i][nt] = cat8(lds_tr(a), lds_tr(a + 512));
      }
    BAR_L();
    u32x4 xb[8];
    poll_ind<8, 3, 7>(xb, srdS, ((pn * 64 + 16 * kq + c.h) * 64 + prow) * 16u, S_U, 2048u);
    STAMP(2, 3);
    tile_mm8(af, xb, v, lds + LA, rt, kq, l);
#pragma unroll
    for (int k = 0; k < 8; ++k) v[k] = gelu_tanh(v[k]);
    pub16(srdS, ovo, S_H1, (u32x4){cvtpk(v[0], v[1]), cvtpk(v[2], v[3]), cvtpk(v[4], v[5]), cvtpk(v[6], v[7])});
    if (w < 7) {
#pragma unroll
      for (int ks = 0; ks < 4; ++ks) dma16(lds0 + r3off(ks, rc, rb), v3, srd3, s30 + (unsigned)(16 * ks) * 3584u);
    }
  }
  STAMP(2, 4);
  float y[8];
  float cs[16];
  const unsigned cso = w < 7 ? (3072 + 224 * j + 32 * w + 8 * c.h) * 8u : OOB;
  {
    bf16x8 af[8][2];
#pragma unroll
    for (int i = 0; i < 8; ++i)
#pragma unroll
      for (int nt = 0; nt < 2; ++nt) {
        const lds_cptr a = lds + LB + (8 * kq + i) * 2048 + c.afr[nt];
        af[i][nt] = cat8(lds_tr(a), lds_tr(a + 512));
      }
    BAR_L();
    const int sh = kq == 0 ? -1 : kq == 1 ? 1 : kq == 2 ? -2 : 2;
    const int srow = (int)trow + sh;
    const unsigned xvo = (srow >= 0 && srow < T) ? ((((unsigned)srow >> 6) * 64 + 16 * kq + c.h) * 64 + ((unsigned)srow & 63)) * 16u : OOB;
    u32x4 xb[8];
    poll_ind<8, 3, 7>(xb, srdS, xvo, S_H1, 2048u);
    STAMP(2, 5);
    u32x4 hz[4];
    const unsigned hzo = ((pn * 64 + och) * 256 + prow) * 16u;
#pragma unroll
    for (int i = 0; i < 4; ++i) hz[i] = __builtin_amdgcn_raw_buffer_load_b128(srdS, hzo, S_HZ + i * 1024u, 16);
    u32x4 cg[8];
#pragma unroll
    for (int i = 0; i < 8; ++i) cg[i] = __builtin_amdgcn_raw_buffer_load_b128(srdS, cso, W_MOD + 128u * (i >> 2) + 16u * (i & 3), 16);
    tile_mm8(af, xb, v, lds + LA, rt, kq, l);
    if (has_sent(hz, 4)) poll_load<4>(hz, srdS, hzo, S_HZ, 1024u);
    float x1[8];
#pragma unroll
    for (int k = 0; k < 8; ++k) {
      x1[k] = U2F(hz[k >> 2][k & 3]) + v[k];
      y[k] = __builtin_fmaf(gb[k], v[k], U2F(hz[2 + (k >> 2)][k & 3]));
    }
    pub16(srdS, ovo, S_X1, (u32x4){cvtpk(x1[0], x1[1]), cvtpk(x1[2], x1[3]), cvtpk(x1[4], x1[5]), cvtpk(x1[6], x1[7])});
    if (ANY(cso < OOB && bad_tag(cg, 8, tag))) {
      poll_tag<4>(cg, srdS, cso, W_MOD, 16u, tag);
      poll_tag<4>(cg + 4, srdS, cso, W_MOD + 128u, 16u, tag);
    }
#pragma unroll
    for (int i = 0; i < 16; ++i) cs[i] = U2F(cg[i >> 1][2 * (i & 1)]);
    if (w < 7) {
#pragma unroll
      for (int ks = 4; ks < RING3; ++ks) dma16(lds0 + r3off(ks, rc, rb), v3, srd3, s30 + (unsigned)(16 * ks) * 3584u);
    }
  }
  STAMP(2, 6);
  const __amdgpu_buffer_rsrc_t srd4 = mksrd(P.w4, (unsigned)F * 512u * 2u);
  const unsigned s40 = (448u * kq + 8u * rt) * 1024u + j * 128u;
  {
    u32x4 xr[8];
    BAR_L();
    poll_ind<8, 3, 7>(xr, srdS, ((pn * 64 + 8 * w) * 64 + l) * 16u, S_X1, 1024u);
    STAMP(2, 7);
#pragma unroll
    for (int i = 0; i < 8; ++i) *(LAS u32x4*)(lds + LA + ximg(8 * w + i, l)) = xr[i];
    {
      float s0 = 0.f, s1 = 0.f, q0 = 0.f, q1 = 0.f;
#pragma unroll
      for (int i = 0; i < 8; ++i)
#pragma unroll
        for (int k = 0; k < 4; k += 2) {
          s0 = dot2bf(xr[i][k], 0x3F803F80u, s0); s1 = dot2bf(xr[i][k + 1], 0x3F803F80u, s1);
          q0 = dot2bf(xr[i][k], xr[i][k], q0); q1 = dot2bf(xr[i][k + 1], xr[i][k + 1], q1);
        }
      *(LAS f32x2*)(lds + LST + (w * 64 + l) * 8) = (f32x2){s0 + s1, q0 + q1};
    }
    BAR_L();
    STAMP(2, 8);
    if (w < 7) {
      f32x16 acc[2] = {};
      const unsigned a3 = (8 * c.h + c.q) * 64 + 32 * c.blk + 8 * (2 * (c.p & 1) + (c.p >> 1));
      Frag3 f0, f1, f2;
      wait_vm<RING3 - 2>();
      load_frags3<0>(f0, lds, rc, rb, a3, c);
      load_frags3<1>(f1, lds, rc, rb, a3, c);
      float mu[2], rs[2];
#pragma unroll
      for (int r2 = 0; r2 < 2; ++r2) {
        float s = 0.f, q = 0.f;
#pragma unroll
        for (int i = 0; i < 8; ++i) {
          const f32x2 t = *(const LAS f32x2*)(lds + LST + (i * 64 + 32 * r2 + c.r) * 8);
          s += t[0]; q += t[1];
        }
        mu[r2] = s * (1.0f / D);
        rs[r2] = __builtin_amdgcn_rsqf(__builtin_fmaxf(__builtin_fmaf(-mu[r2], mu[r2], q * (1.0f / D)), 0.f) + LN_EPS);
      }
      if (w < 4) __builtin_amdgcn_s_setprio(2); else __builtin_amdgcn_s_setprio(1);
      g3_step<0>(acc, f0, f1, f2, lds, lds0, rc, rb, v3, srd3, s30, a3, c, vw, srd4, s40);
      __builtin_amdgcn_s_setprio(0);
      STAMP(2, 9);
#pragma unroll
      for (int r2 = 0; r2 < 2; ++r2) {
#pragma unroll
        for (int i = 0; i < 16; ++i) acc[r2][i] = gelu_tanh(rs[r2] * __builtin_fmaf(-mu[r2], cs[i], acc[r2][i]));
#pragma unroll
        for (int s2 = 0; s2 < 2; ++s2)
          pub16(srdS, ((pn * 224 + 28 * j + 4 * w + 2 * s2 + c.h) * 64 + 32 * r2 + c.r) * 16u, S_H3, pack8(acc[r2], s2, 1.f));
      }
    } else {
#pragma unroll
      for (int n = 0; n < 8; ++n) dma16(lds0 + r3off(w4slot(n), rc, rb), vw, srd4, s40 + (unsigned)n * 16384u);
    }
    BAR_L();
  }
  STAMP(2, 10);
  {
    auto poff = [&](int ks, unsigned wv) -> unsigned {
      return (ks >= 12 && ks < 20) ? LA + wv * 8192u + (unsigned)(ks - 12) * 1024u : r3off(w4slot(ks % 20), LC + wv * 4096u, LB + wv * 8192u);
    };
    auto w4dma = [&](int ks) { dma16(lds0 + poff(ks, w), vw, srd4, s40 + (unsigned)ks * 16384u); };
    if (w == 7) {
#pragma unroll
      for (int ks = 8; ks < 12; ++ks) w4dma(ks);
    }
#pragma unroll
    for (int ks = 12; ks < 20; ++ks) w4dma(ks);
    const unsigned wsrc = 2 * kq + c.h;
    const unsigned hvo = ((pn * 224 + 56 * kq + c.h) * 64 + prow) * 16u;
    u32x4 ha[14], hb[14];
    poll_ind<14, 9, 13>(ha, srdS, hvo, S_H3, 2048u);
    BAR_L();
    STAMP(2, 11);
    f32x16 acc[2] = {};
    bf16x8 fa[2][2];
    auto ld = [&](int ks) {
#pragma unroll
      for (int nt = 0; nt < 2; ++nt) {
        const lds_cptr a = lds + poff(ks, wsrc) + (c.afr[nt] - 1024u * c.h);
        fa[ks & 1][nt] = cat8(lds_tr(a), lds_tr(a + 512));
      }
    };
    auto mm = [&](int ks, const u32x4& b) {
#pragma unroll
      for (int nt = 0; nt < 2; ++nt) acc[nt] = MFMA32(fa[ks & 1][nt], __builtin_bit_cast(bf16x8, b), acc[nt]);
    };
    auto hbl = [&](int i) { bld16_sc1(hb[i], hvo, srdS, S_H3 + (14u + i) * 2048u); };
    ld(0);
#pragma unroll
    for (int ks = 0; ks < 8; ++ks) {
      if (ks < 7) ld(ks + 1);
      SBAR(); mm(ks, ha[ks]);
      if (ks < 7) { hbl(2 * ks); hbl(2 * ks + 1); }
      SBAR();
    }
    BAR_L();
    ld(8);
#pragma unroll
    for (int ks = 8; ks < 14; ++ks) {
      if (ks < 13) ld(ks + 1);
      SBAR(); mm(ks, ha[ks]);
      if (ks < 12) { w4dma(20 + 2 * (ks - 8)); w4dma(21 + 2 * (ks - 8)); }
      SBAR();
    }
    asm volatile("s_waitcnt vmcnt(8) lgkmcnt(0)\n\ts_barrier" : "+v"(hb[0]), "+v"(hb[1]), "+v"(hb[2]), "+v"(hb[3]), "+v"(hb[4]), "+v"(hb[5]),
                 "+v"(hb[6]), "+v"(hb[7]), "+v"(hb[8]), "+v"(hb[9]), "+v"(hb[10]), "+v"(hb[11]), "+v"(hb[12]), "+v"(hb[13]) :: "memory");
    if (has_sent(hb, 14)) poll_load<14>(hb, srdS, hvo, S_H3 + 14u * 2048u, 2048u);
    STAMP(2, 12);
    ld(14);
#pragma unroll
    for (int ks = 14; ks < 19; ++ks) { ld(ks + 1); SBAR(); mm(ks, hb[ks - 14]); SBAR(); }
    mm(19, hb[5]);
    WAIT_BAR(0);
    ld(20);
#pragma unroll
    for (int ks = 20; ks < 27; ++ks) { ld(ks + 1); SBAR(); mm(ks, hb[ks - 14]); SBAR(); }
    mm(27, hb[13]);
    BAR_L();
    STAMP(2, 13);
    red4_put(acc[0], 0, lds + LA, rt, kq, l);
    red4_put(acc[1], 1, lds + LA, rt, kq, l);
    BAR_L();
    red4_get(v, lds + LA, rt, kq, l);
#pragma unroll
    for (int k = 0; k < 8; ++k) v[k] = __builtin_fmaf(gb[k], v[k], y[k]);
    P.out[(size_t)trow * 64 + och] = (u32x4){cvtpk(v[0], v[1]), cvtpk(v[2], v[3]), cvtpk(v[4], v[5]), cvtpk(v[6], v[7])};
  }
  STAMP(2, 14);
}

__global__ __launch_bounds__(512, 2) void k_fused(Params P) {
  extern __shared__ __attribute__((aligned(16))) char smem[];
  const lds_ptr lds = (lds_ptr)smem;
  const unsigned lds0 = (unsigned)(uintptr_t)smem;
  const unsigned w = rfl(TIDX >> 6);
  const __amdgpu_buffer_rsrc_t srdW = mksrd(P.ws, W_END);
  {
    unsigned long long pc; unsigned len;
    asm volatile("s_getpc_b64 %0\n.Lkf_here:\n\ts_mov_b32 %1, .Lkf_end-.Lkf_here" : "=s"(pc), "=s"(len));
    dma16(lds0 + LDS_R + w * RINGB + 8192u, (TIDX & 63) * 16u, mksrd((const void*)(pc & ~15ull), len & ~15u), ((BIDX >> 3) * 8 + w) * 1024u);
  }
  const unsigned tag = attn_part(P, lds, lds0, w, srdW);
  mlp_part(P, lds, lds0, w, srdW, tag);
  asm volatile(".Lkf_end:");
}

#if defined(__HIP_DEVICE_COMPILE__)
#pragma clang attribute pop
#endif
extern "C" void kernel_launch(void* const* d_in, const int* in_sizes, int n_in, void* d_out, int out_size, void* d_ws,
                              size_t ws_size, hipStream_t stream) {
  Params P;
  P.x = (const unsigned short*)d_in[0];
  P.kc = (const unsigned short*)d_in[1];
  P.vc = (const unsigned short*)d_in[2];
  P.wq = (const unsigned short*)d_in[3];
  P.wo = (const unsigned short*)d_in[4];
  P.w1 = (const unsigned short*)d_in[6];
  P.w2 = (const unsigned short*)d_in[7];
  P.w3 = (const unsigned short*)d_in[8];
  P.w4 = (const unsigned short*)d_in[9];
  P.c = (const unsigned short*)d_in[10];
  P.wmod = (const unsigned short*)d_in[11];
  P.ws = d_ws;
  P.out = (u32x4*)d_out;
  (void)hipFuncSetAttribute((const void*)k_fused, hipFuncAttributeMaxDynamicSharedMemorySize, LDS_TOTAL);
  k_fused<<<256, 512, LDS_TOTAL, stream>>>(P);
}
```

```cpp
#include <hip/hip_runtime.h>
#include <hip/hip_bf16.h>
#include <stdint.h>

#if defined(__HIP_DEVICE_COMPILE__)
#pragma clang attribute push(__attribute__((target("no-packed-fp32-ops"))), apply_to = function)
#endif
typedef short bf16x8 __attribute__((ext_vector_type(8)));
typedef short s16x4 __attribute__((ext_vector_type(4)));
typedef float f32x16 __attribute__((ext_vector_type(16)));
typedef float f32x4 __attribute__((ext_vector_type(4)));
typedef float f32x2 __attribute__((ext_vector_type(2)));
typedef unsigned u32x4 __attribute__((ext_vector_type(4)));
typedef unsigned u32x2 __attribute__((ext_vector_type(2)));
typedef __bf16 bf16x2_t __attribute__((ext_vector_type(2)));
typedef short v4i16_t __attribute__((ext_vector_type(4)));
#define LAS __attribute__((address_space(3)))
typedef LAS const char* lds_cptr;
typedef LAS char* lds_ptr;
#define DI __device__ __forceinline__
#define F2U(x) __builtin_bit_cast(unsigned, (float)(x))
#define U2F(x) __builtin_bit_cast(float, (unsigned)(x))
#define F2I(x) __builtin_bit_cast(int, (float)(x))
#define I2F(x) __builtin_bit_cast(float, (int)(x))
#define TIDX __builtin_amdgcn_workitem_id_x()
#define BIDX __builtin_amdgcn_workgroup_id_x()
#define ANY(p) (__builtin_amdgcn_ballot_w64(p) != 0ull)
#define SBAR() __builtin_amdgcn_sched_barrier(0)
#define MFMA32(a, b, c) __builtin_amdgcn_mfma_f32_32x32x16_bf16((a), (b), (c), 0, 0, 0)
#define WAIT_V(n) asm volatile("s_waitcnt vmcnt(" #n ")" ::: "memory")
#define WAIT_L0() asm volatile("s_waitcnt lgkmcnt(0)" ::: "memory")
#define BAR_L() asm volatile("s_waitcnt lgkmcnt(0)\n\ts_barrier" ::: "memory")
#define WAIT_BAR(n) asm volatile("s_waitcnt vmcnt(" #n ") lgkmcnt(0)\n\ts_barrier" ::: "memory")

constexpr int T = 2048, D = 512, HKV = 8, SK = 2048, DH = 128, WIN = 512, F = 1792;
constexpr float LN_EPS = 1e-5f;
constexpr float QSCALE = 0.088388347648318440f * 1.4426950408889634f;

constexpr unsigned W_EPOCH = 0;
constexpr unsigned W_FLAG = 4096;
constexpr unsigned W_MOD = 32768;
constexpr unsigned W_MP = W_MOD + 40960;
constexpr unsigned W_P1 = 393216;
constexpr unsigned S_U = W_P1 + 8u * 64u * T * 16u;
constexpr unsigned S_H1 = S_U + 64u * T * 16u;
constexpr unsigned S_X1 = S_H1 + 64u * T * 16u;
constexpr unsigned S_HZ = S_X1 + 64u * T * 16u;
constexpr unsigned S_H3 = S_HZ + 256u * T * 16u;
constexpr unsigned W_END = S_H3 + 224u * T * 16u;
static_assert(W_MP + 8u * 4864u * 8u <= W_P1, "ws");
constexpr unsigned OOB = 0xF0000000u;
#define K1MAP 0
#define FLAG_SLEEP 4
#define GSPLIT 2
#define ATHR 32.0f
constexpr float THR = ATHR;
constexpr unsigned SENT = 0xFFFFFFFFu;
constexpr unsigned SPIN_CAP = 1u << 16;

constexpr int LDS_X = 0;
constexpr int LDS_R = 65536;
constexpr int RING = 5, RINGB = RING * 2048;
constexpr int LDS_D = LDS_R + 8 * RINGB;
constexpr int LDS_TOTAL = LDS_D + 16384;
static_assert(LDS_TOTAL == 163840, "lds");

#define STAMP(kid, i) do {} while (0)
DI unsigned cvtpk(float lo, float hi) {
  f32x2 v = {lo, hi};
  bf16x2_t b = __builtin_convertvector(v, bf16x2_t);
  return __builtin_bit_cast(unsigned, b);
}
DI float bflo(unsigned u) { return U2F(u << 16); }
DI float bfhi(unsigned u) { return U2F(u & 0xffff0000u); }
DI unsigned rfl(unsigned x) { return __builtin_amdgcn_readfirstlane(x); }
DI __amdgpu_buffer_rsrc_t mksrd(const void* p, unsigned bytes) {
  return __builtin_amdgcn_make_buffer_rsrc((void*)p, (short)0, (int)bytes, 0x00020000);
}
DI void dma16(unsigned lds, unsigned voff, __amdgpu_buffer_rsrc_t srd, unsigned soff) {
  asm volatile("s_mov_b32 m0, %0\n\ts_nop 0\n\tbuffer_load_dwordx4 %1, %2, %3 offen lds"
               :: "s"(lds), "v"(voff), "s"(srd), "s"(soff) : "memory");
}
DI void gld16(u32x4& d, const void* p) { asm volatile("global_load_dwordx4 %0, %1, off" : "=v"(d) : "v"(p) : "memory"); }
DI void bld16_sc1(u32x4& d, unsigned voff, __amdgpu_buffer_rsrc_t srd, unsigned soff) {
  asm volatile("buffer_load_dwordx4 %0, %1, %2, %3 offen sc1" : "=v"(d) : "v"(voff), "s"(srd), "s"(soff) : "memory");
}
DI void dma16_sc1(unsigned lds, unsigned voff, __amdgpu_buffer_rsrc_t srd, unsigned soff) {
  asm volatile("s_mov_b32 m0, %0\n\ts_nop 0\n\tbuffer_load_dwordx4 %1, %2, %3 offen sc1 lds"
               :: "s"(lds), "v"(voff), "s"(srd), "s"(soff) : "memory");
}
DI bf16x8 lds_b128(lds_cptr p) { return *(const LAS bf16x8*)p; }
DI s16x4 lds_tr(lds_cptr p) {
  return __builtin_bit_cast(s16x4, __builtin_amdgcn_ds_read_tr16_b64_v4i16((LAS v4i16_t*)p));
}
DI bf16x8 cat8(s16x4 lo, s16x4 hi) { return __builtin_shufflevector(lo, hi, 0, 1, 2, 3, 4, 5, 6, 7); }
DI float swap32_add(float x) {
  auto r = __builtin_amdgcn_permlane32_swap(F2U(x), F2U(x), false, false);
  return U2F(r[0]) + U2F(r[1]);
}
DI float swap32_max(float x) {
  auto r = __builtin_amdgcn_permlane32_swap(F2U(x), F2U(x), false, false);
  return __builtin_fmaxf(U2F(r[0]), U2F(r[1]));
}
template <int CTRL> DI float dpp_add(float x) {
  int y = __builtin_amdgcn_update_dpp(0, F2I(x), CTRL, 0xF, 0xF, true);
  return x + I2F(y);
}
DI float max3f(float a, float b, float c) { float r; asm("v_max3_f32 %0, %1, %2, %3" : "=v"(r) : "v"(a), "v"(b), "v"(c)); return r; }
DI float max2f(float a, float b) { float r; asm("v_max_f32_e32 %0, %1, %2" : "=v"(r) : "v"(a), "v"(b)); return r; }
DI float dot2bf(unsigned a, unsigned b, float c) {
  return __builtin_amdgcn_fdot2_f32_bf16(__builtin_bit_cast(bf16x2_t, a), __builtin_bit_cast(bf16x2_t, b), c, false);
}
#define PIN(x) asm volatile("" : "+v"(x))
DI float swap16_add(float x) {
  auto r = __builtin_amdgcn_permlane16_swap(F2U(x), F2U(x), false, false);
  return U2F(r[0]) + U2F(r[1]);
}
DI float sum_hi3(float x) { return swap32_add(swap16_add(dpp_add<0x128>(x))); }
DI float gelu_tanh(float x) {
  const float k0 = -2.0f * 0.7978845608028654f * 1.4426950408889634f;
  const float t = x * x;
  const float z = x * __builtin_fmaf(t, 0.044715f * k0, k0);
  const float e = __builtin_amdgcn_exp2f(z);
  return x * __builtin_amdgcn_rcpf(1.0f + e);
}
DI void wave_sum8(const float (&a)[8], float (&out)[8]) {
  float b[4], c[2];
#pragma unroll
  for (int i = 0; i < 4; ++i) {
    auto r = __builtin_amdgcn_permlane32_swap(F2U(a[i]), F2U(a[i + 4]), false, false);
    b[i] = U2F(r[0]) + U2F(r[1]);
  }
#pragma unroll
  for (int i = 0; i < 2; ++i) {
    auto r = __builtin_amdgcn_permlane16_swap(F2U(b[i]), F2U(b[i + 2]), false, false);
    c[i] = U2F(r[0]) + U2F(r[1]);
  }
#pragma unroll
  for (int i = 0; i < 2; ++i) {
    c[i] = dpp_add<0xB1>(c[i]);
    c[i] = dpp_add<0x4E>(c[i]);
    c[i] = dpp_add<0x141>(c[i]);
    c[i] = dpp_add<0x140>(c[i]);
  }
#pragma unroll
  for (int q = 0; q < 4; ++q)
#pragma unroll
    for (int i = 0; i < 2; ++i)
      out[i + 2 * (q & 1) + 4 * (q >> 1)] = I2F(__builtin_amdgcn_readlane(F2I(c[i]), 16 * q));
}

DI unsigned nosent(unsigned v) { return v < 0xFFFFFFFEu ? v : 0xFFFFFFFEu; }
DI void pub16(__amdgpu_buffer_rsrc_t srd, unsigned voff, unsigned soff, u32x4 v) {
  v = (u32x4){nosent(v[0]), nosent(v[1]), nosent(v[2]), nosent(v[3])};
  __builtin_amdgcn_raw_buffer_store_b128(v, srd, voff + soff, 0, 16);
}
DI unsigned umax3(unsigned a, unsigned b, unsigned c) { const unsigned m = a > b ? a : b; return m > c ? m : c; }
DI bool has_sent(const u32x4* v, int n) {
  unsigned m = 0;
#pragma unroll
  for (int i = 0; i < n; ++i) { m = umax3(m, v[i][0], v[i][1]); m = umax3(m, v[i][2], v[i][3]); }
  return ANY(m == SENT);
}
template <int N> DI void poll_load(u32x4* v, __amdgpu_buffer_rsrc_t srd, unsigned voff, unsigned soff0, unsigned step) {
  for (unsigned spin = 0;; ++spin) {
    asm volatile("" ::: "memory");
#pragma unroll
    for (int i = 0; i < N; ++i) v[i] = __builtin_amdgcn_raw_buffer_load_b128(srd, voff, soff0 + (unsigned)i * step, 16);
    if (!has_sent(v, N) || spin > SPIN_CAP) break;
    __builtin_amdgcn_s_sleep(1);
  }
}
template <int N, int I0, int I1> DI void poll_ind(u32x4* v, __amdgpu_buffer_rsrc_t srd, unsigned voff, unsigned soff0, unsigned step) {
#pragma unroll
  for (int i = 0; i < N; ++i) v[i] = __builtin_amdgcn_raw_buffer_load_b128(srd, voff, soff0 + (unsigned)i * step, 16);
  if (has_sent(v, N)) {
    u32x4 ind[2];
    poll_load<2>(ind, srd, voff, soff0 + (unsigned)I0 * step, (unsigned)(I1 - I0) * step);
    poll_load<N>(v, srd, voff, soff0, step);
  }
}

DI bool bad_tag(const u32x4* v, int n, unsigned tag) {
  unsigned m = 0;
#pragma unroll
  for (int i = 0; i < n; ++i) m |= (v[i][1] ^ tag) | (v[i][3] ^ tag);
  return m != 0;
}
template <int N> DI void poll_tag(u32x4* v, __amdgpu_buffer_rsrc_t srd, unsigned voff, unsigned soff0, unsigned step, unsigned tag) {
  for (unsigned spin = 0;; ++spin) {
    asm volatile("" ::: "memory");
#pragma unroll
    for (int i = 0; i < N; ++i) v[i] = __builtin_amdgcn_raw_buffer_load_b128(srd, voff, soff0 + (unsigned)i * step, 16);
    if (!ANY(voff < OOB && bad_tag(v, N, tag)) || spin > SPIN_CAP) break;
    __builtin_amdgcn_s_sleep(1);
  }
}

struct LaneC {
  unsigned lane, r, h, q, p, blk;
  unsigned wv;
  unsigned afr[2];
  unsigned xb[4];
};
DI void lane_consts(LaneC& c) {
  unsigned l = TIDX & 63;
  asm volatile("" : "+v"(l));
  c.lane = l; c.r = l & 31; c.h = l >> 5; c.q = (l & 15) >> 2; c.p = l & 3; c.blk = (l >> 4) & 1;
  const unsigned sp = 2 * (c.p & 1) + (c.p >> 1);
#pragma unroll
  for (int nt = 0; nt < 2; ++nt) c.afr[nt] = (8 * c.h + c.q) * 128 + 64 * (nt ^ (c.q >> 1)) + 32 * c.blk + 8 * sp;
#pragma unroll
  for (int s = 0; s < 4; ++s) c.xb[s] = 1024 * c.h + ((c.r ^ (2 * s + c.h)) << 4);
}
DI unsigned wdma_voff(unsigned l, unsigned ld) { return (l >> 3) * ld + (((l & 7) ^ (((l >> 4) & 1) << 2)) << 4); }

DI void wring_issue(int ks, unsigned ring, unsigned voff, __amdgpu_buffer_rsrc_t srd, unsigned soff0, unsigned ld) {
  const unsigned slot = ring + (unsigned)(ks % RING) * 2048u;
  dma16(slot, voff, srd, soff0 + (unsigned)(16 * ks) * ld);
  dma16(slot + 1024u, voff ^ 32u, srd, soff0 + (unsigned)(16 * ks + 8) * ld);
}
template <int N> DI void wait_vm() {
  if constexpr (N == 0) WAIT_V(0); else if constexpr (N == 1) WAIT_V(1); else if constexpr (N == 2) WAIT_V(2);
  else if constexpr (N == 3) WAIT_V(3); else if constexpr (N == 4) WAIT_V(4); else if constexpr (N == 5) WAIT_V(5);
  else if constexpr (N == 6) WAIT_V(6); else if constexpr (N == 7) WAIT_V(7); else if constexpr (N == 8) WAIT_V(8);
  else if constexpr (N == 9) WAIT_V(9); else if constexpr (N == 10) WAIT_V(10); else if constexpr (N == 11) WAIT_V(11);
  else if constexpr (N == 12) WAIT_V(12); else if constexpr (N == 13) WAIT_V(13); else if constexpr (N == 14) WAIT_V(14);
  else if constexpr (N == 15) WAIT_V(15); else if constexpr (N == 16) WAIT_V(16);
  else if constexpr (N == 17) WAIT_V(17); else if constexpr (N == 18) WAIT_V(18);
  else static_assert(N < 0, "wait_vm");
}

#define MFMA16(a, b, c) __builtin_amdgcn_mfma_f32_16x16x32_bf16((a), (b), (c), 0, 0, 0)
struct G16 { unsigned a[4], hi, x[2]; };
DI void g16_consts(G16& k, unsigned l, unsigned ringoff) {
  const unsigned i = l & 15, g = l >> 4, q = i >> 2, p = l & 3, x = 2 * (q >> 1) + (g & 1);
#pragma unroll
  for (int m = 0; m < 4; ++m) k.a[m] = ringoff + (8 * (g & 1) + q) * 128 + ((2 * (m ^ x) + (p >> 1)) << 4) + 8 * (p & 1);
  k.hi = g >> 1;
#pragma unroll
  for (int e = 0; e < 2; ++e) k.x[e] = LDS_X + 1024 * g + ((i ^ (4 * e + g)) << 4);
}
struct Frag { bf16x8 a[4], b[4]; };
template <int KK> DI void load_frags(Frag& f, lds_cptr lds, const G16& k) {
  constexpr int sA = (2 * KK) % RING;
  const unsigned d = sA == RING - 1 ? (1u - k.hi) * (unsigned)((RING - 1) * 2048) : k.hi * 2048u;
  constexpr int o = sA == RING - 1 ? 0 : sA * 2048;
#pragma unroll
  for (int m = 0; m < 4; ++m) f.a[m] = cat8(lds_tr(lds + (k.a[m] + d) + o), lds_tr(lds + (k.a[m] + d) + o + 512));
#pragma unroll
  for (int r = 0; r < 4; ++r) f.b[r] = lds_b128(lds + k.x[KK & 1] + 4096 * KK + 256 * r);
}
template <int KK, int EXTRA>
DI void gemm_step(f32x4 (&acc)[4][4], Frag& cur, Frag& nxt, lds_cptr lds, unsigned ring, unsigned voff,
                  __amdgpu_buffer_rsrc_t srd, unsigned soff0, unsigned ld, const G16& k) {
  if constexpr (2 * KK + RING < 32) {
    asm volatile("" :: "v"(cur.a[0]), "v"(cur.a[1]), "v"(cur.a[2]), "v"(cur.a[3]));
    wring_issue(2 * KK + RING, ring, voff, srd, soff0, ld);
    if constexpr (2 * KK + RING + 1 < 32) wring_issue(2 * KK + RING + 1, ring, voff, srd, soff0, ld);
  }
  SBAR();
#pragma unroll
  for (int m = 0; m < GSPLIT; ++m)
#pragma unroll
    for (int r = 0; r < 4; ++r) acc[m][r] = MFMA16(cur.a[m], cur.b[r], acc[m][r]);
  SBAR();
  if constexpr (KK + 1 < 16) {
    constexpr int last = 2 * KK + RING + 1 < 32 ? 2 * KK + RING + 1 : 31;
    wait_vm<2 * (last - (2 * KK + 3)) + ((2 * KK + 3 < RING) ? EXTRA : 0)>();
    load_frags<KK + 1>(nxt, lds, k);
  }
  SBAR();
#pragma unroll
  for (int m = GSPLIT; m < 4; ++m)
#pragma unroll
    for (int r = 0; r < 4; ++r) acc[m][r] = MFMA16(cur.a[m], cur.b[r], acc[m][r]);
  SBAR();
  if constexpr (KK + 1 < 16) gemm_step<KK + 1, EXTRA>(acc, nxt, cur, lds, ring, voff, srd, soff0, ld, k);
}
template <int EXTRA>
DI void gemm_phase(f32x4 (&acc)[4][4], lds_cptr lds, unsigned ringoff, unsigned ring, unsigned voff,
                   __amdgpu_buffer_rsrc_t srd, unsigned soff0, unsigned ld, unsigned l) {
  G16 k; g16_consts(k, l, ringoff);
  Frag f0, f1;
  wait_vm<2 * (RING - 2) + EXTRA>();
  load_frags<0>(f0, lds, k);
  gemm_step<0, EXTRA>(acc, f0, f1, lds, ring, voff, srd, soff0, ld, k);
}
DI u32x4 pack16(const f32x4& a, const f32x4& b, float sc) {
  auto r0 = __builtin_amdgcn_permlane16_swap(cvtpk(a[0] * sc, a[1] * sc), cvtpk(b[0] * sc, b[1] * sc), false, false);
  auto r1 = __builtin_amdgcn_permlane16_swap(cvtpk(a[2] * sc, a[3] * sc), cvtpk(b[2] * sc, b[3] * sc), false, false);
  return (u32x4){r0[0], r1[0], r0[1], r1[1]};
}
DI u32x4 pack8(const f32x16& a, int s, float sc) {
  return (u32x4){cvtpk(a[8 * s] * sc, a[8 * s + 1] * sc), cvtpk(a[8 * s + 2] * sc, a[8 * s + 3] * sc),
                 cvtpk(a[8 * s + 4] * sc, a[8 * s + 5] * sc), cvtpk(a[8 * s + 6] * sc, a[8 * s + 7] * sc)};
}
DI unsigned ximg(unsigned chunk, unsigned row) { return chunk * 1024u + ((row ^ (chunk & 7u)) << 4); }

constexpr int KSLOT[8] = {LDS_D, LDS_R, LDS_R + 32768, LDS_X, LDS_X + 32768, LDS_D, LDS_X, LDS_X + 32768};
constexpr int VSLOT[8] = {LDS_R + 16384, LDS_R + 49152, LDS_X + 16384, LDS_X + 49152, LDS_R + 65536, LDS_X + 16384, LDS_X + 49152, LDS_D};
template <int N> struct IC { static constexpr int value = N; };
struct Params {
  const unsigned short *x, *kc, *vc, *wq, *wo, *w1, *w2, *w3, *w4, *c, *wmod;
  void* ws;
  u32x4* out;
};
DI unsigned attn_part(const Params& P, lds_ptr lds, unsigned lds0, unsigned w, __amdgpu_buffer_rsrc_t srdW) {
  LaneC c; lane_consts(c);
  const unsigned g = BIDX & 7, pn = BIDX >> 3;
  const unsigned l = c.lane, tid = TIDX;
  const unsigned ringoff = LDS_R + w * RINGB, ring = lds0 + ringoff;
  STAMP(1, 0);

  unsigned ep;
  asm volatile("buffer_load_dword %0, off, %1, 0 sc1" : "=v"(ep) : "s"(srdW) : "memory");
  u32x4 jw[4]; unsigned jc[4];
  auto c_load = [&](unsigned jks) __attribute__((always_inline)) {
#pragma unroll
    for (int i = 0; i < 4; ++i) asm volatile("global_load_ushort %0, %1, off" : "=v"(jc[i]) : "v"(P.c + jks * 64 + (l >> 2) + 16 * i) : "memory");
  };
  auto job_run = [&](unsigned jcol, unsigned jks, unsigned tag) __attribute__((always_inline)) {
    float acc[8] = {0, 0, 0, 0, 0, 0, 0, 0};
#pragma unroll
    for (int i = 0; i < 4; ++i) {
      const float v = U2F(jc[i] << 16);
      const float sv = v * __builtin_amdgcn_rcpf(1.0f + __builtin_amdgcn_exp2f(-1.4426950408889634f * v));
      const float sc = jcol >= 96u ? 1.0f : sv;
#pragma unroll
      for (int k = 0; k < 4; ++k) {
        acc[2 * k] = __builtin_fmaf(sc, bflo(jw[i][k]), acc[2 * k]);
        acc[2 * k + 1] = __builtin_fmaf(sc, bfhi(jw[i][k]), acc[2 * k + 1]);
      }
    }
#pragma unroll
    for (int k = 0; k < 8; ++k) acc[k] = sum_hi3(dpp_add<0x124>(acc[k]));
    const unsigned e = (l >> 2) & 3;
    const float v0 = e == 0 ? acc[0] : e == 1 ? acc[2] : e == 2 ? acc[4] : acc[6], v1 = e == 0 ? acc[1] : e == 1 ? acc[3] : e == 2 ? acc[5] : acc[7];
    __builtin_amdgcn_raw_buffer_store_b128((u32x4){F2U(v0), tag, F2U(v1), tag}, srdW,
                                           l < 16 ? W_MP + (jks * 4864u + jcol * 32u + (l & 3) * 8u + e * 2u) * 8u : OOB, 0, 16);
  };
  u32x4 xr[8];
  auto x_load = [&]() __attribute__((always_inline)) {
#pragma unroll
    for (int i = 0; i < 8; ++i) gld16(xr[i], P.x + (size_t)(pn * 64 + w * 8 + i) * D + l * 8);
  };
  if (w == 0) {
#pragma unroll
    for (int i = 0; i < 4; ++i) gld16(jw[i], P.wmod + (BIDX & 31u) * 32 + (l & 3) * 8 + (size_t)((BIDX >> 5) * 64 + (l >> 2) + 16 * i) * 3072);
    c_load(BIDX >> 5);
  } else x_load();
  asm volatile("s_waitcnt vmcnt(8)\n\ts_barrier" : "+v"(ep) :: "memory");
  const unsigned tag = rfl(ep) + 1u;
  if (w == 0) {
    asm volatile("s_waitcnt vmcnt(0)" : "+v"(jw[0]), "+v"(jw[1]), "+v"(jw[2]), "+v"(jw[3]), "+v"(jc[0]), "+v"(jc[1]), "+v"(jc[2]), "+v"(jc[3]) :: "memory");
    job_run(BIDX & 31u, BIDX >> 5, tag); x_load();
  }
  const unsigned jid = w * 256u + BIDX, jcol2 = 32u + jid % 120u, jks2 = jid / 120u;
  const bool job2 = w < 4 && jid < 960u;
  const __amdgpu_buffer_rsrc_t srdQ = mksrd(P.wq, 512u * 4096u * 2u);
  const unsigned vq = wdma_voff(l, 8192u), sq0 = (g * 512u + w * 64u) * 2u;
  const __amdgpu_buffer_rsrc_t srdK = mksrd(P.kc + ((size_t)g * SK + (SK - WIN)) * DH, WIN * DH * 2u);
  const __amdgpu_buffer_rsrc_t srdV = mksrd(P.vc + ((size_t)g * SK + (SK - WIN)) * DH, WIN * DH * 2u);
  const unsigned vk = (l >> 4) * 256u + (((l & 15) ^ (8 * (w & 1) + (l >> 4))) << 4);
  const unsigned vv = (l >> 3) * 256u + (((l & 7) ^ (((l >> 4) & 3) << 1)) << 4);
  auto dma_k = [&](int t, unsigned slot) {
    dma16(lds0 + slot + 2048u * w, vk, srdK, (unsigned)t * 16384u + 2048u * w);
    dma16(lds0 + slot + 2048u * w + 1024u, vk ^ 64u, srdK, (unsigned)t * 16384u + 2048u * w + 1024u);
  };
  auto dma_v = [&](int t, unsigned slot) {
    dma16(lds0 + slot + 1024u * w, vv, srdV, (unsigned)t * 16384u + 2048u * w);
    dma16(lds0 + slot + 8192u + 1024u * w, vv, srdV, (unsigned)t * 16384u + 2048u * w + 128u);
  };

  {
    asm volatile("s_waitcnt vmcnt(0)" : "+v"(xr[0]), "+v"(xr[1]), "+v"(xr[2]), "+v"(xr[3]), "+v"(xr[4]), "+v"(xr[5]), "+v"(xr[6]),
                 "+v"(xr[7]) :: "memory");
    dma16(ring + 8192u, l * 16u, srdQ, (2 * (pn * 8 + w)) * 8192u + g * 1024u);
    dma16(ring + 9216u, l * 16u, srdQ, (2 * (pn * 8 + w) + 1) * 8192u + g * 1024u);
    wring_issue(0, ring, vq, srdQ, sq0, 8192u);
    wring_issue(1, ring, vq, srdQ, sq0, 8192u);
    float v[8][8], s[8], m[8];
#pragma unroll
    for (int i = 0; i < 8; ++i) {
#pragma unroll
      for (int j = 0; j < 4; ++j) { v[i][2 * j] = bflo(xr[i][j]); v[i][2 * j + 1] = bfhi(xr[i][j]); }
      s[i] = ((v[i][0] + v[i][1]) + (v[i][2] + v[i][3])) + ((v[i][4] + v[i][5]) + (v[i][6] + v[i][7]));
    }
    wave_sum8(s, m);
#pragma unroll
    for (int i = 0; i < 8; ++i) {
      const float mu = m[i] * (1.0f / D);
      float q = 0.f;
#pragma unroll
      for (int j = 0; j < 8; ++j) { v[i][j] -= mu; q = __builtin_fmaf(v[i][j], v[i][j], q); }
      s[i] = q;
    }
    wave_sum8(s, m);
    float rs[8];
#pragma unroll
    for (int i = 0; i < 8; ++i) rs[i] = __builtin_amdgcn_rsqf(m[i] * (1.0f / D) + LN_EPS);
    STAMP(1, 8);
    {
      u32x4 mp[8];
      poll_tag<8>(mp, srdW, tid * 16u, W_MP, 4864u * 8u, tag);
      f32x2 t;
#pragma unroll
      for (int k = 0; k < 2; ++k)
        t[k] = ((U2F(mp[0][2 * k]) + U2F(mp[1][2 * k])) + (U2F(mp[2][2 * k]) + U2F(mp[3][2 * k]))) +
               ((U2F(mp[4][2 * k]) + U2F(mp[5][2 * k])) + (U2F(mp[6][2 * k]) + U2F(mp[7][2 * k])));
      *(LAS f32x2*)(lds + LDS_D + tid * 8) = t;
    }
    BAR_L();
    if (BIDX == 0 && tid == 0) __builtin_amdgcn_raw_buffer_store_b32(tag, srdW, W_EPOCH, 0, 16);
    u32x4 mo[4];
#pragma unroll
    for (int i = 0; i < 4; ++i) mo[i] = *(const LAS u32x4*)(lds + LDS_D + (i >> 1) * 2048 + l * 32 + (i & 1) * 16);
    STAMP(1, 9);
#pragma unroll
    for (int i = 0; i < 8; ++i) {
      float hh[8];
#pragma unroll
      for (int j = 0; j < 8; ++j) hh[j] = __builtin_fmaf(v[i][j] * rs[i], 1.f + U2F(mo[2 + (j >> 2)][j & 3]), U2F(mo[j >> 2][j & 3]));
      const u32x4 o = {cvtpk(hh[0], hh[1]), cvtpk(hh[2], hh[3]), cvtpk(hh[4], hh[5]), cvtpk(hh[6], hh[7])};
      *(LAS u32x4*)(lds + LDS_X + ximg(l, w * 8 + i)) = o;
      if (i == 1 || i == 3 || i == 5) wring_issue(2 + i / 2, ring, vq, srdQ, sq0, 8192u);
    }
  }
  BAR_L();
  dma_k(0, LDS_D);
  STAMP(1, 1);

  {
    f32x4 acc[4][4] = {};
    gemm_phase<2>(acc, lds, ringoff, ring, vq, srdQ, sq0, 8192u, l);
    STAMP(1, 2);
    WAIT_BAR(0);
    dma_k(1, KSLOT[1]); dma_v(0, VSLOT[0]); dma_k(2, KSLOT[2]); dma_v(1, VSLOT[1]);
    if (job2) {
      const bool isw3 = jcol2 >= 96u;
      const unsigned ldb = isw3 ? 3584u : 6144u;
      const __amdgpu_buffer_rsrc_t srdJ = isw3 ? mksrd(P.w3, 512u * 3584u) : mksrd(P.wmod, 512u * 6144u);
#pragma unroll
      for (int i = 0; i < 4; ++i)
        dma16(lds0 + LDS_R + 65536u + w * 4096u + i * 1024u, (l >> 2) * ldb + (l & 3) * 16u, srdJ, (jks2 * 64 + 16 * i) * ldb + (isw3 ? jcol2 - 96u : jcol2) * 64u);
      c_load(jks2);
    }
    const unsigned cl = 8 * w + 2 * ((l >> 4) & 1) + (l >> 5);
#pragma unroll
    for (int r = 0; r < 4; ++r)
#pragma unroll
      for (int mp = 0; mp < 2; ++mp)
        *(LAS u32x4*)(lds + LDS_X + ximg(cl + 4 * mp, 16 * r + (l & 15))) = pack16(acc[2 * mp][r], acc[2 * mp + 1][r], QSCALE);
    BAR_L();
  }

  STAMP(1, 3);
  const unsigned hh = w >> 1, rt = w & 1, li = l & 15, g4 = l >> 4;
  bf16x8 qf[4][2];
#pragma unroll
  for (int ks = 0; ks < 4; ++ks)
#pragma unroll
    for (int qt = 0; qt < 2; ++qt) qf[ks][qt] = lds_b128(lds + LDS_X + ximg(16 * hh + 4 * ks + g4, 32 * rt + 16 * qt + li));
  unsigned kb[4], vb[4];
#pragma unroll
  for (int i = 0; i < 4; ++i) {
    kb[i] = li * 256 + (((4 * i + g4) ^ li) << 4);
    vb[i] = (4 * g4 + c.q) * 128 + ((2 * (i ^ (2 * (g4 & 1) + (c.q >> 1))) + (c.p >> 1)) << 4) + 8 * (c.p & 1);
  }
  const __amdgpu_buffer_rsrc_t srdO = mksrd(P.wo, 4096u * 512u * 2u);
  const unsigned vo = wdma_voff(l, 1024u), so0 = g * 512u * 1024u + w * 128u;

  f32x4 o[8][2] = {};
  f32x4 sa[4][2];
  f32x4 negm[2];
  f32x4 lacc[2] = {};
  bf16x8 ones = {0x3F80, 0x3F80, 0x3F80, 0x3F80, 0x3F80, 0x3F80, 0x3F80, 0x3F80};
  PIN(ones);
  u32x4 pk[2][2];
  constexpr int PD = 6;
  bf16x8 fr[PD];
  const bool lag = w >= 4;
  auto frag = [&](int t, int it) -> bf16x8 {
    if (it < 16) {
      const lds_cptr a = lds + VSLOT[t > 0 ? t - 1 : 0] + vb[it & 3] + 8192 * ((it >> 2) & 1) + 4096 * (it >> 3);
      return cat8(lds_tr(a), lds_tr(a + 2048));
    }
    return lds_b128(lds + KSLOT[t < 8 ? t : 0] + kb[(it - 16) >> 2] + 4096 * (it & 3));
  };
  auto seam = [&](int t, int tx) __attribute__((always_inline)) {
    if (t == 7) { BAR_L(); } else if (tx >= 1 && tx <= 5) { WAIT_BAR(4); } else if (tx == 6) { WAIT_BAR(2); } else { WAIT_BAR(0); }
  };
  auto refill = [&](int t) __attribute__((always_inline)) {
    if (t + 3 < 8) dma_k(t + 3, KSLOT[t + 3 < 8 ? t + 3 : 0]);
    if (t + 2 < 8) dma_v(t + 2, VSLOT[t + 2 < 8 ? t + 2 : 0]);
    if (t == 6) {
#pragma unroll
      for (int ks = 0; ks < RING; ++ks) wring_issue(ks, ring, vo, srdO, so0, 1024u);
    }
  };
  auto extra = [&](int e) __attribute__((always_inline)) {
    const unsigned dump = lds0 + LDS_R + w * 3072u + (unsigned)(e % 3) * 1024u;
    const unsigned id = (BIDX >> 3) * 8 + w, jh = BIDX & 1;
    const unsigned vrow = (l >> 5) * 1024u + (l & 31) * 16u + jh * 512u;
    auto w3v = [&](unsigned piece) { const unsigned byte = piece * 1024u + l * 16u, row = byte / 1792u;
                                     return row * 3584u + jh * 1792u + (byte - row * 1792u); };
    if (e < 4 || e >= 15) {
      const int i = e < 4 ? e : e - 11;
      const unsigned off = i < 3 ? S_U + (unsigned)i * (64u * T * 16u) + pn * 65536u + g * 8192u
                         : i < 7 ? S_HZ + pn * 262144u + g * 32768u + (unsigned)(i - 3) * 8192u
                                 : S_H3 + pn * 229376u + g * 28672u + (unsigned)(i - 7) * 8192u;
      const u32x4 sv = {SENT, SENT, SENT, SENT};
      __builtin_amdgcn_raw_buffer_store_b128(sv, srdW, (i == 10 && tid >= 256) ? OOB : tid * 16u + off, 0, 16);
    } else if (e < 6) dma16(dump, l * 16u, srdO, (g * 512u + (pn * 8 + w) * 2 + (e - 4)) * 1024u);
    else if (e == 6) dma16(dump, vrow, mksrd(P.w1, 512u * 1024u), id * 2048u);
    else if (e == 7) dma16(dump, vrow, mksrd(P.w2, 512u * 1024u), id * 2048u);
    else if (e < 11) dma16(dump, w3v(id + 256u * (e - 8)), mksrd(P.w3, 512u * 3584u), 0u);
    else if (e == 11) {
      if (id < 128) dma16(dump, w3v(id + 768u), mksrd(P.w3, 512u * 3584u), 0u);
      else dma16(dump, vrow, mksrd(P.w4, 1792u * 1024u), (id - 128u + 768u) * 2048u);
    } else dma16(dump, vrow, mksrd(P.w4, 1792u * 1024u), (id + 256u * (e - 12)) * 2048u);
  };
  auto step = [&](auto tc) __attribute__((always_inline)) {
    constexpr int t = decltype(tc)::value;
    constexpr int i0 = t == 0 ? 16 : 0, i1 = t == 8 ? 16 : 32;
    if (t <= 1) {
#pragma unroll
      for (int it = i0; it < i0 + PD; ++it) fr[it % PD] = frag(t, it);
    }
    __builtin_amdgcn_s_setprio(1);
    SBAR();
#pragma unroll
    for (int it = i0; it < i1; ++it) {
#pragma unroll
      for (int qt = 0; qt < 2; ++qt) {
        if (it < 16) o[it & 7][qt] = MFMA16(fr[it % PD], __builtin_bit_cast(bf16x8, pk[it >> 3][qt]), o[it & 7][qt]);
        else sa[it & 3][qt] = MFMA16(fr[it % PD], qf[(it - 16) >> 2][qt], it >= 20 ? sa[it & 3][qt] : t == 0 ? (f32x4){} : negm[qt]);
      }
      if (it < 16 && (it & 7) == 7) {
#pragma unroll
        for (int qt = 0; qt < 2; ++qt) lacc[qt] = MFMA16(ones, __builtin_bit_cast(bf16x8, pk[it >> 3][qt]), lacc[qt]);
      }
      if (it + PD < i1) fr[it % PD] = frag(t, it + PD);
      if (t >= 1 && it == 1) { if (!lag) refill(t - 1); }
      SBAR();
    }
    if constexpr (t == 8) return;
    __builtin_amdgcn_s_setprio(0);
    if (lag) { seam(t, t - 1); refill(t); }
    asm volatile("s_nop 15" : "+v"(sa[0][0]), "+v"(sa[0][1]), "+v"(sa[1][0]), "+v"(sa[1][1]), "+v"(sa[2][0]), "+v"(sa[2][1]),
                 "+v"(sa[3][0]), "+v"(sa[3][1]));
    float tm[2];
#pragma unroll
    for (int qt = 0; qt < 2; ++qt) {
      float ta = max3f(sa[0][qt][0], sa[0][qt][1], sa[0][qt][2]), tb = max3f(sa[0][qt][3], sa[1][qt][0], sa[1][qt][1]);
      ta = max3f(ta, sa[1][qt][2], sa[1][qt][3]); tb = max3f(tb, sa[2][qt][0], sa[2][qt][1]);
      ta = max3f(ta, sa[2][qt][2], sa[2][qt][3]); tb = max3f(tb, sa[3][qt][0], sa[3][qt][1]);
      tm[qt] = max3f(max3f(ta, sa[3][qt][2], sa[3][qt][3]), tb, tb);
    }
    if (t == 0 || __builtin_expect(rfl(ANY(max2f(tm[0], tm[1]) > THR)), 0)) {
      const float aone = g4 ? 0.f : 1.f;
      __builtin_amdgcn_s_setprio(2);
#pragma unroll
      for (int qt = 0; qt < 2; ++qt) {
        auto r = __builtin_amdgcn_permlane16_swap(F2U(tm[qt]), F2U(tm[qt]), false, false);
        const float m1 = max2f(U2F(r[0]), U2F(r[1]));
        auto r2 = __builtin_amdgcn_permlane32_swap(F2U(m1), F2U(m1), false, false);
        const float m2 = max2f(U2F(r2[0]), U2F(r2[1]));
        const float ndl = t == 0 ? -m2 : -max2f(m2, 0.f);
        if (t == 0) negm[qt] = (f32x4){};
#pragma unroll
        for (int kt = 0; kt < 4; ++kt) sa[kt][qt] = __builtin_amdgcn_mfma_f32_16x16x4f32(aone, ndl, sa[kt][qt], 0, 0, 0);
        negm[qt] = __builtin_amdgcn_mfma_f32_16x16x4f32(aone, ndl, negm[qt], 0, 0, 0);
        if (t > 0) {
          const float al = __builtin_amdgcn_exp2f(ndl);
          lacc[qt][0] *= al;
#pragma unroll
          for (int dt = 0; dt < 8; ++dt)
#pragma unroll
            for (int e = 0; e < 4; ++e) o[dt][qt][e] *= al;
        }
      }
      __builtin_amdgcn_s_setprio(0);
    }
    SBAR();
    if (t >= 1) {
#pragma unroll
      for (int it = 0; it < PD; ++it) fr[it] = frag(t + 1, it);
    }
#pragma unroll
    for (int kt = 0; kt < 4; ++kt) {
#pragma unroll
      for (int qt = 0; qt < 2; ++qt) {
#pragma unroll
        for (int e = 0; e < 4; ++e) sa[kt][qt][e] = __builtin_amdgcn_exp2f(sa[kt][qt][e]);
        pk[kt >> 1][qt][2 * (kt & 1)] = cvtpk(sa[kt][qt][0], sa[kt][qt][1]);
        pk[kt >> 1][qt][2 * (kt & 1) + 1] = cvtpk(sa[kt][qt][2], sa[kt][qt][3]);
      }
      if (t >= 1 && 4 * (t - 1) + kt < 22) { PIN(sa[kt][0]); extra(4 * (t - 1) + kt); }
    }
    PIN(pk[0][0]); PIN(pk[0][1]); PIN(pk[1][0]); PIN(pk[1][1]);
    if constexpr (t == 1) {
      if (job2) {
        asm volatile("" : "+v"(jc[0]), "+v"(jc[1]), "+v"(jc[2]), "+v"(jc[3]));
#pragma unroll
        for (int i = 0; i < 4; ++i) jw[i] = *(const LAS u32x4*)(lds + LDS_R + 65536 + w * 4096 + i * 1024 + l * 16);
        job_run(jcol2, jks2, tag);
      }
    }
    if constexpr (t == 4) {
      if (BIDX < 120 && w == 0) {
        u32x4 mp[2];
        poll_tag<2>(mp, srdW, ((l >> 3) * 4864u + (32u + BIDX) * 32u + (l & 7) * 2u) * 8u, W_MP, 128u, tag);
        const float a0 = sum_hi3(U2F(mp[0][0])), a1 = sum_hi3(U2F(mp[0][2])), b0 = sum_hi3(U2F(mp[1][0])), b1 = sum_hi3(U2F(mp[1][2]));
        const bool hi = (l >> 3) & 1;
        __builtin_amdgcn_raw_buffer_store_b128((u32x4){F2U(hi ? b0 : a0), tag, F2U(hi ? b1 : a1), tag}, srdW,
                                               l < 16 ? W_MOD + ((32u + BIDX) * 32u + l * 2u) * 8u : OOB, 0, 16);
      }
    }
    SBAR();
    if (!lag) seam(t, t);
  };
  step(IC<0>{}); step(IC<1>{}); step(IC<2>{}); step(IC<3>{}); step(IC<4>{}); step(IC<5>{}); step(IC<6>{}); step(IC<7>{}); step(IC<8>{});
  __builtin_amdgcn_s_setprio(0);
  STAMP(1, 4);
  {
#pragma unroll
    for (int qt = 0; qt < 2; ++qt) {
      const float il = __builtin_amdgcn_rcpf(lacc[qt][0]);
#pragma unroll
      for (int mp = 0; mp < 4; ++mp)
        *(LAS u32x4*)(lds + LDS_X + ximg(16 * hh + 4 * mp + 2 * (g4 & 1) + (g4 >> 1), 32 * rt + 16 * qt + li)) =
            pack16(o[2 * mp][qt], o[2 * mp + 1][qt], il);
    }
  }
  BAR_L();
  STAMP(1, 5);
  dma16_sc1(lds0 + LDS_D + w * 2048u, l * 16u, srdW, W_MOD + 8192u + w * 2048u);
  dma16_sc1(lds0 + LDS_D + w * 2048u + 1024u, l * 16u, srdW, W_MOD + 8192u + w * 2048u + 1024u);
  {
    unsigned l2 = TIDX & 63;
    asm volatile("" : "+v"(l2));
    f32x4 acc[4][4] = {};
    gemm_phase<2>(acc, lds, ringoff, ring, vo, srdO, so0, 1024u, l2);
    STAMP(1, 6);
    const unsigned dst = W_P1 + ((((pn * 8 + ((l2 >> 3) & 1)) * 8 + g) * 64 + 8 * w + 2 * ((l2 >> 4) & 1) + (l2 >> 5)) * 8 + (l2 & 7)) * 16u;
#pragma unroll
    for (int r = 0; r < 4; ++r)
#pragma unroll
      for (int mp = 0; mp < 2; ++mp)
        __builtin_amdgcn_raw_buffer_store_b128(pack16(acc[2 * mp][r], acc[2 * mp + 1][r], 1.f), srdW,
                                               dst + (4 * mp * 8 + r * 2 * 8 * 64 * 8) * 16u, 0, 16);
  }
  WAIT_BAR(0);
  if (tid == 0) __builtin_amdgcn_raw_buffer_store_b32(tag, srdW, W_FLAG + (pn * 8 + g) * 64u, 0, 16);
  STAMP(1, 7);
  return tag;
}

DI void ln8(float (&v)[8], LAS float* red  , unsigned tid) {
  float s = ((v[0] + v[1]) + (v[2] + v[3])) + ((v[4] + v[5]) + (v[6] + v[7]));
  float q = (__builtin_fmaf(v[0], v[0], v[1] * v[1]) + __builtin_fmaf(v[2], v[2], v[3] * v[3])) +
            (__builtin_fmaf(v[4], v[4], v[5] * v[5]) + __builtin_fmaf(v[6], v[6], v[7] * v[7]));
  s = sum_hi3(s); q = sum_hi3(q);
  if ((tid & 63) < 8) *(LAS f32x2*)(red + ((tid >> 6) * 8 + (tid & 7)) * 2) = (f32x2){s, q};
  BAR_L();
  f32x2 e[8];
#pragma unroll
  for (int i = 0; i < 8; ++i) e[i] = *(const LAS f32x2*)(red + (i * 8 + (tid & 7)) * 2);
  const float mu = (((e[0][0] + e[1][0]) + (e[2][0] + e[3][0])) + ((e[4][0] + e[5][0]) + (e[6][0] + e[7][0]))) * (1.0f / D);
  const float ms = (((e[0][1] + e[1][1]) + (e[2][1] + e[3][1])) + ((e[4][1] + e[5][1]) + (e[6][1] + e[7][1]))) * (1.0f / D);
  const float rs = __builtin_amdgcn_rsqf(__builtin_fmaxf(__builtin_fmaf(-mu, mu, ms), 0.f) + LN_EPS);
  const float nb = -mu * rs;
#pragma unroll
  for (int k = 0; k < 8; ++k) v[k] = __builtin_fmaf(v[k], rs, nb);
}
DI void red4_put(const f32x16& a, int nt, lds_ptr scr, unsigned rt, unsigned kq, unsigned l) {
#pragma unroll
  for (int g = 0; g < 2; ++g)
#pragma unroll
    for (int i = 0; i < 2; ++i) {
      const int o = 8 * g + 4 * i;
      *(LAS f32x4*)(scr + ((rt * 4 + 2 * nt + g) * 4 + kq) * 2048 + i * 1024 + l * 16) = (f32x4){a[o], a[o + 1], a[o + 2], a[o + 3]};
    }
}
DI void red4_get(float (&v)[8], lds_cptr scr, unsigned rt, unsigned kq, unsigned l) {
  f32x4 t[4][2];
#pragma unroll
  for (int s = 0; s < 4; ++s)
#pragma unroll
    for (int i = 0; i < 2; ++i) t[s][i] = *(const LAS f32x4*)(scr + ((rt * 4 + kq) * 4 + s) * 2048 + i * 1024 + l * 16);
#pragma unroll
  for (int i = 0; i < 2; ++i)
#pragma unroll
    for (int k = 0; k < 4; ++k) v[4 * i + k] = (t[0][i][k] + t[1][i][k]) + (t[2][i][k] + t[3][i][k]);
}
DI void tile_mm8(const bf16x8 (&af)[8][2], const u32x4 (&xb)[8], float (&v)[8], lds_ptr scr, unsigned rt, unsigned kq, unsigned l) {
  f32x16 a0 = {}, a1 = {};
#pragma unroll
  for (int i = 0; i < 8; ++i) a0 = MFMA32(af[i][0], __builtin_bit_cast(bf16x8, xb[i]), a0);
  SBAR();
#pragma unroll
  for (int i = 0; i < 4; ++i) a1 = MFMA32(af[i][1], __builtin_bit_cast(bf16x8, xb[i]), a1);
  SBAR();
  red4_put(a0, 0, scr, rt, kq, l);
#pragma unroll
  for (int i = 4; i < 8; ++i) a1 = MFMA32(af[i][1], __builtin_bit_cast(bf16x8, xb[i]), a1);
  SBAR();
  red4_put(a1, 1, scr, rt, kq, l);
  BAR_L();
  red4_get(v, scr, rt, kq, l);
}

struct Frag3 { bf16x8 a, b[2]; };
constexpr int RING3 = 12;
constexpr int LA = 0, LB = 65536, LC = 131072;
DI unsigned r3off(int s, unsigned rc, unsigned rb) { return s < 4 ? rc + s * 1024u : rb + (s - 4) * 1024u; }
template <int KS> DI void load_frags3(Frag3& f, lds_cptr lds, unsigned rc, unsigned rb, unsigned a3, const LaneC& c) {
  const lds_cptr sl = lds + r3off(KS % RING3, rc, rb) + a3;
  f.a = cat8(lds_tr(sl), lds_tr(sl + 256));
#pragma unroll
  for (int rt = 0; rt < 2; ++rt) f.b[rt] = lds_b128(lds + LA + c.xb[KS & 3] + 2048 * KS + 512 * rt);
}
constexpr int w4slot(int n) { return (8 + n) % RING3; }
template <int KS>
DI void g3_step(f32x16 (&acc)[2], Frag3& cur, Frag3& n1, Frag3& n2, lds_cptr lds, unsigned lds0, unsigned rc, unsigned rb,
                unsigned voff, __amdgpu_buffer_rsrc_t srd, unsigned soff0, unsigned a3, const LaneC& c,
                unsigned v4, __amdgpu_buffer_rsrc_t srd4, unsigned s40) {
  constexpr int NKS = 32;
  if constexpr (KS + 2 < NKS) {
    constexpr int last = (KS + RING3 < NKS ? KS + RING3 : NKS) - 1;
    wait_vm<last - (KS + 2) + (KS > NKS - RING3 ? KS - (NKS - RING3) : 0)>();
    load_frags3<KS + 2>(n2, lds, rc, rb, a3, c);
  }
  SBAR();
  acc[0] = MFMA32(cur.a, cur.b[0], acc[0]);
  acc[1] = MFMA32(cur.a, cur.b[1], acc[1]);
  asm volatile("" :: "v"(cur.a));
  if constexpr (KS + RING3 < NKS) dma16(lds0 + r3off(KS % RING3, rc, rb), voff, srd, soff0 + (unsigned)(16 * (KS + RING3)) * 3584u);
  else dma16(lds0 + r3off(KS % RING3, rc, rb), v4, srd4, s40 + (unsigned)(KS - (NKS - RING3)) * 16384u);
  SBAR();
  if constexpr (KS + 1 < NKS) g3_step<KS + 1>(acc, n1, n2, cur, lds, lds0, rc, rb, voff, srd, soff0, a3, c, v4, srd4, s40);
}

DI void mlp_part(const Params& P, lds_ptr lds, unsigned lds0, unsigned w, __amdgpu_buffer_rsrc_t srdS, unsigned tag) {
  LaneC c; lane_consts(c);
  const unsigned idx = BIDX >> 3, pn = 8 * ((BIDX & 7) >> 1) + (idx >> 2), j = 4 * (BIDX & 1) + (idx & 3);
  const unsigned tid = TIDX, l = c.lane;
  const unsigned vw = wdma_voff(l, 1024u);
  const unsigned rt = w & 1, kq = w >> 1;
  constexpr int LMOD = LDS_D;
  constexpr int LRED = LC + 12288;
  constexpr int LST = LB + 7 * 8192;
  const unsigned prow = 32 * rt + c.r;
  const unsigned trow = pn * 64 + prow;
  const unsigned och = 8 * j + 2 * kq + c.h;
  const unsigned ovo = ((pn * 64 + och) * 64 + prow) * 16u;
  float gb[8];
  STAMP(2, 0);

  {
    const unsigned row = pn * 64 + j * 8 + (tid & 7), ch = tid >> 3;
    u32x4 xr;
    gld16(xr, P.x + (size_t)row * D + ch * 8);
    auto wait_flags = [&](unsigned fo) {
      for (unsigned spin = 0;; ++spin) {
        asm volatile("" ::: "memory");
        const unsigned f = __builtin_amdgcn_raw_buffer_load_b32(srdS, fo, 0, 16);
        if (!ANY(fo != OOB && f != tag) || spin > SPIN_CAP) break;
        __builtin_amdgcn_s_sleep(FLAG_SLEEP);
      }
    };
    const __amdgpu_buffer_rsrc_t srd1 = mksrd(P.w1, 512u * 512u * 2u), srd2 = mksrd(P.w2, 512u * 512u * 2u);
#pragma unroll
    for (int i = 0; i < 8; ++i) dma16(lds0 + LA + (8 * w + i) * 1024u, vw, srd1, (8 * w + i) * 8192u + j * 128u);
    {
      const int fp = (int)pn - 1 + (int)(l >> 3);
      if (w == 0) wait_flags((l < 24 && fp >= 0 && fp < 32) ? W_FLAG + ((unsigned)fp * 8 + (l & 7)) * 64u : OOB);
      BAR_L();
    }
    STAMP(2, 15);
    u32x4 p[8];
#pragma unroll
    for (int g = 0; g < 8; ++g) bld16_sc1(p[g], tid * 16u, srdS, W_P1 + ((pn * 8 + j) * 8 + g) * 8192u);
    float ga[8], sb[8], cb[8], g2[8];
    {
      u32x4 mv[20];
#pragma unroll
      for (int i = 0; i < 16; ++i) mv[i] = *(const LAS u32x4*)(lds + LMOD + (i >> 2) * 4096 + ch * 64 + 16 * (i & 3));
#pragma unroll
      for (int i = 0; i < 4; ++i) mv[16 + i] = *(const LAS u32x4*)(lds + LMOD + 12288 + och * 64 + 16 * i);
      if (ANY(bad_tag(mv, 20, tag))) {
#pragma unroll
        for (int i = 0; i < 4; ++i) poll_tag<4>(mv + 4 * i, srdS, ch * 64u, W_MOD + 8192u + i * 4096u, 16u, tag);
        poll_tag<4>(mv + 16, srdS, och * 64u, W_MOD + 20480u, 16u, tag);
      }
#pragma unroll
      for (int k = 0; k < 8; ++k) {
        ga[k] = U2F(mv[k >> 1][2 * (k & 1)]); sb[k] = U2F(mv[4 + (k >> 1)][2 * (k & 1)]); cb[k] = 1.f + U2F(mv[8 + (k >> 1)][2 * (k & 1)]);
        g2[k] = U2F(mv[12 + (k >> 1)][2 * (k & 1)]); gb[k] = U2F(mv[16 + (k >> 1)][2 * (k & 1)]);
      }
    }
    asm volatile("s_waitcnt vmcnt(0)" : "+v"(xr), "+v"(p[0]), "+v"(p[1]), "+v"(p[2]), "+v"(p[3]), "+v"(p[4]), "+v"(p[5]),
                 "+v"(p[6]), "+v"(p[7]) :: "memory");
    STAMP(2, 1);
    float v[8], x2[8];
#pragma unroll
    for (int i = 0; i < 4; ++i) {
      const float lo = ((bflo(p[0][i]) + bflo(p[1][i])) + (bflo(p[2][i]) + bflo(p[3][i]))) + ((bflo(p[4][i]) + bflo(p[5][i])) + (bflo(p[6][i]) + bflo(p[7][i])));
      const float hi = ((bfhi(p[0][i]) + bfhi(p[1][i])) + (bfhi(p[2][i]) + bfhi(p[3][i]))) + ((bfhi(p[4][i]) + bfhi(p[5][i])) + (bfhi(p[6][i]) + bfhi(p[7][i])));
      v[2 * i] = x2[2 * i] = __builtin_fmaf(ga[2 * i], lo, bflo(xr[i]));
      v[2 * i + 1] = x2[2 * i + 1] = __builtin_fmaf(ga[2 * i + 1], hi, bfhi(xr[i]));
    }
    ln8(v, (LAS float*)(lds + LRED), tid);
    float hb[8];
#pragma unroll
    for (int k = 0; k < 8; ++k) v[k] = hb[k] = __builtin_fmaf(v[k], cb[k], sb[k]);
    ln8(v, (LAS float*)(lds + LRED + 512), tid);
    const unsigned vo = ((pn * 64 + ch) * 64 + j * 8 + (tid & 7)) * 16u, vz = ((pn * 64 + ch) * 256 + j * 8 + (tid & 7)) * 16u;
    pub16(srdS, vo, S_U, (u32x4){cvtpk(v[0], v[1]), cvtpk(v[2], v[3]), cvtpk(v[4], v[5]), cvtpk(v[6], v[7])});
#pragma unroll
    for (int i = 0; i < 2; ++i) {
      pub16(srdS, vz, S_HZ + i * 1024u, (u32x4){F2U(hb[4 * i]), F2U(hb[4 * i + 1]),
                                                F2U(hb[4 * i + 2]), F2U(hb[4 * i + 3])});
      f32x4 z;
#pragma unroll
      for (int e = 0; e < 4; ++e) z[e] = __builtin_fmaf(g2[4 * i + e], hb[4 * i + e], x2[4 * i + e]);
      pub16(srdS, vz, S_HZ + (2 + i) * 1024u, __builtin_bit_cast(u32x4, z));
    }
#pragma unroll
    for (int i = 0; i < 8; ++i) dma16(lds0 + LB + (8 * w + i) * 1024u, vw, srd2, (8 * w + i) * 8192u + j * 128u);
  }
  STAMP(2, 2);
  const __amdgpu_buffer_rsrc_t srd3 = mksrd(P.w3, 512u * F * 2u);
  const unsigned rc = LC + w * 4096u, rb = LB + w * 8192u;
  const unsigned v3 = (l >> 2) * 3584u + ((l & 3) << 4), s30 = (j * 224u + w * 32u) * 2u;
  float v[8];
  {
    bf16x8 af[8][2];
#pragma unroll
    for (int i = 0; i < 8; ++i)
#pragma unroll
      for (int nt = 0; nt < 2; ++nt) {
        const lds_cptr a = lds + LA + (8 * kq + i) * 2048 + c.afr[nt];
        af[i][nt] = cat8(lds_tr(a), lds_tr(a + 512));
      }
    BAR_L();
    u32x4 xb[8];
    poll_ind<8, 3, 7>(xb, srdS, ((pn * 64 + 16 * kq + c.h) * 64 + prow) * 16u, S_U, 2048u);
    STAMP(2, 3);
    tile_mm8(af, xb, v, lds + LA, rt, kq, l);
#pragma unroll
    for (int k = 0; k < 8; ++k) v[k] = gelu_tanh(v[k]);
    pub16(srdS, ovo, S_H1, (u32x4){cvtpk(v[0], v[1]), cvtpk(v[2], v[3]), cvtpk(v[4], v[5]), cvtpk(v[6], v[7])});
    if (w < 7) {
#pragma unroll
      for (int ks = 0; ks < 4; ++ks) dma16(lds0 + r3off(ks, rc, rb), v3, srd3, s30 + (unsigned)(16 * ks) * 3584u);
    }
  }
  STAMP(2, 4);
  float y[8];
  float cs[16];
  const unsigned cso = w < 7 ? (3072 + 224 * j + 32 * w + 8 * c.h) * 8u : OOB;
  {
    bf16x8 af[8][2];
#pragma unroll
    for (int i = 0; i < 8; ++i)
#pragma unroll
      for (int nt = 0; nt < 2; ++nt) {
        const lds_cptr a = lds + LB + (8 * kq + i) * 2048 + c.afr[nt];
        af[i][nt] = cat8(lds_tr(a), lds_tr(a + 512));
      }
    BAR_L();
    const int sh = kq == 0 ? -1 : kq == 1 ? 1 : kq == 2 ? -2 : 2;
    const int srow = (int)trow + sh;
    const unsigned xvo = (srow >= 0 && srow < T) ? ((((unsigned)srow >> 6) * 64 + 16 * kq + c.h) * 64 + ((unsigned)srow & 63)) * 16u : OOB;
    u32x4 xb[8];
    poll_ind<8, 3, 7>(xb, srdS, xvo, S_H1, 2048u);
    STAMP(2, 5);
    u32x4 hz[4];
    const unsigned hzo = ((pn * 64 + och) * 256 + prow) * 16u;
#pragma unroll
    for (int i = 0; i < 4; ++i) hz[i] = __builtin_amdgcn_raw_buffer_load_b128(srdS, hzo, S_HZ + i * 1024u, 16);
    u32x4 cg[8];
#pragma unroll
    for (int i = 0; i < 8; ++i) cg[i] = __builtin_amdgcn_raw_buffer_load_b128(srdS, cso, W_MOD + 128u * (i >> 2) + 16u * (i & 3), 16);
    tile_mm8(af, xb, v, lds + LA, rt, kq, l);
    if (has_sent(hz, 4)) poll_load<4>(hz, srdS, hzo, S_HZ, 1024u);
    float x1[8];
#pragma unroll
    for (int k = 0; k < 8; ++k) {
      x1[k] = U2F(hz[k >> 2][k & 3]) + v[k];
      y[k] = __builtin_fmaf(gb[k], v[k], U2F(hz[2 + (k >> 2)][k & 3]));
    }
    pub16(srdS, ovo, S_X1, (u32x4){cvtpk(x1[0], x1[1]), cvtpk(x1[2], x1[3]), cvtpk(x1[4], x1[5]), cvtpk(x1[6], x1[7])});
    if (ANY(cso < OOB && bad_tag(cg, 8, tag))) {
      poll_tag<4>(cg, srdS, cso, W_MOD, 16u, tag);
      poll_tag<4>(cg + 4, srdS, cso, W_MOD + 128u, 16u, tag);
    }
#pragma unroll
    for (int i = 0; i < 16; ++i) cs[i] = U2F(cg[i >> 1][2 * (i & 1)]);
    if (w < 7) {
#pragma unroll
      for (int ks = 4; ks < RING3; ++ks) dma16(lds0 + r3off(ks, rc, rb), v3, srd3, s30 + (unsigned)(16 * ks) * 3584u);
    }
  }
  STAMP(2, 6);
  const __amdgpu_buffer_rsrc_t srd4 = mksrd(P.w4, (unsigned)F * 512u * 2u);
  const unsigned s40 = (448u * kq + 8u * rt) * 1024u + j * 128u;
  {
    u32x4 xr[8];
    BAR_L();
    poll_ind<8, 3, 7>(xr, srdS, ((pn * 64 + 8 * w) * 64 + l) * 16u, S_X1, 1024u);
    STAMP(2, 7);
#pragma unroll
    for (int i = 0; i < 8; ++i) *(LAS u32x4*)(lds + LA + ximg(8 * w + i, l)) = xr[i];
    {
      float s0 = 0.f, s1 = 0.f, q0 = 0.f, q1 = 0.f;
#pragma unroll
      for (int i = 0; i < 8; ++i)
#pragma unroll
        for (int k = 0; k < 4; k += 2) {
          s0 = dot2bf(xr[i][k], 0x3F803F80u, s0); s1 = dot2bf(xr[i][k + 1], 0x3F803F80u, s1);
          q0 = dot2bf(xr[i][k], xr[i][k], q0); q1 = dot2bf(xr[i][k + 1], xr[i][k + 1], q1);
        }
      *(LAS f32x2*)(lds + LST + (w * 64 + l) * 8) = (f32x2){s0 + s1, q0 + q1};
    }
    BAR_L();
    STAMP(2, 8);
    if (w < 7) {
      f32x16 acc[2] = {};
      const unsigned a3 = (8 * c.h + c.q) * 64 + 32 * c.blk + 8 * (2 * (c.p & 1) + (c.p >> 1));
      Frag3 f0, f1, f2;
      wait_vm<RING3 - 2>();
      load_frags3<0>(f0, lds, rc, rb, a3, c);
      load_frags3<1>(f1, lds, rc, rb, a3, c);
      float mu[2], rs[2];
#pragma unroll
      for (int r2 = 0; r2 < 2; ++r2) {
        float s = 0.f, q = 0.f;
#pragma unroll
        for (int i = 0; i < 8; ++i) {
          const f32x2 t = *(const LAS f32x2*)(lds + LST + (i * 64 + 32 * r2 + c.r) * 8);
          s += t[0]; q += t[1];
        }
        mu[r2] = s * (1.0f / D);
        rs[r2] = __builtin_amdgcn_rsqf(__builtin_fmaxf(__builtin_fmaf(-mu[r2], mu[r2], q * (1.0f / D)), 0.f) + LN_EPS);
      }
      if (w < 4) __builtin_amdgcn_s_setprio(2); else __builtin_amdgcn_s_setprio(1);
      g3_step<0>(acc, f0, f1, f2, lds, lds0, rc, rb, v3, srd3, s30, a3, c, vw, srd4, s40);
      __builtin_amdgcn_s_setprio(0);
      STAMP(2, 9);
#pragma unroll
      for (int r2 = 0; r2 < 2; ++r2) {
#pragma unroll
        for (int i = 0; i < 16; ++i) acc[r2][i] = gelu_tanh(rs[r2] * __builtin_fmaf(-mu[r2], cs[i], acc[r2][i]));
#pragma unroll
        for (int s2 = 0; s2 < 2; ++s2)
          pub16(srdS, ((pn * 224 + 28 * j + 4 * w + 2 * s2 + c.h) * 64 + 32 * r2 + c.r) * 16u, S_H3, pack8(acc[r2], s2, 1.f));
      }
    } else {
#pragma unroll
      for (int n = 0; n < 8; ++n) dma16(lds0 + r3off(w4slot(n), rc, rb), vw, srd4, s40 + (unsigned)n * 16384u);
    }
    BAR_L();
  }
  STAMP(2, 10);
  {
    auto poff = [&](int ks, unsigned wv) -> unsigned {
      return (ks >= 12 && ks < 20) ? LA + wv * 8192u + (unsigned)(ks - 12) * 1024u : r3off(w4slot(ks % 20), LC + wv * 4096u, LB + wv * 8192u);
    };
    auto w4dma = [&](int ks) { dma16(lds0 + poff(ks, w), vw, srd4, s40 + (unsigned)ks * 16384u); };
    if (w == 7) {
#pragma unroll
      for (int ks = 8; ks < 12; ++ks) w4dma(ks);
    }
#pragma unroll
    for (int ks = 12; ks < 20; ++ks) w4dma(ks);
    const unsigned wsrc = 2 * kq + c.h;
    const unsigned hvo = ((pn * 224 + 56 * kq + c.h) * 64 + prow) * 16u;
    u32x4 ha[14], hb[14];
    poll_ind<14, 9, 13>(ha, srdS, hvo, S_H3, 2048u);
    BAR_L();
    STAMP(2, 11);
    f32x16 acc[2] = {};
    bf16x8 fa[2][2];
    auto ld = [&](int ks) {
#pragma unroll
      for (int nt = 0; nt < 2; ++nt) {
        const lds_cptr a = lds + poff(ks, wsrc) + (c.afr[nt] - 1024u * c.h);
        fa[ks & 1][nt] = cat8(lds_tr(a), lds_tr(a + 512));
      }
    };
    auto mm = [&](int ks, const u32x4& b) {
#pragma unroll
      for (int nt = 0; nt < 2; ++nt) acc[nt] = MFMA32(fa[ks & 1][nt], __builtin_bit_cast(bf16x8, b), acc[nt]);
    };
    auto hbl = [&](int i) { bld16_sc1(hb[i], hvo, srdS, S_H3 + (14u + i) * 2048u); };
    ld(0);
#pragma unroll
    for (int ks = 0; ks < 8; ++ks) {
      if (ks < 7) ld(ks + 1);
      SBAR(); mm(ks, ha[ks]);
      if (ks < 7) { hbl(2 * ks); hbl(2 * ks + 1); }
      SBAR();
    }
    BAR_L();
    ld(8);
#pragma unroll
    for (int ks = 8; ks < 14; ++ks) {
      if (ks < 13) ld(ks + 1);
      SBAR(); mm(ks, ha[ks]);
      if (ks < 12) { w4dma(20 + 2 * (ks - 8)); w4dma(21 + 2 * (ks - 8)); }
      SBAR();
    }
    asm volatile("s_waitcnt vmcnt(8) lgkmcnt(0)\n\ts_barrier" : "+v"(hb[0]), "+v"(hb[1]), "+v"(hb[2]), "+v"(hb[3]), "+v"(hb[4]), "+v"(hb[5]),
                 "+v"(hb[6]), "+v"(hb[7]), "+v"(hb[8]), "+v"(hb[9]), "+v"(hb[10]), "+v"(hb[11]), "+v"(hb[12]), "+v"(hb[13]) :: "memory");
    if (has_sent(hb, 14)) poll_load<14>(hb, srdS, hvo, S_H3 + 14u * 2048u, 2048u);
    STAMP(2, 12);
    ld(14);
#pragma unroll
    for (int ks = 14; ks < 19; ++ks) { ld(ks + 1); SBAR(); mm(ks, hb[ks - 14]); SBAR(); }
    mm(19, hb[5]);
    WAIT_BAR(0);
    ld(20);
#pragma unroll
    for (int ks = 20; ks < 27; ++ks) { ld(ks + 1); SBAR(); mm(ks, hb[ks - 14]); SBAR(); }
    mm(27, hb[13]);
    BAR_L();
    STAMP(2, 13);
    red4_put(acc[0], 0, lds + LA, rt, kq, l);
    red4_put(acc[1], 1, lds + LA, rt, kq, l);
    BAR_L();
    red4_get(v, lds + LA, rt, kq, l);
#pragma unroll
    for (int k = 0; k < 8; ++k) v[k] = __builtin_fmaf(gb[k], v[k], y[k]);
    P.out[(size_t)trow * 64 + och] = (u32x4){cvtpk(v[0], v[1]), cvtpk(v[2], v[3]), cvtpk(v[4], v[5]), cvtpk(v[6], v[7])};
  }
  STAMP(2, 14);
}

__global__ __launch_bounds__(512, 2) void k_fused(Params P) {
  extern __shared__ __attribute__((aligned(16))) char smem[];
  const lds_ptr lds = (lds_ptr)smem;
  const unsigned lds0 = (unsigned)(uintptr_t)smem;
  const unsigned w = rfl(TIDX >> 6);
  const __amdgpu_buffer_rsrc_t srdW = mksrd(P.ws, W_END);
  {
    unsigned long long pc; unsigned len;
    asm volatile("s_getpc_b64 %0\n.Lkf_here:\n\ts_mov_b32 %1, .Lkf_end-.Lkf_here" : "=s"(pc), "=s"(len));
    dma16(lds0 + LDS_R + w * RINGB + 8192u, (TIDX & 63) * 16u, mksrd((const void*)(pc & ~15ull), len & ~15u), ((BIDX >> 3) * 8 + w) * 1024u);
  }
  const unsigned tag = attn_part(P, lds, lds0, w, srdW);
  mlp_part(P, lds, lds0, w, srdW, tag);
  asm volatile(".Lkf_end:");
}

#if defined(__HIP_DEVICE_COMPILE__)
#pragma clang attribute pop
#endif
extern "C" void kernel_launch(void* const* d_in, const int* in_sizes, int n_in, void* d_out, int out_size, void* d_ws,
                              size_t ws_size, hipStream_t stream) {
  Params P;
  P.x = (const unsigned short*)d_in[0];
  P.kc = (const unsigned short*)d_in[1];
  P.vc = (const unsigned short*)d_in[2];
  P.wq = (const unsigned short*)d_in[3];
  P.wo = (const unsigned short*)d_in[4];
  P.w1 = (const unsigned short*)d_in[6];
  P.w2 = (const unsigned short*)d_in[7];
  P.w3 = (const unsigned short*)d_in[8];
  P.w4 = (const unsigned short*)d_in[9];
  P.c = (const unsigned short*)d_in[10];
  P.wmod = (const unsigned short*)d_in[11];
  P.ws = d_ws;
  P.out = (u32x4*)d_out;
  (void)hipFuncSetAttribute((const void*)k_fused, hipFuncAttributeMaxDynamicSharedMemorySize, LDS_TOTAL);
  k_fused<<<256, 512, LDS_TOTAL, stream>>>(P);
}
```

```cpp
#include <hip/hip_runtime.h>
#include <hip/hip_bf16.h>
#include <stdint.h>

#if defined(__HIP_DEVICE_COMPILE__)
#pragma clang attribute push(__attribute__((target("no-packed-fp32-ops"))), apply_to = function)
#endif
typedef short bf16x8 __attribute__((ext_vector_type(8)));
typedef short s16x4 __attribute__((ext_vector_type(4)));
typedef float f32x16 __attribute__((ext_vector_type(16)));
typedef float f32x4 __attribute__((ext_vector_type(4)));
typedef float f32x2 __attribute__((ext_vector_type(2)));
typedef unsigned u32x4 __attribute__((ext_vector_type(4)));
typedef unsigned u32x2 __attribute__((ext_vector_type(2)));
typedef __bf16 bf16x2_t __attribute__((ext_vector_type(2)));
typedef short v4i16_t __attribute__((ext_vector_type(4)));
#define LAS __attribute__((address_space(3)))
typedef LAS const char* lds_cptr;
typedef LAS char* lds_ptr;
#define DI __device__ __forceinline__
#define F2U(x) __builtin_bit_cast(unsigned, (float)(x))
#define U2F(x) __builtin_bit_cast(float, (unsigned)(x))
#define F2I(x) __builtin_bit_cast(int, (float)(x))
#define I2F(x) __builtin_bit_cast(float, (int)(x))
#define TIDX __builtin_amdgcn_workitem_id_x()
#define BIDX __builtin_amdgcn_workgroup_id_x()
#define ANY(p) (__builtin_amdgcn_ballot_w64(p) != 0ull)
#define SBAR() __builtin_amdgcn_sched_barrier(0)
#define MFMA32(a, b, c) __builtin_amdgcn_mfma_f32_32x32x16_bf16((a), (b), (c), 0, 0, 0)
#define WAIT_V(n) asm volatile("s_waitcnt vmcnt(" #n ")" ::: "memory")
#define WAIT_L0() asm volatile("s_waitcnt lgkmcnt(0)" ::: "memory")
#define BAR_L() asm volatile("s_waitcnt lgkmcnt(0)\n\ts_barrier" ::: "memory")
#define WAIT_BAR(n) asm volatile("s_waitcnt vmcnt(" #n ") lgkmcnt(0)\n\ts_barrier" ::: "memory")

constexpr int T = 2048, D = 512, HKV = 8, SK = 2048, DH = 128, WIN = 512, F = 1792;
constexpr float LN_EPS = 1e-5f;
constexpr float QSCALE = 0.088388347648318440f * 1.4426950408889634f;

constexpr unsigned W_EPOCH = 0;
constexpr unsigned W_FLAG = 4096;
constexpr unsigned W_MOD = 32768;
constexpr unsigned W_MP = W_MOD + 40960;
constexpr unsigned W_P1 = 393216;
constexpr unsigned S_U = W_P1 + 8u * 64u * T * 16u;
constexpr unsigned S_H1 = S_U + 64u * T * 16u;
constexpr unsigned S_X1 = S_H1 + 64u * T * 16u;
constexpr unsigned S_HZ = S_X1 + 64u * T * 16u;
constexpr unsigned S_H3 = S_HZ + 256u * T * 16u;
constexpr unsigned W_END = S_H3 + 224u * T * 16u;
static_assert(W_MP + 8u * 4864u * 8u <= W_P1, "ws");
constexpr unsigned OOB = 0xF0000000u;
#define K1MAP 0
#define FLAG_SLEEP 4
#define GSPLIT 2
#define ATHR 32.0f
constexpr float THR = ATHR;
constexpr unsigned SENT = 0xFFFFFFFFu;
constexpr unsigned SPIN_CAP = 1u << 16;

constexpr int LDS_X = 0;
constexpr int LDS_R = 65536;
constexpr int RING = 5, RINGB = RING * 2048;
constexpr int LDS_D = LDS_R + 8 * RINGB;
constexpr int LDS_TOTAL = LDS_D + 16384;
static_assert(LDS_TOTAL == 163840, "lds");

#define STAMP(kid, i) do {} while (0)
DI unsigned cvtpk(float lo, float hi) {
  f32x2 v = {lo, hi};
  bf16x2_t b = __builtin_convertvector(v, bf16x2_t);
  return __builtin_bit_cast(unsigned, b);
}
DI float bflo(unsigned u) { return U2F(u << 16); }
DI float bfhi(unsigned u) { return U2F(u & 0xffff0000u); }
DI unsigned rfl(unsigned x) { return __builtin_amdgcn_readfirstlane(x); }
DI __amdgpu_buffer_rsrc_t mksrd(const void* p, unsigned bytes) {
  return __builtin_amdgcn_make_buffer_rsrc((void*)p, (short)0, (int)bytes, 0x00020000);
}
DI void dma16(unsigned lds, unsigned voff, __amdgpu_buffer_rsrc_t srd, unsigned soff) {
  asm volatile("s_mov_b32 m0, %0\n\ts_nop 0\n\tbuffer_load_dwordx4 %1, %2, %3 offen lds"
               :: "s"(lds), "v"(voff), "s"(srd), "s"(soff) : "memory");
}
DI void gld16(u32x4& d, const void* p) { asm volatile("global_load_dwordx4 %0, %1, off" : "=v"(d) : "v"(p) : "memory"); }
DI void bld16_sc1(u32x4& d, unsigned voff, __amdgpu_buffer_rsrc_t srd, unsigned soff) {
  asm volatile("buffer_load_dwordx4 %0, %1, %2, %3 offen sc1" : "=v"(d) : "v"(voff), "s"(srd), "s"(soff) : "memory");
}
DI void dma16_sc1(unsigned lds, unsigned voff, __amdgpu_buffer_rsrc_t srd, unsigned soff) {
  asm volatile("s_mov_b32 m0, %0\n\ts_nop 0\n\tbuffer_load_dwordx4 %1, %2, %3 offen sc1 lds"
               :: "s"(lds), "v"(voff), "s"(srd), "s"(soff) : "memory");
}
DI bf16x8 lds_b128(lds_cptr p) { return *(const LAS bf16x8*)p; }
DI s16x4 lds_tr(lds_cptr p) {
  return __builtin_bit_cast(s16x4, __builtin_amdgcn_ds_read_tr16_b64_v4i16((LAS v4i16_t*)p));
}
DI bf16x8 cat8(s16x4 lo, s16x4 hi) { return __builtin_shufflevector(lo, hi, 0, 1, 2, 3, 4, 5, 6, 7); }
DI float swap32_add(float x) {
  auto r = __builtin_amdgcn_permlane32_swap(F2U(x), F2U(x), false, false);
  return U2F(r[0]) + U2F(r[1]);
}
DI float swap32_max(float x) {
  auto r = __builtin_amdgcn_permlane32_swap(F2U(x), F2U(x), false, false);
  return __builtin_fmaxf(U2F(r[0]), U2F(r[1]));
}
template <int CTRL> DI float dpp_add(float x) {
  int y = __builtin_amdgcn_update_dpp(0, F2I(x), CTRL, 0xF, 0xF, true);
  return x + I2F(y);
}
DI float max3f(float a, float b, float c) { float r; asm("v_max3_f32 %0, %1, %2, %3" : "=v"(r) : "v"(a), "v"(b), "v"(c)); return r; }
DI float max2f(float a, float b) { float r; asm("v_max_f32_e32 %0, %1, %2" : "=v"(r) : "v"(a), "v"(b)); return r; }
DI float dot2bf(unsigned a, unsigned b, float c) {
  return __builtin_amdgcn_fdot2_f32_bf16(__builtin_bit_cast(bf16x2_t, a), __builtin_bit_cast(bf16x2_t, b), c, false);
}
#define PIN(x) asm volatile("" : "+v"(x))
DI float swap16_add(float x) {
  auto r = __builtin_amdgcn_permlane16_swap(F2U(x), F2U(x), false, false);
  return U2F(r[0]) + U2F(r[1]);
}
DI float sum_hi3(float x) { return swap32_add(swap16_add(dpp_add<0x128>(x))); }
DI float gelu_tanh(float x) {
  const float k0 = -2.0f * 0.7978845608028654f * 1.4426950408889634f;
  const float t = x * x;
  const float z = x * __builtin_fmaf(t, 0.044715f * k0, k0);
  const float e = __builtin_amdgcn_exp2f(z);
  return x * __builtin_amdgcn_rcpf(1.0f + e);
}
DI void wave_sum8(const float (&a)[8], float (&out)[8]) {
  float b[4], c[2];
#pragma unroll
  for (int i = 0; i < 4; ++i) {
    auto r = __builtin_amdgcn_permlane32_swap(F2U(a[i]), F2U(a[i + 4]), false, false);
    b[i] = U2F(r[0]) + U2F(r[1]);
  }
#pragma unroll
  for (int i = 0; i < 2; ++i) {
    auto r = __builtin_amdgcn_permlane16_swap(F2U(b[i]), F2U(b[i + 2]), false, false);
    c[i] = U2F(r[0]) + U2F(r[1]);
  }
#pragma unroll
  for (int i = 0; i < 2; ++i) {
    c[i] = dpp_add<0xB1>(c[i]);
    c[i] = dpp_add<0x4E>(c[i]);
    c[i] = dpp_add<0x141>(c[i]);
    c[i] = dpp_add<0x140>(c[i]);
  }
#pragma unroll
  for (int q = 0; q < 4; ++q)
#pragma unroll
    for (int i = 0; i < 2; ++i)
      out[i + 2 * (q & 1) + 4 * (q >> 1)] = I2F(__builtin_amdgcn_readlane(F2I(c[i]), 16 * q));
}

DI unsigned nosent(unsigned v) { return v < 0xFFFFFFFEu ? v : 0xFFFFFFFEu; }
DI void pub16(__amdgpu_buffer_rsrc_t srd, unsigned voff, unsigned soff, u32x4 v) {
  v = (u32x4){nosent(v[0]), nosent(v[1]), nosent(v[2]), nosent(v[3])};
  __builtin_amdgcn_raw_buffer_store_b128(v, srd, voff + soff, 0, 16);
}
DI unsigned umax3(unsigned a, unsigned b, unsigned c) { const unsigned m = a > b ? a : b; return m > c ? m : c; }
DI bool has_sent(const u32x4* v, int n) {
  unsigned m = 0;
#pragma unroll
  for (int i = 0; i < n; ++i) { m = umax3(m, v[i][0], v[i][1]); m = umax3(m, v[i][2], v[i][3]); }
  return ANY(m == SENT);
}
template <int N> DI void poll_load(u32x4* v, __amdgpu_buffer_rsrc_t srd, unsigned voff, unsigned soff0, unsigned step) {
  for (unsigned spin = 0;; ++spin) {
    asm volatile("" ::: "memory");
#pragma unroll
    for (int i = 0; i < N; ++i) v[i] = __builtin_amdgcn_raw_buffer_load_b128(srd, voff, soff0 + (unsigned)i * step, 16);
    if (!has_sent(v, N) || spin > SPIN_CAP) break;
    __builtin_amdgcn_s_sleep(1);
  }
}
template <int N, int I0, int I1> DI void poll_ind(u32x4* v, __amdgpu_buffer_rsrc_t srd, unsigned voff, unsigned soff0, unsigned step) {
#pragma unroll
  for (int i = 0; i < N; ++i) v[i] = __builtin_amdgcn_raw_buffer_load_b128(srd, voff, soff0 + (unsigned)i * step, 16);
  if (has_sent(v, N)) {
    u32x4 ind[2];
    poll_load<2>(ind, srd, voff, soff0 + (unsigned)I0 * step, (unsigned)(I1 - I0) * step);
    poll_load<N>(v, srd, voff, soff0, step);
  }
}

DI bool bad_tag(const u32x4* v, int n, unsigned tag) {
  unsigned m = 0;
#pragma unroll
  for (int i = 0; i < n; ++i) m |= (v[i][1] ^ tag) | (v[i][3] ^ tag);
  return m != 0;
}
template <int N> DI void poll_tag(u32x4* v, __amdgpu_buffer_rsrc_t srd, unsigned voff, unsigned soff0, unsigned step, unsigned tag) {
  for (unsigned spin = 0;; ++spin) {
    asm volatile("" ::: "memory");
#pragma unroll
    for (int i = 0; i < N; ++i) v[i] = __builtin_amdgcn_raw_buffer_load_b128(srd, voff, soff0 + (unsigned)i * step, 16);
    if (!ANY(voff < OOB && bad_tag(v, N, tag)) || spin > SPIN_CAP) break;
    __builtin_amdgcn_s_sleep(1);
  }
}

struct LaneC {
  unsigned lane, r, h, q, p, blk;
  unsigned wv;
  unsigned afr[2];
  unsigned xb[4];
};
DI void lane_consts(LaneC& c) {
  unsigned l = TIDX & 63;
  asm volatile("" : "+v"(l));
  c.lane = l; c.r = l & 31; c.h = l >> 5; c.q = (l & 15) >> 2; c.p = l & 3; c.blk = (l >> 4) & 1;
  const unsigned sp = 2 * (c.p & 1) + (c.p >> 1);
#pragma unroll
  for (int nt = 0; nt < 2; ++nt) c.afr[nt] = (8 * c.h + c.q) * 128 + 64 * (nt ^ (c.q >> 1)) + 32 * c.blk + 8 * sp;
#pragma unroll
  for (int s = 0; s < 4; ++s) c.xb[s] = 1024 * c.h + ((c.r ^ (2 * s + c.h)) << 4);
}
DI unsigned wdma_voff(unsigned l, unsigned ld) { return (l >> 3) * ld + (((l & 7) ^ (((l >> 4) & 1) << 2)) << 4); }

DI void wring_issue(int ks, unsigned ring, unsigned voff, __amdgpu_buffer_rsrc_t srd, unsigned soff0, unsigned ld) {
  const unsigned slot = ring + (unsigned)(ks % RING) * 2048u;
  dma16(slot, voff, srd, soff0 + (unsigned)(16 * ks) * ld);
  dma16(slot + 1024u, voff ^ 32u, srd, soff0 + (unsigned)(16 * ks + 8) * ld);
}
template <int N> DI void wait_vm() {
  if constexpr (N == 0) WAIT_V(0); else if constexpr (N == 1) WAIT_V(1); else if constexpr (N == 2) WAIT_V(2);
  else if constexpr (N == 3) WAIT_V(3); else if constexpr (N == 4) WAIT_V(4); else if constexpr (N == 5) WAIT_V(5);
  else if constexpr (N == 6) WAIT_V(6); else if constexpr (N == 7) WAIT_V(7); else if constexpr (N == 8) WAIT_V(8);
  else if constexpr (N == 9) WAIT_V(9); else if constexpr (N == 10) WAIT_V(10); else if constexpr (N == 11) WAIT_V(11);
  else if constexpr (N == 12) WAIT_V(12); else if constexpr (N == 13) WAIT_V(13); else if constexpr (N == 14) WAIT_V(14);
  else if constexpr (N == 15) WAIT_V(15); else if constexpr (N == 16) WAIT_V(16);
  else if constexpr (N == 17) WAIT_V(17); else if constexpr (N == 18) WAIT_V(18);
  else static_assert(N < 0, "wait_vm");
}

#define MFMA16(a, b, c) __builtin_amdgcn_mfma_f32_16x16x32_bf16((a), (b), (c), 0, 0, 0)
struct G16 { unsigned a[4], hi, x[2]; };
DI void g16_consts(G16& k, unsigned l, unsigned ringoff) {
  const unsigned i = l & 15, g = l >> 4, q = i >> 2, p = l & 3, x = 2 * (q >> 1) + (g & 1);
#pragma unroll
  for (int m = 0; m < 4; ++m) k.a[m] = ringoff + (8 * (g & 1) + q) * 128 + ((2 * (m ^ x) + (p >> 1)) << 4) + 8 * (p & 1);
  k.hi = g >> 1;
#pragma unroll
  for (int e = 0; e < 2; ++e) k.x[e] = LDS_X + 1024 * g + ((i ^ (4 * e + g)) << 4);
}
struct Frag { bf16x8 a[4], b[4]; };
template <int KK> DI void load_frags(Frag& f, lds_cptr lds, const G16& k) {
  constexpr int sA = (2 * KK) % RING;
  const unsigned d = sA == RING - 1 ? (1u - k.hi) * (unsigned)((RING - 1) * 2048) : k.hi * 2048u;
  constexpr int o = sA == RING - 1 ? 0 : sA * 2048;
#pragma unroll
  for (int m = 0; m < 4; ++m) f.a[m] = cat8(lds_tr(lds + (k.a[m] + d) + o), lds_tr(lds + (k.a[m] + d) + o + 512));
#pragma unroll
  for (int r = 0; r < 4; ++r) f.b[r] = lds_b128(lds + k.x[KK & 1] + 4096 * KK + 256 * r);
}
template <int KK, int EXTRA>
DI void gemm_step(f32x4 (&acc)[4][4], Frag& cur, Frag& nxt, lds_cptr lds, unsigned ring, unsigned voff,
                  __amdgpu_buffer_rsrc_t srd, unsigned soff0, unsigned ld, const G16& k) {
  if constexpr (2 * KK + RING < 32) {
    asm volatile("" :: "v"(cur.a[0]), "v"(cur.a[1]), "v"(cur.a[2]), "v"(cur.a[3]));
    wring_issue(2 * KK + RING, ring, voff, srd, soff0, ld);
    if constexpr (2 * KK + RING + 1 < 32) wring_issue(2 * KK + RING + 1, ring, voff, srd, soff0, ld);
  }
  SBAR();
#pragma unroll
  for (int m = 0; m < GSPLIT; ++m)
#pragma unroll
    for (int r = 0; r < 4; ++r) acc[m][r] = MFMA16(cur.a[m], cur.b[r], acc[m][r]);
  SBAR();
  if constexpr (KK + 1 < 16) {
    constexpr int last = 2 * KK + RING + 1 < 32 ? 2 * KK + RING + 1 : 31;
    wait_vm<2 * (last - (2 * KK + 3)) + ((2 * KK + 3 < RING) ? EXTRA : 0)>();
    load_frags<KK + 1>(nxt, lds, k);
  }
  SBAR();
#pragma unroll
  for (int m = GSPLIT; m < 4; ++m)
#pragma unroll
    for (int r = 0; r < 4; ++r) acc[m][r] = MFMA16(cur.a[m], cur.b[r], acc[m][r]);
  SBAR();
  if constexpr (KK + 1 < 16) gemm_step<KK + 1, EXTRA>(acc, nxt, cur, lds, ring, voff, srd, soff0, ld, k);
}
template <int EXTRA>
DI void gemm_phase(f32x4 (&acc)[4][4], lds_cptr lds, unsigned ringoff, unsigned ring, unsigned voff,
                   __amdgpu_buffer_rsrc_t srd, unsigned soff0, unsigned ld, unsigned l) {
  G16 k; g16_consts(k, l, ringoff);
  Frag f0, f1;
  wait_vm<2 * (RING - 2) + EXTRA>();
  load_frags<0>(f0, lds, k);
  gemm_step<0, EXTRA>(acc, f0, f1, lds, ring, voff, srd, soff0, ld, k);
}
DI u32x4 pack16(const f32x4& a, const f32x4& b, float sc) {
  auto r0 = __builtin_amdgcn_permlane16_swap(cvtpk(a[0] * sc, a[1] * sc), cvtpk(b[0] * sc, b[1] * sc), false, false);
  auto r1 = __builtin_amdgcn_permlane16_swap(cvtpk(a[2] * sc, a[3] * sc), cvtpk(b[2] * sc, b[3] * sc), false, false);
  return (u32x4){r0[0], r1[0], r0[1], r1[1]};
}
DI u32x4 pack8(const f32x16& a, int s, float sc) {
  return (u32x4){cvtpk(a[8 * s] * sc, a[8 * s + 1] * sc), cvtpk(a[8 * s + 2] * sc, a[8 * s + 3] * sc),
                 cvtpk(a[8 * s + 4] * sc, a[8 * s + 5] * sc), cvtpk(a[8 * s + 6] * sc, a[8 * s + 7] * sc)};
}
DI unsigned ximg(unsigned chunk, unsigned row) { return chunk * 1024u + ((row ^ (chunk & 7u)) << 4); }

constexpr int KSLOT[8] = {LDS_D, LDS_R, LDS_R + 32768, LDS_X, LDS_X + 32768, LDS_D, LDS_X, LDS_X + 32768};
constexpr int VSLOT[8] = {LDS_R + 16384, LDS_R + 49152, LDS_X + 16384, LDS_X + 49152, LDS_R + 65536, LDS_X + 16384, LDS_X + 49152, LDS_D};
template <int N> struct IC { static constexpr int value = N; };
struct Params {
  const unsigned short *x, *kc, *vc, *wq, *wo, *w1, *w2, *w3, *w4, *c, *wmod;
  void* ws;
  u32x4* out;
};
DI unsigned attn_part(const Params& P, lds_ptr lds, unsigned lds0, unsigned w, __amdgpu_buffer_rsrc_t srdW) {
  LaneC c; lane_consts(c);
  const unsigned g = BIDX & 7, pn = BIDX >> 3;
  const unsigned l = c.lane, tid = TIDX;
  const unsigned ringoff = LDS_R + w * RINGB, ring = lds0 + ringoff;
  STAMP(1, 0);

  unsigned ep;
  asm volatile("buffer_load_dword %0, off, %1, 0 sc1" : "=v"(ep) : "s"(srdW) : "memory");
  u32x4 jw[4]; unsigned jc[4];
  auto c_load = [&](unsigned jks) __attribute__((always_inline)) {
#pragma unroll
    for (int i = 0; i < 4; ++i) asm volatile("global_load_ushort %0, %1, off" : "=v"(jc[i]) : "v"(P.c + jks * 64 + (l >> 2) + 16 * i) : "memory");
  };
  auto job_run = [&](unsigned jcol, unsigned jks, unsigned tag) __attribute__((always_inline)) {
    float acc[8] = {0, 0, 0, 0, 0, 0, 0, 0};
#pragma unroll
    for (int i = 0; i < 4; ++i) {
      const float v = U2F(jc[i] << 16);
      const float sv = v * __builtin_amdgcn_rcpf(1.0f + __builtin_amdgcn_exp2f(-1.4426950408889634f * v));
      const float sc = jcol >= 96u ? 1.0f : sv;
#pragma unroll
      for (int k = 0; k < 4; ++k) {
        acc[2 * k] = __builtin_fmaf(sc, bflo(jw[i][k]), acc[2 * k]);
        acc[2 * k + 1] = __builtin_fmaf(sc, bfhi(jw[i][k]), acc[2 * k + 1]);
      }
    }
#pragma unroll
    for (int k = 0; k < 8; ++k) acc[k] = sum_hi3(dpp_add<0x124>(acc[k]));
    const unsigned e = (l >> 2) & 3;
    const float v0 = e == 0 ? acc[0] : e == 1 ? acc[2] : e == 2 ? acc[4] : acc[6], v1 = e == 0 ? acc[1] : e == 1 ? acc[3] : e == 2 ? acc[5] : acc[7];
    __builtin_amdgcn_raw_buffer_store_b128((u32x4){F2U(v0), tag, F2U(v1), tag}, srdW,
                                           l < 16 ? W_MP + (jks * 4864u + jcol * 32u + (l & 3) * 8u + e * 2u) * 8u : OOB, 0, 16);
  };
  u32x4 xr[8];
  auto x_load = [&]() __attribute__((always_inline)) {
#pragma unroll
    for (int i = 0; i < 8; ++i) gld16(xr[i], P.x + (size_t)(pn * 64 + w * 8 + i) * D + l * 8);
  };
  if (w == 0) {
#pragma unroll
    for (int i = 0; i < 4; ++i) gld16(jw[i], P.wmod + (BIDX & 31u) * 32 + (l & 3) * 8 + (size_t)((BIDX >> 5) * 64 + (l >> 2) + 16 * i) * 3072);
    c_load(BIDX >> 5);
  } else x_load();
  asm volatile("s_waitcnt vmcnt(8)\n\ts_barrier" : "+v"(ep) :: "memory");
  const unsigned tag = rfl(ep) + 1u;
  if (w == 0) {
    asm volatile("s_waitcnt vmcnt(0)" : "+v"(jw[0]), "+v"(jw[1]), "+v"(jw[2]), "+v"(jw[3]), "+v"(jc[0]), "+v"(jc[1]), "+v"(jc[2]), "+v"(jc[3]) :: "memory");
    job_run(BIDX & 31u, BIDX >> 5, tag); x_load();
  }
  const unsigned jid = w * 256u + BIDX, jcol2 = 32u + jid % 120u, jks2 = jid / 120u;
  const bool job2 = w < 4 && jid < 960u;
  const __amdgpu_buffer_rsrc_t srdQ = mksrd(P.wq, 512u * 4096u * 2u);
  const unsigned vq = wdma_voff(l, 8192u), sq0 = (g * 512u + w * 64u) * 2u;
  const __amdgpu_buffer_rsrc_t srdK = mksrd(P.kc + ((size_t)g * SK + (SK - WIN)) * DH, WIN * DH * 2u);
  const __amdgpu_buffer_rsrc_t srdV = mksrd(P.vc + ((size_t)g * SK + (SK - WIN)) * DH, WIN * DH * 2u);
  const unsigned vk = ((l >> 2) & 7) * 256u + (l >> 5) * 64u + (((l & 3) ^ (2 * (w & 1) + ((l >> 4) & 1))) << 4);
  const unsigned vv = wdma_voff(l, 256u);
  auto dma_k = [&](int t, unsigned slot) {
    dma16(lds0 + slot + 2048u * w, vk, srdK, (unsigned)t * 16384u + 2048u * w);
    dma16(lds0 + slot + 2048u * w + 1024u, vk, srdK, (unsigned)t * 16384u + 2048u * w + 128u);
  };
  auto dma_v = [&](int t, unsigned slot) {
    dma16(lds0 + slot + 1024u * w, vv, srdV, (unsigned)t * 16384u + 2048u * w);
    dma16(lds0 + slot + 8192u + 1024u * w, vv, srdV, (unsigned)t * 16384u + 2048u * w + 128u);
  };

  {
    asm volatile("s_waitcnt vmcnt(0)" : "+v"(xr[0]), "+v"(xr[1]), "+v"(xr[2]), "+v"(xr[3]), "+v"(xr[4]), "+v"(xr[5]), "+v"(xr[6]),
                 "+v"(xr[7]) :: "memory");
    dma16(ring + 8192u, l * 16u, srdQ, (2 * (pn * 8 + w)) * 8192u + g * 1024u);
    dma16(ring + 9216u, l * 16u, srdQ, (2 * (pn * 8 + w) + 1) * 8192u + g * 1024u);
    wring_issue(0, ring, vq, srdQ, sq0, 8192u);
    wring_issue(1, ring, vq, srdQ, sq0, 8192u);
    float v[8][8], s[8], m[8];
#pragma unroll
    for (int i = 0; i < 8; ++i) {
#pragma unroll
      for (int j = 0; j < 4; ++j) { v[i][2 * j] = bflo(xr[i][j]); v[i][2 * j + 1] = bfhi(xr[i][j]); }
      s[i] = ((v[i][0] + v[i][1]) + (v[i][2] + v[i][3])) + ((v[i][4] + v[i][5]) + (v[i][6] + v[i][7]));
    }
    wave_sum8(s, m);
#pragma unroll
    for (int i = 0; i < 8; ++i) {
      const float mu = m[i] * (1.0f / D);
      float q = 0.f;
#pragma unroll
      for (int j = 0; j < 8; ++j) { v[i][j] -= mu; q = __builtin_fmaf(v[i][j], v[i][j], q); }
      s[i] = q;
    }
    wave_sum8(s, m);
    float rs[8];
#pragma unroll
    for (int i = 0; i < 8; ++i) rs[i] = __builtin_amdgcn_rsqf(m[i] * (1.0f / D) + LN_EPS);
    STAMP(1, 8);
    {
      u32x4 mp[8];
      poll_tag<8>(mp, srdW, tid * 16u, W_MP, 4864u * 8u, tag);
      f32x2 t;
#pragma unroll
      for (int k = 0; k < 2; ++k)
        t[k] = ((U2F(mp[0][2 * k]) + U2F(mp[1][2 * k])) + (U2F(mp[2][2 * k]) + U2F(mp[3][2 * k]))) +
               ((U2F(mp[4][2 * k]) + U2F(mp[5][2 * k])) + (U2F(mp[6][2 * k]) + U2F(mp[7][2 * k])));
      *(LAS f32x2*)(lds + LDS_D + tid * 8) = t;
    }
    BAR_L();
    if (BIDX == 0 && tid == 0) __builtin_amdgcn_raw_buffer_store_b32(tag, srdW, W_EPOCH, 0, 16);
    u32x4 mo[4];
#pragma unroll
    for (int i = 0; i < 4; ++i) mo[i] = *(const LAS u32x4*)(lds + LDS_D + (i >> 1) * 2048 + l * 32 + (i & 1) * 16);
    STAMP(1, 9);
#pragma unroll
    for (int i = 0; i < 8; ++i) {
      float hh[8];
#pragma unroll
      for (int j = 0; j < 8; ++j) hh[j] = __builtin_fmaf(v[i][j] * rs[i], 1.f + U2F(mo[2 + (j >> 2)][j & 3]), U2F(mo[j >> 2][j & 3]));
      const u32x4 o = {cvtpk(hh[0], hh[1]), cvtpk(hh[2], hh[3]), cvtpk(hh[4], hh[5]), cvtpk(hh[6], hh[7])};
      *(LAS u32x4*)(lds + LDS_X + ximg(l, w * 8 + i)) = o;
      if (i == 1 || i == 3 || i == 5) wring_issue(2 + i / 2, ring, vq, srdQ, sq0, 8192u);
    }
  }
  BAR_L();
  dma_k(0, LDS_D);
  STAMP(1, 1);

  {
    f32x4 acc[4][4] = {};
    gemm_phase<2>(acc, lds, ringoff, ring, vq, srdQ, sq0, 8192u, l);
    STAMP(1, 2);
    WAIT_BAR(0);
    dma_k(1, KSLOT[1]); dma_v(0, VSLOT[0]); dma_k(2, KSLOT[2]); dma_v(1, VSLOT[1]);
    if (job2) {
      const bool isw3 = jcol2 >= 96u;
      const unsigned ldb = isw3 ? 3584u : 6144u;
      const __amdgpu_buffer_rsrc_t srdJ = isw3 ? mksrd(P.w3, 512u * 3584u) : mksrd(P.wmod, 512u * 6144u);
#pragma unroll
      for (int i = 0; i < 4; ++i)
        dma16(lds0 + LDS_R + 65536u + w * 4096u + i * 1024u, (l >> 2) * ldb + (l & 3) * 16u, srdJ, (jks2 * 64 + 16 * i) * ldb + (isw3 ? jcol2 - 96u : jcol2) * 64u);
      c_load(jks2);
    }
    const unsigned cl = 8 * w + 2 * ((l >> 4) & 1) + (l >> 5);
#pragma unroll
    for (int r = 0; r < 4; ++r)
#pragma unroll
      for (int mp = 0; mp < 2; ++mp)
        *(LAS u32x4*)(lds + LDS_X + ximg(cl + 4 * mp, 16 * r + (l & 15))) = pack16(acc[2 * mp][r], acc[2 * mp + 1][r], QSCALE);
    BAR_L();
  }

  STAMP(1, 3);
  const unsigned hh = w >> 1, rt = w & 1;
  bf16x8 qf[8];
#pragma unroll
  for (int s = 0; s < 8; ++s) qf[s] = lds_b128(lds + LDS_X + ximg(16 * hh + 2 * s + c.h, 32 * rt + c.r));
  unsigned kb[2], vb[2];
#pragma unroll
  for (int i = 0; i < 2; ++i) {
    kb[i] = 2048 * (c.r >> 3) + 64 * (c.r & 7) + (((2 * i + c.h) ^ ((c.r >> 2) & 3)) << 4);
    vb[i] = (4 * c.h + c.q) * 128 + 64 * (i ^ (c.q >> 1)) + 32 * c.blk + 8 * (2 * (c.p & 1) + (c.p >> 1));
  }
  const __amdgpu_buffer_rsrc_t srdO = mksrd(P.wo, 4096u * 512u * 2u);
  const unsigned vo = wdma_voff(l, 1024u), so0 = g * 512u * 1024u + w * 128u;

  f32x16 o[4] = {};
  f32x16 s0, s1;
  f32x16 negm;
  float lsum = 0.f;
  u32x4 pk[4];
  constexpr int PD = 6;
  bf16x8 fr[PD];
  const bool lag = w >= 4;
  auto frag = [&](int t, int it) -> bf16x8 {
    if (it < 16) {
      const lds_cptr a = lds + VSLOT[t > 0 ? t - 1 : 0] + vb[it & 1] + 8192 * ((it >> 1) & 1) + 2048 * (it >> 2);
      return cat8(lds_tr(a), lds_tr(a + 1024));
    }
    const int s = (it - 16) >> 1;
    return lds_b128(lds + KSLOT[t < 8 ? t : 0] + kb[s & 1] + 512 * (s >> 1) + 8192 * (it & 1));
  };
  auto seam = [&](int t, int tx) __attribute__((always_inline)) {
    if (t == 7) { BAR_L(); } else if (tx >= 1 && tx <= 5) { WAIT_BAR(4); } else if (tx == 6) { WAIT_BAR(2); } else { WAIT_BAR(0); }
  };
  auto refill = [&](int t) __attribute__((always_inline)) {
    if (t + 3 < 8) dma_k(t + 3, KSLOT[t + 3 < 8 ? t + 3 : 0]);
    if (t + 2 < 8) dma_v(t + 2, VSLOT[t + 2 < 8 ? t + 2 : 0]);
    if (t == 6) {
#pragma unroll
      for (int ks = 0; ks < RING; ++ks) wring_issue(ks, ring, vo, srdO, so0, 1024u);
    }
  };
  auto extra = [&](int e) __attribute__((always_inline)) {
    const unsigned dump = lds0 + LDS_R + w * 3072u + (unsigned)(e % 3) * 1024u;
    const unsigned id = (BIDX >> 3) * 8 + w, jh = BIDX & 1;
    const unsigned vrow = (l >> 5) * 1024u + (l & 31) * 16u + jh * 512u;
    auto w3v = [&](unsigned piece) { const unsigned byte = piece * 1024u + l * 16u, row = byte / 1792u;
                                     return row * 3584u + jh * 1792u + (byte - row * 1792u); };
    if (e < 4 || e >= 15) {
      const int i = e < 4 ? e : e - 11;
      const unsigned off = i < 3 ? S_U + (unsigned)i * (64u * T * 16u) + pn * 65536u + g * 8192u
                         : i < 7 ? S_HZ + pn * 262144u + g * 32768u + (unsigned)(i - 3) * 8192u
                                 : S_H3 + pn * 229376u + g * 28672u + (unsigned)(i - 7) * 8192u;
      const u32x4 sv = {SENT, SENT, SENT, SENT};
      __builtin_amdgcn_raw_buffer_store_b128(sv, srdW, (i == 10 && tid >= 256) ? OOB : tid * 16u + off, 0, 16);
    } else if (e < 6) dma16(dump, l * 16u, srdO, (g * 512u + (pn * 8 + w) * 2 + (e - 4)) * 1024u);
    else if (e == 6) dma16(dump, vrow, mksrd(P.w1, 512u * 1024u), id * 2048u);
    else if (e == 7) dma16(dump, vrow, mksrd(P.w2, 512u * 1024u), id * 2048u);
    else if (e < 11) dma16(dump, w3v(id + 256u * (e - 8)), mksrd(P.w3, 512u * 3584u), 0u);
    else if (e == 11) {
      if (id < 128) dma16(dump, w3v(id + 768u), mksrd(P.w3, 512u * 3584u), 0u);
      else dma16(dump, vrow, mksrd(P.w4, 1792u * 1024u), (id - 128u + 768u) * 2048u);
    } else dma16(dump, vrow, mksrd(P.w4, 1792u * 1024u), (id + 256u * (e - 12)) * 2048u);
  };
  auto step = [&](auto tc) __attribute__((always_inline)) {
    constexpr int t = decltype(tc)::value;
    constexpr int i0 = t == 0 ? 16 : 0, i1 = t == 8 ? 16 : 32;
    if (t <= 1) {
#pragma unroll
      for (int it = i0; it < i0 + PD; ++it) fr[it % PD] = frag(t, it);
    }
    if (t == 0) { s0 = (f32x16){}; s1 = (f32x16){}; } else if (t < 8) { s0 = negm; s1 = negm; }
    __builtin_amdgcn_s_setprio(1);
    SBAR();
#pragma unroll
    for (int it = i0; it < i1; ++it) {
      if (it < 16) o[it & 3] = MFMA32(fr[it % PD], __builtin_bit_cast(bf16x8, pk[it >> 2]), o[it & 3]);
      else if (it & 1) s1 = MFMA32(fr[it % PD], qf[(it - 16) >> 1], s1);
      else s0 = MFMA32(fr[it % PD], qf[(it - 16) >> 1], s0);
      if (it + PD < i1) fr[it % PD] = frag(t, it + PD);
      if (t >= 1 && it == 1) { if (!lag) refill(t - 1); }
      SBAR();
    }
    if constexpr (t == 8) return;
    __builtin_amdgcn_s_setprio(0);
    if (lag) { seam(t, t - 1); refill(t); }
    asm volatile("s_nop 15" : "+v"(s0), "+v"(s1));
    float ta = max3f(s0[0], s0[1], s1[0]), tb = max3f(s0[2], s0[3], s1[1]);
    ta = max3f(ta, s1[2], s1[3]);
#pragma unroll
    for (int i = 4; i < 16; i += 4) {
      ta = max3f(ta, s0[i], s0[i + 1]); tb = max3f(tb, s0[i + 2], s0[i + 3]);
      ta = max3f(ta, s1[i], s1[i + 1]); tb = max3f(tb, s1[i + 2], s1[i + 3]);
    }
    float tm = max2f(ta, tb);
    if (t == 0 || __builtin_expect(rfl(ANY(tm > THR)), 0)) {
      {
        auto r = __builtin_amdgcn_permlane32_swap(F2U(tm), F2U(tm), false, false);
        tm = max2f(U2F(r[0]), U2F(r[1]));
      }
      const float ndl = t == 0 ? -tm : -max2f(tm, 0.f);
      const float aone = c.h ? 0.f : 1.f;
      if (t == 0) negm = (f32x16){};
      __builtin_amdgcn_s_setprio(2);
      s0 = __builtin_amdgcn_mfma_f32_32x32x2f32(aone, ndl, s0, 0, 0, 0);
      s1 = __builtin_amdgcn_mfma_f32_32x32x2f32(aone, ndl, s1, 0, 0, 0);
      negm = __builtin_amdgcn_mfma_f32_32x32x2f32(aone, ndl, negm, 0, 0, 0);
      __builtin_amdgcn_s_setprio(0);
      if (t > 0) {
        const float al = __builtin_amdgcn_exp2f(ndl);
        lsum *= al;
#pragma unroll
        for (int dt = 0; dt < 4; ++dt)
#pragma unroll
          for (int i = 0; i < 16; ++i) o[dt][i] *= al;
      }
    }
    SBAR();
    if (t >= 1) {
#pragma unroll
      for (int it = 0; it < PD; ++it) fr[it] = frag(t + 1, it);
    }
#pragma unroll
    for (int i = 0; i < 16; ++i) {
      s0[i] = __builtin_amdgcn_exp2f(s0[i]); s1[i] = __builtin_amdgcn_exp2f(s1[i]);
      if (t >= 1 && (i & 3) == 1 && 4 * (t - 1) + (i >> 2) < 22) { PIN(s0[i]); extra(4 * (t - 1) + (i >> 2)); }
    }
    pk[0] = pack8(s0, 0, 1.f); pk[1] = pack8(s0, 1, 1.f); pk[2] = pack8(s1, 0, 1.f); pk[3] = pack8(s1, 1, 1.f);
    {
      float l0 = lsum, l1 = 0.f, l2 = 0.f, l3 = 0.f;
#pragma unroll
      for (int i = 0; i < 16; i += 2) { l0 += s0[i]; l1 += s0[i + 1]; l2 += s1[i]; l3 += s1[i + 1]; }
      lsum = (l0 + l1) + (l2 + l3);
    }
    PIN(pk[0]); PIN(pk[1]); PIN(pk[2]); PIN(pk[3]); PIN(lsum);
    if constexpr (t == 1) {
      if (job2) {
        asm volatile("" : "+v"(jc[0]), "+v"(jc[1]), "+v"(jc[2]), "+v"(jc[3]));
#pragma unroll
        for (int i = 0; i < 4; ++i) jw[i] = *(const LAS u32x4*)(lds + LDS_R + 65536 + w * 4096 + i * 1024 + l * 16);
        job_run(jcol2, jks2, tag);
      }
    }
    if constexpr (t == 4) {
      if (BIDX < 120 && w == 0) {
        u32x4 mp[2];
        poll_tag<2>(mp, srdW, ((l >> 3) * 4864u + (32u + BIDX) * 32u + (l & 7) * 2u) * 8u, W_MP, 128u, tag);
        const float a0 = sum_hi3(U2F(mp[0][0])), a1 = sum_hi3(U2F(mp[0][2])), b0 = sum_hi3(U2F(mp[1][0])), b1 = sum_hi3(U2F(mp[1][2]));
        const bool hi = (l >> 3) & 1;
        __builtin_amdgcn_raw_buffer_store_b128((u32x4){F2U(hi ? b0 : a0), tag, F2U(hi ? b1 : a1), tag}, srdW,
                                               l < 16 ? W_MOD + ((32u + BIDX) * 32u + l * 2u) * 8u : OOB, 0, 16);
      }
    }
    SBAR();
    if (!lag) seam(t, t);
  };
  step(IC<0>{}); step(IC<1>{}); step(IC<2>{}); step(IC<3>{}); step(IC<4>{}); step(IC<5>{}); step(IC<6>{}); step(IC<7>{}); step(IC<8>{});
  __builtin_amdgcn_s_setprio(0);
  STAMP(1, 4);
  {
    const float il = __builtin_amdgcn_rcpf(swap32_add(lsum));
#pragma unroll
    for (int dt = 0; dt < 4; ++dt)
#pragma unroll
      for (int s = 0; s < 2; ++s)
        *(LAS u32x4*)(lds + LDS_X + ximg(16 * hh + 4 * dt + 2 * s + c.h, 32 * rt + c.r)) = pack8(o[dt], s, il);
  }
  BAR_L();
  STAMP(1, 5);
  dma16_sc1(lds0 + LDS_D + w * 2048u, l * 16u, srdW, W_MOD + 8192u + w * 2048u);
  dma16_sc1(lds0 + LDS_D + w * 2048u + 1024u, l * 16u, srdW, W_MOD + 8192u + w * 2048u + 1024u);
  {
    unsigned l2 = TIDX & 63;
    asm volatile("" : "+v"(l2));
    f32x4 acc[4][4] = {};
    gemm_phase<2>(acc, lds, ringoff, ring, vo, srdO, so0, 1024u, l2);
    STAMP(1, 6);
    const unsigned dst = W_P1 + ((((pn * 8 + ((l2 >> 3) & 1)) * 8 + g) * 64 + 8 * w + 2 * ((l2 >> 4) & 1) + (l2 >> 5)) * 8 + (l2 & 7)) * 16u;
#pragma unroll
    for (int r = 0; r < 4; ++r)
#pragma unroll
      for (int mp = 0; mp < 2; ++mp)
        __builtin_amdgcn_raw_buffer_store_b128(pack16(acc[2 * mp][r], acc[2 * mp + 1][r], 1.f), srdW,
                                               dst + (4 * mp * 8 + r * 2 * 8 * 64 * 8) * 16u, 0, 16);
  }
  WAIT_BAR(0);
  if (tid == 0) __builtin_amdgcn_raw_buffer_store_b32(tag, srdW, W_FLAG + (pn * 8 + g) * 64u, 0, 16);
  STAMP(1, 7);
  return tag;
}

DI void ln8(float (&v)[8], LAS float* red  , unsigned tid) {
  float s = ((v[0] + v[1]) + (v[2] + v[3])) + ((v[4] + v[5]) + (v[6] + v[7]));
  float q = (__builtin_fmaf(v[0], v[0], v[1] * v[1]) + __builtin_fmaf(v[2], v[2], v[3] * v[3])) +
            (__builtin_fmaf(v[4], v[4], v[5] * v[5]) + __builtin_fmaf(v[6], v[6], v[7] * v[7]));
  s = sum_hi3(s); q = sum_hi3(q);
  if ((tid & 63) < 8) *(LAS f32x2*)(red + ((tid >> 6) * 8 + (tid & 7)) * 2) = (f32x2){s, q};
  BAR_L();
  f32x2 e[8];
#pragma unroll
  for (int i = 0; i < 8; ++i) e[i] = *(const LAS f32x2*)(red + (i * 8 + (tid & 7)) * 2);
  const float mu = (((e[0][0] + e[1][0]) + (e[2][0] + e[3][0])) + ((e[4][0] + e[5][0]) + (e[6][0] + e[7][0]))) * (1.0f / D);
  const float ms = (((e[0][1] + e[1][1]) + (e[2][1] + e[3][1])) + ((e[4][1] + e[5][1]) + (e[6][1] + e[7][1]))) * (1.0f / D);
  const float rs = __builtin_amdgcn_rsqf(__builtin_fmaxf(__builtin_fmaf(-mu, mu, ms), 0.f) + LN_EPS);
  const float nb = -mu * rs;
#pragma unroll
  for (int k = 0; k < 8; ++k) v[k] = __builtin_fmaf(v[k], rs, nb);
}
DI void red4_put(const f32x16& a, int nt, lds_ptr scr, unsigned rt, unsigned kq, unsigned l) {
#pragma unroll
  for (int g = 0; g < 2; ++g)
#pragma unroll
    for (int i = 0; i < 2; ++i) {
      const int o = 8 * g + 4 * i;
      *(LAS f32x4*)(scr + ((rt * 4 + 2 * nt + g) * 4 + kq) * 2048 + i * 1024 + l * 16) = (f32x4){a[o], a[o + 1], a[o + 2], a[o + 3]};
    }
}
DI void red4_get(float (&v)[8], lds_cptr scr, unsigned rt, unsigned kq, unsigned l) {
  f32x4 t[4][2];
#pragma unroll
  for (int s = 0; s < 4; ++s)
#pragma unroll
    for (int i = 0; i < 2; ++i) t[s][i] = *(const LAS f32x4*)(scr + ((rt * 4 + kq) * 4 + s) * 2048 + i * 1024 + l * 16);
#pragma unroll
  for (int i = 0; i < 2; ++i)
#pragma unroll
    for (int k = 0; k < 4; ++k) v[4 * i + k] = (t[0][i][k] + t[1][i][k]) + (t[2][i][k] + t[3][i][k]);
}
DI void tile_mm8(const bf16x8 (&af)[8][2], const u32x4 (&xb)[8], float (&v)[8], lds_ptr scr, unsigned rt, unsigned kq, unsigned l) {
  f32x16 a0 = {}, a1 = {};
#pragma unroll
  for (int i = 0; i < 8; ++i) a0 = MFMA32(af[i][0], __builtin_bit_cast(bf16x8, xb[i]), a0);
  SBAR();
#pragma unroll
  for (int i = 0; i < 4; ++i) a1 = MFMA32(af[i][1], __builtin_bit_cast(bf16x8, xb[i]), a1);
  SBAR();
  red4_put(a0, 0, scr, rt, kq, l);
#pragma unroll
  for (int i = 4; i < 8; ++i) a1 = MFMA32(af[i][1], __builtin_bit_cast(bf16x8, xb[i]), a1);
  SBAR();
  red4_put(a1, 1, scr, rt, kq, l);
  BAR_L();
  red4_get(v, scr, rt, kq, l);
}

struct Frag3 { bf16x8 a, b[2]; };
constexpr int RING3 = 12;
constexpr int LA = 0, LB = 65536, LC = 131072;
DI unsigned r3off(int s, unsigned rc, unsigned rb) { return s < 4 ? rc + s * 1024u : rb + (s - 4) * 1024u; }
template <int KS> DI void load_frags3(Frag3& f, lds_cptr lds, unsigned rc, unsigned rb, unsigned a3, const LaneC& c) {
  const lds_cptr sl = lds + r3off(KS % RING3, rc, rb) + a3;
  f.a = cat8(lds_tr(sl), lds_tr(sl + 256));
#pragma unroll
  for (int rt = 0; rt < 2; ++rt) f.b[rt] = lds_b128(lds + LA + c.xb[KS & 3] + 2048 * KS + 512 * rt);
}
constexpr int w4slot(int n) { return (8 + n) % RING3; }
template <int KS>
DI void g3_step(f32x16 (&acc)[2], Frag3& cur, Frag3& n1, Frag3& n2, lds_cptr lds, unsigned lds0, unsigned rc, unsigned rb,
                unsigned voff, __amdgpu_buffer_rsrc_t srd, unsigned soff0, unsigned a3, const LaneC& c,
                unsigned v4, __amdgpu_buffer_rsrc_t srd4, unsigned s40) {
  constexpr int NKS = 32;
  if constexpr (KS + 2 < NKS) {
    constexpr int last = (KS + RING3 < NKS ? KS + RING3 : NKS) - 1;
    wait_vm<last - (KS + 2) + (KS > NKS - RING3 ? KS - (NKS - RING3) : 0)>();
    load_frags3<KS + 2>(n2, lds, rc, rb, a3, c);
  }
  SBAR();
  acc[0] = MFMA32(cur.a, cur.b[0], acc[0]);
  acc[1] = MFMA32(cur.a, cur.b[1], acc[1]);
  asm volatile("" :: "v"(cur.a));
  if constexpr (KS + RING3 < NKS) dma16(lds0 + r3off(KS % RING3, rc, rb), voff, srd, soff0 + (unsigned)(16 * (KS + RING3)) * 3584u);
  else dma16(lds0 + r3off(KS % RING3, rc, rb), v4, srd4, s40 + (unsigned)(KS - (NKS - RING3)) * 16384u);
  SBAR();
  if constexpr (KS + 1 < NKS) g3_step<KS + 1>(acc, n1, n2, cur, lds, lds0, rc, rb, voff, srd, soff0, a3, c, v4, srd4, s40);
}

DI void mlp_part(const Params& P, lds_ptr lds, unsigned lds0, unsigned w, __amdgpu_buffer_rsrc_t srdS, unsigned tag) {
  LaneC c; lane_consts(c);
  const unsigned idx = BIDX >> 3, pn = 8 * ((BIDX & 7) >> 1) + (idx >> 2), j = 4 * (BIDX & 1) + (idx & 3);
  const unsigned tid = TIDX, l = c.lane;
  const unsigned vw = wdma_voff(l, 1024u);
  const unsigned rt = w & 1, kq = w >> 1;
  constexpr int LMOD = LDS_D;
  constexpr int LRED = LC + 12288;
  constexpr int LST = LB + 7 * 8192;
  const unsigned prow = 32 * rt + c.r;
  const unsigned trow = pn * 64 + prow;
  const unsigned och = 8 * j + 2 * kq + c.h;
  const unsigned ovo = ((pn * 64 + och) * 64 + prow) * 16u;
  float gb[8];
  STAMP(2, 0);

  {
    const unsigned row = pn * 64 + j * 8 + (tid & 7), ch = tid >> 3;
    u32x4 xr;
    gld16(xr, P.x + (size_t)row * D + ch * 8);
    auto wait_flags = [&](unsigned fo) {
      for (unsigned spin = 0;; ++spin) {
        asm volatile("" ::: "memory");
        const unsigned f = __builtin_amdgcn_raw_buffer_load_b32(srdS, fo, 0, 16);
        if (!ANY(fo != OOB && f != tag) || spin > SPIN_CAP) break;
        __builtin_amdgcn_s_sleep(FLAG_SLEEP);
      }
    };
    const __amdgpu_buffer_rsrc_t srd1 = mksrd(P.w1, 512u * 512u * 2u), srd2 = mksrd(P.w2, 512u * 512u * 2u);
#pragma unroll
    for (int i = 0; i < 8; ++i) dma16(lds0 + LA + (8 * w + i) * 1024u, vw, srd1, (8 * w + i) * 8192u + j * 128u);
    {
      const int fp = (int)pn - 1 + (int)(l >> 3);
      if (w == 0) wait_flags((l < 24 && fp >= 0 && fp < 32) ? W_FLAG + ((unsigned)fp * 8 + (l & 7)) * 64u : OOB);
      BAR_L();
    }
    STAMP(2, 15);
    u32x4 p[8];
#pragma unroll
    for (int g = 0; g < 8; ++g) bld16_sc1(p[g], tid * 16u, srdS, W_P1 + ((pn * 8 + j) * 8 + g) * 8192u);
    float ga[8], sb[8], cb[8], g2[8];
    {
      u32x4 mv[20];
#pragma unroll
      for (int i = 0; i < 16; ++i) mv[i] = *(const LAS u32x4*)(lds + LMOD + (i >> 2) * 4096 + ch * 64 + 16 * (i & 3));
#pragma unroll
      for (int i = 0; i < 4; ++i) mv[16 + i] = *(const LAS u32x4*)(lds + LMOD + 12288 + och * 64 + 16 * i);
      if (ANY(bad_tag(mv, 20, tag))) {
#pragma unroll
        for (int i = 0; i < 4; ++i) poll_tag<4>(mv + 4 * i, srdS, ch * 64u, W_MOD + 8192u + i * 4096u, 16u, tag);
        poll_tag<4>(mv + 16, srdS, och * 64u, W_MOD + 20480u, 16u, tag);
      }
#pragma unroll
      for (int k = 0; k < 8; ++k) {
        ga[k] = U2F(mv[k >> 1][2 * (k & 1)]); sb[k] = U2F(mv[4 + (k >> 1)][2 * (k & 1)]); cb[k] = 1.f + U2F(mv[8 + (k >> 1)][2 * (k & 1)]);
        g2[k] = U2F(mv[12 + (k >> 1)][2 * (k & 1)]); gb[k] = U2F(mv[16 + (k >> 1)][2 * (k & 1)]);
      }
    }
    asm volatile("s_waitcnt vmcnt(0)" : "+v"(xr), "+v"(p[0]), "+v"(p[1]), "+v"(p[2]), "+v"(p[3]), "+v"(p[4]), "+v"(p[5]),
                 "+v"(p[6]), "+v"(p[7]) :: "memory");
    STAMP(2, 1);
    float v[8], x2[8];
#pragma unroll
    for (int i = 0; i < 4; ++i) {
      const float lo = ((bflo(p[0][i]) + bflo(p[1][i])) + (bflo(p[2][i]) + bflo(p[3][i]))) + ((bflo(p[4][i]) + bflo(p[5][i])) + (bflo(p[6][i]) + bflo(p[7][i])));
      const float hi = ((bfhi(p[0][i]) + bfhi(p[1][i])) + (bfhi(p[2][i]) + bfhi(p[3][i]))) + ((bfhi(p[4][i]) + bfhi(p[5][i])) + (bfhi(p[6][i]) + bfhi(p[7][i])));
      v[2 * i] = x2[2 * i] = __builtin_fmaf(ga[2 * i], lo, bflo(xr[i]));
      v[2 * i + 1] = x2[2 * i + 1] = __builtin_fmaf(ga[2 * i + 1], hi, bfhi(xr[i]));
    }
    ln8(v, (LAS float*)(lds + LRED), tid);
    float hb[8];
#pragma unroll
    for (int k = 0; k < 8; ++k) v[k] = hb[k] = __builtin_fmaf(v[k], cb[k], sb[k]);
    ln8(v, (LAS float*)(lds + LRED + 512), tid);
    const unsigned vo = ((pn * 64 + ch) * 64 + j * 8 + (tid & 7)) * 16u, vz = ((pn * 64 + ch) * 256 + j * 8 + (tid & 7)) * 16u;
    pub16(srdS, vo, S_U, (u32x4){cvtpk(v[0], v[1]), cvtpk(v[2], v[3]), cvtpk(v[4], v[5]), cvtpk(v[6], v[7])});
#pragma unroll
    for (int i = 0; i < 2; ++i) {
      pub16(srdS, vz, S_HZ + i * 1024u, (u32x4){F2U(hb[4 * i]), F2U(hb[4 * i + 1]),
                                                F2U(hb[4 * i + 2]), F2U(hb[4 * i + 3])});
      f32x4 z;
#pragma unroll
      for (int e = 0; e < 4; ++e) z[e] = __builtin_fmaf(g2[4 * i + e], hb[4 * i + e], x2[4 * i + e]);
      pub16(srdS, vz, S_HZ + (2 + i) * 1024u, __builtin_bit_cast(u32x4, z));
    }
#pragma unroll
    for (int i = 0; i < 8; ++i) dma16(lds0 + LB + (8 * w + i) * 1024u, vw, srd2, (8 * w + i) * 8192u + j * 128u);
  }
  STAMP(2, 2);
  const __amdgpu_buffer_rsrc_t srd3 = mksrd(P.w3, 512u * F * 2u);
  const unsigned rc = LC + w * 4096u, rb = LB + w * 8192u;
  const unsigned v3 = (l >> 2) * 3584u + ((l & 3) << 4), s30 = (j * 224u + w * 32u) * 2u;
  float v[8];
  {
    bf16x8 af[8][2];
#pragma unroll
    for (int i = 0; i < 8; ++i)
#pragma unroll
      for (int nt = 0; nt < 2; ++nt) {
        const lds_cptr a = lds + LA + (8 * kq + i) * 2048 + c.afr[nt];
        af[i][nt] = cat8(lds_tr(a), lds_tr(a + 512));
      }
    BAR_L();
    u32x4 xb[8];
    poll_ind<8, 3, 7>(xb, srdS, ((pn * 64 + 16 * kq + c.h) * 64 + prow) * 16u, S_U, 2048u);
    STAMP(2, 3);
    tile_mm8(af, xb, v, lds + LA, rt, kq, l);
#pragma unroll
    for (int k = 0; k < 8; ++k) v[k] = gelu_tanh(v[k]);
    pub16(srdS, ovo, S_H1, (u32x4){cvtpk(v[0], v[1]), cvtpk(v[2], v[3]), cvtpk(v[4], v[5]), cvtpk(v[6], v[7])});
    if (w < 7) {
#pragma unroll
      for (int ks = 0; ks < 4; ++ks) dma16(lds0 + r3off(ks, rc, rb), v3, srd3, s30 + (unsigned)(16 * ks) * 3584u);
    }
  }
  STAMP(2, 4);
  float y[8];
  float cs[16];
  const unsigned cso = w < 7 ? (3072 + 224 * j + 32 * w + 8 * c.h) * 8u : OOB;
  {
    bf16x8 af[8][2];
#pragma unroll
    for (int i = 0; i < 8; ++i)
#pragma unroll
      for (int nt = 0; nt < 2; ++nt) {
        const lds_cptr a = lds + LB + (8 * kq + i) * 2048 + c.afr[nt];
        af[i][nt] = cat8(lds_tr(a), lds_tr(a + 512));
      }
    BAR_L();
    const int sh = kq == 0 ? -1 : kq == 1 ? 1 : kq == 2 ? -2 : 2;
    const int srow = (int)trow + sh;
    const unsigned xvo = (srow >= 0 && srow < T) ? ((((unsigned)srow >> 6) * 64 + 16 * kq + c.h) * 64 + ((unsigned)srow & 63)) * 16u : OOB;
    u32x4 xb[8];
    poll_ind<8, 3, 7>(xb, srdS, xvo, S_H1, 2048u);
    STAMP(2, 5);
    u32x4 hz[4];
    const unsigned hzo = ((pn * 64 + och) * 256 + prow) * 16u;
#pragma unroll
    for (int i = 0; i < 4; ++i) hz[i] = __builtin_amdgcn_raw_buffer_load_b128(srdS, hzo, S_HZ + i * 1024u, 16);
    u32x4 cg[8];
#pragma unroll
    for (int i = 0; i < 8; ++i) cg[i] = __builtin_amdgcn_raw_buffer_load_b128(srdS, cso, W_MOD + 128u * (i >> 2) + 16u * (i & 3), 16);
    tile_mm8(af, xb, v, lds + LA, rt, kq, l);
    if (has_sent(hz, 4)) poll_load<4>(hz, srdS, hzo, S_HZ, 1024u);
    float x1[8];
#pragma unroll
    for (int k = 0; k < 8; ++k) {
      x1[k] = U2F(hz[k >> 2][k & 3]) + v[k];
      y[k] = __builtin_fmaf(gb[k], v[k], U2F(hz[2 + (k >> 2)][k & 3]));
    }
    pub16(srdS, ovo, S_X1, (u32x4){cvtpk(x1[0], x1[1]), cvtpk(x1[2], x1[3]), cvtpk(x1[4], x1[5]), cvtpk(x1[6], x1[7])});
    if (ANY(cso < OOB && bad_tag(cg, 8, tag))) {
      poll_tag<4>(cg, srdS, cso, W_MOD, 16u, tag);
      poll_tag<4>(cg + 4, srdS, cso, W_MOD + 128u, 16u, tag);
    }
#pragma unroll
    for (int i = 0; i < 16; ++i) cs[i] = U2F(cg[i >> 1][2 * (i & 1)]);
    if (w < 7) {
#pragma unroll
      for (int ks = 4; ks < RING3; ++ks) dma16(lds0 + r3off(ks, rc, rb), v3, srd3, s30 + (unsigned)(16 * ks) * 3584u);
    }
  }
  STAMP(2, 6);
  const __amdgpu_buffer_rsrc_t srd4 = mksrd(P.w4, (unsigned)F * 512u * 2u);
  const unsigned s40 = (448u * kq + 8u * rt) * 1024u + j * 128u;
  {
    u32x4 xr[8];
    BAR_L();
    poll_ind<8, 3, 7>(xr, srdS, ((pn * 64 + 8 * w) * 64 + l) * 16u, S_X1, 1024u);
    STAMP(2, 7);
#pragma unroll
    for (int i = 0; i < 8; ++i) *(LAS u32x4*)(lds + LA + ximg(8 * w + i, l)) = xr[i];
    {
      float s0 = 0.f, s1 = 0.f, q0 = 0.f, q1 = 0.f;
#pragma unroll
      for (int i = 0; i < 8; ++i)
#pragma unroll
        for (int k = 0; k < 4; k += 2) {
          s0 = dot2bf(xr[i][k], 0x3F803F80u, s0); s1 = dot2bf(xr[i][k + 1], 0x3F803F80u, s1);
          q0 = dot2bf(xr[i][k], xr[i][k], q0); q1 = dot2bf(xr[i][k + 1], xr[i][k + 1], q1);
        }
      *(LAS f32x2*)(lds + LST + (w * 64 + l) * 8) = (f32x2){s0 + s1, q0 + q1};
    }
    BAR_L();
    STAMP(2, 8);
    if (w < 7) {
      f32x16 acc[2] = {};
      const unsigned a3 = (8 * c.h + c.q) * 64 + 32 * c.blk + 8 * (2 * (c.p & 1) + (c.p >> 1));
      Frag3 f0, f1, f2;
      wait_vm<RING3 - 2>();
      load_frags3<0>(f0, lds, rc, rb, a3, c);
      load_frags3<1>(f1, lds, rc, rb, a3, c);
      float mu[2], rs[2];
#pragma unroll
      for (int r2 = 0; r2 < 2; ++r2) {
        float s = 0.f, q = 0.f;
#pragma unroll
        for (int i = 0; i < 8; ++i) {
          const f32x2 t = *(const LAS f32x2*)(lds + LST + (i * 64 + 32 * r2 + c.r) * 8);
          s += t[0]; q += t[1];
        }
        mu[r2] = s * (1.0f / D);
        rs[r2] = __builtin_amdgcn_rsqf(__builtin_fmaxf(__builtin_fmaf(-mu[r2], mu[r2], q * (1.0f / D)), 0.f) + LN_EPS);
      }
      if (w < 4) __builtin_amdgcn_s_setprio(2); else __builtin_amdgcn_s_setprio(1);
      g3_step<0>(acc, f0, f1, f2, lds, lds0, rc, rb, v3, srd3, s30, a3, c, vw, srd4, s40);
      __builtin_amdgcn_s_setprio(0);
      STAMP(2, 9);
#pragma unroll
      for (int r2 = 0; r2 < 2; ++r2) {
#pragma unroll
        for (int i = 0; i < 16; ++i) acc[r2][i] = gelu_tanh(rs[r2] * __builtin_fmaf(-mu[r2], cs[i], acc[r2][i]));
#pragma unroll
        for (int s2 = 0; s2 < 2; ++s2)
          pub16(srdS, ((pn * 224 + 28 * j + 4 * w + 2 * s2 + c.h) * 64 + 32 * r2 + c.r) * 16u, S_H3, pack8(acc[r2], s2, 1.f));
      }
    } else {
#pragma unroll
      for (int n = 0; n < 8; ++n) dma16(lds0 + r3off(w4slot(n), rc, rb), vw, srd4, s40 + (unsigned)n * 16384u);
    }
    BAR_L();
  }
  STAMP(2, 10);
  {
    auto poff = [&](int ks, unsigned wv) -> unsigned {
      return (ks >= 12 && ks < 20) ? LA + wv * 8192u + (unsigned)(ks - 12) * 1024u : r3off(w4slot(ks % 20), LC + wv * 4096u, LB + wv * 8192u);
    };
    auto w4dma = [&](int ks) { dma16(lds0 + poff(ks, w), vw, srd4, s40 + (unsigned)ks * 16384u); };
    if (w == 7) {
#pragma unroll
      for (int ks = 8; ks < 12; ++ks) w4dma(ks);
    }
#pragma unroll
    for (int ks = 12; ks < 20; ++ks) w4dma(ks);
    const unsigned wsrc = 2 * kq + c.h;
    const unsigned hvo = ((pn * 224 + 56 * kq + c.h) * 64 + prow) * 16u;
    u32x4 ha[14], hb[14];
    poll_ind<14, 9, 13>(ha, srdS, hvo, S_H3, 2048u);
    BAR_L();
    STAMP(2, 11);
    f32x16 acc[2] = {};
    bf16x8 fa[2][2];
    auto ld = [&](int ks) {
#pragma unroll
      for (int nt = 0; nt < 2; ++nt) {
        const lds_cptr a = lds + poff(ks, wsrc) + (c.afr[nt] - 1024u * c.h);
        fa[ks & 1][nt] = cat8(lds_tr(a), lds_tr(a + 512));
      }
    };
    auto mm = [&](int ks, const u32x4& b) {
#pragma unroll
      for (int nt = 0; nt < 2; ++nt) acc[nt] = MFMA32(fa[ks & 1][nt], __builtin_bit_cast(bf16x8, b), acc[nt]);
    };
    auto hbl = [&](int i) { bld16_sc1(hb[i], hvo, srdS, S_H3 + (14u + i) * 2048u); };
    ld(0);
#pragma unroll
    for (int ks = 0; ks < 8; ++ks) {
      if (ks < 7) ld(ks + 1);
      SBAR(); mm(ks, ha[ks]);
      if (ks < 7) { hbl(2 * ks); hbl(2 * ks + 1); }
      SBAR();
    }
    BAR_L();
    ld(8);
#pragma unroll
    for (int ks = 8; ks < 14; ++ks) {
      if (ks < 13) ld(ks + 1);
      SBAR(); mm(ks, ha[ks]);
      if (ks < 12) { w4dma(20 + 2 * (ks - 8)); w4dma(21 + 2 * (ks - 8)); }
      SBAR();
    }
    asm volatile("s_waitcnt vmcnt(8) lgkmcnt(0)\n\ts_barrier" : "+v"(hb[0]), "+v"(hb[1]), "+v"(hb[2]), "+v"(hb[3]), "+v"(hb[4]), "+v"(hb[5]),
                 "+v"(hb[6]), "+v"(hb[7]), "+v"(hb[8]), "+v"(hb[9]), "+v"(hb[10]), "+v"(hb[11]), "+v"(hb[12]), "+v"(hb[13]) :: "memory");
    if (has_sent(hb, 14)) poll_load<14>(hb, srdS, hvo, S_H3 + 14u * 2048u, 2048u);
    STAMP(2, 12);
    ld(14);
#pragma unroll
    for (int ks = 14; ks < 19; ++ks) { ld(ks + 1); SBAR(); mm(ks, hb[ks - 14]); SBAR(); }
    mm(19, hb[5]);
    WAIT_BAR(0);
    ld(20);
#pragma unroll
    for (int ks = 20; ks < 27; ++ks) { ld(ks + 1); SBAR(); mm(ks, hb[ks - 14]); SBAR(); }
    mm(27, hb[13]);
    BAR_L();
    STAMP(2, 13);
    red4_put(acc[0], 0, lds + LA, rt, kq, l);
    red4_put(acc[1], 1, lds + LA, rt, kq, l);
    BAR_L();
    red4_get(v, lds + LA, rt, kq, l);
#pragma unroll
    for (int k = 0; k < 8; ++k) v[k] = __builtin_fmaf(gb[k], v[k], y[k]);
    P.out[(size_t)trow * 64 + och] = (u32x4){cvtpk(v[0], v[1]), cvtpk(v[2], v[3]), cvtpk(v[4], v[5]), cvtpk(v[6], v[7])};
  }
  STAMP(2, 14);
}

__global__ __launch_bounds__(512, 2) void k_fused(Params P) {
  extern __shared__ __attribute__((aligned(16))) char smem[];
  const lds_ptr lds = (lds_ptr)smem;
  const unsigned lds0 = (unsigned)(uintptr_t)smem;
  const unsigned w = rfl(TIDX >> 6);
  const __amdgpu_buffer_rsrc_t srdW = mksrd(P.ws, W_END);
  {
    unsigned long long pc; unsigned len;
    asm volatile("s_getpc_b64 %0\n.Lkf_here:\n\ts_mov_b32 %1, .Lkf_end-.Lkf_here" : "=s"(pc), "=s"(len));
    dma16(lds0 + LDS_R + w * RINGB + 8192u, (TIDX & 63) * 16u, mksrd((const void*)(pc & ~15ull), len & ~15u), ((BIDX >> 3) * 8 + w) * 1024u);
  }
  const unsigned tag = attn_part(P, lds, lds0, w, srdW);
  mlp_part(P, lds, lds0, w, srdW, tag);
  asm volatile(".Lkf_end:");
}

#if defined(__HIP_DEVICE_COMPILE__)
#pragma clang attribute pop
#endif
extern "C" void kernel_launch(void* const* d_in, const int* in_sizes, int n_in, void* d_out, int out_size, void* d_ws,
                              size_t ws_size, hipStream_t stream) {
  Params P;
  P.x = (const unsigned short*)d_in[0];
  P.kc = (const unsigned short*)d_in[1];
  P.vc = (const unsigned short*)d_in[2];
  P.wq = (const unsigned short*)d_in[3];
  P.wo = (const unsigned short*)d_in[4];
  P.w1 = (const unsigned short*)d_in[6];
  P.w2 = (const unsigned short*)d_in[7];
  P.w3 = (const unsigned short*)d_in[8];
  P.w4 = (const unsigned short*)d_in[9];
  P.c = (const unsigned short*)d_in[10];
  P.wmod = (const unsigned short*)d_in[11];
  P.ws = d_ws;
  P.out = (u32x4*)d_out;
  (void)hipFuncSetAttribute((const void*)k_fused, hipFuncAttributeMaxDynamicSharedMemorySize, LDS_TOTAL);
  k_fused<<<256, 512, LDS_TOTAL, stream>>>(P);
}
```

```cpp
#include <hip/hip_runtime.h>
#include <hip/hip_bf16.h>
#include <stdint.h>

#if defined(__HIP_DEVICE_COMPILE__)
#pragma clang attribute push(__attribute__((target("no-packed-fp32-ops"))), apply_to = function)
#endif
typedef short bf16x8 __attribute__((ext_vector_type(8)));
typedef short s16x4 __attribute__((ext_vector_type(4)));
typedef float f32x16 __attribute__((ext_vector_type(16)));
typedef float f32x4 __attribute__((ext_vector_type(4)));
typedef float f32x2 __attribute__((ext_vector_type(2)));
typedef unsigned u32x4 __attribute__((ext_vector_type(4)));
typedef unsigned u32x2 __attribute__((ext_vector_type(2)));
typedef __bf16 bf16x2_t __attribute__((ext_vector_type(2)));
typedef short v4i16_t __attribute__((ext_vector_type(4)));
#define LAS __attribute__((address_space(3)))
typedef LAS const char* lds_cptr;
typedef LAS char* lds_ptr;
#define DI __device__ __forceinline__
#define F2U(x) __builtin_bit_cast(unsigned, (float)(x))
#define U2F(x) __builtin_bit_cast(float, (unsigned)(x))
#define F2I(x) __builtin_bit_cast(int, (float)(x))
#define I2F(x) __builtin_bit_cast(float, (int)(x))
#define TIDX __builtin_amdgcn_workitem_id_x()
#define BIDX __builtin_amdgcn_workgroup_id_x()
#define ANY(p) (__builtin_amdgcn_ballot_w64(p) != 0ull)
#define SBAR() __builtin_amdgcn_sched_barrier(0)
#define MFMA32(a, b, c) __builtin_amdgcn_mfma_f32_32x32x16_bf16((a), (b), (c), 0, 0, 0)
#define WAIT_V(n) asm volatile("s_waitcnt vmcnt(" #n ")" ::: "memory")
#define WAIT_L0() asm volatile("s_waitcnt lgkmcnt(0)" ::: "memory")
#define BAR_L() asm volatile("s_waitcnt lgkmcnt(0)\n\ts_barrier" ::: "memory")
#define WAIT_BAR(n) asm volatile("s_waitcnt vmcnt(" #n ") lgkmcnt(0)\n\ts_barrier" ::: "memory")

constexpr int T = 2048, D = 512, HKV = 8, SK = 2048, DH = 128, WIN = 512, F = 1792;
constexpr float LN_EPS = 1e-5f;
constexpr float QSCALE = 0.088388347648318440f * 1.4426950408889634f;

constexpr unsigned W_EPOCH = 0;
constexpr unsigned W_FLAG = 4096;
constexpr unsigned W_MOD = 32768;
constexpr unsigned W_MP = W_MOD + 40960;
constexpr unsigned W_P1 = 393216;
constexpr unsigned S_U = W_P1 + 8u * 64u * T * 16u;
constexpr unsigned S_H1 = S_U + 64u * T * 16u;
constexpr unsigned S_X1 = S_H1 + 64u * T * 16u;
constexpr unsigned S_HZ = S_X1 + 64u * T * 16u;
constexpr unsigned S_H3 = S_HZ + 256u * T * 16u;
constexpr unsigned W_END = S_H3 + 224u * T * 16u;
static_assert(W_MP + 8u * 4864u * 8u <= W_P1, "ws");
constexpr unsigned OOB = 0xF0000000u;
#define K1MAP 0
#define FLAG_SLEEP 4
#define GSPLIT 2
#define ATHR 32.0f
constexpr float THR = ATHR;
constexpr unsigned SENT = 0xFFFFFFFFu;
constexpr unsigned SPIN_CAP = 1u << 16;

constexpr int LDS_X = 0;
constexpr int LDS_R = 65536;
constexpr int RING = 5, RINGB = RING * 2048;
constexpr int LDS_D = LDS_R + 8 * RINGB;
constexpr int LDS_TOTAL = LDS_D + 16384;
static_assert(LDS_TOTAL == 163840, "lds");

#define STAMP(kid, i) do {} while (0)
DI unsigned cvtpk(float lo, float hi) {
  f32x2 v = {lo, hi};
  bf16x2_t b = __builtin_convertvector(v, bf16x2_t);
  return __builtin_bit_cast(unsigned, b);
}
DI float bflo(unsigned u) { return U2F(u << 16); }
DI float bfhi(unsigned u) { return U2F(u & 0xffff0000u); }
DI unsigned rfl(unsigned x) { return __builtin_amdgcn_readfirstlane(x); }
DI __amdgpu_buffer_rsrc_t mksrd(const void* p, unsigned bytes) {
  return __builtin_amdgcn_make_buffer_rsrc((void*)p, (short)0, (int)bytes, 0x00020000);
}
DI void dma16(unsigned lds, unsigned voff, __amdgpu_buffer_rsrc_t srd, unsigned soff) {
  asm volatile("s_mov_b32 m0, %0\n\ts_nop 0\n\tbuffer_load_dwordx4 %1, %2, %3 offen lds"
               :: "s"(lds), "v"(voff), "s"(srd), "s"(soff) : "memory");
}
DI void gld16(u32x4& d, const void* p) { asm volatile("global_load_dwordx4 %0, %1, off" : "=v"(d) : "v"(p) : "memory"); }
DI void bld16_sc1(u32x4& d, unsigned voff, __amdgpu_buffer_rsrc_t srd, unsigned soff) {
  asm volatile("buffer_load_dwordx4 %0, %1, %2, %3 offen sc1" : "=v"(d) : "v"(voff), "s"(srd), "s"(soff) : "memory");
}
DI void dma16_sc1(unsigned lds, unsigned voff, __amdgpu_buffer_rsrc_t srd, unsigned soff) {
  asm volatile("s_mov_b32 m0, %0\n\ts_nop 0\n\tbuffer_load_dwordx4 %1, %2, %3 offen sc1 lds"
               :: "s"(lds), "v"(voff), "s"(srd), "s"(soff) : "memory");
}
DI bf16x8 lds_b128(lds_cptr p) { return *(const LAS bf16x8*)p; }
DI s16x4 lds_tr(lds_cptr p) {
  return __builtin_bit_cast(s16x4, __builtin_amdgcn_ds_read_tr16_b64_v4i16((LAS v4i16_t*)p));
}
DI bf16x8 cat8(s16x4 lo, s16x4 hi) { return __builtin_shufflevector(lo, hi, 0, 1, 2, 3, 4, 5, 6, 7); }
DI float swap32_add(float x) {
  auto r = __builtin_amdgcn_permlane32_swap(F2U(x), F2U(x), false, false);
  return U2F(r[0]) + U2F(r[1]);
}
DI float swap32_max(float x) {
  auto r = __builtin_amdgcn_permlane32_swap(F2U(x), F2U(x), false, false);
  return __builtin_fmaxf(U2F(r[0]), U2F(r[1]));
}
template <int CTRL> DI float dpp_add(float x) {
  int y = __builtin_amdgcn_update_dpp(0, F2I(x), CTRL, 0xF, 0xF, true);
  return x + I2F(y);
}
DI float max3f(float a, float b, float c) { float r; asm("v_max3_f32 %0, %1, %2, %3" : "=v"(r) : "v"(a), "v"(b), "v"(c)); return r; }
DI float max2f(float a, float b) { float r; asm("v_max_f32_e32 %0, %1, %2" : "=v"(r) : "v"(a), "v"(b)); return r; }
DI float dot2bf(unsigned a, unsigned b, float c) {
  return __builtin_amdgcn_fdot2_f32_bf16(__builtin_bit_cast(bf16x2_t, a), __builtin_bit_cast(bf16x2_t, b), c, false);
}
#define PIN(x) asm volatile("" : "+v"(x))
DI float swap16_add(float x) {
  auto r = __builtin_amdgcn_permlane16_swap(F2U(x), F2U(x), false, false);
  return U2F(r[0]) + U2F(r[1]);
}
DI float sum_hi3(float x) { return swap32_add(swap16_add(dpp_add<0x128>(x))); }
DI float gelu_tanh(float x) {
  const float k0 = -2.0f * 0.7978845608028654f * 1.4426950408889634f;
  const float t = x * x;
  const float z = x * __builtin_fmaf(t, 0.044715f * k0, k0);
  const float e = __builtin_amdgcn_exp2f(z);
  return x * __builtin_amdgcn_rcpf(1.0f + e);
}
DI void wave_sum8(const float (&a)[8], float (&out)[8]) {
  float b[4], c[2];
#pragma unroll
  for (int i = 0; i < 4; ++i) {
    auto r = __builtin_amdgcn_permlane32_swap(F2U(a[i]), F2U(a[i + 4]), false, false);
    b[i] = U2F(r[0]) + U2F(r[1]);
  }
#pragma unroll
  for (int i = 0; i < 2; ++i) {
    auto r = __builtin_amdgcn_permlane16_swap(F2U(b[i]), F2U(b[i + 2]), false, false);
    c[i] = U2F(r[0]) + U2F(r[1]);
  }
#pragma unroll
  for (int i = 0; i < 2; ++i) {
    c[i] = dpp_add<0xB1>(c[i]);
    c[i] = dpp_add<0x4E>(c[i]);
    c[i] = dpp_add<0x141>(c[i]);
    c[i] = dpp_add<0x140>(c[i]);
  }
#pragma unroll
  for (int q = 0; q < 4; ++q)
#pragma unroll
    for (int i = 0; i < 2; ++i)
      out[i + 2 * (q & 1) + 4 * (q >> 1)] = I2F(__builtin_amdgcn_readlane(F2I(c[i]), 16 * q));
}

DI unsigned nosent(unsigned v) { return v < 0xFFFFFFFEu ? v : 0xFFFFFFFEu; }
DI void pub16(__amdgpu_buffer_rsrc_t srd, unsigned voff, unsigned soff, u32x4 v) {
  v = (u32x4){nosent(v[0]), nosent(v[1]), nosent(v[2]), nosent(v[3])};
  __builtin_amdgcn_raw_buffer_store_b128(v, srd, voff + soff, 0, 16);
}
DI unsigned umax3(unsigned a, unsigned b, unsigned c) { const unsigned m = a > b ? a : b; return m > c ? m : c; }
DI bool has_sent(const u32x4* v, int n) {
  unsigned m = 0;
#pragma unroll
  for (int i = 0; i < n; ++i) { m = umax3(m, v[i][0], v[i][1]); m = umax3(m, v[i][2], v[i][3]); }
  return ANY(m == SENT);
}
template <int N> DI void poll_load(u32x4* v, __amdgpu_buffer_rsrc_t srd, unsigned voff, unsigned soff0, unsigned step) {
  for (unsigned spin = 0;; ++spin) {
    asm volatile("" ::: "memory");
#pragma unroll
    for (int i = 0; i < N; ++i) v[i] = __builtin_amdgcn_raw_buffer_load_b128(srd, voff, soff0 + (unsigned)i * step, 16);
    if (!has_sent(v, N) || spin > SPIN_CAP) break;
    __builtin_amdgcn_s_sleep(1);
  }
}
template <int N, int I0, int I1> DI void poll_ind(u32x4* v, __amdgpu_buffer_rsrc_t srd, unsigned voff, unsigned soff0, unsigned step) {
#pragma unroll
  for (int i = 0; i < N; ++i) v[i] = __builtin_amdgcn_raw_buffer_load_b128(srd, voff, soff0 + (unsigned)i * step, 16);
  if (has_sent(v, N)) {
    u32x4 ind[2];
    poll_load<2>(ind, srd, voff, soff0 + (unsigned)I0 * step, (unsigned)(I1 - I0) * step);
    poll_load<N>(v, srd, voff, soff0, step);
  }
}

DI bool bad_tag(const u32x4* v, int n, unsigned tag) {
  unsigned m = 0;
#pragma unroll
  for (int i = 0; i < n; ++i) m |= (v[i][1] ^ tag) | (v[i][3] ^ tag);
  return m != 0;
}
template <int N> DI void poll_tag(u32x4* v, __amdgpu_buffer_rsrc_t srd, unsigned voff, unsigned soff0, unsigned step, unsigned tag) {
  for (unsigned spin = 0;; ++spin) {
    asm volatile("" ::: "memory");
#pragma unroll
    for (int i = 0; i < N; ++i) v[i] = __builtin_amdgcn_raw_buffer_load_b128(srd, voff, soff0 + (unsigned)i * step, 16);
    if (!ANY(voff < OOB && bad_tag(v, N, tag)) || spin > SPIN_CAP) break;
    __builtin_amdgcn_s_sleep(1);
  }
}

struct LaneC {
  unsigned lane, r, h, q, p, blk;
  unsigned wv;
  unsigned afr[2];
  unsigned xb[4];
};
DI void lane_consts(LaneC& c) {
  unsigned l = TIDX & 63;
  asm volatile("" : "+v"(l));
  c.lane = l; c.r = l & 31; c.h = l >> 5; c.q = (l & 15) >> 2; c.p = l & 3; c.blk = (l >> 4) & 1;
  const unsigned sp = 2 * (c.p & 1) + (c.p >> 1);
#pragma unroll
  for (int nt = 0; nt < 2; ++nt) c.afr[nt] = (8 * c.h + c.q) * 128 + 64 * (nt ^ (c.q >> 1)) + 32 * c.blk + 8 * sp;
#pragma unroll
  for (int s = 0; s < 4; ++s) c.xb[s] = 1024 * c.h + ((c.r ^ (2 * s + c.h)) << 4);
}
DI unsigned wdma_voff(unsigned l, unsigned ld) { return (l >> 3) * ld + (((l & 7) ^ (((l >> 4) & 1) << 2)) << 4); }

DI void wring_issue(int ks, unsigned ring, unsigned voff, __amdgpu_buffer_rsrc_t srd, unsigned soff0, unsigned ld) {
  const unsigned slot = ring + (unsigned)(ks % RING) * 2048u;
  dma16(slot, voff, srd, soff0 + (unsigned)(16 * ks) * ld);
  dma16(slot + 1024u, voff ^ 32u, srd, soff0 + (unsigned)(16 * ks + 8) * ld);
}
template <int N> DI void wait_vm() {
  if constexpr (N == 0) WAIT_V(0); else if constexpr (N == 1) WAIT_V(1); else if constexpr (N == 2) WAIT_V(2);
  else if constexpr (N == 3) WAIT_V(3); else if constexpr (N == 4) WAIT_V(4); else if constexpr (N == 5) WAIT_V(5);
  else if constexpr (N == 6) WAIT_V(6); else if constexpr (N == 7) WAIT_V(7); else if constexpr (N == 8) WAIT_V(8);
  else if constexpr (N == 9) WAIT_V(9); else if constexpr (N == 10) WAIT_V(10); else if constexpr (N == 11) WAIT_V(11);
  else if constexpr (N == 12) WAIT_V(12); else if constexpr (N == 13) WAIT_V(13); else if constexpr (N == 14) WAIT_V(14);
  else if constexpr (N == 15) WAIT_V(15); else if constexpr (N == 16) WAIT_V(16);
  else if constexpr (N == 17) WAIT_V(17); else if constexpr (N == 18) WAIT_V(18);
  else static_assert(N < 0, "wait_vm");
}

#define MFMA16(a, b, c) __builtin_amdgcn_mfma_f32_16x16x32_bf16((a), (b), (c), 0, 0, 0)
struct G16 { unsigned a[4], hi, x[2]; };
DI void g16_consts(G16& k, unsigned l, unsigned ringoff) {
  const unsigned i = l & 15, g = l >> 4, q = i >> 2, p = l & 3, x = 2 * (q >> 1) + (g & 1);
#pragma unroll
  for (int m = 0; m < 4; ++m) k.a[m] = ringoff + (8 * (g & 1) + q) * 128 + ((2 * (m ^ x) + (p >> 1)) << 4) + 8 * (p & 1);
  k.hi = g >> 1;
#pragma unroll
  for (int e = 0; e < 2; ++e) k.x[e] = LDS_X + 1024 * g + ((i ^ (4 * e + g)) << 4);
}
struct Frag { bf16x8 a[4], b[4]; };
template <int KK> DI void load_frags(Frag& f, lds_cptr lds, const G16& k) {
  constexpr int sA = (2 * KK) % RING;
  const unsigned d = sA == RING - 1 ? (1u - k.hi) * (unsigned)((RING - 1) * 2048) : k.hi * 2048u;
  constexpr int o = sA == RING - 1 ? 0 : sA * 2048;
#pragma unroll
  for (int m = 0; m < 4; ++m) f.a[m] = cat8(lds_tr(lds + (k.a[m] + d) + o), lds_tr(lds + (k.a[m] + d) + o + 512));
#pragma unroll
  for (int r = 0; r < 4; ++r) f.b[r] = lds_b128(lds + k.x[KK & 1] + 4096 * KK + 256 * r);
}
template <int KK, int EXTRA>
DI void gemm_step(f32x4 (&acc)[4][4], Frag& cur, Frag& nxt, lds_cptr lds, unsigned ring, unsigned voff,
                  __amdgpu_buffer_rsrc_t srd, unsigned soff0, unsigned ld, const G16& k) {
  if constexpr (2 * KK + RING < 32) {
    asm volatile("" :: "v"(cur.a[0]), "v"(cur.a[1]), "v"(cur.a[2]), "v"(cur.a[3]));
    wring_issue(2 * KK + RING, ring, voff, srd, soff0, ld);
    if constexpr (2 * KK + RING + 1 < 32) wring_issue(2 * KK + RING + 1, ring, voff, srd, soff0, ld);
  }
  SBAR();
#pragma unroll
  for (int m = 0; m < GSPLIT; ++m)
#pragma unroll
    for (int r = 0; r < 4; ++r) acc[m][r] = MFMA16(cur.a[m], cur.b[r], acc[m][r]);
  SBAR();
  if constexpr (KK + 1 < 16) {
    constexpr int last = 2 * KK + RING + 1 < 32 ? 2 * KK + RING + 1 : 31;
    wait_vm<2 * (last - (2 * KK + 3)) + ((2 * KK + 3 < RING) ? EXTRA : 0)>();
    load_frags<KK + 1>(nxt, lds, k);
  }
  SBAR();
#pragma unroll
  for (int m = GSPLIT; m < 4; ++m)
#pragma unroll
    for (int r = 0; r < 4; ++r) acc[m][r] = MFMA16(cur.a[m], cur.b[r], acc[m][r]);
  SBAR();
  if constexpr (KK + 1 < 16) gemm_step<KK + 1, EXTRA>(acc, nxt, cur, lds, ring, voff, srd, soff0, ld, k);
}
template <int EXTRA>
DI void gemm_phase(f32x4 (&acc)[4][4], lds_cptr lds, unsigned ringoff, unsigned ring, unsigned voff,
                   __amdgpu_buffer_rsrc_t srd, unsigned soff0, unsigned ld, unsigned l) {
  G16 k; g16_consts(k, l, ringoff);
  Frag f0, f1;
  wait_vm<2 * (RING - 2) + EXTRA>();
  load_frags<0>(f0, lds, k);
  gemm_step<0, EXTRA>(acc, f0, f1, lds, ring, voff, srd, soff0, ld, k);
}
DI u32x4 pack16(const f32x4& a, const f32x4& b, float sc) {
  auto r0 = __builtin_amdgcn_permlane16_swap(cvtpk(a[0] * sc, a[1] * sc), cvtpk(b[0] * sc, b[1] * sc), false, false);
  auto r1 = __builtin_amdgcn_permlane16_swap(cvtpk(a[2] * sc, a[3] * sc), cvtpk(b[2] * sc, b[3] * sc), false, false);
  return (u32x4){r0[0], r1[0], r0[1], r1[1]};
}
DI u32x4 pack8(const f32x16& a, int s, float sc) {
  return (u32x4){cvtpk(a[8 * s] * sc, a[8 * s + 1] * sc), cvtpk(a[8 * s + 2] * sc, a[8 * s + 3] * sc),
                 cvtpk(a[8 * s + 4] * sc, a[8 * s + 5] * sc), cvtpk(a[8 * s + 6] * sc, a[8 * s + 7] * sc)};
}
DI unsigned ximg(unsigned chunk, unsigned row) { return chunk * 1024u + ((row ^ (chunk & 7u)) << 4); }

constexpr int KSLOT[8] = {LDS_D, LDS_R, LDS_R + 32768, LDS_X, LDS_X + 32768, LDS_D, LDS_X, LDS_X + 32768};
constexpr int VSLOT[8] = {LDS_R + 16384, LDS_R + 49152, LDS_X + 16384, LDS_X + 49152, LDS_R + 65536, LDS_X + 16384, LDS_X + 49152, LDS_D};
template <int N> struct IC { static constexpr int value = N; };
struct Params {
  const unsigned short *x, *kc, *vc, *wq, *wo, *w1, *w2, *w3, *w4, *c, *wmod;
  void* ws;
  u32x4* out;
};
DI unsigned attn_part(const Params& P, lds_ptr lds, unsigned lds0, unsigned w, __amdgpu_buffer_rsrc_t srdW) {
  LaneC c; lane_consts(c);
  const unsigned g = BIDX & 7, pn = BIDX >> 3;
  const unsigned l = c.lane, tid = TIDX;
  const unsigned ringoff = LDS_R + w * RINGB, ring = lds0 + ringoff;
  STAMP(1, 0);

  unsigned ep;
  asm volatile("buffer_load_dword %0, off, %1, 0 sc1" : "=v"(ep) : "s"(srdW) : "memory");
  u32x4 jw[4]; unsigned jc[4];
  auto c_load = [&](unsigned jks) __attribute__((always_inline)) {
#pragma unroll
    for (int i = 0; i < 4; ++i) asm volatile("global_load_ushort %0, %1, off" : "=v"(jc[i]) : "v"(P.c + jks * 64 + (l >> 2) + 16 * i) : "memory");
  };
  auto job_run = [&](unsigned jcol, unsigned jks, unsigned tag) __attribute__((always_inline)) {
    float acc[8] = {0, 0, 0, 0, 0, 0, 0, 0};
#pragma unroll
    for (int i = 0; i < 4; ++i) {
      const float v = U2F(jc[i] << 16);
      const float sv = v * __builtin_amdgcn_rcpf(1.0f + __builtin_amdgcn_exp2f(-1.4426950408889634f * v));
      const float sc = jcol >= 96u ? 1.0f : sv;
#pragma unroll
      for (int k = 0; k < 4; ++k) {
        acc[2 * k] = __builtin_fmaf(sc, bflo(jw[i][k]), acc[2 * k]);
        acc[2 * k + 1] = __builtin_fmaf(sc, bfhi(jw[i][k]), acc[2 * k + 1]);
      }
    }
#pragma unroll
    for (int k = 0; k < 8; ++k) acc[k] = sum_hi3(dpp_add<0x124>(acc[k]));
    const unsigned e = (l >> 2) & 3;
    const float v0 = e == 0 ? acc[0] : e == 1 ? acc[2] : e == 2 ? acc[4] : acc[6], v1 = e == 0 ? acc[1] : e == 1 ? acc[3] : e == 2 ? acc[5] : acc[7];
    __builtin_amdgcn_raw_buffer_store_b128((u32x4){F2U(v0), tag, F2U(v1), tag}, srdW,
                                           l < 16 ? W_MP + (jks * 4864u + jcol * 32u + (l & 3) * 8u + e * 2u) * 8u : OOB, 0, 16);
  };
  u32x4 xr[8];
  auto x_load = [&]() __attribute__((always_inline)) {
#pragma unroll
    for (int i = 0; i < 8; ++i) gld16(xr[i], P.x + (size_t)(pn * 64 + w * 8 + i) * D + l * 8);
  };
  if (w == 0) {
#pragma unroll
    for (int i = 0; i < 4; ++i) gld16(jw[i], P.wmod + (BIDX & 31u) * 32 + (l & 3) * 8 + (size_t)((BIDX >> 5) * 64 + (l >> 2) + 16 * i) * 3072);
    c_load(BIDX >> 5);
  } else x_load();
  asm volatile("s_waitcnt vmcnt(8)\n\ts_barrier" : "+v"(ep) :: "memory");
  const unsigned tag = rfl(ep) + 1u;
  if (w == 0) {
    asm volatile("s_waitcnt vmcnt(0)" : "+v"(jw[0]), "+v"(jw[1]), "+v"(jw[2]), "+v"(jw[3]), "+v"(jc[0]), "+v"(jc[1]), "+v"(jc[2]), "+v"(jc[3]) :: "memory");
    job_run(BIDX & 31u, BIDX >> 5, tag); x_load();
  }
  const unsigned jid = w * 256u + BIDX, jcol2 = 32u + jid % 120u, jks2 = jid / 120u;
  const bool job2 = w < 4 && jid < 960u;
  const __amdgpu_buffer_rsrc_t srdQ = mksrd(P.wq, 512u * 4096u * 2u);
  const unsigned vq = wdma_voff(l, 8192u), sq0 = (g * 512u + w * 64u) * 2u;
  const __amdgpu_buffer_rsrc_t srdK = mksrd(P.kc + ((size_t)g * SK + (SK - WIN)) * DH, WIN * DH * 2u);
  const __amdgpu_buffer_rsrc_t srdV = mksrd(P.vc + ((size_t)g * SK + (SK - WIN)) * DH, WIN * DH * 2u);
  const unsigned vk = ((l >> 2) & 7) * 256u + (l >> 5) * 64u + (((l & 3) ^ (2 * (w & 1) + ((l >> 4) & 1))) << 4);
  const unsigned vv = wdma_voff(l, 256u);
  auto dma_k = [&](int t, unsigned slot) {
    dma16(lds0 + slot + 2048u * w, vk, srdK, (unsigned)t * 16384u + 2048u * w);
    dma16(lds0 + slot + 2048u * w + 1024u, vk, srdK, (unsigned)t * 16384u + 2048u * w + 128u);
  };
  auto dma_v = [&](int t, unsigned slot) {
    dma16(lds0 + slot + 1024u * w, vv, srdV, (unsigned)t * 16384u + 2048u * w);
    dma16(lds0 + slot + 8192u + 1024u * w, vv, srdV, (unsigned)t * 16384u + 2048u * w + 128u);
  };

  {
    asm volatile("s_waitcnt vmcnt(0)" : "+v"(xr[0]), "+v"(xr[1]), "+v"(xr[2]), "+v"(xr[3]), "+v"(xr[4]), "+v"(xr[5]), "+v"(xr[6]),
                 "+v"(xr[7]) :: "memory");
    dma16(ring + 8192u, l * 16u, srdQ, (2 * (pn * 8 + w)) * 8192u + g * 1024u);
    dma16(ring + 9216u, l * 16u, srdQ, (2 * (pn * 8 + w) + 1) * 8192u + g * 1024u);
    wring_issue(0, ring, vq, srdQ, sq0, 8192u);
    wring_issue(1, ring, vq, srdQ, sq0, 8192u);
    float v[8][8], s[8], m[8];
#pragma unroll
    for (int i = 0; i < 8; ++i) {
#pragma unroll
      for (int j = 0; j < 4; ++j) { v[i][2 * j] = bflo(xr[i][j]); v[i][2 * j + 1] = bfhi(xr[i][j]); }
      s[i] = ((v[i][0] + v[i][1]) + (v[i][2] + v[i][3])) + ((v[i][4] + v[i][5]) + (v[i][6] + v[i][7]));
    }
    wave_sum8(s, m);
#pragma unroll
    for (int i = 0; i < 8; ++i) {
      const float mu = m[i] * (1.0f / D);
      float q = 0.f;
#pragma unroll
      for (int j = 0; j < 8; ++j) { v[i][j] -= mu; q = __builtin_fmaf(v[i][j], v[i][j], q); }
      s[i] = q;
    }
    wave_sum8(s, m);
    float rs[8];
#pragma unroll
    for (int i = 0; i < 8; ++i) rs[i] = __builtin_amdgcn_rsqf(m[i] * (1.0f / D) + LN_EPS);
    STAMP(1, 8);
    {
      u32x4 mp[8];
      poll_tag<8>(mp, srdW, tid * 16u, W_MP, 4864u * 8u, tag);
      f32x2 t;
#pragma unroll
      for (int k = 0; k < 2; ++k)
        t[k] = ((U2F(mp[0][2 * k]) + U2F(mp[1][2 * k])) + (U2F(mp[2][2 * k]) + U2F(mp[3][2 * k]))) +
               ((U2F(mp[4][2 * k]) + U2F(mp[5][2 * k])) + (U2F(mp[6][2 * k]) + U2F(mp[7][2 * k])));
      *(LAS f32x2*)(lds + LDS_D + tid * 8) = t;
    }
    BAR_L();
    if (BIDX == 0 && tid == 0) __builtin_amdgcn_raw_buffer_store_b32(tag, srdW, W_EPOCH, 0, 16);
    u32x4 mo[4];
#pragma unroll
    for (int i = 0; i < 4; ++i) mo[i] = *(const LAS u32x4*)(lds + LDS_D + (i >> 1) * 2048 + l * 32 + (i & 1) * 16);
    STAMP(1, 9);
#pragma unroll
    for (int i = 0; i < 8; ++i) {
      float hh[8];
#pragma unroll
      for (int j = 0; j < 8; ++j) hh[j] = __builtin_fmaf(v[i][j] * rs[i], 1.f + U2F(mo[2 + (j >> 2)][j & 3]), U2F(mo[j >> 2][j & 3]));
      const u32x4 o = {cvtpk(hh[0], hh[1]), cvtpk(hh[2], hh[3]), cvtpk(hh[4], hh[5]), cvtpk(hh[6], hh[7])};
      *(LAS u32x4*)(lds + LDS_X + ximg(l, w * 8 + i)) = o;
      if (i == 1 || i == 3 || i == 5) wring_issue(2 + i / 2, ring, vq, srdQ, sq0, 8192u);
    }
  }
  BAR_L();
  dma_k(0, LDS_D);
  STAMP(1, 1);

  {
    f32x4 acc[4][4] = {};
    gemm_phase<2>(acc, lds, ringoff, ring, vq, srdQ, sq0, 8192u, l);
    STAMP(1, 2);
    WAIT_BAR(0);
    dma_k(1, KSLOT[1]); dma_v(0, VSLOT[0]); dma_k(2, KSLOT[2]); dma_v(1, VSLOT[1]);
    if (job2) {
      const bool isw3 = jcol2 >= 96u;
      const unsigned ldb = isw3 ? 3584u : 6144u;
      const __amdgpu_buffer_rsrc_t srdJ = isw3 ? mksrd(P.w3, 512u * 3584u) : mksrd(P.wmod, 512u * 6144u);
#pragma unroll
      for (int i = 0; i < 4; ++i)
        dma16(lds0 + LDS_R + 65536u + w * 4096u + i * 1024u, (l >> 2) * ldb + (l & 3) * 16u, srdJ, (jks2 * 64 + 16 * i) * ldb + (isw3 ? jcol2 - 96u : jcol2) * 64u);
      c_load(jks2);
    }
    const unsigned cl = 8 * w + 2 * ((l >> 4) & 1) + (l >> 5);
#pragma unroll
    for (int r = 0; r < 4; ++r)
#pragma unroll
      for (int mp = 0; mp < 2; ++mp)
        *(LAS u32x4*)(lds + LDS_X + ximg(cl + 4 * mp, 16 * r + (l & 15))) = pack16(acc[2 * mp][r], acc[2 * mp + 1][r], QSCALE);
    BAR_L();
  }

  STAMP(1, 3);
  const unsigned hh = w >> 1, rt = w & 1;
  bf16x8 qf[8];
#pragma unroll
  for (int s = 0; s < 8; ++s) qf[s] = lds_b128(lds + LDS_X + ximg(16 * hh + 2 * s + c.h, 32 * rt + c.r));
  unsigned kb[2], vb[2];
#pragma unroll
  for (int i = 0; i < 2; ++i) {
    kb[i] = 2048 * (c.r >> 3) + 64 * (c.r & 7) + (((2 * i + c.h) ^ ((c.r >> 2) & 3)) << 4);
    vb[i] = (4 * c.h + c.q) * 128 + 64 * (i ^ (c.q >> 1)) + 32 * c.blk + 8 * (2 * (c.p & 1) + (c.p >> 1));
  }
  const __amdgpu_buffer_rsrc_t srdO = mksrd(P.wo, 4096u * 512u * 2u);
  const unsigned vo = wdma_voff(l, 1024u), so0 = g * 512u * 1024u + w * 128u;

  f32x16 o[4] = {};
  f32x16 s0, s1;
  f32x16 negm;
  float lsum = 0.f;
  u32x4 pk[4];
  constexpr int PD = 6;
  bf16x8 fr[PD];
  const bool lag = w >= 4;
  auto frag = [&](int t, int it) -> bf16x8 {
    if (it < 16) {
      const lds_cptr a = lds + VSLOT[t > 0 ? t - 1 : 0] + vb[it & 1] + 8192 * ((it >> 1) & 1) + 2048 * (it >> 2);
      return cat8(lds_tr(a), lds_tr(a + 1024));
    }
    const int s = (it - 16) >> 1;
    return lds_b128(lds + KSLOT[t < 8 ? t : 0] + kb[s & 1] + 512 * (s >> 1) + 8192 * (it & 1));
  };
  auto seam = [&](int t, int tx) __attribute__((always_inline)) {
    if (t == 7) { BAR_L(); } else if (tx >= 1 && tx <= 6) { WAIT_BAR(4); } else { WAIT_BAR(0); }
  };
  auto refill = [&](int t) __attribute__((always_inline)) {
    if (t + 3 < 8) dma_k(t + 3, KSLOT[t + 3 < 8 ? t + 3 : 0]);
    if (t + 2 < 8) dma_v(t + 2, VSLOT[t + 2 < 8 ? t + 2 : 0]);
    if (t == 6) {
#pragma unroll
      for (int ks = 0; ks < RING; ++ks) wring_issue(ks, ring, vo, srdO, so0, 1024u);
    }
  };
  auto extra = [&](int e) __attribute__((always_inline)) {
    const unsigned dump = lds0 + LDS_R + w * 3072u + (unsigned)(e % 3) * 1024u;
    const unsigned id = (BIDX >> 3) * 8 + w, jh = BIDX & 1;
    const unsigned vrow = (l >> 5) * 1024u + (l & 31) * 16u + jh * 512u;
    auto w3v = [&](unsigned piece) { const unsigned byte = piece * 1024u + l * 16u, row = byte / 1792u;
                                     return row * 3584u + jh * 1792u + (byte - row * 1792u); };
    if (e == 15) {
      const unsigned pn2 = 8 * ((BIDX & 7) >> 1) + (BIDX >> 5);
      dma16(dump, l * 16u, srdW, W_P1 + ((w & 1) ? pn2 : pn) * 524288u + (w >> 1) * 131072u);
    } else if (e == 16) {
      dma16(dump, l * 16u, mksrd(P.out, T * 1024u), (8 * ((BIDX & 7) >> 1) + (BIDX >> 5)) * 65536u + w * 8192u);
    } else if (e < 4 || e >= 17) {
      const int i = e < 4 ? e : e - 13;
      const unsigned off = i < 3 ? S_U + (unsigned)i * (64u * T * 16u) + pn * 65536u + g * 8192u
                         : i < 7 ? S_HZ + pn * 262144u + g * 32768u + (unsigned)(i - 3) * 8192u
                                 : S_H3 + pn * 229376u + g * 28672u + (unsigned)(i - 7) * 8192u;
      const u32x4 sv = {SENT, SENT, SENT, SENT};
      __builtin_amdgcn_raw_buffer_store_b128(sv, srdW, (i == 10 && tid >= 256) ? OOB : tid * 16u + off, 0, 16);
    } else if (e < 6) dma16(dump, l * 16u, srdO, (g * 512u + (pn * 8 + w) * 2 + (e - 4)) * 1024u);
    else if (e == 6) dma16(dump, vrow, mksrd(P.w1, 512u * 1024u), id * 2048u);
    else if (e == 7) dma16(dump, vrow, mksrd(P.w2, 512u * 1024u), id * 2048u);
    else if (e < 11) dma16(dump, w3v(id + 256u * (e - 8)), mksrd(P.w3, 512u * 3584u), 0u);
    else if (e == 11) {
      if (id < 128) dma16(dump, w3v(id + 768u), mksrd(P.w3, 512u * 3584u), 0u);
      else dma16(dump, vrow, mksrd(P.w4, 1792u * 1024u), (id - 128u + 768u) * 2048u);
    } else dma16(dump, vrow, mksrd(P.w4, 1792u * 1024u), (id + 256u * (e - 12)) * 2048u);
  };
  auto step = [&](auto tc) __attribute__((always_inline)) {
    constexpr int t = decltype(tc)::value;
    constexpr int i0 = t == 0 ? 16 : 0, i1 = t == 8 ? 16 : 32;
    if (t <= 1) {
#pragma unroll
      for (int it = i0; it < i0 + PD; ++it) fr[it % PD] = frag(t, it);
    }
    if (t == 0) { s0 = (f32x16){}; s1 = (f32x16){}; } else if (t < 8) { s0 = negm; s1 = negm; }
    __builtin_amdgcn_s_setprio(1);
    SBAR();
#pragma unroll
    for (int it = i0; it < i1; ++it) {
      if (it < 16) o[it & 3] = MFMA32(fr[it % PD], __builtin_bit_cast(bf16x8, pk[it >> 2]), o[it & 3]);
      else if (it & 1) s1 = MFMA32(fr[it % PD], qf[(it - 16) >> 1], s1);
      else s0 = MFMA32(fr[it % PD], qf[(it - 16) >> 1], s0);
      if (it + PD < i1) fr[it % PD] = frag(t, it + PD);
      if (t >= 1 && it == 1) { if (!lag) refill(t - 1); }
      SBAR();
    }
    if constexpr (t == 8) return;
    __builtin_amdgcn_s_setprio(0);
    if (lag) { seam(t, t - 1); refill(t); }
    asm volatile("s_nop 15" : "+v"(s0), "+v"(s1));
    float ta = max3f(s0[0], s0[1], s1[0]), tb = max3f(s0[2], s0[3], s1[1]);
    ta = max3f(ta, s1[2], s1[3]);
#pragma unroll
    for (int i = 4; i < 16; i += 4) {
      ta = max3f(ta, s0[i], s0[i + 1]); tb = max3f(tb, s0[i + 2], s0[i + 3]);
      ta = max3f(ta, s1[i], s1[i + 1]); tb = max3f(tb, s1[i + 2], s1[i + 3]);
    }
    float tm = max2f(ta, tb);
    if (t == 0 || __builtin_expect(rfl(ANY(tm > THR)), 0)) {
      {
        auto r = __builtin_amdgcn_permlane32_swap(F2U(tm), F2U(tm), false, false);
        tm = max2f(U2F(r[0]), U2F(r[1]));
      }
      const float ndl = t == 0 ? -tm : -max2f(tm, 0.f);
      const float aone = c.h ? 0.f : 1.f;
      if (t == 0) negm = (f32x16){};
      __builtin_amdgcn_s_setprio(2);
      s0 = __builtin_amdgcn_mfma_f32_32x32x2f32(aone, ndl, s0, 0, 0, 0);
      s1 = __builtin_amdgcn_mfma_f32_32x32x2f32(aone, ndl, s1, 0, 0, 0);
      negm = __builtin_amdgcn_mfma_f32_32x32x2f32(aone, ndl, negm, 0, 0, 0);
      __builtin_amdgcn_s_setprio(0);
      if (t > 0) {
        const float al = __builtin_amdgcn_exp2f(ndl);
        lsum *= al;
#pragma unroll
        for (int dt = 0; dt < 4; ++dt)
#pragma unroll
          for (int i = 0; i < 16; ++i) o[dt][i] *= al;
      }
    }
    SBAR();
    if (t >= 1) {
#pragma unroll
      for (int it = 0; it < PD; ++it) fr[it] = frag(t + 1, it);
    }
#pragma unroll
    for (int i = 0; i < 16; ++i) {
      s0[i] = __builtin_amdgcn_exp2f(s0[i]); s1[i] = __builtin_amdgcn_exp2f(s1[i]);
      if (t >= 1 && (i & 3) == 1 && 4 * (t - 1) + (i >> 2) < 24) { PIN(s0[i]); extra(4 * (t - 1) + (i >> 2)); }
    }
    pk[0] = pack8(s0, 0, 1.f); pk[1] = pack8(s0, 1, 1.f); pk[2] = pack8(s1, 0, 1.f); pk[3] = pack8(s1, 1, 1.f);
    {
      float l0 = lsum, l1 = 0.f, l2 = 0.f, l3 = 0.f;
#pragma unroll
      for (int i = 0; i < 16; i += 2) { l0 += s0[i]; l1 += s0[i + 1]; l2 += s1[i]; l3 += s1[i + 1]; }
      lsum = (l0 + l1) + (l2 + l3);
    }
    PIN(pk[0]); PIN(pk[1]); PIN(pk[2]); PIN(pk[3]); PIN(lsum);
    if constexpr (t == 1) {
      if (job2) {
        asm volatile("" : "+v"(jc[0]), "+v"(jc[1]), "+v"(jc[2]), "+v"(jc[3]));
#pragma unroll
        for (int i = 0; i < 4; ++i) jw[i] = *(const LAS u32x4*)(lds + LDS_R + 65536 + w * 4096 + i * 1024 + l * 16);
        job_run(jcol2, jks2, tag);
      }
    }
    if constexpr (t == 4) {
      if (BIDX < 120 && w == 0) {
        u32x4 mp[2];
        poll_tag<2>(mp, srdW, ((l >> 3) * 4864u + (32u + BIDX) * 32u + (l & 7) * 2u) * 8u, W_MP, 128u, tag);
        const float a0 = sum_hi3(U2F(mp[0][0])), a1 = sum_hi3(U2F(mp[0][2])), b0 = sum_hi3(U2F(mp[1][0])), b1 = sum_hi3(U2F(mp[1][2]));
        const bool hi = (l >> 3) & 1;
        __builtin_amdgcn_raw_buffer_store_b128((u32x4){F2U(hi ? b0 : a0), tag, F2U(hi ? b1 : a1), tag}, srdW,
                                               l < 16 ? W_MOD + ((32u + BIDX) * 32u + l * 2u) * 8u : OOB, 0, 16);
      }
    }
    SBAR();
    if (!lag) seam(t, t);
  };
  step(IC<0>{}); step(IC<1>{}); step(IC<2>{}); step(IC<3>{}); step(IC<4>{}); step(IC<5>{}); step(IC<6>{}); step(IC<7>{}); step(IC<8>{});
  __builtin_amdgcn_s_setprio(0);
  STAMP(1, 4);
  {
    const float il = __builtin_amdgcn_rcpf(swap32_add(lsum));
#pragma unroll
    for (int dt = 0; dt < 4; ++dt)
#pragma unroll
      for (int s = 0; s < 2; ++s)
        *(LAS u32x4*)(lds + LDS_X + ximg(16 * hh + 4 * dt + 2 * s + c.h, 32 * rt + c.r)) = pack8(o[dt], s, il);
  }
  BAR_L();
  STAMP(1, 5);
  dma16_sc1(lds0 + LDS_D + w * 2048u, l * 16u, srdW, W_MOD + 8192u + w * 2048u);
  dma16_sc1(lds0 + LDS_D + w * 2048u + 1024u, l * 16u, srdW, W_MOD + 8192u + w * 2048u + 1024u);
  {
    unsigned l2 = TIDX & 63;
    asm volatile("" : "+v"(l2));
    f32x4 acc[4][4] = {};
    gemm_phase<2>(acc, lds, ringoff, ring, vo, srdO, so0, 1024u, l2);
    STAMP(1, 6);
    const unsigned dst = W_P1 + ((((pn * 8 + ((l2 >> 3) & 1)) * 8 + g) * 64 + 8 * w + 2 * ((l2 >> 4) & 1) + (l2 >> 5)) * 8 + (l2 & 7)) * 16u;
#pragma unroll
    for (int r = 0; r < 4; ++r)
#pragma unroll
      for (int mp = 0; mp < 2; ++mp)
        __builtin_amdgcn_raw_buffer_store_b128(pack16(acc[2 * mp][r], acc[2 * mp + 1][r], 1.f), srdW,
                                               dst + (4 * mp * 8 + r * 2 * 8 * 64 * 8) * 16u, 0, 16);
  }
  WAIT_BAR(0);
  if (tid == 0) __builtin_amdgcn_raw_buffer_store_b32(tag, srdW, W_FLAG + (pn * 8 + g) * 64u, 0, 16);
  STAMP(1, 7);
  return tag;
}

DI void ln8(float (&v)[8], LAS float* red  , unsigned tid) {
  float s = ((v[0] + v[1]) + (v[2] + v[3])) + ((v[4] + v[5]) + (v[6] + v[7]));
  float q = (__builtin_fmaf(v[0], v[0], v[1] * v[1]) + __builtin_fmaf(v[2], v[2], v[3] * v[3])) +
            (__builtin_fmaf(v[4], v[4], v[5] * v[5]) + __builtin_fmaf(v[6], v[6], v[7] * v[7]));
  s = sum_hi3(s); q = sum_hi3(q);
  if ((tid & 63) < 8) *(LAS f32x2*)(red + ((tid >> 6) * 8 + (tid & 7)) * 2) = (f32x2){s, q};
  BAR_L();
  f32x2 e[8];
#pragma unroll
  for (int i = 0; i < 8; ++i) e[i] = *(const LAS f32x2*)(red + (i * 8 + (tid & 7)) * 2);
  const float mu = (((e[0][0] + e[1][0]) + (e[2][0] + e[3][0])) + ((e[4][0] + e[5][0]) + (e[6][0] + e[7][0]))) * (1.0f / D);
  const float ms = (((e[0][1] + e[1][1]) + (e[2][1] + e[3][1])) + ((e[4][1] + e[5][1]) + (e[6][1] + e[7][1]))) * (1.0f / D);
  const float rs = __builtin_amdgcn_rsqf(__builtin_fmaxf(__builtin_fmaf(-mu, mu, ms), 0.f) + LN_EPS);
  const float nb = -mu * rs;
#pragma unroll
  for (int k = 0; k < 8; ++k) v[k] = __builtin_fmaf(v[k], rs, nb);
}
DI void red4_put(const f32x16& a, int nt, lds_ptr scr, unsigned rt, unsigned kq, unsigned l) {
#pragma unroll
  for (int g = 0; g < 2; ++g)
#pragma unroll
    for (int i = 0; i < 2; ++i) {
      const int o = 8 * g + 4 * i;
      *(LAS f32x4*)(scr + ((rt * 4 + 2 * nt + g) * 4 + kq) * 2048 + i * 1024 + l * 16) = (f32x4){a[o], a[o + 1], a[o + 2], a[o + 3]};
    }
}
DI void red4_get(float (&v)[8], lds_cptr scr, unsigned rt, unsigned kq, unsigned l) {
  f32x4 t[4][2];
#pragma unroll
  for (int s = 0; s < 4; ++s)
#pragma unroll
    for (int i = 0; i < 2; ++i) t[s][i] = *(const LAS f32x4*)(scr + ((rt * 4 + kq) * 4 + s) * 2048 + i * 1024 + l * 16);
#pragma unroll
  for (int i = 0; i < 2; ++i)
#pragma unroll
    for (int k = 0; k < 4; ++k) v[4 * i + k] = (t[0][i][k] + t[1][i][k]) + (t[2][i][k] + t[3][i][k]);
}
DI void tile_mm8(const bf16x8 (&af)[8][2], const u32x4 (&xb)[8], float (&v)[8], lds_ptr scr, unsigned rt, unsigned kq, unsigned l) {
  f32x16 a0 = {}, a1 = {};
#pragma unroll
  for (int i = 0; i < 8; ++i) a0 = MFMA32(af[i][0], __builtin_bit_cast(bf16x8, xb[i]), a0);
  SBAR();
#pragma unroll
  for (int i = 0; i < 4; ++i) a1 = MFMA32(af[i][1], __builtin_bit_cast(bf16x8, xb[i]), a1);
  SBAR();
  red4_put(a0, 0, scr, rt, kq, l);
#pragma unroll
  for (int i = 4; i < 8; ++i) a1 = MFMA32(af[i][1], __builtin_bit_cast(bf16x8, xb[i]), a1);
  SBAR();
  red4_put(a1, 1, scr, rt, kq, l);
  BAR_L();
  red4_get(v, scr, rt, kq, l);
}

struct Frag3 { bf16x8 a, b[2]; };
constexpr int RING3 = 12;
constexpr int LA = 0, LB = 65536, LC = 131072;
DI unsigned r3off(int s, unsigned rc, unsigned rb) { return s < 4 ? rc + s * 1024u : rb + (s - 4) * 1024u; }
template <int KS> DI void load_frags3(Frag3& f, lds_cptr lds, unsigned rc, unsigned rb, unsigned a3, const LaneC& c) {
  const lds_cptr sl = lds + r3off(KS % RING3, rc, rb) + a3;
  f.a = cat8(lds_tr(sl), lds_tr(sl + 256));
#pragma unroll
  for (int rt = 0; rt < 2; ++rt) f.b[rt] = lds_b128(lds + LA + c.xb[KS & 3] + 2048 * KS + 512 * rt);
}
constexpr int w4slot(int n) { return (8 + n) % RING3; }
template <int KS>
DI void g3_step(f32x16 (&acc)[2], Frag3& cur, Frag3& n1, Frag3& n2, lds_cptr lds, unsigned lds0, unsigned rc, unsigned rb,
                unsigned voff, __amdgpu_buffer_rsrc_t srd, unsigned soff0, unsigned a3, const LaneC& c,
                unsigned v4, __amdgpu_buffer_rsrc_t srd4, unsigned s40) {
  constexpr int NKS = 32;
  if constexpr (KS + 2 < NKS) {
    constexpr int last = (KS + RING3 < NKS ? KS + RING3 : NKS) - 1;
    wait_vm<last - (KS + 2) + (KS > NKS - RING3 ? KS - (NKS - RING3) : 0)>();
    load_frags3<KS + 2>(n2, lds, rc, rb, a3, c);
  }
  SBAR();
  acc[0] = MFMA32(cur.a, cur.b[0], acc[0]);
  acc[1] = MFMA32(cur.a, cur.b[1], acc[1]);
  asm volatile("" :: "v"(cur.a));
  if constexpr (KS + RING3 < NKS) dma16(lds0 + r3off(KS % RING3, rc, rb), voff, srd, soff0 + (unsigned)(16 * (KS + RING3)) * 3584u);
  else dma16(lds0 + r3off(KS % RING3, rc, rb), v4, srd4, s40 + (unsigned)(KS - (NKS - RING3)) * 16384u);
  SBAR();
  if constexpr (KS + 1 < NKS) g3_step<KS + 1>(acc, n1, n2, cur, lds, lds0, rc, rb, voff, srd, soff0, a3, c, v4, srd4, s40);
}

DI void mlp_part(const Params& P, lds_ptr lds, unsigned lds0, unsigned w, __amdgpu_buffer_rsrc_t srdS, unsigned tag) {
  LaneC c; lane_consts(c);
  const unsigned idx = BIDX >> 3, pn = 8 * ((BIDX & 7) >> 1) + (idx >> 2), j = 4 * (BIDX & 1) + (idx & 3);
  const unsigned tid = TIDX, l = c.lane;
  const unsigned vw = wdma_voff(l, 1024u);
  const unsigned rt = w & 1, kq = w >> 1;
  constexpr int LMOD = LDS_D;
  constexpr int LRED = LC + 12288;
  constexpr int LST = LB + 7 * 8192;
  const unsigned prow = 32 * rt + c.r;
  const unsigned trow = pn * 64 + prow;
  const unsigned och = 8 * j + 2 * kq + c.h;
  const unsigned ovo = ((pn * 64 + och) * 64 + prow) * 16u;
  float gb[8];
  STAMP(2, 0);

  {
    const unsigned row = pn * 64 + j * 8 + (tid & 7), ch = tid >> 3;
    u32x4 xr;
    gld16(xr, P.x + (size_t)row * D + ch * 8);
    auto wait_flags = [&](unsigned fo) {
      for (unsigned spin = 0;; ++spin) {
        asm volatile("" ::: "memory");
        const unsigned f = __builtin_amdgcn_raw_buffer_load_b32(srdS, fo, 0, 16);
        if (!ANY(fo != OOB && f != tag) || spin > SPIN_CAP) break;
        __builtin_amdgcn_s_sleep(FLAG_SLEEP);
      }
    };
    const __amdgpu_buffer_rsrc_t srd1 = mksrd(P.w1, 512u * 512u * 2u), srd2 = mksrd(P.w2, 512u * 512u * 2u);
#pragma unroll
    for (int i = 0; i < 8; ++i) dma16(lds0 + LA + (8 * w + i) * 1024u, vw, srd1, (8 * w + i) * 8192u + j * 128u);
    {
      const int fp = (int)pn - 1 + (int)(l >> 3);
      if (w == 0) wait_flags((l < 24 && fp >= 0 && fp < 32) ? W_FLAG + ((unsigned)fp * 8 + (l & 7)) * 64u : OOB);
      BAR_L();
    }
    STAMP(2, 15);
    u32x4 p[8];
#pragma unroll
    for (int g = 0; g < 8; ++g) bld16_sc1(p[g], tid * 16u, srdS, W_P1 + ((pn * 8 + j) * 8 + g) * 8192u);
    float ga[8], sb[8], cb[8], g2[8];
    {
      u32x4 mv[20];
#pragma unroll
      for (int i = 0; i < 16; ++i) mv[i] = *(const LAS u32x4*)(lds + LMOD + (i >> 2) * 4096 + ch * 64 + 16 * (i & 3));
#pragma unroll
      for (int i = 0; i < 4; ++i) mv[16 + i] = *(const LAS u32x4*)(lds + LMOD + 12288 + och * 64 + 16 * i);
      if (ANY(bad_tag(mv, 20, tag))) {
#pragma unroll
        for (int i = 0; i < 4; ++i) poll_tag<4>(mv + 4 * i, srdS, ch * 64u, W_MOD + 8192u + i * 4096u, 16u, tag);
        poll_tag<4>(mv + 16, srdS, och * 64u, W_MOD + 20480u, 16u, tag);
      }
#pragma unroll
      for (int k = 0; k < 8; ++k) {
        ga[k] = U2F(mv[k >> 1][2 * (k & 1)]); sb[k] = U2F(mv[4 + (k >> 1)][2 * (k & 1)]); cb[k] = 1.f + U2F(mv[8 + (k >> 1)][2 * (k & 1)]);
        g2[k] = U2F(mv[12 + (k >> 1)][2 * (k & 1)]); gb[k] = U2F(mv[16 + (k >> 1)][2 * (k & 1)]);
      }
    }
    asm volatile("s_waitcnt vmcnt(0)" : "+v"(xr), "+v"(p[0]), "+v"(p[1]), "+v"(p[2]), "+v"(p[3]), "+v"(p[4]), "+v"(p[5]),
                 "+v"(p[6]), "+v"(p[7]) :: "memory");
    STAMP(2, 1);
    float v[8], x2[8];
#pragma unroll
    for (int i = 0; i < 4; ++i) {
      const float lo = ((bflo(p[0][i]) + bflo(p[1][i])) + (bflo(p[2][i]) + bflo(p[3][i]))) + ((bflo(p[4][i]) + bflo(p[5][i])) + (bflo(p[6][i]) + bflo(p[7][i])));
      const float hi = ((bfhi(p[0][i]) + bfhi(p[1][i])) + (bfhi(p[2][i]) + bfhi(p[3][i]))) + ((bfhi(p[4][i]) + bfhi(p[5][i])) + (bfhi(p[6][i]) + bfhi(p[7][i])));
      v[2 * i] = x2[2 * i] = __builtin_fmaf(ga[2 * i], lo, bflo(xr[i]));
      v[2 * i + 1] = x2[2 * i + 1] = __builtin_fmaf(ga[2 * i + 1], hi, bfhi(xr[i]));
    }
    ln8(v, (LAS float*)(lds + LRED), tid);
    float hb[8];
#pragma unroll
    for (int k = 0; k < 8; ++k) v[k] = hb[k] = __builtin_fmaf(v[k], cb[k], sb[k]);
    ln8(v, (LAS float*)(lds + LRED + 512), tid);
    const unsigned vo = ((pn * 64 + ch) * 64 + j * 8 + (tid & 7)) * 16u, vz = ((pn * 64 + ch) * 256 + j * 8 + (tid & 7)) * 16u;
    pub16(srdS, vo, S_U, (u32x4){cvtpk(v[0], v[1]), cvtpk(v[2], v[3]), cvtpk(v[4], v[5]), cvtpk(v[6], v[7])});
#pragma unroll
    for (int i = 0; i < 2; ++i) {
      pub16(srdS, vz, S_HZ + i * 1024u, (u32x4){F2U(hb[4 * i]), F2U(hb[4 * i + 1]),
                                                F2U(hb[4 * i + 2]), F2U(hb[4 * i + 3])});
      f32x4 z;
#pragma unroll
      for (int e = 0; e < 4; ++e) z[e] = __builtin_fmaf(g2[4 * i + e], hb[4 * i + e], x2[4 * i + e]);
      pub16(srdS, vz, S_HZ + (2 + i) * 1024u, __builtin_bit_cast(u32x4, z));
    }
#pragma unroll
    for (int i = 0; i < 8; ++i) dma16(lds0 + LB + (8 * w + i) * 1024u, vw, srd2, (8 * w + i) * 8192u + j * 128u);
  }
  STAMP(2, 2);
  const __amdgpu_buffer_rsrc_t srd3 = mksrd(P.w3, 512u * F * 2u);
  const unsigned rc = LC + w * 4096u, rb = LB + w * 8192u;
  const unsigned v3 = (l >> 2) * 3584u + ((l & 3) << 4), s30 = (j * 224u + w * 32u) * 2u;
  float v[8];
  {
    bf16x8 af[8][2];
#pragma unroll
    for (int i = 0; i < 8; ++i)
#pragma unroll
      for (int nt = 0; nt < 2; ++nt) {
        const lds_cptr a = lds + LA + (8 * kq + i) * 2048 + c.afr[nt];
        af[i][nt] = cat8(lds_tr(a), lds_tr(a + 512));
      }
    BAR_L();
    u32x4 xb[8];
    poll_ind<8, 3, 7>(xb, srdS, ((pn * 64 + 16 * kq + c.h) * 64 + prow) * 16u, S_U, 2048u);
    STAMP(2, 3);
    tile_mm8(af, xb, v, lds + LA, rt, kq, l);
#pragma unroll
    for (int k = 0; k < 8; ++k) v[k] = gelu_tanh(v[k]);
    pub16(srdS, ovo, S_H1, (u32x4){cvtpk(v[0], v[1]), cvtpk(v[2], v[3]), cvtpk(v[4], v[5]), cvtpk(v[6], v[7])});
    if (w < 7) {
#pragma unroll
      for (int ks = 0; ks < 4; ++ks) dma16(lds0 + r3off(ks, rc, rb), v3, srd3, s30 + (unsigned)(16 * ks) * 3584u);
    }
  }
  STAMP(2, 4);
  float y[8];
  float cs[16];
  const unsigned cso = w < 7 ? (3072 + 224 * j + 32 * w + 8 * c.h) * 8u : OOB;
  {
    bf16x8 af[8][2];
#pragma unroll
    for (int i = 0; i < 8; ++i)
#pragma unroll
      for (int nt = 0; nt < 2; ++nt) {
        const lds_cptr a = lds + LB + (8 * kq + i) * 2048 + c.afr[nt];
        af[i][nt] = cat8(lds_tr(a), lds_tr(a + 512));
      }
    BAR_L();
    const int sh = kq == 0 ? -1 : kq == 1 ? 1 : kq == 2 ? -2 : 2;
    const int srow = (int)trow + sh;
    const unsigned xvo = (srow >= 0 && srow < T) ? ((((unsigned)srow >> 6) * 64 + 16 * kq + c.h) * 64 + ((unsigned)srow & 63)) * 16u : OOB;
    u32x4 xb[8];
    poll_ind<8, 3, 7>(xb, srdS, xvo, S_H1, 2048u);
    STAMP(2, 5);
    u32x4 hz[4];
    const unsigned hzo = ((pn * 64 + och) * 256 + prow) * 16u;
#pragma unroll
    for (int i = 0; i < 4; ++i) hz[i] = __builtin_amdgcn_raw_buffer_load_b128(srdS, hzo, S_HZ + i * 1024u, 16);
    u32x4 cg[8];
#pragma unroll
    for (int i = 0; i < 8; ++i) cg[i] = __builtin_amdgcn_raw_buffer_load_b128(srdS, cso, W_MOD + 128u * (i >> 2) + 16u * (i & 3), 16);
    tile_mm8(af, xb, v, lds + LA, rt, kq, l);
    if (has_sent(hz, 4)) poll_load<4>(hz, srdS, hzo, S_HZ, 1024u);
    float x1[8];
#pragma unroll
    for (int k = 0; k < 8; ++k) {
      x1[k] = U2F(hz[k >> 2][k & 3]) + v[k];
      y[k] = __builtin_fmaf(gb[k], v[k], U2F(hz[2 + (k >> 2)][k & 3]));
    }
    pub16(srdS, ovo, S_X1, (u32x4){cvtpk(x1[0], x1[1]), cvtpk(x1[2], x1[3]), cvtpk(x1[4], x1[5]), cvtpk(x1[6], x1[7])});
    if (ANY(cso < OOB && bad_tag(cg, 8, tag))) {
      poll_tag<4>(cg, srdS, cso, W_MOD, 16u, tag);
      poll_tag<4>(cg + 4, srdS, cso, W_MOD + 128u, 16u, tag);
    }
#pragma unroll
    for (int i = 0; i < 16; ++i) cs[i] = U2F(cg[i >> 1][2 * (i & 1)]);
    if (w < 7) {
#pragma unroll
      for (int ks = 4; ks < RING3; ++ks) dma16(lds0 + r3off(ks, rc, rb), v3, srd3, s30 + (unsigned)(16 * ks) * 3584u);
    }
  }
  STAMP(2, 6);
  const __amdgpu_buffer_rsrc_t srd4 = mksrd(P.w4, (unsigned)F * 512u * 2u);
  const unsigned s40 = (448u * kq + 8u * rt) * 1024u + j * 128u;
  {
    u32x4 xr[8];
    BAR_L();
    poll_ind<8, 3, 7>(xr, srdS, ((pn * 64 + 8 * w) * 64 + l) * 16u, S_X1, 1024u);
    STAMP(2, 7);
#pragma unroll
    for (int i = 0; i < 8; ++i) *(LAS u32x4*)(lds + LA + ximg(8 * w + i, l)) = xr[i];
    {
      float s0 = 0.f, s1 = 0.f, q0 = 0.f, q1 = 0.f;
#pragma unroll
      for (int i = 0; i < 8; ++i)
#pragma unroll
        for (int k = 0; k < 4; k += 2) {
          s0 = dot2bf(xr[i][k], 0x3F803F80u, s0); s1 = dot2bf(xr[i][k + 1], 0x3F803F80u, s1);
          q0 = dot2bf(xr[i][k], xr[i][k], q0); q1 = dot2bf(xr[i][k + 1], xr[i][k + 1], q1);
        }
      *(LAS f32x2*)(lds + LST + (w * 64 + l) * 8) = (f32x2){s0 + s1, q0 + q1};
    }
    BAR_L();
    STAMP(2, 8);
    if (w < 7) {
      f32x16 acc[2] = {};
      const unsigned a3 = (8 * c.h + c.q) * 64 + 32 * c.blk + 8 * (2 * (c.p & 1) + (c.p >> 1));
      Frag3 f0, f1, f2;
      wait_vm<RING3 - 2>();
      load_frags3<0>(f0, lds, rc, rb, a3, c);
      load_frags3<1>(f1, lds, rc, rb, a3, c);
      float mu[2], rs[2];
#pragma unroll
      for (int r2 = 0; r2 < 2; ++r2) {
        float s = 0.f, q = 0.f;
#pragma unroll
        for (int i = 0; i < 8; ++i) {
          const f32x2 t = *(const LAS f32x2*)(lds + LST + (i * 64 + 32 * r2 + c.r) * 8);
          s += t[0]; q += t[1];
        }
        mu[r2] = s * (1.0f / D);
        rs[r2] = __builtin_amdgcn_rsqf(__builtin_fmaxf(__builtin_fmaf(-mu[r2], mu[r2], q * (1.0f / D)), 0.f) + LN_EPS);
      }
      if (w < 4) __builtin_amdgcn_s_setprio(2); else __builtin_amdgcn_s_setprio(1);
      g3_step<0>(acc, f0, f1, f2, lds, lds0, rc, rb, v3, srd3, s30, a3, c, vw, srd4, s40);
      __builtin_amdgcn_s_setprio(0);
      STAMP(2, 9);
#pragma unroll
      for (int r2 = 0; r2 < 2; ++r2) {
#pragma unroll
        for (int i = 0; i < 16; ++i) acc[r2][i] = gelu_tanh(rs[r2] * __builtin_fmaf(-mu[r2], cs[i], acc[r2][i]));
#pragma unroll
        for (int s2 = 0; s2 < 2; ++s2)
          pub16(srdS, ((pn * 224 + 28 * j + 4 * w + 2 * s2 + c.h) * 64 + 32 * r2 + c.r) * 16u, S_H3, pack8(acc[r2], s2, 1.f));
      }
    } else {
#pragma unroll
      for (int n = 0; n < 8; ++n) dma16(lds0 + r3off(w4slot(n), rc, rb), vw, srd4, s40 + (unsigned)n * 16384u);
    }
    BAR_L();
  }
  STAMP(2, 10);
  {
    auto poff = [&](int ks, unsigned wv) -> unsigned {
      return (ks >= 12 && ks < 20) ? LA + wv * 8192u + (unsigned)(ks - 12) * 1024u : r3off(w4slot(ks % 20), LC + wv * 4096u, LB + wv * 8192u);
    };
    auto w4dma = [&](int ks) { dma16(lds0 + poff(ks, w), vw, srd4, s40 + (unsigned)ks * 16384u); };
    if (w == 7) {
#pragma unroll
      for (int ks = 8; ks < 12; ++ks) w4dma(ks);
    }
#pragma unroll
    for (int ks = 12; ks < 20; ++ks) w4dma(ks);
    const unsigned wsrc = 2 * kq + c.h;
    const unsigned hvo = ((pn * 224 + 56 * kq + c.h) * 64 + prow) * 16u;
    u32x4 ha[14], hb[14];
    poll_ind<14, 9, 13>(ha, srdS, hvo, S_H3, 2048u);
    BAR_L();
    STAMP(2, 11);
    f32x16 acc[2] = {};
    bf16x8 fa[2][2];
    auto ld = [&](int ks) {
#pragma unroll
      for (int nt = 0; nt < 2; ++nt) {
        const lds_cptr a = lds + poff(ks, wsrc) + (c.afr[nt] - 1024u * c.h);
        fa[ks & 1][nt] = cat8(lds_tr(a), lds_tr(a + 512));
      }
    };
    auto mm = [&](int ks, const u32x4& b) {
#pragma unroll
      for (int nt = 0; nt < 2; ++nt) acc[nt] = MFMA32(fa[ks & 1][nt], __builtin_bit_cast(bf16x8, b), acc[nt]);
    };
    auto hbl = [&](int i) { bld16_sc1(hb[i], hvo, srdS, S_H3 + (14u + i) * 2048u); };
    ld(0);
#pragma unroll
    for (int ks = 0; ks < 8; ++ks) {
      if (ks < 7) ld(ks + 1);
      SBAR(); mm(ks, ha[ks]);
      if (ks < 7) { hbl(2 * ks); hbl(2 * ks + 1); }
      SBAR();
    }
    BAR_L();
    ld(8);
#pragma unroll
    for (int ks = 8; ks < 14; ++ks) {
      if (ks < 13) ld(ks + 1);
      SBAR(); mm(ks, ha[ks]);
      if (ks < 12) { w4dma(20 + 2 * (ks - 8)); w4dma(21 + 2 * (ks - 8)); }
      SBAR();
    }
    asm volatile("s_waitcnt vmcnt(8) lgkmcnt(0)\n\ts_barrier" : "+v"(hb[0]), "+v"(hb[1]), "+v"(hb[2]), "+v"(hb[3]), "+v"(hb[4]), "+v"(hb[5]),
                 "+v"(hb[6]), "+v"(hb[7]), "+v"(hb[8]), "+v"(hb[9]), "+v"(hb[10]), "+v"(hb[11]), "+v"(hb[12]), "+v"(hb[13]) :: "memory");
    if (has_sent(hb, 14)) poll_load<14>(hb, srdS, hvo, S_H3 + 14u * 2048u, 2048u);
    STAMP(2, 12);
    ld(14);
#pragma unroll
    for (int ks = 14; ks < 19; ++ks) { ld(ks + 1); SBAR(); mm(ks, hb[ks - 14]); SBAR(); }
    mm(19, hb[5]);
    WAIT_BAR(0);
    ld(20);
#pragma unroll
    for (int ks = 20; ks < 27; ++ks) { ld(ks + 1); SBAR(); mm(ks, hb[ks - 14]); SBAR(); }
    mm(27, hb[13]);
    BAR_L();
    STAMP(2, 13);
    red4_put(acc[0], 0, lds + LA, rt, kq, l);
    red4_put(acc[1], 1, lds + LA, rt, kq, l);
    BAR_L();
    red4_get(v, lds + LA, rt, kq, l);
#pragma unroll
    for (int k = 0; k < 8; ++k) v[k] = __builtin_fmaf(gb[k], v[k], y[k]);
    P.out[(size_t)trow * 64 + och] = (u32x4){cvtpk(v[0], v[1]), cvtpk(v[2], v[3]), cvtpk(v[4], v[5]), cvtpk(v[6], v[7])};
  }
  STAMP(2, 14);
}

__global__ __launch_bounds__(512, 2) void k_fused(Params P) {
  extern __shared__ __attribute__((aligned(16))) char smem[];
  const lds_ptr lds = (lds_ptr)smem;
  const unsigned lds0 = (unsigned)(uintptr_t)smem;
  const unsigned w = rfl(TIDX >> 6);
  const __amdgpu_buffer_rsrc_t srdW = mksrd(P.ws, W_END);
  {
    unsigned long long pc; unsigned len;
    asm volatile("s_getpc_b64 %0\n.Lkf_here:\n\ts_mov_b32 %1, .Lkf_end-.Lkf_here" : "=s"(pc), "=s"(len));
    dma16(lds0 + LDS_R + w * RINGB + 8192u, (TIDX & 63) * 16u, mksrd((const void*)(pc & ~15ull), len & ~15u), ((BIDX >> 3) * 8 + w) * 1024u);
  }
  const unsigned tag = attn_part(P, lds, lds0, w, srdW);
  mlp_part(P, lds, lds0, w, srdW, tag);
  asm volatile(".Lkf_end:");
}

#if defined(__HIP_DEVICE_COMPILE__)
#pragma clang attribute pop
#endif
extern "C" void kernel_launch(void* const* d_in, const int* in_sizes, int n_in, void* d_out, int out_size, void* d_ws,
                              size_t ws_size, hipStream_t stream) {
  Params P;
  P.x = (const unsigned short*)d_in[0];
  P.kc = (const unsigned short*)d_in[1];
  P.vc = (const unsigned short*)d_in[2];
  P.wq = (const unsigned short*)d_in[3];
  P.wo = (const unsigned short*)d_in[4];
  P.w1 = (const unsigned short*)d_in[6];
  P.w2 = (const unsigned short*)d_in[7];
  P.w3 = (const unsigned short*)d_in[8];
  P.w4 = (const unsigned short*)d_in[9];
  P.c = (const unsigned short*)d_in[10];
  P.wmod = (const unsigned short*)d_in[11];
  P.ws = d_ws;
  P.out = (u32x4*)d_out;
  (void)hipFuncSetAttribute((const void*)k_fused, hipFuncAttributeMaxDynamicSharedMemorySize, LDS_TOTAL);
  k_fused<<<256, 512, LDS_TOTAL, stream>>>(P);
}
```

```cpp
#include <hip/hip_runtime.h>
#include <hip/hip_bf16.h>
#include <stdint.h>

#if defined(__HIP_DEVICE_COMPILE__)
#pragma clang attribute push(__attribute__((target("no-packed-fp32-ops"))), apply_to = function)
#endif
typedef short bf16x8 __attribute__((ext_vector_type(8)));
typedef short s16x4 __attribute__((ext_vector_type(4)));
typedef float f32x16 __attribute__((ext_vector_type(16)));
typedef float f32x4 __attribute__((ext_vector_type(4)));
typedef float f32x2 __attribute__((ext_vector_type(2)));
typedef unsigned u32x4 __attribute__((ext_vector_type(4)));
typedef __bf16 bf16x2_t __attribute__((ext_vector_type(2)));
typedef short v4i16_t __attribute__((ext_vector_type(4)));
#define LAS __attribute__((address_space(3)))
typedef LAS const char* lds_cptr;
typedef LAS char* lds_ptr;
#define DI __device__ __forceinline__
#define F2U(x) __builtin_bit_cast(unsigned, (float)(x))
#define U2F(x) __builtin_bit_cast(float, (unsigned)(x))
#define F2I(x) __builtin_bit_cast(int, (float)(x))
#define I2F(x) __builtin_bit_cast(float, (int)(x))
#define TIDX __builtin_amdgcn_workitem_id_x()
#define BIDX __builtin_amdgcn_workgroup_id_x()
#define ANY(p) (__builtin_amdgcn_ballot_w64(p) != 0ull)
#define SBAR() __builtin_amdgcn_sched_barrier(0)
#define MFMA32(a, b, c) __builtin_amdgcn_mfma_f32_32x32x16_bf16((a), (b), (c), 0, 0, 0)
#define WAIT_V(n) asm volatile("s_waitcnt vmcnt(" #n ")" ::: "memory")
#define BAR_L() asm volatile("s_waitcnt lgkmcnt(0)\n\ts_barrier" ::: "memory")
#define WAIT_BAR(n) asm volatile("s_waitcnt vmcnt(" #n ") lgkmcnt(0)\n\ts_barrier" ::: "memory")

constexpr int T = 2048, D = 512, HKV = 8, SK = 2048, DH = 128, WIN = 512, F = 1792;
constexpr float LN_EPS = 1e-5f;
constexpr float QSCALE = 0.088388347648318440f * 1.4426950408889634f;

constexpr unsigned W_EPOCH = 0;
constexpr unsigned W_FLAG = 4096;
constexpr unsigned W_MOD = 32768;
constexpr unsigned W_MP = W_MOD + 40960;
constexpr unsigned W_P1 = 393216;
constexpr unsigned S_U = W_P1 + 8u * 64u * T * 16u;
constexpr unsigned S_H1 = S_U + 64u * T * 16u;
constexpr unsigned S_X1 = S_H1 + 64u * T * 16u;
constexpr unsigned S_HZ = S_X1 + 64u * T * 16u;
constexpr unsigned S_H3 = S_HZ + 256u * T * 16u;
constexpr unsigned W_END = S_H3 + 224u * T * 16u;
static_assert(W_MP + 8u * 4864u * 8u <= W_P1, "ws");
constexpr unsigned OOB = 0xF0000000u;
constexpr float THR = 32.0f;
constexpr unsigned SENT = 0xFFFFFFFFu;
constexpr unsigned SPIN_CAP = 1u << 16;

constexpr int LDS_X = 0;
constexpr int LDS_R = 65536;
constexpr int RING = 5, RINGB = RING * 2048;
constexpr int LDS_D = LDS_R + 8 * RINGB;
constexpr int LDS_TOTAL = LDS_D + 16384;
static_assert(LDS_TOTAL == 163840, "lds");

DI unsigned cvtpk(float lo, float hi) {
  f32x2 v = {lo, hi};
  bf16x2_t b = __builtin_convertvector(v, bf16x2_t);
  return __builtin_bit_cast(unsigned, b);
}
DI float bflo(unsigned u) { return U2F(u << 16); }
DI float bfhi(unsigned u) { return U2F(u & 0xffff0000u); }
DI unsigned rfl(unsigned x) { return __builtin_amdgcn_readfirstlane(x); }
DI __amdgpu_buffer_rsrc_t mksrd(const void* p, unsigned bytes) {
  return __builtin_amdgcn_make_buffer_rsrc((void*)p, (short)0, (int)bytes, 0x00020000);
}
DI void dma16(unsigned lds, unsigned voff, __amdgpu_buffer_rsrc_t srd, unsigned soff) {
  asm volatile("s_mov_b32 m0, %0\n\ts_nop 0\n\tbuffer_load_dwordx4 %1, %2, %3 offen lds"
               :: "s"(lds), "v"(voff), "s"(srd), "s"(soff) : "memory");
}
DI void gld16(u32x4& d, const void* p) { asm volatile("global_load_dwordx4 %0, %1, off" : "=v"(d) : "v"(p) : "memory"); }
DI void bld16_sc1(u32x4& d, unsigned voff, __amdgpu_buffer_rsrc_t srd, unsigned soff) {
  asm volatile("buffer_load_dwordx4 %0, %1, %2, %3 offen sc1" : "=v"(d) : "v"(voff), "s"(srd), "s"(soff) : "memory");
}
DI void dma16_sc1(unsigned lds, unsigned voff, __amdgpu_buffer_rsrc_t srd, unsigned soff) {
  asm volatile("s_mov_b32 m0, %0\n\ts_nop 0\n\tbuffer_load_dwordx4 %1, %2, %3 offen sc1 lds"
               :: "s"(lds), "v"(voff), "s"(srd), "s"(soff) : "memory");
}
DI bf16x8 lds_b128(lds_cptr p) { return *(const LAS bf16x8*)p; }
DI s16x4 lds_tr(lds_cptr p) {
  return __builtin_bit_cast(s16x4, __builtin_amdgcn_ds_read_tr16_b64_v4i16((LAS v4i16_t*)p));
}
DI bf16x8 cat8(s16x4 lo, s16x4 hi) { return __builtin_shufflevector(lo, hi, 0, 1, 2, 3, 4, 5, 6, 7); }
DI float swap32_add(float x) {
  auto r = __builtin_amdgcn_permlane32_swap(F2U(x), F2U(x), false, false);
  return U2F(r[0]) + U2F(r[1]);
}
template <int CTRL> DI float dpp_add(float x) {
  int y = __builtin_amdgcn_update_dpp(0, F2I(x), CTRL, 0xF, 0xF, true);
  return x + I2F(y);
}
DI float max3f(float a, float b, float c) { float r; asm("v_max3_f32 %0, %1, %2, %3" : "=v"(r) : "v"(a), "v"(b), "v"(c)); return r; }
DI float max2f(float a, float b) { float r; asm("v_max_f32_e32 %0, %1, %2" : "=v"(r) : "v"(a), "v"(b)); return r; }
DI float dot2bf(unsigned a, unsigned b, float c) {
  return __builtin_amdgcn_fdot2_f32_bf16(__builtin_bit_cast(bf16x2_t, a), __builtin_bit_cast(bf16x2_t, b), c, false);
}
#define PIN(x) asm volatile("" : "+v"(x))
DI float swap16_add(float x) {
  auto r = __builtin_amdgcn_permlane16_swap(F2U(x), F2U(x), false, false);
  return U2F(r[0]) + U2F(r[1]);
}
DI float sum_hi3(float x) { return swap32_add(swap16_add(dpp_add<0x128>(x))); }
DI float gelu_tanh(float x) {
  const float k0 = -2.0f * 0.7978845608028654f * 1.4426950408889634f;
  const float t = x * x;
  const float z = x * __builtin_fmaf(t, 0.044715f * k0, k0);
  const float e = __builtin_amdgcn_exp2f(z);
  return x * __builtin_amdgcn_rcpf(1.0f + e);
}
DI void wave_sum8(const float (&a)[8], float (&out)[8]) {
  float b[4], c[2];
#pragma unroll
  for (int i = 0; i < 4; ++i) {
    auto r = __builtin_amdgcn_permlane32_swap(F2U(a[i]), F2U(a[i + 4]), false, false);
    b[i] = U2F(r[0]) + U2F(r[1]);
  }
#pragma unroll
  for (int i = 0; i < 2; ++i) {
    auto r = __builtin_amdgcn_permlane16_swap(F2U(b[i]), F2U(b[i + 2]), false, false);
    c[i] = U2F(r[0]) + U2F(r[1]);
  }
#pragma unroll
  for (int i = 0; i < 2; ++i) {
    c[i] = dpp_add<0xB1>(c[i]);
    c[i] = dpp_add<0x4E>(c[i]);
    c[i] = dpp_add<0x141>(c[i]);
    c[i] = dpp_add<0x140>(c[i]);
  }
#pragma unroll
  for (int q = 0; q < 4; ++q)
#pragma unroll
    for (int i = 0; i < 2; ++i)
      out[i + 2 * (q & 1) + 4 * (q >> 1)] = I2F(__builtin_amdgcn_readlane(F2I(c[i]), 16 * q));
}

DI unsigned nosent(unsigned v) { return v < 0xFFFFFFFEu ? v : 0xFFFFFFFEu; }
DI void pub16(__amdgpu_buffer_rsrc_t srd, unsigned voff, unsigned soff, u32x4 v) {
  v = (u32x4){nosent(v[0]), nosent(v[1]), nosent(v[2]), nosent(v[3])};
  __builtin_amdgcn_raw_buffer_store_b128(v, srd, voff + soff, 0, 16);
}
DI unsigned umax3(unsigned a, unsigned b, unsigned c) { const unsigned m = a > b ? a : b; return m > c ? m : c; }
DI bool has_sent(const u32x4* v, int n) {
  unsigned m = 0;
#pragma unroll
  for (int i = 0; i < n; ++i) { m = umax3(m, v[i][0], v[i][1]); m = umax3(m, v[i][2], v[i][3]); }
  return ANY(m == SENT);
}
template <int N> DI void poll_load(u32x4* v, __amdgpu_buffer_rsrc_t srd, unsigned voff, unsigned soff0, unsigned step) {
  for (unsigned spin = 0;; ++spin) {
    asm volatile("" ::: "memory");
#pragma unroll
    for (int i = 0; i < N; ++i) v[i] = __builtin_amdgcn_raw_buffer_load_b128(srd, voff, soff0 + (unsigned)i * step, 16);
    if (!has_sent(v, N) || spin > SPIN_CAP) break;
    __builtin_amdgcn_s_sleep(1);
  }
}
template <int N, int I0, int I1> DI void poll_ind(u32x4* v, __amdgpu_buffer_rsrc_t srd, unsigned voff, unsigned soff0, unsigned step) {
#pragma unroll
  for (int i = 0; i < N; ++i) v[i] = __builtin_amdgcn_raw_buffer_load_b128(srd, voff, soff0 + (unsigned)i * step, 16);
  if (has_sent(v, N)) {
    u32x4 ind[2];
    poll_load<2>(ind, srd, voff, soff0 + (unsigned)I0 * step, (unsigned)(I1 - I0) * step);
    poll_load<N>(v, srd, voff, soff0, step);
  }
}

DI bool bad_tag(const u32x4* v, int n, unsigned tag) {
  unsigned m = 0;
#pragma unroll
  for (int i = 0; i < n; ++i) m |= (v[i][1] ^ tag) | (v[i][3] ^ tag);
  return m != 0;
}
template <int N> DI void poll_tag(u32x4* v, __amdgpu_buffer_rsrc_t srd, unsigned voff, unsigned soff0, unsigned step, unsigned tag) {
  for (unsigned spin = 0;; ++spin) {
    asm volatile("" ::: "memory");
#pragma unroll
    for (int i = 0; i < N; ++i) v[i] = __builtin_amdgcn_raw_buffer_load_b128(srd, voff, soff0 + (unsigned)i * step, 16);
    if (!ANY(voff < OOB && bad_tag(v, N, tag)) || spin > SPIN_CAP) break;
    __builtin_amdgcn_s_sleep(1);
  }
}

struct LaneC {
  unsigned lane, r, h, q, p, blk;
  unsigned afr[2];
  unsigned xb[4];
};
DI void lane_consts(LaneC& c) {
  unsigned l = TIDX & 63;
  asm volatile("" : "+v"(l));
  c.lane = l; c.r = l & 31; c.h = l >> 5; c.q = (l & 15) >> 2; c.p = l & 3; c.blk = (l >> 4) & 1;
  const unsigned sp = 2 * (c.p & 1) + (c.p >> 1);
#pragma unroll
  for (int nt = 0; nt < 2; ++nt) c.afr[nt] = (8 * c.h + c.q) * 128 + 64 * (nt ^ (c.q >> 1)) + 32 * c.blk + 8 * sp;
#pragma unroll
  for (int s = 0; s < 4; ++s) c.xb[s] = 1024 * c.h + ((c.r ^ (2 * s + c.h)) << 4);
}
DI unsigned wdma_voff(unsigned l, unsigned ld) { return (l >> 3) * ld + (((l & 7) ^ (((l >> 4) & 1) << 2)) << 4); }

DI void wring_issue(int ks, unsigned ring, unsigned voff, __amdgpu_buffer_rsrc_t srd, unsigned soff0, unsigned ld) {
  const unsigned slot = ring + (unsigned)(ks % RING) * 2048u;
  dma16(slot, voff, srd, soff0 + (unsigned)(16 * ks) * ld);
  dma16(slot + 1024u, voff ^ 32u, srd, soff0 + (unsigned)(16 * ks + 8) * ld);
}
template <int N> DI void wait_vm() {
  if constexpr (N == 0) WAIT_V(0); else if constexpr (N == 1) WAIT_V(1); else if constexpr (N == 2) WAIT_V(2);
  else if constexpr (N == 3) WAIT_V(3); else if constexpr (N == 4) WAIT_V(4); else if constexpr (N == 5) WAIT_V(5);
  else if constexpr (N == 6) WAIT_V(6); else if constexpr (N == 7) WAIT_V(7); else if constexpr (N == 8) WAIT_V(8);
  else if constexpr (N == 9) WAIT_V(9); else if constexpr (N == 10) WAIT_V(10); else if constexpr (N == 11) WAIT_V(11);
  else if constexpr (N == 12) WAIT_V(12); else if constexpr (N == 13) WAIT_V(13); else if constexpr (N == 14) WAIT_V(14);
  else if constexpr (N == 15) WAIT_V(15); else if constexpr (N == 16) WAIT_V(16);
  else if constexpr (N == 17) WAIT_V(17); else if constexpr (N == 18) WAIT_V(18);
  else static_assert(N < 0, "wait_vm");
}

#define MFMA16(a, b, c) __builtin_amdgcn_mfma_f32_16x16x32_bf16((a), (b), (c), 0, 0, 0)
struct G16 { unsigned a[4], hi, x[2]; };
DI void g16_consts(G16& k, unsigned l, unsigned ringoff) {
  const unsigned i = l & 15, g = l >> 4, q = i >> 2, p = l & 3, x = 2 * (q >> 1) + (g & 1);
#pragma unroll
  for (int m = 0; m < 4; ++m) k.a[m] = ringoff + (8 * (g & 1) + q) * 128 + ((2 * (m ^ x) + (p >> 1)) << 4) + 8 * (p & 1);
  k.hi = g >> 1;
#pragma unroll
  for (int e = 0; e < 2; ++e) k.x[e] = LDS_X + 1024 * g + ((i ^ (4 * e + g)) << 4);
}
struct Frag { bf16x8 a[4], b[4]; };
template <int KK> DI void load_frags(Frag& f, lds_cptr lds, const G16& k) {
  constexpr int sA = (2 * KK) % RING;
  const unsigned d = sA == RING - 1 ? (1u - k.hi) * (unsigned)((RING - 1) * 2048) : k.hi * 2048u;
  constexpr int o = sA == RING - 1 ? 0 : sA * 2048;
#pragma unroll
  for (int m = 0; m < 4; ++m) f.a[m] = cat8(lds_tr(lds + (k.a[m] + d) + o), lds_tr(lds + (k.a[m] + d) + o + 512));
#pragma unroll
  for (int r = 0; r < 4; ++r) f.b[r] = lds_b128(lds + k.x[KK & 1] + 4096 * KK + 256 * r);
}
template <int KK, int EXTRA>
DI void gemm_step(f32x4 (&acc)[4][4], Frag& cur, Frag& nxt, lds_cptr lds, unsigned ring, unsigned voff,
                  __amdgpu_buffer_rsrc_t srd, unsigned soff0, unsigned ld, const G16& k) {
  if constexpr (2 * KK + RING < 32) {
    asm volatile("" :: "v"(cur.a[0]), "v"(cur.a[1]), "v"(cur.a[2]), "v"(cur.a[3]));
    wring_issue(2 * KK + RING, ring, voff, srd, soff0, ld);
    if constexpr (2 * KK + RING + 1 < 32) wring_issue(2 * KK + RING + 1, ring, voff, srd, soff0, ld);
  }
  SBAR();
#pragma unroll
  for (int m = 0; m < 2; ++m)
#pragma unroll
    for (int r = 0; r < 4; ++r) acc[m][r] = MFMA16(cur.a[m], cur.b[r], acc[m][r]);
  SBAR();
  if constexpr (KK + 1 < 16) {
    constexpr int last = 2 * KK + RING + 1 < 32 ? 2 * KK + RING + 1 : 31;
    wait_vm<2 * (last - (2 * KK + 3)) + ((2 * KK + 3 < RING) ? EXTRA : 0)>();
    load_frags<KK + 1>(nxt, lds, k);
  }
  SBAR();
#pragma unroll
  for (int m = 2; m < 4; ++m)
#pragma unroll
    for (int r = 0; r < 4; ++r) acc[m][r] = MFMA16(cur.a[m], cur.b[r], acc[m][r]);
  SBAR();
  if constexpr (KK + 1 < 16) gemm_step<KK + 1, EXTRA>(acc, nxt, cur, lds, ring, voff, srd, soff0, ld, k);
}
template <int EXTRA>
DI void gemm_phase(f32x4 (&acc)[4][4], lds_cptr lds, unsigned ringoff, unsigned ring, unsigned voff,
                   __amdgpu_buffer_rsrc_t srd, unsigned soff0, unsigned ld, unsigned l) {
  G16 k; g16_consts(k, l, ringoff);
  Frag f0, f1;
  wait_vm<2 * (RING - 2) + EXTRA>();
  load_frags<0>(f0, lds, k);
  gemm_step<0, EXTRA>(acc, f0, f1, lds, ring, voff, srd, soff0, ld, k);
}
DI u32x4 pack16(const f32x4& a, const f32x4& b, float sc) {
  auto r0 = __builtin_amdgcn_permlane16_swap(cvtpk(a[0] * sc, a[1] * sc), cvtpk(b[0] * sc, b[1] * sc), false, false);
  auto r1 = __builtin_amdgcn_permlane16_swap(cvtpk(a[2] * sc, a[3] * sc), cvtpk(b[2] * sc, b[3] * sc), false, false);
  return (u32x4){r0[0], r1[0], r0[1], r1[1]};
}
DI u32x4 pack8(const f32x16& a, int s, float sc) {
  return (u32x4){cvtpk(a[8 * s] * sc, a[8 * s + 1] * sc), cvtpk(a[8 * s + 2] * sc, a[8 * s + 3] * sc),
                 cvtpk(a[8 * s + 4] * sc, a[8 * s + 5] * sc), cvtpk(a[8 * s + 6] * sc, a[8 * s + 7] * sc)};
}
DI unsigned ximg(unsigned chunk, unsigned row) { return chunk * 1024u + ((row ^ (chunk & 7u)) << 4); }

constexpr int KSLOT[8] = {LDS_D, LDS_R, LDS_R + 32768, LDS_X, LDS_X + 32768, LDS_D, LDS_X, LDS_X + 32768};
constexpr int VSLOT[8] = {LDS_R + 16384, LDS_R + 49152, LDS_X + 16384, LDS_X + 49152, LDS_R + 65536, LDS_X + 16384, LDS_X + 49152, LDS_D};
template <int N> struct IC { static constexpr int value = N; };
struct Params {
  const unsigned short *x, *kc, *vc, *wq, *wo, *w1, *w2, *w3, *w4, *c, *wmod;
  void* ws;
  u32x4* out;
};
DI unsigned attn_part(const Params& P, lds_ptr lds, unsigned lds0, unsigned w, __amdgpu_buffer_rsrc_t srdW) {
  LaneC c; lane_consts(c);
  const unsigned g = BIDX & 7, pn = BIDX >> 3;
  const unsigned l = c.lane, tid = TIDX;
  const unsigned ringoff = LDS_R + w * RINGB, ring = lds0 + ringoff;
  unsigned ep;
  asm volatile("buffer_load_dword %0, off, %1, 0 sc1" : "=v"(ep) : "s"(srdW) : "memory");
  u32x4 jw[4]; unsigned jc[4];
  auto c_load = [&](unsigned jks) __attribute__((always_inline)) {
#pragma unroll
    for (int i = 0; i < 4; ++i) asm volatile("global_load_ushort %0, %1, off" : "=v"(jc[i]) : "v"(P.c + jks * 64 + (l >> 2) + 16 * i) : "memory");
  };
  auto job_run = [&](unsigned jcol, unsigned jks, unsigned tag) __attribute__((always_inline)) {
    float acc[8] = {0, 0, 0, 0, 0, 0, 0, 0};
#pragma unroll
    for (int i = 0; i < 4; ++i) {
      const float v = U2F(jc[i] << 16);
      const float sv = v * __builtin_amdgcn_rcpf(1.0f + __builtin_amdgcn_exp2f(-1.4426950408889634f * v));
      const float sc = jcol >= 96u ? 1.0f : sv;
#pragma unroll
      for (int k = 0; k < 4; ++k) {
        acc[2 * k] = __builtin_fmaf(sc, bflo(jw[i][k]), acc[2 * k]);
        acc[2 * k + 1] = __builtin_fmaf(sc, bfhi(jw[i][k]), acc[2 * k + 1]);
      }
    }
#pragma unroll
    for (int k = 0; k < 8; ++k) acc[k] = sum_hi3(dpp_add<0x124>(acc[k]));
    const unsigned e = (l >> 2) & 3;
    const float v0 = e == 0 ? acc[0] : e == 1 ? acc[2] : e == 2 ? acc[4] : acc[6], v1 = e == 0 ? acc[1] : e == 1 ? acc[3] : e == 2 ? acc[5] : acc[7];
    __builtin_amdgcn_raw_buffer_store_b128((u32x4){F2U(v0), tag, F2U(v1), tag}, srdW,
                                           l < 16 ? W_MP + (jks * 4864u + jcol * 32u + (l & 3) * 8u + e * 2u) * 8u : OOB, 0, 16);
  };
  u32x4 xr[8];
  auto x_load = [&]() __attribute__((always_inline)) {
#pragma unroll
    for (int i = 0; i < 8; ++i) gld16(xr[i], P.x + (size_t)(pn * 64 + w * 8 + i) * D + l * 8);
  };
  if (w == 0) {
#pragma unroll
    for (int i = 0; i < 4; ++i) gld16(jw[i], P.wmod + (BIDX & 31u) * 32 + (l & 3) * 8 + (size_t)((BIDX >> 5) * 64 + (l >> 2) + 16 * i) * 3072);
    c_load(BIDX >> 5);
  } else x_load();
  asm volatile("s_waitcnt vmcnt(8)\n\ts_barrier" : "+v"(ep) :: "memory");
  const unsigned tag = rfl(ep) + 1u;
  if (w == 0) {
    asm volatile("s_waitcnt vmcnt(0)" : "+v"(jw[0]), "+v"(jw[1]), "+v"(jw[2]), "+v"(jw[3]), "+v"(jc[0]), "+v"(jc[1]), "+v"(jc[2]), "+v"(jc[3]) :: "memory");
    job_run(BIDX & 31u, BIDX >> 5, tag); x_load();
  }
  const unsigned jid = w * 256u + BIDX, jcol2 = 32u + jid % 120u, jks2 = jid / 120u;
  const bool job2 = w < 4 && jid < 960u;
  const __amdgpu_buffer_rsrc_t srdQ = mksrd(P.wq, 512u * 4096u * 2u);
  const unsigned vq = wdma_voff(l, 8192u), sq0 = (g * 512u + w * 64u) * 2u;
  const __amdgpu_buffer_rsrc_t srdK = mksrd(P.kc + ((size_t)g * SK + (SK - WIN)) * DH, WIN * DH * 2u);
  const __amdgpu_buffer_rsrc_t srdV = mksrd(P.vc + ((size_t)g * SK + (SK - WIN)) * DH, WIN * DH * 2u);
  const unsigned vk = ((l >> 2) & 7) * 256u + (l >> 5) * 64u + (((l & 3) ^ (2 * (w & 1) + ((l >> 4) & 1))) << 4);
  const unsigned vv = wdma_voff(l, 256u);
  auto dma_k = [&](int t, unsigned slot) {
    dma16(lds0 + slot + 2048u * w, vk, srdK, (unsigned)t * 16384u + 2048u * w);
    dma16(lds0 + slot + 2048u * w + 1024u, vk, srdK, (unsigned)t * 16384u + 2048u * w + 128u);
  };
  auto dma_v = [&](int t, unsigned slot) {
    dma16(lds0 + slot + 1024u * w, vv, srdV, (unsigned)t * 16384u + 2048u * w);
    dma16(lds0 + slot + 8192u + 1024u * w, vv, srdV, (unsigned)t * 16384u + 2048u * w + 128u);
  };

  {
    asm volatile("s_waitcnt vmcnt(0)" : "+v"(xr[0]), "+v"(xr[1]), "+v"(xr[2]), "+v"(xr[3]), "+v"(xr[4]), "+v"(xr[5]), "+v"(xr[6]),
                 "+v"(xr[7]) :: "memory");
    dma16(ring + 8192u, l * 16u, srdQ, (2 * (pn * 8 + w)) * 8192u + g * 1024u);
    dma16(ring + 9216u, l * 16u, srdQ, (2 * (pn * 8 + w) + 1) * 8192u + g * 1024u);
    wring_issue(0, ring, vq, srdQ, sq0, 8192u);
    wring_issue(1, ring, vq, srdQ, sq0, 8192u);
    float v[8][8], s[8], m[8];
#pragma unroll
    for (int i = 0; i < 8; ++i) {
#pragma unroll
      for (int j = 0; j < 4; ++j) { v[i][2 * j] = bflo(xr[i][j]); v[i][2 * j + 1] = bfhi(xr[i][j]); }
      s[i] = ((v[i][0] + v[i][1]) + (v[i][2] + v[i][3])) + ((v[i][4] + v[i][5]) + (v[i][6] + v[i][7]));
    }
    wave_sum8(s, m);
#pragma unroll
    for (int i = 0; i < 8; ++i) {
      const float mu = m[i] * (1.0f / D);
      float q = 0.f;
#pragma unroll
      for (int j = 0; j < 8; ++j) { v[i][j] -= mu; q = __builtin_fmaf(v[i][j], v[i][j], q); }
      s[i] = q;
    }
    wave_sum8(s, m);
    float rs[8];
#pragma unroll
    for (int i = 0; i < 8; ++i) rs[i] = __builtin_amdgcn_rsqf(m[i] * (1.0f / D) + LN_EPS);
    {
      u32x4 mp[8];
      poll_tag<8>(mp, srdW, tid * 16u, W_MP, 4864u * 8u, tag);
      f32x2 t;
#pragma unroll
      for (int k = 0; k < 2; ++k)
        t[k] = ((U2F(mp[0][2 * k]) + U2F(mp[1][2 * k])) + (U2F(mp[2][2 * k]) + U2F(mp[3][2 * k]))) +
               ((U2F(mp[4][2 * k]) + U2F(mp[5][2 * k])) + (U2F(mp[6][2 * k]) + U2F(mp[7][2 * k])));
      *(LAS f32x2*)(lds + LDS_D + tid * 8) = t;
    }
    BAR_L();
    if (BIDX == 0 && tid == 0) __builtin_amdgcn_raw_buffer_store_b32(tag, srdW, W_EPOCH, 0, 16);
    u32x4 mo[4];
#pragma unroll
    for (int i = 0; i < 4; ++i) mo[i] = *(const LAS u32x4*)(lds + LDS_D + (i >> 1) * 2048 + l * 32 + (i & 1) * 16);
#pragma unroll
    for (int i = 0; i < 8; ++i) {
      float hh[8];
#pragma unroll
      for (int j = 0; j < 8; ++j) hh[j] = __builtin_fmaf(v[i][j] * rs[i], 1.f + U2F(mo[2 + (j >> 2)][j & 3]), U2F(mo[j >> 2][j & 3]));
      const u32x4 o = {cvtpk(hh[0], hh[1]), cvtpk(hh[2], hh[3]), cvtpk(hh[4], hh[5]), cvtpk(hh[6], hh[7])};
      *(LAS u32x4*)(lds + LDS_X + ximg(l, w * 8 + i)) = o;
      if (i == 1 || i == 3 || i == 5) wring_issue(2 + i / 2, ring, vq, srdQ, sq0, 8192u);
    }
  }
  BAR_L();
  dma_k(0, LDS_D);
  {
    f32x4 acc[4][4] = {};
    gemm_phase<2>(acc, lds, ringoff, ring, vq, srdQ, sq0, 8192u, l);
    WAIT_BAR(0);
    dma_k(1, KSLOT[1]); dma_v(0, VSLOT[0]); dma_k(2, KSLOT[2]); dma_v(1, VSLOT[1]);
    if (job2) {
      const bool isw3 = jcol2 >= 96u;
      const unsigned ldb = isw3 ? 3584u : 6144u;
      const __amdgpu_buffer_rsrc_t srdJ = isw3 ? mksrd(P.w3, 512u * 3584u) : mksrd(P.wmod, 512u * 6144u);
#pragma unroll
      for (int i = 0; i < 4; ++i)
        dma16(lds0 + LDS_R + 65536u + w * 4096u + i * 1024u, (l >> 2) * ldb + (l & 3) * 16u, srdJ, (jks2 * 64 + 16 * i) * ldb + (isw3 ? jcol2 - 96u : jcol2) * 64u);
      c_load(jks2);
    }
    const unsigned cl = 8 * w + 2 * ((l >> 4) & 1) + (l >> 5);
#pragma unroll
    for (int r = 0; r < 4; ++r)
#pragma unroll
      for (int mp = 0; mp < 2; ++mp)
        *(LAS u32x4*)(lds + LDS_X + ximg(cl + 4 * mp, 16 * r + (l & 15))) = pack16(acc[2 * mp][r], acc[2 * mp + 1][r], QSCALE);
    BAR_L();
  }
  const unsigned hh = w >> 1, rt = w & 1;
  bf16x8 qf[8];
#pragma unroll
  for (int s = 0; s < 8; ++s) qf[s] = lds_b128(lds + LDS_X + ximg(16 * hh + 2 * s + c.h, 32 * rt + c.r));
  unsigned kb[2], vb[2];
#pragma unroll
  for (int i = 0; i < 2; ++i) {
    kb[i] = 2048 * (c.r >> 3) + 64 * (c.r & 7) + (((2 * i + c.h) ^ ((c.r >> 2) & 3)) << 4);
    vb[i] = (4 * c.h + c.q) * 128 + 64 * (i ^ (c.q >> 1)) + 32 * c.blk + 8 * (2 * (c.p & 1) + (c.p >> 1));
  }
  const __amdgpu_buffer_rsrc_t srdO = mksrd(P.wo, 4096u * 512u * 2u);
  const unsigned vo = wdma_voff(l, 1024u), so0 = g * 512u * 1024u + w * 128u;

  f32x16 o[4] = {};
  f32x16 s0, s1;
  f32x16 negm;
  float lsum = 0.f;
  u32x4 pk[4];
  constexpr int PD = 6;
  bf16x8 fr[PD];
  const bool lag = w >= 4;
  auto frag = [&](int t, int it) -> bf16x8 {
    if (it < 16) {
      const lds_cptr a = lds + VSLOT[t > 0 ? t - 1 : 0] + vb[it & 1] + 8192 * ((it >> 1) & 1) + 2048 * (it >> 2);
      return cat8(lds_tr(a), lds_tr(a + 1024));
    }
    const int s = (it - 16) >> 1;
    return lds_b128(lds + KSLOT[t < 8 ? t : 0] + kb[s & 1] + 512 * (s >> 1) + 8192 * (it & 1));
  };
  auto seam = [&](int t, int tx) __attribute__((always_inline)) {
    if (t == 7) { BAR_L(); } else if (tx >= 1 && tx <= 6) { WAIT_BAR(4); } else { WAIT_BAR(0); }
  };
  auto refill = [&](int t) __attribute__((always_inline)) {
    if (t + 3 < 8) dma_k(t + 3, KSLOT[t + 3 < 8 ? t + 3 : 0]);
    if (t + 2 < 8) dma_v(t + 2, VSLOT[t + 2 < 8 ? t + 2 : 0]);
    if (t == 6) {
#pragma unroll
      for (int ks = 0; ks < RING; ++ks) wring_issue(ks, ring, vo, srdO, so0, 1024u);
    }
  };
  auto extra = [&](int e) __attribute__((always_inline)) {
    const unsigned dump = lds0 + LDS_R + w * 3072u + (unsigned)(e % 3) * 1024u;
    const unsigned id = (BIDX >> 3) * 8 + w, jh = BIDX & 1;
    const unsigned vrow = (l >> 5) * 1024u + (l & 31) * 16u + jh * 512u;
    auto w3v = [&](unsigned piece) { const unsigned byte = piece * 1024u + l * 16u, row = byte / 1792u;
                                     return row * 3584u + jh * 1792u + (byte - row * 1792u); };
    if (e == 15) {
      const unsigned pn2 = 8 * ((BIDX & 7) >> 1) + (BIDX >> 5);
      dma16_sc1(dump, l * 16u, srdW, W_P1 + ((w & 1) ? pn2 : pn) * 524288u + (w >> 1) * 131072u);
    } else if (e == 16) {
      dma16_sc1(dump, l * 16u, mksrd(P.out, T * 1024u), (8 * ((BIDX & 7) >> 1) + (BIDX >> 5)) * 65536u + w * 8192u);
    } else if (e < 4 || e >= 17) {
      const int i = e < 4 ? e : e - 13;
      const unsigned off = i < 3 ? S_U + (unsigned)i * (64u * T * 16u) + pn * 65536u + g * 8192u
                         : i < 7 ? S_HZ + pn * 262144u + g * 32768u + (unsigned)(i - 3) * 8192u
                                 : S_H3 + pn * 229376u + g * 28672u + (unsigned)(i - 7) * 8192u;
      const u32x4 sv = {SENT, SENT, SENT, SENT};
      __builtin_amdgcn_raw_buffer_store_b128(sv, srdW, (i == 10 && tid >= 256) ? OOB : tid * 16u + off, 0, 16);
    } else if (e < 6) dma16(dump, l * 16u, srdO, (g * 512u + (pn * 8 + w) * 2 + (e - 4)) * 1024u);
    else if (e == 6) dma16(dump, vrow, mksrd(P.w1, 512u * 1024u), id * 2048u);
    else if (e == 7) dma16(dump, vrow, mksrd(P.w2, 512u * 1024u), id * 2048u);
    else if (e < 11) dma16(dump, w3v(id + 256u * (e - 8)), mksrd(P.w3, 512u * 3584u), 0u);
    else if (e == 11) {
      if (id < 128) dma16(dump, w3v(id + 768u), mksrd(P.w3, 512u * 3584u), 0u);
      else dma16(dump, vrow, mksrd(P.w4, 1792u * 1024u), (id - 128u + 768u) * 2048u);
    } else dma16(dump, vrow, mksrd(P.w4, 1792u * 1024u), (id + 256u * (e - 12)) * 2048u);
  };
  auto step = [&](auto tc) __attribute__((always_inline)) {
    constexpr int t = decltype(tc)::value;
    constexpr int i0 = t == 0 ? 16 : 0, i1 = t == 8 ? 16 : 32;
    if (t <= 1) {
#pragma unroll
      for (int it = i0; it < i0 + PD; ++it) fr[it % PD] = frag(t, it);
    }
    if (t == 0) { s0 = (f32x16){}; s1 = (f32x16){}; } else if (t < 8) { s0 = negm; s1 = negm; }
    __builtin_amdgcn_s_setprio(1);
    SBAR();
#pragma unroll
    for (int it = i0; it < i1; ++it) {
      if (it < 16) o[it & 3] = MFMA32(fr[it % PD], __builtin_bit_cast(bf16x8, pk[it >> 2]), o[it & 3]);
      else if (it & 1) s1 = MFMA32(fr[it % PD], qf[(it - 16) >> 1], s1);
      else s0 = MFMA32(fr[it % PD], qf[(it - 16) >> 1], s0);
      if (it + PD < i1) fr[it % PD] = frag(t, it + PD);
      if (t >= 1 && it == 1) { if (!lag) refill(t - 1); }
      SBAR();
    }
    if constexpr (t == 8) return;
    __builtin_amdgcn_s_setprio(0);
    if (lag) { seam(t, t - 1); refill(t); }
    asm volatile("s_nop 15" : "+v"(s0), "+v"(s1));
    float ta = max3f(s0[0], s0[1], s1[0]), tb = max3f(s0[2], s0[3], s1[1]);
    ta = max3f(ta, s1[2], s1[3]);
#pragma unroll
    for (int i = 4; i < 16; i += 4) {
      ta = max3f(ta, s0[i], s0[i + 1]); tb = max3f(tb, s0[i + 2], s0[i + 3]);
      ta = max3f(ta, s1[i], s1[i + 1]); tb = max3f(tb, s1[i + 2], s1[i + 3]);
    }
    float tm = max2f(ta, tb);
    if (t == 0 || __builtin_expect(rfl(ANY(tm > THR)), 0)) {
      {
        auto r = __builtin_amdgcn_permlane32_swap(F2U(tm), F2U(tm), false, false);
        tm = max2f(U2F(r[0]), U2F(r[1]));
      }
      const float ndl = t == 0 ? -tm : -max2f(tm, 0.f);
      const float aone = c.h ? 0.f : 1.f;
      if (t == 0) negm = (f32x16){};
      __builtin_amdgcn_s_setprio(2);
      s0 = __builtin_amdgcn_mfma_f32_32x32x2f32(aone, ndl, s0, 0, 0, 0);
      s1 = __builtin_amdgcn_mfma_f32_32x32x2f32(aone, ndl, s1, 0, 0, 0);
      negm = __builtin_amdgcn_mfma_f32_32x32x2f32(aone, ndl, negm, 0, 0, 0);
      __builtin_amdgcn_s_setprio(0);
      if (t > 0) {
        const float al = __builtin_amdgcn_exp2f(ndl);
        lsum *= al;
#pragma unroll
        for (int dt = 0; dt < 4; ++dt)
#pragma unroll
          for (int i = 0; i < 16; ++i) o[dt][i] *= al;
      }
    }
    SBAR();
    if (t >= 1) {
#pragma unroll
      for (int it = 0; it < PD; ++it) fr[it] = frag(t + 1, it);
    }
#pragma unroll
    for (int i = 0; i < 16; ++i) {
      s0[i] = __builtin_amdgcn_exp2f(s0[i]); s1[i] = __builtin_amdgcn_exp2f(s1[i]);
      if (t >= 1 && (i & 3) == 1 && 4 * (t - 1) + (i >> 2) < 24) { PIN(s0[i]); extra(4 * (t - 1) + (i >> 2)); }
    }
    pk[0] = pack8(s0, 0, 1.f); pk[1] = pack8(s0, 1, 1.f); pk[2] = pack8(s1, 0, 1.f); pk[3] = pack8(s1, 1, 1.f);
    {
      float l0 = lsum, l1 = 0.f, l2 = 0.f, l3 = 0.f;
#pragma unroll
      for (int i = 0; i < 16; i += 2) { l0 += s0[i]; l1 += s0[i + 1]; l2 += s1[i]; l3 += s1[i + 1]; }
      lsum = (l0 + l1) + (l2 + l3);
    }
    PIN(pk[0]); PIN(pk[1]); PIN(pk[2]); PIN(pk[3]); PIN(lsum);
    if constexpr (t == 1) {
      if (job2) {
        asm volatile("" : "+v"(jc[0]), "+v"(jc[1]), "+v"(jc[2]), "+v"(jc[3]));
#pragma unroll
        for (int i = 0; i < 4; ++i) jw[i] = *(const LAS u32x4*)(lds + LDS_R + 65536 + w * 4096 + i * 1024 + l * 16);
        job_run(jcol2, jks2, tag);
      }
    }
    if constexpr (t == 4) {
      if (BIDX < 120 && w == 0) {
        u32x4 mp[2];
        poll_tag<2>(mp, srdW, ((l >> 3) * 4864u + (32u + BIDX) * 32u + (l & 7) * 2u) * 8u, W_MP, 128u, tag);
        const float a0 = sum_hi3(U2F(mp[0][0])), a1 = sum_hi3(U2F(mp[0][2])), b0 = sum_hi3(U2F(mp[1][0])), b1 = sum_hi3(U2F(mp[1][2]));
        const bool hi = (l >> 3) & 1;
        __builtin_amdgcn_raw_buffer_store_b128((u32x4){F2U(hi ? b0 : a0), tag, F2U(hi ? b1 : a1), tag}, srdW,
                                               l < 16 ? W_MOD + ((32u + BIDX) * 32u + l * 2u) * 8u : OOB, 0, 16);
      }
    }
    SBAR();
    if (!lag) seam(t, t);
  };
  step(IC<0>{}); step(IC<1>{}); step(IC<2>{}); step(IC<3>{}); step(IC<4>{}); step(IC<5>{}); step(IC<6>{}); step(IC<7>{}); step(IC<8>{});
  __builtin_amdgcn_s_setprio(0);
  {
    const float il = __builtin_amdgcn_rcpf(swap32_add(lsum));
#pragma unroll
    for (int dt = 0; dt < 4; ++dt)
#pragma unroll
      for (int s = 0; s < 2; ++s)
        *(LAS u32x4*)(lds + LDS_X + ximg(16 * hh + 4 * dt + 2 * s + c.h, 32 * rt + c.r)) = pack8(o[dt], s, il);
  }
  BAR_L();
  dma16_sc1(lds0 + LDS_D + w * 2048u, l * 16u, srdW, W_MOD + 8192u + w * 2048u);
  dma16_sc1(lds0 + LDS_D + w * 2048u + 1024u, l * 16u, srdW, W_MOD + 8192u + w * 2048u + 1024u);
  {
    unsigned l2 = TIDX & 63;
    asm volatile("" : "+v"(l2));
    f32x4 acc[4][4] = {};
    gemm_phase<2>(acc, lds, ringoff, ring, vo, srdO, so0, 1024u, l2);
    const unsigned dst = W_P1 + ((((pn * 8 + ((l2 >> 3) & 1)) * 8 + g) * 64 + 8 * w + 2 * ((l2 >> 4) & 1) + (l2 >> 5)) * 8 + (l2 & 7)) * 16u;
#pragma unroll
    for (int r = 0; r < 4; ++r)
#pragma unroll
      for (int mp = 0; mp < 2; ++mp)
        __builtin_amdgcn_raw_buffer_store_b128(pack16(acc[2 * mp][r], acc[2 * mp + 1][r], 1.f), srdW,
                                               dst + (4 * mp * 8 + r * 2 * 8 * 64 * 8) * 16u, 0, 16);
  }
  WAIT_BAR(0);
  if (tid == 0) __builtin_amdgcn_raw_buffer_store_b32(tag, srdW, W_FLAG + (pn * 8 + g) * 64u, 0, 16);
  return tag;
}

DI void ln8(float (&v)[8], LAS float* red  , unsigned tid) {
  float s = ((v[0] + v[1]) + (v[2] + v[3])) + ((v[4] + v[5]) + (v[6] + v[7]));
  float q = (__builtin_fmaf(v[0], v[0], v[1] * v[1]) + __builtin_fmaf(v[2], v[2], v[3] * v[3])) +
            (__builtin_fmaf(v[4], v[4], v[5] * v[5]) + __builtin_fmaf(v[6], v[6], v[7] * v[7]));
  s = sum_hi3(s); q = sum_hi3(q);
  if ((tid & 63) < 8) *(LAS f32x2*)(red + ((tid >> 6) * 8 + (tid & 7)) * 2) = (f32x2){s, q};
  BAR_L();
  f32x2 e[8];
#pragma unroll
  for (int i = 0; i < 8; ++i) e[i] = *(const LAS f32x2*)(red + (i * 8 + (tid & 7)) * 2);
  const float mu = (((e[0][0] + e[1][0]) + (e[2][0] + e[3][0])) + ((e[4][0] + e[5][0]) + (e[6][0] + e[7][0]))) * (1.0f / D);
  const float ms = (((e[0][1] + e[1][1]) + (e[2][1] + e[3][1])) + ((e[4][1] + e[5][1]) + (e[6][1] + e[7][1]))) * (1.0f / D);
  const float rs = __builtin_amdgcn_rsqf(__builtin_fmaxf(__builtin_fmaf(-mu, mu, ms), 0.f) + LN_EPS);
  const float nb = -mu * rs;
#pragma unroll
  for (int k = 0; k < 8; ++k) v[k] = __builtin_fmaf(v[k], rs, nb);
}
DI void red4_put(const f32x16& a, int nt, lds_ptr scr, unsigned rt, unsigned kq, unsigned l) {
#pragma unroll
  for (int g = 0; g < 2; ++g)
#pragma unroll
    for (int i = 0; i < 2; ++i) {
      const int o = 8 * g + 4 * i;
      *(LAS f32x4*)(scr + ((rt * 4 + 2 * nt + g) * 4 + kq) * 2048 + i * 1024 + l * 16) = (f32x4){a[o], a[o + 1], a[o + 2], a[o + 3]};
    }
}
DI void red4_get(float (&v)[8], lds_cptr scr, unsigned rt, unsigned kq, unsigned l) {
  f32x4 t[4][2];
#pragma unroll
  for (int s = 0; s < 4; ++s)
#pragma unroll
    for (int i = 0; i < 2; ++i) t[s][i] = *(const LAS f32x4*)(scr + ((rt * 4 + kq) * 4 + s) * 2048 + i * 1024 + l * 16);
#pragma unroll
  for (int i = 0; i < 2; ++i)
#pragma unroll
    for (int k = 0; k < 4; ++k) v[4 * i + k] = (t[0][i][k] + t[1][i][k]) + (t[2][i][k] + t[3][i][k]);
}
DI void tile_mm8(const bf16x8 (&af)[8][2], const u32x4 (&xb)[8], float (&v)[8], lds_ptr scr, unsigned rt, unsigned kq, unsigned l) {
  f32x16 a0 = {}, a1 = {};
#pragma unroll
  for (int i = 0; i < 8; ++i) a0 = MFMA32(af[i][0], __builtin_bit_cast(bf16x8, xb[i]), a0);
  SBAR();
#pragma unroll
  for (int i = 0; i < 4; ++i) a1 = MFMA32(af[i][1], __builtin_bit_cast(bf16x8, xb[i]), a1);
  SBAR();
  red4_put(a0, 0, scr, rt, kq, l);
#pragma unroll
  for (int i = 4; i < 8; ++i) a1 = MFMA32(af[i][1], __builtin_bit_cast(bf16x8, xb[i]), a1);
  SBAR();
  red4_put(a1, 1, scr, rt, kq, l);
  BAR_L();
  red4_get(v, scr, rt, kq, l);
}

struct Frag3 { bf16x8 a, b[2]; };
constexpr int RING3 = 12;
constexpr int LA = 0, LB = 65536, LC = 131072;
DI unsigned r3off(int s, unsigned rc, unsigned rb) { return s < 4 ? rc + s * 1024u : rb + (s - 4) * 1024u; }
template <int KS> DI void load_frags3(Frag3& f, lds_cptr lds, unsigned rc, unsigned rb, unsigned a3, const LaneC& c) {
  const lds_cptr sl = lds + r3off(KS % RING3, rc, rb) + a3;
  f.a = cat8(lds_tr(sl), lds_tr(sl + 256));
#pragma unroll
  for (int rt = 0; rt < 2; ++rt) f.b[rt] = lds_b128(lds + LA + c.xb[KS & 3] + 2048 * KS + 512 * rt);
}
constexpr int w4slot(int n) { return (8 + n) % RING3; }
template <int KS>
DI void g3_step(f32x16 (&acc)[2], Frag3& cur, Frag3& n1, Frag3& n2, lds_cptr lds, unsigned lds0, unsigned rc, unsigned rb,
                unsigned voff, __amdgpu_buffer_rsrc_t srd, unsigned soff0, unsigned a3, const LaneC& c,
                unsigned v4, __amdgpu_buffer_rsrc_t srd4, unsigned s40) {
  constexpr int NKS = 32;
  if constexpr (KS + 2 < NKS) {
    constexpr int last = (KS + RING3 < NKS ? KS + RING3 : NKS) - 1;
    wait_vm<last - (KS + 2) + (KS > NKS - RING3 ? KS - (NKS - RING3) : 0)>();
    load_frags3<KS + 2>(n2, lds, rc, rb, a3, c);
  }
  SBAR();
  acc[0] = MFMA32(cur.a, cur.b[0], acc[0]);
  acc[1] = MFMA32(cur.a, cur.b[1], acc[1]);
  asm volatile("" :: "v"(cur.a));
  if constexpr (KS + RING3 < NKS) dma16(lds0 + r3off(KS % RING3, rc, rb), voff, srd, soff0 + (unsigned)(16 * (KS + RING3)) * 3584u);
  else dma16(lds0 + r3off(KS % RING3, rc, rb), v4, srd4, s40 + (unsigned)(KS - (NKS - RING3)) * 16384u);
  SBAR();
  if constexpr (KS + 1 < NKS) g3_step<KS + 1>(acc, n1, n2, cur, lds, lds0, rc, rb, voff, srd, soff0, a3, c, v4, srd4, s40);
}

DI void mlp_part(const Params& P, lds_ptr lds, unsigned lds0, unsigned w, __amdgpu_buffer_rsrc_t srdS, unsigned tag) {
  LaneC c; lane_consts(c);
  const unsigned idx = BIDX >> 3, pn = 8 * ((BIDX & 7) >> 1) + (idx >> 2), j = 4 * (BIDX & 1) + (idx & 3);
  const unsigned tid = TIDX, l = c.lane;
  const unsigned vw = wdma_voff(l, 1024u);
  const unsigned rt = w & 1, kq = w >> 1;
  constexpr int LMOD = LDS_D;
  constexpr int LRED = LC + 12288;
  constexpr int LST = LB + 7 * 8192;
  const unsigned prow = 32 * rt + c.r;
  const unsigned trow = pn * 64 + prow;
  const unsigned och = 8 * j + 2 * kq + c.h;
  const unsigned ovo = ((pn * 64 + och) * 64 + prow) * 16u;
  float gb[8];
  {
    const unsigned row = pn * 64 + j * 8 + (tid & 7), ch = tid >> 3;
    u32x4 xr;
    gld16(xr, P.x + (size_t)row * D + ch * 8);
    auto wait_flags = [&](unsigned fo) {
      for (unsigned spin = 0;; ++spin) {
        asm volatile("" ::: "memory");
        const unsigned f = __builtin_amdgcn_raw_buffer_load_b32(srdS, fo, 0, 16);
        if (!ANY(fo != OOB && f != tag) || spin > SPIN_CAP) break;
        __builtin_amdgcn_s_sleep(4);
      }
    };
    const __amdgpu_buffer_rsrc_t srd1 = mksrd(P.w1, 512u * 512u * 2u), srd2 = mksrd(P.w2, 512u * 512u * 2u);
#pragma unroll
    for (int i = 0; i < 8; ++i) dma16(lds0 + LA + (8 * w + i) * 1024u, vw, srd1, (8 * w + i) * 8192u + j * 128u);
    {
      const int fp = (int)pn - 1 + (int)(l >> 3);
      if (w == 0) wait_flags((l < 24 && fp >= 0 && fp < 32) ? W_FLAG + ((unsigned)fp * 8 + (l & 7)) * 64u : OOB);
      BAR_L();
    }
    u32x4 p[8];
#pragma unroll
    for (int g = 0; g < 8; ++g) bld16_sc1(p[g], tid * 16u, srdS, W_P1 + ((pn * 8 + j) * 8 + g) * 8192u);
    float ga[8], sb[8], cb[8], g2[8];
    {
      u32x4 mv[20];
#pragma unroll
      for (int i = 0; i < 16; ++i) mv[i] = *(const LAS u32x4*)(lds + LMOD + (i >> 2) * 4096 + ch * 64 + 16 * (i & 3));
#pragma unroll
      for (int i = 0; i < 4; ++i) mv[16 + i] = *(const LAS u32x4*)(lds + LMOD + 12288 + och * 64 + 16 * i);
      if (ANY(bad_tag(mv, 20, tag))) {
#pragma unroll
        for (int i = 0; i < 4; ++i) poll_tag<4>(mv + 4 * i, srdS, ch * 64u, W_MOD + 8192u + i * 4096u, 16u, tag);
        poll_tag<4>(mv + 16, srdS, och * 64u, W_MOD + 20480u, 16u, tag);
      }
#pragma unroll
      for (int k = 0; k < 8; ++k) {
        ga[k] = U2F(mv[k >> 1][2 * (k & 1)]); sb[k] = U2F(mv[4 + (k >> 1)][2 * (k & 1)]); cb[k] = 1.f + U2F(mv[8 + (k >> 1)][2 * (k & 1)]);
        g2[k] = U2F(mv[12 + (k >> 1)][2 * (k & 1)]); gb[k] = U2F(mv[16 + (k >> 1)][2 * (k & 1)]);
      }
    }
    asm volatile("s_waitcnt vmcnt(0)" : "+v"(xr), "+v"(p[0]), "+v"(p[1]), "+v"(p[2]), "+v"(p[3]), "+v"(p[4]), "+v"(p[5]),
                 "+v"(p[6]), "+v"(p[7]) :: "memory");
    float v[8], x2[8];
#pragma unroll
    for (int i = 0; i < 4; ++i) {
      const float lo = ((bflo(p[0][i]) + bflo(p[1][i])) + (bflo(p[2][i]) + bflo(p[3][i]))) + ((bflo(p[4][i]) + bflo(p[5][i])) + (bflo(p[6][i]) + bflo(p[7][i])));
      const float hi = ((bfhi(p[0][i]) + bfhi(p[1][i])) + (bfhi(p[2][i]) + bfhi(p[3][i]))) + ((bfhi(p[4][i]) + bfhi(p[5][i])) + (bfhi(p[6][i]) + bfhi(p[7][i])));
      v[2 * i] = x2[2 * i] = __builtin_fmaf(ga[2 * i], lo, bflo(xr[i]));
      v[2 * i + 1] = x2[2 * i + 1] = __builtin_fmaf(ga[2 * i + 1], hi, bfhi(xr[i]));
    }
    ln8(v, (LAS float*)(lds + LRED), tid);
    float hb[8];
#pragma unroll
    for (int k = 0; k < 8; ++k) v[k] = hb[k] = __builtin_fmaf(v[k], cb[k], sb[k]);
    ln8(v, (LAS float*)(lds + LRED + 512), tid);
    const unsigned vo = ((pn * 64 + ch) * 64 + j * 8 + (tid & 7)) * 16u, vz = ((pn * 64 + ch) * 256 + j * 8 + (tid & 7)) * 16u;
    pub16(srdS, vo, S_U, (u32x4){cvtpk(v[0], v[1]), cvtpk(v[2], v[3]), cvtpk(v[4], v[5]), cvtpk(v[6], v[7])});
#pragma unroll
    for (int i = 0; i < 2; ++i) {
      pub16(srdS, vz, S_HZ + i * 1024u, (u32x4){F2U(hb[4 * i]), F2U(hb[4 * i + 1]),
                                                F2U(hb[4 * i + 2]), F2U(hb[4 * i + 3])});
      f32x4 z;
#pragma unroll
      for (int e = 0; e < 4; ++e) z[e] = __builtin_fmaf(g2[4 * i + e], hb[4 * i + e], x2[4 * i + e]);
      pub16(srdS, vz, S_HZ + (2 + i) * 1024u, __builtin_bit_cast(u32x4, z));
    }
#pragma unroll
    for (int i = 0; i < 8; ++i) dma16(lds0 + LB + (8 * w + i) * 1024u, vw, srd2, (8 * w + i) * 8192u + j * 128u);
  }
  const __amdgpu_buffer_rsrc_t srd3 = mksrd(P.w3, 512u * F * 2u);
  const unsigned rc = LC + w * 4096u, rb = LB + w * 8192u;
  const unsigned v3 = (l >> 2) * 3584u + ((l & 3) << 4), s30 = (j * 224u + w * 32u) * 2u;
  float v[8];
  {
    bf16x8 af[8][2];
#pragma unroll
    for (int i = 0; i < 8; ++i)
#pragma unroll
      for (int nt = 0; nt < 2; ++nt) {
        const lds_cptr a = lds + LA + (8 * kq + i) * 2048 + c.afr[nt];
        af[i][nt] = cat8(lds_tr(a), lds_tr(a + 512));
      }
    BAR_L();
    u32x4 xb[8];
    poll_ind<8, 3, 7>(xb, srdS, ((pn * 64 + 16 * kq + c.h) * 64 + prow) * 16u, S_U, 2048u);
    tile_mm8(af, xb, v, lds + LA, rt, kq, l);
#pragma unroll
    for (int k = 0; k < 8; ++k) v[k] = gelu_tanh(v[k]);
    pub16(srdS, ovo, S_H1, (u32x4){cvtpk(v[0], v[1]), cvtpk(v[2], v[3]), cvtpk(v[4], v[5]), cvtpk(v[6], v[7])});
    if (w < 7) {
#pragma unroll
      for (int ks = 0; ks < 4; ++ks) dma16(lds0 + r3off(ks, rc, rb), v3, srd3, s30 + (unsigned)(16 * ks) * 3584u);
    }
  }
  float y[8];
  float cs[16];
  const unsigned cso = w < 7 ? (3072 + 224 * j + 32 * w + 8 * c.h) * 8u : OOB;
  {
    bf16x8 af[8][2];
#pragma unroll
    for (int i = 0; i < 8; ++i)
#pragma unroll
      for (int nt = 0; nt < 2; ++nt) {
        const lds_cptr a = lds + LB + (8 * kq + i) * 2048 + c.afr[nt];
        af[i][nt] = cat8(lds_tr(a), lds_tr(a + 512));
      }
    BAR_L();
    const int sh = kq == 0 ? -1 : kq == 1 ? 1 : kq == 2 ? -2 : 2;
    const int srow = (int)trow + sh;
    const unsigned xvo = (srow >= 0 && srow < T) ? ((((unsigned)srow >> 6) * 64 + 16 * kq + c.h) * 64 + ((unsigned)srow & 63)) * 16u : OOB;
    u32x4 xb[8];
    poll_ind<8, 3, 7>(xb, srdS, xvo, S_H1, 2048u);
    u32x4 hz[4];
    const unsigned hzo = ((pn * 64 + och) * 256 + prow) * 16u;
#pragma unroll
    for (int i = 0; i < 4; ++i) hz[i] = __builtin_amdgcn_raw_buffer_load_b128(srdS, hzo, S_HZ + i * 1024u, 16);
    u32x4 cg[8];
#pragma unroll
    for (int i = 0; i < 8; ++i) cg[i] = __builtin_amdgcn_raw_buffer_load_b128(srdS, cso, W_MOD + 128u * (i >> 2) + 16u * (i & 3), 16);
    tile_mm8(af, xb, v, lds + LA, rt, kq, l);
    if (has_sent(hz, 4)) poll_load<4>(hz, srdS, hzo, S_HZ, 1024u);
    float x1[8];
#pragma unroll
    for (int k = 0; k < 8; ++k) {
      x1[k] = U2F(hz[k >> 2][k & 3]) + v[k];
      y[k] = __builtin_fmaf(gb[k], v[k], U2F(hz[2 + (k >> 2)][k & 3]));
    }
    pub16(srdS, ovo, S_X1, (u32x4){cvtpk(x1[0], x1[1]), cvtpk(x1[2], x1[3]), cvtpk(x1[4], x1[5]), cvtpk(x1[6], x1[7])});
    if (ANY(cso < OOB && bad_tag(cg, 8, tag))) {
      poll_tag<4>(cg, srdS, cso, W_MOD, 16u, tag);
      poll_tag<4>(cg + 4, srdS, cso, W_MOD + 128u, 16u, tag);
    }
#pragma unroll
    for (int i = 0; i < 16; ++i) cs[i] = U2F(cg[i >> 1][2 * (i & 1)]);
    if (w < 7) {
#pragma unroll
      for (int ks = 4; ks < RING3; ++ks) dma16(lds0 + r3off(ks, rc, rb), v3, srd3, s30 + (unsigned)(16 * ks) * 3584u);
    }
  }
  const __amdgpu_buffer_rsrc_t srd4 = mksrd(P.w4, (unsigned)F * 512u * 2u);
  const unsigned s40 = (448u * kq + 8u * rt) * 1024u + j * 128u;
  {
    u32x4 xr[8];
    BAR_L();
    poll_ind<8, 3, 7>(xr, srdS, ((pn * 64 + 8 * w) * 64 + l) * 16u, S_X1, 1024u);
#pragma unroll
    for (int i = 0; i < 8; ++i) *(LAS u32x4*)(lds + LA + ximg(8 * w + i, l)) = xr[i];
    {
      float s0 = 0.f, s1 = 0.f, q0 = 0.f, q1 = 0.f;
#pragma unroll
      for (int i = 0; i < 8; ++i)
#pragma unroll
        for (int k = 0; k < 4; k += 2) {
          s0 = dot2bf(xr[i][k], 0x3F803F80u, s0); s1 = dot2bf(xr[i][k + 1], 0x3F803F80u, s1);
          q0 = dot2bf(xr[i][k], xr[i][k], q0); q1 = dot2bf(xr[i][k + 1], xr[i][k + 1], q1);
        }
      *(LAS f32x2*)(lds + LST + (w * 64 + l) * 8) = (f32x2){s0 + s1, q0 + q1};
    }
    BAR_L();
    if (w < 7) {
      f32x16 acc[2] = {};
      const unsigned a3 = (8 * c.h + c.q) * 64 + 32 * c.blk + 8 * (2 * (c.p & 1) + (c.p >> 1));
      Frag3 f0, f1, f2;
      wait_vm<RING3 - 2>();
      load_frags3<0>(f0, lds, rc, rb, a3, c);
      load_frags3<1>(f1, lds, rc, rb, a3, c);
      float mu[2], rs[2];
#pragma unroll
      for (int r2 = 0; r2 < 2; ++r2) {
        float s = 0.f, q = 0.f;
#pragma unroll
        for (int i = 0; i < 8; ++i) {
          const f32x2 t = *(const LAS f32x2*)(lds + LST + (i * 64 + 32 * r2 + c.r) * 8);
          s += t[0]; q += t[1];
        }
        mu[r2] = s * (1.0f / D);
        rs[r2] = __builtin_amdgcn_rsqf(__builtin_fmaxf(__builtin_fmaf(-mu[r2], mu[r2], q * (1.0f / D)), 0.f) + LN_EPS);
      }
      if (w < 4) __builtin_amdgcn_s_setprio(2); else __builtin_amdgcn_s_setprio(1);
      g3_step<0>(acc, f0, f1, f2, lds, lds0, rc, rb, v3, srd3, s30, a3, c, vw, srd4, s40);
      __builtin_amdgcn_s_setprio(0);
#pragma unroll
      for (int r2 = 0; r2 < 2; ++r2) {
#pragma unroll
        for (int i = 0; i < 16; ++i) acc[r2][i] = gelu_tanh(rs[r2] * __builtin_fmaf(-mu[r2], cs[i], acc[r2][i]));
#pragma unroll
        for (int s2 = 0; s2 < 2; ++s2)
          pub16(srdS, ((pn * 224 + 28 * j + 4 * w + 2 * s2 + c.h) * 64 + 32 * r2 + c.r) * 16u, S_H3, pack8(acc[r2], s2, 1.f));
      }
    } else {
#pragma unroll
      for (int n = 0; n < 8; ++n) dma16(lds0 + r3off(w4slot(n), rc, rb), vw, srd4, s40 + (unsigned)n * 16384u);
    }
    BAR_L();
  }
  {
    auto poff = [&](int ks, unsigned wv) -> unsigned {
      return (ks >= 12 && ks < 20) ? LA + wv * 8192u + (unsigned)(ks - 12) * 1024u : r3off(w4slot(ks % 20), LC + wv * 4096u, LB + wv * 8192u);
    };
    auto w4dma = [&](int ks) { dma16(lds0 + poff(ks, w), vw, srd4, s40 + (unsigned)ks * 16384u); };
    if (w == 7) {
#pragma unroll
      for (int ks = 8; ks < 12; ++ks) w4dma(ks);
    }
#pragma unroll
    for (int ks = 12; ks < 20; ++ks) w4dma(ks);
    const unsigned wsrc = 2 * kq + c.h;
    const unsigned hvo = ((pn * 224 + 56 * kq + c.h) * 64 + prow) * 16u;
    u32x4 ha[14], hb[14];
    poll_ind<14, 9, 13>(ha, srdS, hvo, S_H3, 2048u);
    BAR_L();
    f32x16 acc[2] = {};
    bf16x8 fa[2][2];
    auto ld = [&](int ks) {
#pragma unroll
      for (int nt = 0; nt < 2; ++nt) {
        const lds_cptr a = lds + poff(ks, wsrc) + (c.afr[nt] - 1024u * c.h);
        fa[ks & 1][nt] = cat8(lds_tr(a), lds_tr(a + 512));
      }
    };
    auto mm = [&](int ks, const u32x4& b) {
#pragma unroll
      for (int nt = 0; nt < 2; ++nt) acc[nt] = MFMA32(fa[ks & 1][nt], __builtin_bit_cast(bf16x8, b), acc[nt]);
    };
    auto hbl = [&](int i) { bld16_sc1(hb[i], hvo, srdS, S_H3 + (14u + i) * 2048u); };
    ld(0);
#pragma unroll
    for (int ks = 0; ks < 8; ++ks) {
      if (ks < 7) ld(ks + 1);
      SBAR(); mm(ks, ha[ks]);
      if (ks < 7) { hbl(2 * ks); hbl(2 * ks + 1); }
      SBAR();
    }
    BAR_L();
    ld(8);
#pragma unroll
    for (int ks = 8; ks < 14; ++ks) {
      if (ks < 13) ld(ks + 1);
      SBAR(); mm(ks, ha[ks]);
      if (ks < 12) { w4dma(20 + 2 * (ks - 8)); w4dma(21 + 2 * (ks - 8)); }
      SBAR();
    }
    asm volatile("s_waitcnt vmcnt(8) lgkmcnt(0)\n\ts_barrier" : "+v"(hb[0]), "+v"(hb[1]), "+v"(hb[2]), "+v"(hb[3]), "+v"(hb[4]), "+v"(hb[5]),
                 "+v"(hb[6]), "+v"(hb[7]), "+v"(hb[8]), "+v"(hb[9]), "+v"(hb[10]), "+v"(hb[11]), "+v"(hb[12]), "+v"(hb[13]) :: "memory");
    if (has_sent(hb, 14)) poll_load<14>(hb, srdS, hvo, S_H3 + 14u * 2048u, 2048u);
    ld(14);
#pragma unroll
    for (int ks = 14; ks < 19; ++ks) { ld(ks + 1); SBAR(); mm(ks, hb[ks - 14]); SBAR(); }
    mm(19, hb[5]);
    WAIT_BAR(0);
    ld(20);
#pragma unroll
    for (int ks = 20; ks < 27; ++ks) { ld(ks + 1); SBAR(); mm(ks, hb[ks - 14]); SBAR(); }
    mm(27, hb[13]);
    BAR_L();
    red4_put(acc[0], 0, lds + LA, rt, kq, l);
    red4_put(acc[1], 1, lds + LA, rt, kq, l);
    BAR_L();
    red4_get(v, lds + LA, rt, kq, l);
#pragma unroll
    for (int k = 0; k < 8; ++k) v[k] = __builtin_fmaf(gb[k], v[k], y[k]);
    P.out[(size_t)trow * 64 + och] = (u32x4){cvtpk(v[0], v[1]), cvtpk(v[2], v[3]), cvtpk(v[4], v[5]), cvtpk(v[6], v[7])};
  }
}

__global__ __launch_bounds__(512, 2) void k_fused(Params P) {
  extern __shared__ __attribute__((aligned(16))) char smem[];
  const lds_ptr lds = (lds_ptr)smem;
  const unsigned lds0 = (unsigned)(uintptr_t)smem;
  const unsigned w = rfl(TIDX >> 6);
  const __amdgpu_buffer_rsrc_t srdW = mksrd(P.ws, W_END);
  {
    unsigned long long pc; unsigned len;
    asm volatile("s_getpc_b64 %0\n.Lkf_here:\n\ts_mov_b32 %1, .Lkf_end-.Lkf_here" : "=s"(pc), "=s"(len));
    dma16(lds0 + LDS_R + w * RINGB + 8192u, (TIDX & 63) * 16u, mksrd((const void*)(pc & ~15ull), len & ~15u), ((BIDX >> 3) * 8 + w) * 1024u);
  }
  const unsigned tag = attn_part(P, lds, lds0, w, srdW);
  mlp_part(P, lds, lds0, w, srdW, tag);
  asm volatile(".Lkf_end:");
}

#if defined(__HIP_DEVICE_COMPILE__)
#pragma clang attribute pop
#endif
extern "C" void kernel_launch(void* const* d_in, const int* in_sizes, int n_in, void* d_out, int out_size, void* d_ws,
                              size_t ws_size, hipStream_t stream) {
  Params P;
  P.x = (const unsigned short*)d_in[0];
  P.kc = (const unsigned short*)d_in[1];
  P.vc = (const unsigned short*)d_in[2];
  P.wq = (const unsigned short*)d_in[3];
  P.wo = (const unsigned short*)d_in[4];
  P.w1 = (const unsigned short*)d_in[6];
  P.w2 = (const unsigned short*)d_in[7];
  P.w3 = (const unsigned short*)d_in[8];
  P.w4 = (const unsigned short*)d_in[9];
  P.c = (const unsigned short*)d_in[10];
  P.wmod = (const unsigned short*)d_in[11];
  P.ws = d_ws;
  P.out = (u32x4*)d_out;
  (void)hipFuncSetAttribute((const void*)k_fused, hipFuncAttributeMaxDynamicSharedMemorySize, LDS_TOTAL);
  k_fused<<<256, 512, LDS_TOTAL, stream>>>(P);
}
```
